```python
import torch

def _softmax(x, axis=-1):
    return torch.softmax(x, dim=axis)
def _gelu(x):
    return torch.nn.functional.gelu(x, approximate="tanh")
def _silu(x):
    return torch.nn.functional.silu(x)
def _sigmoid(x):
    return torch.sigmoid(x)
def _layernorm(x, axis=-1, eps=1e-5):
    m = x.mean(dim=axis, keepdim=True)
    v = x.var(dim=axis, keepdim=True, unbiased=False)
    return (x - m) / torch.sqrt(v + eps)
def _scatter_add(tgt, idx, src, axis=0):
    return tgt.clone().index_add_(axis, idx, src)
def _segment_sum(src, seg, n):
    out = torch.zeros((n,) + src.shape[1:], dtype=src.dtype)
    return out.index_add_(0, seg, src)
def _pad(x, pw):
    flat = tuple(v for pair in reversed(pw) for v in pair)
    return torch.nn.functional.pad(x, flat)

kernel_name = 'sequential_preLN__spectral_gfnet__dense_activation_silu__prompt'

def setup_inputs():

    _w = (lambda a: (lambda t: t.to(torch.bfloat16) if t.is_floating_point() else t)(torch.as_tensor(a)))
    def _A_setup():
        B, S, D = 1, 2048, 512
        Df = D // 2 + 1
        return {
            "x": _w((lambda *s: torch.randn(*s, dtype=torch.bfloat16) * 0.05)(B, S, D)),
            "w_r": _w((lambda *s: torch.randn(*s, dtype=torch.bfloat16) * 0.4)(S, Df) * 0.02),
            "w_i": _w((lambda *s: torch.randn(*s, dtype=torch.bfloat16) * 0.4)(S, Df) * 0.02),
        }
    _A_in = _A_setup()

    def _B_setup():
        B, S, D, F = 1, 2048, 512, 1792
        return {
            "x": _w((lambda *s: torch.randn(*s, dtype=torch.bfloat16) * 0.05)(B, S, D)),
            "w1": _w((lambda *s: torch.randn(*s, dtype=torch.bfloat16) * 0.8)(D, F) * (D ** -0.5)),
            "w2": _w((lambda *s: torch.randn(*s, dtype=torch.bfloat16) * 0.1)(F, D) * (F ** -0.5)),
        }
    _B_in = _B_setup()

    return {
        "A_x": _A_in["x"],
        "A_w_r": _A_in["w_r"],
        "A_w_i": _A_in["w_i"],
        "B_x": _B_in["x"],
        "B_w1": _B_in["w1"],
        "B_w2": _B_in["w2"],
    }

def reference(A_x, A_w_r, A_w_i, B_x, B_w1, B_w2):
    def _A_ref(x, w_r, w_i):
        B, S, D = x.shape
        X = (lambda x, axes=(-2,-1): torch.fft.rfft2(x.float(), dim=axes))(x, axes=(-2, -1))
        W = w_r + 1j * w_i
        X = X * W
        y = (lambda x, s, axes=(-2,-1): torch.fft.irfft2(x, s=s, dim=axes).to(torch.bfloat16))(X, (S, D), axes=(-2, -1))
        return y

    def _B_ref(x, w1, w2):
        h = torch.einsum("bsd,df->bsf", x, w1)
        h = _silu(h)
        out = torch.einsum("bsf,fd->bsd", h, w2)
        return out

    x = A_x
    h = x + _A_ref(_layernorm(x, axis=-1), A_w_r, A_w_i)
    h2 = h + _B_ref(_layernorm(h, axis=-1), B_w1, B_w2)
    return h2
```

```cpp
#include <hip/hip_runtime.h>
#include <hip/hip_bf16.h>
#include <stdint.h>

typedef _Float16 h8 __attribute__((ext_vector_type(8)));
typedef _Float16 h4 __attribute__((ext_vector_type(4)));
typedef _Float16 h2 __attribute__((ext_vector_type(2)));
typedef __bf16 b8 __attribute__((ext_vector_type(8)));
typedef __bf16 b2 __attribute__((ext_vector_type(2)));
typedef short s4 __attribute__((ext_vector_type(4)));
typedef float f4 __attribute__((ext_vector_type(4)));
typedef float f2 __attribute__((ext_vector_type(2)));
typedef unsigned u32;
typedef u32 u4 __attribute__((ext_vector_type(4)));
typedef u32 u2 __attribute__((ext_vector_type(2)));
#define LAS __attribute__((address_space(3)))
#define DI __device__ __forceinline__

DI u32 pk_h(float a, float b) { f2 v = {a, b}; h2 r = __builtin_convertvector(v, h2); return __builtin_bit_cast(u32, r); }
DI u32 pk_b(float a, float b) { f2 v = {a, b}; b2 r = __builtin_convertvector(v, b2); return __builtin_bit_cast(u32, r); }
DI float bf_lo(u32 v) { return __builtin_bit_cast(float, v << 16); }
DI float bf_hi(u32 v) { return __builtin_bit_cast(float, v & 0xffff0000u); }
DI float rbf(float a) { return bf_lo(pk_b(a, a)); }
DI float cosr(float rev) { return __builtin_amdgcn_cosf(rev); }
DI float sinr(float rev) { return __builtin_amdgcn_sinf(rev); }
DI f4 mma_h(u4 a, u4 b, f4 c) { return __builtin_amdgcn_mfma_f32_16x16x32_f16(__builtin_bit_cast(h8, a), __builtin_bit_cast(h8, b), c, 0, 0, 0); }
DI f4 mma_b(u4 a, u4 b, f4 c) { return __builtin_amdgcn_mfma_f32_16x16x32_bf16(__builtin_bit_cast(b8, a), __builtin_bit_cast(b8, b), c, 0, 0, 0); }
DI f4 mma16_h(u2 a, u2 b, f4 c) { return __builtin_amdgcn_mfma_f32_16x16x16f16(__builtin_bit_cast(h4, a), __builtin_bit_cast(h4, b), c, 0, 0, 0); }
DI f4 mma16_b(u2 a, u2 b, f4 c) { return __builtin_amdgcn_mfma_f32_16x16x16bf16_1k(__builtin_bit_cast(s4, a), __builtin_bit_cast(s4, b), c, 0, 0, 0); }

template <int CTRL> DI float dpp(float v) {
    return __builtin_bit_cast(float, __builtin_amdgcn_update_dpp(0, __builtin_bit_cast(int, v), CTRL, 0xf, 0xf, true));
}
DI float red_rows(float v) {
    u32 x = __builtin_bit_cast(u32, v), y = x;
    asm("" : "+v"(y));
    auto r = __builtin_amdgcn_permlane16_swap(x, y, false, false);
    v = __builtin_bit_cast(float, (u32)r[0]) + __builtin_bit_cast(float, (u32)r[1]);
    x = __builtin_bit_cast(u32, v); y = x;
    asm("" : "+v"(y));
    auto q = __builtin_amdgcn_permlane32_swap(x, y, false, false);
    return __builtin_bit_cast(float, (u32)q[0]) + __builtin_bit_cast(float, (u32)q[1]);
}
DI float red_all(float v) {
    v += dpp<0xB1>(v); v += dpp<0x4E>(v); v += dpp<0x124>(v); v += dpp<0x128>(v);
    return red_rows(v);
}
DI float red_skip1(float v) {
    v += dpp<0xB1>(v); v += dpp<0x124>(v); v += dpp<0x128>(v);
    return red_rows(v);
}
DI float ln_inv(float var) { return 1.0f / rbf(__builtin_sqrtf(rbf(rbf(var) + 1e-5f))); }

constexpr size_t WS_U = 0;
constexpr size_t WS_V = WS_U + (2u << 20);
constexpr size_t WS_WT = WS_V + (2u << 20);
constexpr size_t WS_WSP = WS_WT + (2u << 20);
constexpr size_t WS_LN2 = WS_WSP + (32u << 10);
constexpr size_t WS_H1 = WS_LN2 + (2u << 20);

DI int xcd_remap(int bid) { return (bid & 7) * 32 + ((bid >> 3) & 31); }

DI void p1_main(int b, const uint16_t* __restrict__ x, u32* __restrict__ U, char* smem) {
    const int tid = threadIdx.x, w = tid >> 6, l = tid & 63, g = l >> 4, m = l & 15;
    const int r = (m >> 1) & 1, c = 2 * (m >> 2) + (m & 1);
    const int s = 256 * (2 * w + r) + b;
    const u32* xr = (const u32*)(x + s * 512) + 8 * g + c;
    u32 ld[8];
#pragma unroll
    for (int j = 0; j < 8; ++j) ld[j] = xr[32 * j];
    u4 FAre, FAim, Fre, Fim, Sre, Sim; u2 Mb;
    float twc[4], tws[4];
    {
        float c0[8], s0[8];
#pragma unroll
        for (int j = 0; j < 8; ++j) {
            float a = (float)(((4 * j + g) * m) & 31) * (1.0f / 32);
            c0[j] = cosr(a); s0[j] = m ? -sinr(a) : ((g & 1) ? -1.0f : 1.0f);
        }
#pragma unroll
        for (int j = 0; j < 4; ++j) { FAre[j] = pk_h(c0[2 * j], c0[2 * j + 1]); FAim[j] = pk_h(s0[2 * j], s0[2 * j + 1]); }
#pragma unroll
        for (int u = 0; u < 4; ++u) {
            const int db = 4 * g + u;
            float a = (float)((db * m) & 15) * (1.0f / 16);
            float cc = cosr(a), ss = sinr(a);
            Fre[u] = pk_h(cc, ss); Fim[u] = pk_h(-ss, cc);
            float a2 = (float)((db * (2 * (m & 7) + 1)) & 31) * (1.0f / 32);
            float c2 = cosr(a2), s2 = sinr(a2);
            float im0 = m ? -ss : ((u & 1) ? -1.0f : 1.0f);
            Sre[u] = m < 8 ? pk_h(cc, 0.f) : pk_h(0.f, c2);
            Sim[u] = m < 8 ? pk_h(im0, 0.f) : pk_h(0.f, -s2);
            float a3 = (float)(db * m) * (1.0f / 512);
            twc[u] = cosr(a3); tws[u] = sinr(a3);
        }
#pragma unroll
        for (int e = 0; e < 2; ++e) {
            float a = (float)(((m >> 1) * (256 * (2 * g + e) + b)) & 2047) * (1.0f / 2048);
            float cc = cosr(a), ss = sinr(a);
            Mb[e] = (m & 1) ? pk_h(-ss, cc) : pk_h(cc, ss);
        }
    }
    float v[8][2];
    float sum = 0.f;
#pragma unroll
    for (int j = 0; j < 8; ++j) { v[j][0] = bf_lo(ld[j]); v[j][1] = bf_hi(ld[j]); sum += v[j][0] + v[j][1]; }
    const float mean = red_skip1(sum) * (1.0f / 512);
    float sq = 0.f;
#pragma unroll
    for (int j = 0; j < 8; ++j) { float d0 = v[j][0] - mean, d1 = v[j][1] - mean; sq += d0 * d0 + d1 * d1; }
    const float inv = ln_inv(red_skip1(sq) * (1.0f / 512));
    const float mb = rbf(mean);
#pragma unroll
    for (int j = 0; j < 8; ++j) {
        u32 d = pk_b(v[j][0] - mb, v[j][1] - mb);
        u32 q = pk_b(bf_lo(d) * inv, bf_hi(d) * inv);
        v[j][0] = bf_lo(q); v[j][1] = bf_hi(q);
    }
    f4 Dre[2], Dim[2];
#pragma unroll
    for (int p = 0; p < 2; ++p) {
        u4 A;
#pragma unroll
        for (int j = 0; j < 4; ++j) A[j] = pk_h(v[2 * j][p], v[2 * j + 1][p]);
        Dre[p] = mma_h(A, FAre, (f4){0, 0, 0, 0});
        Dim[p] = mma_h(A, FAim, (f4){0, 0, 0, 0});
    }
#pragma unroll
    for (int rr = 0; rr < 2; ++rr) {
        u4 B;
#pragma unroll
        for (int u = 0; u < 4; ++u) {
            float re = Dre[u & 1][2 * rr + (u >> 1)], im = Dim[u & 1][2 * rr + (u >> 1)];
            B[u] = pk_h(re * twc[u] + im * tws[u], im * twc[u] - re * tws[u]);
        }
        f4 Gre = mma_h(Fre, B, (f4){0, 0, 0, 0}), Gim = mma_h(Fim, B, (f4){0, 0, 0, 0});
        f4 Hre = mma_h(Sre, B, (f4){0, 0, 0, 0}), Him = mma_h(Sim, B, (f4){0, 0, 0, 0});
        u4 Z;
#pragma unroll
        for (int i = 0; i < 4; ++i) Z[i] = m ? pk_h(Gre[i], Gim[i]) : pk_h(Hre[i], Him[i]);
        *(u4*)(smem + (2 * w + rr) * 1024 + m * 64 + g * 16) = Z;
    }
    __syncthreads();
    u4 r0 = *(const u4*)(smem + (2 * g) * 1024 + w * 256 + m * 16);
    u4 r1 = *(const u4*)(smem + (2 * g + 1) * 1024 + w * 256 + m * 16);
#pragma unroll
    for (int t = 0; t < 4; ++t) {
        f4 D = mma16_h(Mb, (u2){r0[t], r1[t]}, (f4){0, 0, 0, 0});
        const int col = 64 * w + 4 * m + t;
        *(u2*)(U + col * 2048 + b * 8 + 2 * g) = (u2){pk_h(D[0], D[1]), pk_h(D[2], D[3])};
    }
}

DI void p1_wt(int jb, const uint16_t* __restrict__ wr, const uint16_t* __restrict__ wi, u32* __restrict__ Wt,
              f4* __restrict__ Wsp, char* smem) {
    const int tid = threadIdx.x;
    constexpr int ARR = 547 * 16;
    for (int it = 0; it < 5; ++it) {
        int ch = tid + 256 * it;
        if (ch < 2 * 547) {
            int arr = ch >= 547; int cc = ch - arr * 547;
            int blk = cc < 257 ? jb : (cc < 514 ? 255 - jb : ((256 - jb) & 255));
            int off = cc < 257 ? cc : (cc < 514 ? cc - 257 : cc - 514);
            const u4* src = (const u4*)(arr ? wi : wr) + blk * 257 + off;
            *(u4*)(smem + arr * ARR + cc * 16) = *src;
        }
    }
    __syncthreads();
    const uint16_t* sr = (const uint16_t*)smem;
    const uint16_t* si = (const uint16_t*)(smem + ARR);
    const int j = tid >> 4, n = tid & 15, col = 16 * n + j;
    const int k = n ? n + 32 * j : (j < 8 ? 32 * j : 16 + 32 * (j - 8));
    const bool mir = k > 256;
    u32 o[8];
#pragma unroll
    for (int i = 0; i < 8; ++i) {
        int e = mir ? ((i ? 2056 + (8 - i) * 257 : 4112) + 512 - k) : (i * 257 + k);
        u32 re = sr[e], im = si[e];
        if (mir) im ^= 0x8000u;
        o[i] = re | (im << 16);
    }
    u4* dst = (u4*)(Wt + col * 2048 + 8 * jb);
    dst[0] = (u4){o[0], o[1], o[2], o[3]};
    dst[1] = (u4){o[4], o[5], o[6], o[7]};
    if (tid < 8) {
        const int i = tid, ed = i * 257, em = i ? 2056 + (8 - i) * 257 : 4112;
        float ar = 0.5f * (bf_lo(sr[ed]) + bf_lo(sr[em])), ai = 0.5f * (bf_lo(si[ed]) - bf_lo(si[em]));
        float br = 0.5f * (bf_lo(sr[ed + 256]) + bf_lo(sr[em + 256])), bi = 0.5f * (bf_lo(si[ed + 256]) - bf_lo(si[em + 256]));
        Wsp[8 * jb + i] = (f4){0.5f * (ar + br), 0.5f * (ai + bi), 0.5f * (ar - br), 0.5f * (ai - bi)};
    }
}

__global__ __launch_bounds__(256) void k_p1(const uint16_t* __restrict__ x, const uint16_t* __restrict__ wr,
                                            const uint16_t* __restrict__ wi, char* __restrict__ ws) {
    __shared__ __attribute__((aligned(16))) char smem[2 * 547 * 16];
    if (blockIdx.x < 256) p1_main(xcd_remap(blockIdx.x), x, (u32*)(ws + WS_U), smem);
    else p1_wt(xcd_remap(blockIdx.x - 256), wr, wi, (u32*)(ws + WS_WT), (f4*)(ws + WS_WSP), smem);
}

DI void p2_main(int col, const u32* __restrict__ U, const u32* __restrict__ Wt, const f4* __restrict__ Wsp,
                u32* __restrict__ V, char* smem) {
    const int tid = threadIdx.x, w = tid >> 6, l = tid & 63, g = l >> 4, m = l & 15;
    const int base = col * 2048 + (64 * g + m) * 8 + 2 * w;
    u2 ld[4], wl[4];
#pragma unroll
    for (int q = 0; q < 4; ++q) ld[q] = *(const u2*)(U + base + 128 * q);
#pragma unroll
    for (int q = 0; q < 4; ++q) wl[q] = *(const u2*)(Wt + base + 128 * q);
    u4 Fre, Fim, Gre, Gim; float tc[4], ts[4];
#pragma unroll
    for (int u = 0; u < 4; ++u) {
        const int e = (4 * g + u) * m;
        float a = (float)(e & 15) * (1.0f / 16);
        float cc = cosr(a), ss = sinr(a);
        Fre[u] = pk_h(0.25f * cc, 0.25f * ss); Fim[u] = pk_h(-0.25f * ss, 0.25f * cc);
        Gre[u] = pk_b(cc * (1.0f / 256), -ss * (1.0f / 256)); Gim[u] = pk_b(ss * (1.0f / 256), cc * (1.0f / 256));
        float a3 = (float)e * (1.0f / 256);
        tc[u] = cosr(a3); ts[u] = sinr(a3);
    }
    f4 Xre[2], Xim[2];
#pragma unroll
    for (int e = 0; e < 2; ++e) {
        u4 A = {ld[0][e], ld[1][e], ld[2][e], ld[3][e]};
        f4 Dre = mma_h(A, Fre, (f4){0, 0, 0, 0}), Dim = mma_h(A, Fim, (f4){0, 0, 0, 0});
        u4 B;
#pragma unroll
        for (int i = 0; i < 4; ++i) B[i] = pk_h(Dre[i] * tc[i] + Dim[i] * ts[i], Dim[i] * tc[i] - Dre[i] * ts[i]);
        Xre[e] = mma_h(Fre, B, (f4){0, 0, 0, 0}); Xim[e] = mma_h(Fim, B, (f4){0, 0, 0, 0});
    }
    u4 Y[2];
    if (col != 0) {
#pragma unroll
        for (int e = 0; e < 2; ++e)
#pragma unroll
            for (int i = 0; i < 4; ++i) {
                float wr = bf_lo(wl[i][e]), wi = bf_hi(wl[i][e]);
                Y[e][i] = pk_b(Xre[e][i] * wr - Xim[e][i] * wi, Xre[e][i] * wi + Xim[e][i] * wr);
            }
    } else {
        f2* sx = (f2*)smem;
#pragma unroll
        for (int e = 0; e < 2; ++e)
#pragma unroll
            for (int i = 0; i < 4; ++i) sx[2 * w + e + 8 * (m + 16 * (4 * g + i))] = (f2){Xre[e][i], Xim[e][i]};
        __syncthreads();
#pragma unroll
        for (int e = 0; e < 2; ++e)
#pragma unroll
            for (int i = 0; i < 4; ++i) {
                const int f = 2 * w + e + 8 * (m + 16 * (4 * g + i));
                f2 xm = sx[(2048 - f) & 2047];
                f4 ww = Wsp[f];
                float yr = Xre[e][i] * ww[0] - Xim[e][i] * ww[1] + xm[0] * ww[2] + xm[1] * ww[3];
                float yi = Xre[e][i] * ww[1] + Xim[e][i] * ww[0] + xm[0] * ww[3] - xm[1] * ww[2];
                Y[e][i] = pk_b(yr, yi);
            }
    }
    u2 o[4];
#pragma unroll
    for (int e = 0; e < 2; ++e) {
        f4 Dre = mma_b(Y[e], Gre, (f4){0, 0, 0, 0}), Dim = mma_b(Y[e], Gim, (f4){0, 0, 0, 0});
        u4 B;
#pragma unroll
        for (int i = 0; i < 4; ++i) B[i] = pk_b(Dre[i] * tc[i] - Dim[i] * ts[i], Dim[i] * tc[i] + Dre[i] * ts[i]);
        f4 Ere = mma_b(Gre, B, (f4){0, 0, 0, 0}), Eim = mma_b(Gim, B, (f4){0, 0, 0, 0});
#pragma unroll
        for (int i = 0; i < 4; ++i) o[i][e] = pk_b(Ere[i], Eim[i]);
    }
#pragma unroll
    for (int q = 0; q < 4; ++q) *(u2*)(V + base + 128 * q) = o[q];
}

__global__ __launch_bounds__(256) void k_p2(char* __restrict__ ws) {
    __shared__ __attribute__((aligned(16))) char smem[16384];
    p2_main(blockIdx.x, (const u32*)(ws + WS_U), (const u32*)(ws + WS_WT), (const f4*)(ws + WS_WSP), (u32*)(ws + WS_V), smem);
}

DI void p3_main(int b, const u32* __restrict__ V, const uint16_t* __restrict__ x, uint16_t* __restrict__ hout,
                char* __restrict__ ln2, char* smem) {
    const int tid = threadIdx.x, w = tid >> 6, l = tid & 63, g = l >> 4, m = l & 15;
    u2 ld[4];
#pragma unroll
    for (int t = 0; t < 4; ++t) ld[t] = *(const u2*)(V + (64 * w + 4 * m + t) * 2048 + b * 8 + 2 * g);
    u2 xl[2][2];
#pragma unroll
    for (int r = 0; r < 2; ++r)
#pragma unroll
        for (int t = 0; t < 2; ++t) xl[r][t] = *(const u2*)(x + (256 * (2 * w + r) + b) * 512 + 256 * t + 16 * m + 4 * g);
    u2 Nb; u4 Gre, Gim, SGre, SGim, GA[2]; float twc[4], tws[4];
#pragma unroll
    for (int e = 0; e < 2; ++e) {
        float a = (float)(((2 * g + e) * (256 * (m >> 1) + b)) & 2047) * (1.0f / 2048);
        float cc = cosr(a), ss = sinr(a);
        Nb[e] = (m & 1) ? pk_b(ss, cc) : pk_b(cc, -ss);
    }
#pragma unroll
    for (int u = 0; u < 4; ++u) {
        const int k = 4 * g + u;
        float a = (float)((k * m) & 15) * (1.0f / 16);
        float cc = cosr(a), ss = sinr(a);
        Gre[u] = pk_b(cc, -ss); Gim[u] = pk_b(ss, cc);
        float a2 = (float)((m * (2 * (k & 7) + 1)) & 31) * (1.0f / 32);
        float c2 = cosr(a2), s2 = sinr(a2);
        SGre[u] = k == 0 ? pk_b(1.0f, (m & 1) ? -1.0f : 1.0f) : (k < 8 ? pk_b(2.f * cc, -2.f * ss) : 0u);
        SGim[u] = k < 8 ? 0u : pk_b(2.f * c2, -2.f * s2);
        float a3 = (float)(k * m) * (1.0f / 512);
        twc[u] = cosr(a3); tws[u] = sinr(a3);
#pragma unroll
        for (int t = 0; t < 2; ++t) {
            float a4 = (float)((k * (16 * t + m)) & 31) * (1.0f / 32);
            GA[t][u] = k == 0 ? pk_b(1.0f, (m & 1) ? -1.0f : 1.0f) : pk_b(2.f * cosr(a4), -2.f * sinr(a4));
        }
    }
    u4 z0, z1;
#pragma unroll
    for (int t = 0; t < 4; ++t) {
        f4 D = mma16_b(Nb, ld[t], (f4){0, 0, 0, 0});
        z0[t] = pk_b(D[0], D[1]); z1[t] = pk_b(D[2], D[3]);
    }
    *(u4*)(smem + (2 * g) * 1024 + w * 256 + m * 16) = z0;
    *(u4*)(smem + (2 * g + 1) * 1024 + w * 256 + m * 16) = z1;
    __syncthreads();
    float h[2][8];
#pragma unroll
    for (int r = 0; r < 2; ++r) {
        u4 A = *(const u4*)(smem + (2 * w + r) * 1024 + m * 64 + g * 16);
        f4 Dre = mma_b(A, Gre, (f4){0, 0, 0, 0}), Dim = mma_b(A, Gim, (f4){0, 0, 0, 0});
        f4 Hre = mma_b(A, SGre, (f4){0, 0, 0, 0}), Him = mma_b(A, SGim, (f4){0, 0, 0, 0});
        if (g == 0) { Dre[0] = Hre[0]; Dim[0] = Him[0]; }
        u4 A2;
#pragma unroll
        for (int i = 0; i < 4; ++i) A2[i] = pk_b(Dre[i] * twc[i] - Dim[i] * tws[i], Dim[i] * twc[i] + Dre[i] * tws[i]);
#pragma unroll
        for (int t = 0; t < 2; ++t) {
            f4 Yv = mma_b(A2, GA[t], (f4){0, 0, 0, 0});
            u32 y01 = pk_b(Yv[0], Yv[1]), y23 = pk_b(Yv[2], Yv[3]);
            u32 h01 = pk_b(bf_lo(xl[r][t][0]) + bf_lo(y01), bf_hi(xl[r][t][0]) + bf_hi(y01));
            u32 h23 = pk_b(bf_lo(xl[r][t][1]) + bf_lo(y23), bf_hi(xl[r][t][1]) + bf_hi(y23));
            *(u2*)(hout + (256 * (2 * w + r) + b) * 512 + 256 * t + 16 * m + 4 * g) = (u2){h01, h23};
            h[r][4 * t] = bf_lo(h01); h[r][4 * t + 1] = bf_hi(h01); h[r][4 * t + 2] = bf_lo(h23); h[r][4 * t + 3] = bf_hi(h23);
        }
    }
    float mean[2], inv[2];
#pragma unroll
    for (int r = 0; r < 2; ++r) {
        float sum = 0.f;
#pragma unroll
        for (int i = 0; i < 8; ++i) sum += h[r][i];
        mean[r] = red_all(sum) * (1.0f / 512);
    }
#pragma unroll
    for (int r = 0; r < 2; ++r) {
        float sq = 0.f;
#pragma unroll
        for (int i = 0; i < 8; ++i) { float d = h[r][i] - mean[r]; sq += d * d; }
        inv[r] = ln_inv(red_all(sq) * (1.0f / 512));
    }
#pragma unroll
    for (int r = 0; r < 2; ++r) {
        const float mb = rbf(mean[r]);
        const int rho = 8 * b + 2 * w + r;
#pragma unroll
        for (int t = 0; t < 2; ++t) {
            u32 d01 = pk_b(h[r][4 * t] - mb, h[r][4 * t + 1] - mb), d23 = pk_b(h[r][4 * t + 2] - mb, h[r][4 * t + 3] - mb);
            u2 o = {pk_b(bf_lo(d01) * inv[r], bf_hi(d01) * inv[r]), pk_b(bf_lo(d23) * inv[r], bf_hi(d23) * inv[r])};
            *(u2*)(ln2 + (rho >> 4) * 16384 + (8 * t + (m >> 1)) * 1024 + (g * 16 + (rho & 15)) * 16 + (m & 1) * 8) = o;
        }
    }
}

__global__ __launch_bounds__(256) void k_p3(const uint16_t* __restrict__ x, uint16_t* __restrict__ out, char* __restrict__ ws) {
    __shared__ __attribute__((aligned(16))) char smem[8192];
    p3_main(xcd_remap(blockIdx.x), (const u32*)(ws + WS_V), x, out, ws + WS_LN2, smem);
}

DI void glds16(const void* gsrc, u32 lds_dst) {
    u32 keep;
    asm volatile("s_mov_b32 %0, m0\n\ts_mov_b32 m0, %2\n\ts_nop 0\n\tglobal_load_lds_dwordx4 %1, off\n\ts_mov_b32 m0, %0"
                 : "=&s"(keep) : "v"(gsrc), "s"(lds_dst) : "memory");
}
DI float silu(float a) { return a * __builtin_amdgcn_rcpf(1.0f + __builtin_amdgcn_exp2f(-1.4426950408889634f * a)); }

__global__ __launch_bounds__(256, 1) void k_g1(const uint16_t* __restrict__ w1, char* __restrict__ ws) {
    extern __shared__ __attribute__((aligned(16))) char smem[];
    const int tid = threadIdx.x, l = tid & 63, g = l >> 4, m = l & 15;
    const int w = __builtin_amdgcn_readfirstlane(tid >> 6);
    const int xcd = blockIdx.x & 7, idx = blockIdx.x >> 3;
    const int mt = (xcd >> 1) * 4 + (idx >> 3), nt = (xcd & 1) * 8 + (idx & 7);
    const u32 lbase = (u32)(uintptr_t)(LAS char*)smem;
    {
        const char* wsrc = (const char*)w1 + nt * 224;
#pragma unroll
        for (int i = 0; i < 28; ++i) {
            const int inst = 4 * i + w, sl = inst * 64 + l, row = sl / 14, c = sl - row * 14;
            glds16(wsrc + row * 3584 + c * 16, lbase + inst * 1024);
        }
    }
    const u4* tok = (const u4*)(ws + WS_LN2) + (mt * 8 + 2 * w) * 1024 + l;
    u4 T[2][16];
#pragma unroll
    for (int kk = 0; kk < 16; ++kk)
#pragma unroll
        for (int tt = 0; tt < 2; ++tt) T[tt][kk] = tok[tt * 1024 + kk * 64];
    asm volatile("s_waitcnt vmcnt(32)" ::: "memory");
    __builtin_amdgcn_s_barrier();
    asm volatile("" ::: "memory");
    f4 acc[2][7];
#pragma unroll
    for (int tt = 0; tt < 2; ++tt)
#pragma unroll
        for (int ft = 0; ft < 7; ++ft) acc[tt][ft] = (f4){0, 0, 0, 0};
    LAS char* wb = (LAS char*)smem + (4 * g + (m >> 2)) * 224 + (m & 3) * 8;
#pragma unroll
    for (int kk = 0; kk < 16; ++kk) {
#pragma unroll
        for (int ft = 0; ft < 7; ++ft) {
            s4 a0 = __builtin_amdgcn_ds_read_tr16_b64_v4i16((LAS s4*)(wb + kk * 32 * 224 + ft * 32));
            s4 a1 = __builtin_amdgcn_ds_read_tr16_b64_v4i16((LAS s4*)(wb + (kk * 32 + 16) * 224 + ft * 32));
            u2 x0 = __builtin_bit_cast(u2, a0), x1 = __builtin_bit_cast(u2, a1);
            u4 A = {x0[0], x0[1], x1[0], x1[1]};
#pragma unroll
            for (int tt = 0; tt < 2; ++tt) acc[tt][ft] = mma_b(A, T[tt][kk], acc[tt][ft]);
        }
    }
    char* h1 = ws + WS_H1;
#pragma unroll
    for (int tt = 0; tt < 2; ++tt) {
        u2 o[7];
#pragma unroll
        for (int ft = 0; ft < 7; ++ft) {
            u32 p0 = pk_b(acc[tt][ft][0], acc[tt][ft][1]), p1 = pk_b(acc[tt][ft][2], acc[tt][ft][3]);
            o[ft] = (u2){pk_b(silu(bf_lo(p0)), silu(bf_hi(p0))), pk_b(silu(bf_lo(p1)), silu(bf_hi(p1)))};
        }
        char* dst = h1 + (size_t)(mt * 8 + 2 * w + tt) * (56 * 1024) + (nt >> 1) * 7 * 1024 + l * 16;
#pragma unroll
        for (int q = 0; q < 3; ++q)
            *(u4*)(dst + ((nt & 1) * 3 + q) * 1024) = (u4){o[2 * q][0], o[2 * q][1], o[2 * q + 1][0], o[2 * q + 1][1]};
        *(u2*)(dst + 6 * 1024 + (nt & 1) * 8) = o[6];
    }
}

__global__ __launch_bounds__(256, 1) void k_g2(const uint16_t* __restrict__ w2, uint16_t* __restrict__ out, char* __restrict__ ws) {
    __shared__ __attribute__((aligned(16))) char smem[65536];
    const int tid = threadIdx.x, l = tid & 63, g = l >> 4, m = l & 15;
    const int w = __builtin_amdgcn_readfirstlane(tid >> 6);
    const int xcd = blockIdx.x & 7, idx = blockIdx.x >> 3;
    const int mt = xcd * 4 + (idx >> 3), nt = idx & 7;
    const u4* tok = (const u4*)(ws + WS_H1) + (size_t)(mt * 4) * (56 * 64) + w * 14 * 64 + l;
    const uint16_t* wb = w2 + (w * 448 + 4 * g) * 512 + nt * 64 + 4 * m;
    const int rho = mt * 64 + w * 16 + m, s = 256 * (rho & 7) + (rho >> 3);
    uint16_t* op = out + s * 512 + nt * 64 + 16 * g;
    const u4 hres0 = *(const u4*)op, hres1 = *(const u4*)(op + 8);
    f4 acc[4][4];
#pragma unroll
    for (int tt = 0; tt < 4; ++tt)
#pragma unroll
        for (int t = 0; t < 4; ++t) acc[tt][t] = (f4){0, 0, 0, 0};
#pragma unroll
    for (int ks = 0; ks < 14; ++ks) {
        const int P = ks / 7, q = ks % 7;
        const int r0 = 224 * P + (q < 3 ? 32 * q : (q < 6 ? 112 + 32 * (q - 3) : 96));
        const int r1 = r0 + (q < 6 ? 16 : 112);
        u2 wv[8];
#pragma unroll
        for (int j = 0; j < 8; ++j) wv[j] = *(const u2*)(wb + ((j < 4 ? r0 : r1) + (j & 3)) * 512);
        u4 T[4];
#pragma unroll
        for (int tt = 0; tt < 4; ++tt) T[tt] = tok[tt * 56 * 64 + ks * 64];
#pragma unroll
        for (int t = 0; t < 4; ++t) {
            u4 A;
#pragma unroll
            for (int jj = 0; jj < 4; ++jj)
                A[jj] = __builtin_amdgcn_perm(wv[2 * jj + 1][t >> 1], wv[2 * jj][t >> 1], (t & 1) ? 0x07060302u : 0x05040100u);
#pragma unroll
            for (int tt = 0; tt < 4; ++tt) acc[tt][t] = mma_b(A, T[tt], acc[tt][t]);
        }
    }
#pragma unroll
    for (int tt = 0; tt < 4; ++tt)
#pragma unroll
        for (int t = 0; t < 4; ++t) *(f4*)(smem + ((w * 4 + tt) * 4 + t) * 1024 + l * 16) = acc[tt][t];
    __syncthreads();
    f4 sum[4];
#pragma unroll
    for (int t = 0; t < 4; ++t) {
        sum[t] = *(const f4*)(smem + ((0 * 4 + w) * 4 + t) * 1024 + l * 16);
#pragma unroll
        for (int ww = 1; ww < 4; ++ww) sum[t] += *(const f4*)(smem + ((ww * 4 + w) * 4 + t) * 1024 + l * 16);
    }
    u32 o[8];
#pragma unroll
    for (int i = 0; i < 4; ++i) {
        u32 p0 = pk_b(sum[0][i], sum[1][i]), p1 = pk_b(sum[2][i], sum[3][i]);
        u32 h0 = i < 2 ? hres0[2 * i] : hres1[2 * i - 4], h1 = i < 2 ? hres0[2 * i + 1] : hres1[2 * i - 3];
        o[2 * i] = pk_b(bf_lo(h0) + bf_lo(p0), bf_hi(h0) + bf_hi(p0));
        o[2 * i + 1] = pk_b(bf_lo(h1) + bf_lo(p1), bf_hi(h1) + bf_hi(p1));
    }
    *(u4*)op = (u4){o[0], o[1], o[2], o[3]};
    *(u4*)(op + 8) = (u4){o[4], o[5], o[6], o[7]};
}

extern "C" void kernel_launch(void* const* d_in, const int* in_sizes, int n_in, void* d_out, int out_size,
                              void* d_ws, size_t ws_size, hipStream_t stream) {
    const uint16_t* x = (const uint16_t*)d_in[0];
    const uint16_t* wr = (const uint16_t*)d_in[1];
    const uint16_t* wi = (const uint16_t*)d_in[2];
    const uint16_t* w1 = (const uint16_t*)d_in[4];
    const uint16_t* w2 = (const uint16_t*)d_in[5];
    uint16_t* out = (uint16_t*)d_out;
    char* ws = (char*)d_ws;
    (void)hipFuncSetAttribute((const void*)k_g1, hipFuncAttributeMaxDynamicSharedMemorySize, 114688);
    k_p1<<<512, 256, 0, stream>>>(x, wr, wi, ws);
    k_p2<<<256, 256, 0, stream>>>(ws);
    k_p3<<<256, 256, 0, stream>>>(x, out, ws);
    k_g1<<<256, 256, 114688, stream>>>(w1, ws);
    k_g2<<<256, 256, 0, stream>>>(w2, out, ws);
}
```

```cpp
#include <hip/hip_runtime.h>
#include <hip/hip_bf16.h>
#include <stdint.h>

typedef _Float16 h8 __attribute__((ext_vector_type(8)));
typedef _Float16 h4 __attribute__((ext_vector_type(4)));
typedef _Float16 h2 __attribute__((ext_vector_type(2)));
typedef __bf16 b8 __attribute__((ext_vector_type(8)));
typedef __bf16 b2 __attribute__((ext_vector_type(2)));
typedef short s4 __attribute__((ext_vector_type(4)));
typedef float f4 __attribute__((ext_vector_type(4)));
typedef float f2 __attribute__((ext_vector_type(2)));
typedef unsigned u32;
typedef u32 u4 __attribute__((ext_vector_type(4)));
typedef u32 u2 __attribute__((ext_vector_type(2)));
#define LAS __attribute__((address_space(3)))
#define GAS __attribute__((address_space(1)))
#define DI __device__ __forceinline__
#define FENCE() asm volatile("" ::: "memory")
#define PIN(x) asm volatile("" : "+v"(x))
#define SCHED() __builtin_amdgcn_sched_barrier(0)
#define WAITV(n) asm volatile("s_waitcnt vmcnt(%0)" :: "n"(n) : "memory")
#define BARRIER() do { asm volatile("s_waitcnt lgkmcnt(0)" ::: "memory"); __builtin_amdgcn_s_barrier(); FENCE(); } while (0)
constexpr int SC1 = 16;
constexpr u32 SENT = 0xffffffffu;
constexpr int SPIN_MAX = 1 << 20;

DI u32 pk_h(float a, float b) { f2 v = {a, b}; h2 r = __builtin_convertvector(v, h2); return __builtin_bit_cast(u32, r); }
DI u32 pk_b(float a, float b) { f2 v = {a, b}; b2 r = __builtin_convertvector(v, b2); return __builtin_bit_cast(u32, r); }
DI float bf_lo(u32 v) { return __builtin_bit_cast(float, v << 16); }
DI float bf_hi(u32 v) { return __builtin_bit_cast(float, v & 0xffff0000u); }
DI f2 bf_2(u32 v) { return (f2){bf_lo(v), bf_hi(v)}; }
DI u32 pk_b2(f2 v) { return pk_b(v[0], v[1]); }
DI float rbf(float a) { return bf_lo(pk_b(a, a)); }
DI float cosr(float rev) { return __builtin_amdgcn_cosf(rev); }
DI float sinr(float rev) { return __builtin_amdgcn_sinf(rev); }
DI f2 lo2(f4 v) { return __builtin_shufflevector(v, v, 0, 1); }
DI f2 hi2(f4 v) { return __builtin_shufflevector(v, v, 2, 3); }
DI f2 fma2(f2 a, f2 b, f2 c) { return __builtin_elementwise_fma(a, b, c); }
DI u32 umin(u32 a, u32 b) { return a < b ? a : b; }
DI u32 umax(u32 a, u32 b) { return a > b ? a : b; }
DI u32 clean(u32 v) { return umin(v, SENT - 1); }
DI u32 max4(u4 v) { return umax(umax(umax(v[0], v[1]), v[2]), v[3]); }
#define Z4 ((f4){0, 0, 0, 0})
DI f4 mma_h(u4 a, u4 b, f4 c) { return __builtin_amdgcn_mfma_f32_16x16x32_f16(__builtin_bit_cast(h8, a), __builtin_bit_cast(h8, b), c, 0, 0, 0); }
DI f4 mma_b(u4 a, u4 b, f4 c) { return __builtin_amdgcn_mfma_f32_16x16x32_bf16(__builtin_bit_cast(b8, a), __builtin_bit_cast(b8, b), c, 0, 0, 0); }
DI f4 mma16_h(u2 a, u2 b, f4 c) { return __builtin_amdgcn_mfma_f32_16x16x16f16(__builtin_bit_cast(h4, a), __builtin_bit_cast(h4, b), c, 0, 0, 0); }
typedef __amdgpu_buffer_rsrc_t rsrc_t;
DI rsrc_t mkrs(const void* p, u32 bytes) { return __builtin_amdgcn_make_buffer_rsrc((void*)p, 0, bytes, 0x00020000); }
template <int AUX> DI u4 bld4(rsrc_t r, int v, int s) { return __builtin_amdgcn_raw_buffer_load_b128(r, v, s, AUX); }
template <int AUX> DI u2 bld2(rsrc_t r, int v, int s) { return __builtin_amdgcn_raw_buffer_load_b64(r, v, s, AUX); }
template <int AUX> DI void bst4(u4 d, rsrc_t r, int v, int s) { __builtin_amdgcn_raw_buffer_store_b128(d, r, v, s, AUX); }
template <int AUX> DI void bst2(u2 d, rsrc_t r, int v, int s) { __builtin_amdgcn_raw_buffer_store_b64(d, r, v, s, AUX); }

template <int CTRL> DI float dpp(float v) {
    return __builtin_bit_cast(float, __builtin_amdgcn_update_dpp(0, __builtin_bit_cast(int, v), CTRL, 0xf, 0xf, true));
}
DI float red_all(float v) {
    v += dpp<0xB1>(v); v += dpp<0x4E>(v); v += dpp<0x124>(v); v += dpp<0x128>(v);
    u32 x = __builtin_bit_cast(u32, v), y = x;
    asm("" : "+v"(y));
    auto r = __builtin_amdgcn_permlane16_swap(x, y, false, false);
    v = __builtin_bit_cast(float, (u32)r[0]) + __builtin_bit_cast(float, (u32)r[1]);
    x = __builtin_bit_cast(u32, v); y = x;
    asm("" : "+v"(y));
    auto q = __builtin_amdgcn_permlane32_swap(x, y, false, false);
    return __builtin_bit_cast(float, (u32)q[0]) + __builtin_bit_cast(float, (u32)q[1]);
}
DI void layernorm8(const f2 (&v)[4], u32 (&o)[4]) {
    f2 s2 = (v[0] + v[1]) + (v[2] + v[3]);
    const float mean = red_all(s2[0] + s2[1]) * (1.0f / 512);
    f2 q2 = {0.f, 0.f};
#pragma unroll
    for (int j = 0; j < 4; ++j) { f2 d = v[j] - mean; q2 = fma2(d, d, q2); }
    const float var = red_all(q2[0] + q2[1]) * (1.0f / 512);
    const float inv = __builtin_amdgcn_rcpf(rbf(__builtin_amdgcn_sqrtf(rbf(rbf(var) + 1e-5f))));
    const float mb = rbf(mean);
#pragma unroll
    for (int j = 0; j < 4; ++j) o[j] = pk_b2(bf_2(pk_b2(v[j] - mb)) * inv);
}

#define ST_DECL
#define ST(i)
#define ST_END(ws, kid)

constexpr u32 WS_U = 0;
constexpr u32 WS_WT = WS_U + (2u << 20);
constexpr u32 WS_V = WS_WT + (2u << 20);
constexpr u32 WS_LN2 = WS_V + (2u << 20);
constexpr u32 WS_W2T = WS_LN2 + (2u << 20);
constexpr u32 WS_WSP = WS_W2T + 1835008u;
constexpr u32 WS_FLAG = WS_WSP + (32u << 10);
constexpr u32 WS_H1 = WS_W2T + (2u << 20);
constexpr u32 WS_END = WS_H1 + 7340032u;

DI int xcd_remap(int bid) { return (bid & 7) * 32 + ((bid >> 3) & 31); }

DI void p1_main(int b, const uint16_t* __restrict__ x, char* __restrict__ ws, char* smem) {
    const int tid = threadIdx.x, w = tid >> 6, l = tid & 63, g = l >> 4, m = l & 15;
    const int hh = (m >> 1) & 1, c = 2 * (m >> 2) + (m & 1);
    const u32* xr = (const u32*)(x + (256 * w + b) * 512) + 128 * hh + 8 * g + c;
    u32 ld[4];
#pragma unroll
    for (int j = 0; j < 4; ++j) ld[j] = xr[32 * j];
    *(u4*)(ws + WS_V + b * 8192 + tid * 16) = (u4){SENT, SENT, SENT, SENT};
    if (b < 3) *(u4*)(ws + WS_FLAG + b * 8192 + tid * 16) = (u4){0, 0, 0, 0};
    FENCE();
    u2 FAre, FAim, Mb; u4 Fre, Fim, Sre, Sim; f2 twc[2], tws[2];
    const float mf = (float)m, gf = (float)g, sg = (m & 1) ? -1.0f : 1.0f;
    {
        float c0[4], s0[4];
#pragma unroll
        for (int j = 0; j < 4; ++j) {
            float a = (gf + 4.0f * j) * (mf * (1.0f / 32));
            c0[j] = cosr(a); s0[j] = m ? -sinr(a) : ((g & 1) ? -1.0f : 1.0f);
        }
        FAre = (u2){pk_h(c0[0], c0[1]), pk_h(c0[2], c0[3])}; FAim = (u2){pk_h(s0[0], s0[1]), pk_h(s0[2], s0[3])};
        const float mo = (float)(2 * (m & 7) + 1) * (1.0f / 32);
#pragma unroll
        for (int u = 0; u < 4; ++u) {
            const float db = 4.0f * gf + u;
            float a = db * (mf * (1.0f / 16));
            float cc = cosr(a), ss = sinr(a);
            Fre[u] = pk_h(cc, ss); Fim[u] = pk_h(-ss, cc);
            float a2 = db * mo;
            u32 re = pk_h(m < 8 ? cc : cosr(a2), 0.f), im = pk_h(m < 8 ? (m ? -ss : ((u & 1) ? -1.0f : 1.0f)) : -sinr(a2), 0.f);
            Sre[u] = m < 8 ? re : re << 16; Sim[u] = m < 8 ? im : im << 16;
            float a3 = a * (1.0f / 32);
            twc[u & 1][u >> 1] = cosr(a3); tws[u & 1][u >> 1] = sinr(a3);
        }
#pragma unroll
        for (int e = 0; e < 2; ++e) {
            float a = (float)(((m >> 1) * (256 * (2 * g + e) + b)) & 2047) * (1.0f / 2048);
            float cc = cosr(a), ss = sinr(a);
            Mb[e] = (m & 1) ? pk_h(-ss, cc) : pk_h(cc, ss);
        }
    }
    f2 v[4]; u32 ln[4];
#pragma unroll
    for (int j = 0; j < 4; ++j) v[j] = bf_2(ld[j]);
    layernorm8(v, ln);
    u4 B;
#pragma unroll
    for (int p = 0; p < 2; ++p) {
        u2 A = p ? (u2){pk_h(bf_hi(ln[0]), bf_hi(ln[1])), pk_h(bf_hi(ln[2]), bf_hi(ln[3]))}
                 : (u2){pk_h(bf_lo(ln[0]), bf_lo(ln[1])), pk_h(bf_lo(ln[2]), bf_lo(ln[3]))};
        f4 Dre = mma16_h(A, FAre, Z4), Dim = mma16_h(A, FAim, Z4);
        f2 re = fma2(hi2(Dre), (f2){sg, sg}, lo2(Dre)), im = fma2(hi2(Dim), (f2){sg, sg}, lo2(Dim));
        f2 tr = fma2(im, tws[p], re * twc[p]), ti = fma2(-re, tws[p], im * twc[p]);
        B[p] = pk_h(tr[0], ti[0]); B[2 + p] = pk_h(tr[1], ti[1]);
    }
    f4 Gre = mma_h(Fre, B, Z4), Gim = mma_h(Fim, B, Z4), Hre = mma_h(Sre, B, Z4), Him = mma_h(Sim, B, Z4);
    u4 Z;
#pragma unroll
    for (int i = 0; i < 4; ++i) Z[i] = pk_h(m ? Gre[i] : Hre[i], m ? Gim[i] : Him[i]);
    *(u4*)(smem + w * 1024 + m * 64 + g * 16) = Z;
    __syncthreads();
    u2 r0 = *(const u2*)(smem + (2 * g) * 1024 + w * 128 + m * 8);
    u2 r1 = *(const u2*)(smem + (2 * g + 1) * 1024 + w * 128 + m * 8);
#pragma unroll
    for (int t = 0; t < 2; ++t) {
        f4 D = mma16_h(Mb, (u2){r0[t], r1[t]}, Z4);
        *(u2*)(ws + WS_U + (32 * w + 2 * m + t) * 8192 + b * 32 + g * 8) = (u2){pk_h(D[0], D[1]), pk_h(D[2], D[3])};
    }
}

DI void p1_wt(int jb, const uint16_t* __restrict__ wr, const uint16_t* __restrict__ wi, char* __restrict__ ws, char* smem) {
    const int tid = threadIdx.x;
    constexpr int NCH = 580, ARR = NCH * 16;
    u4 t[3];
#pragma unroll
    for (int it = 0; it < 3; ++it) {
        int ch = umin(tid + 512 * it, 2 * NCH - 1);
        int arr = ch >= NCH, cc = ch - arr * NCH;
        int src = cc < 257 ? jb * 257 + cc : (cc < 514 ? (255 - jb) * 257 + cc - 257 : (cc < 547 ? ((256 - jb) & 255) * 257 + cc - 514 : (jb + 1) * 257 + cc - 547));
        t[it] = ((const u4*)(arr ? wi : wr))[src];
    }
#pragma unroll
    for (int it = 0; it < 3; ++it) *(u4*)(smem + umin(tid + 512 * it, 2 * NCH - 1) * 16) = t[it];
    __syncthreads();
    const uint16_t* sr = (const uint16_t*)smem;
    const uint16_t* si = (const uint16_t*)(smem + ARR);
    const int sel = tid >> 8, j = (tid >> 4) & 15, n = tid & 15, col = 16 * n + j, blk = sel ? 255 - jb : jb;
    const int k = n ? n + 32 * j : (j < 8 ? 32 * j : 16 + 32 * (j - 8));
    const bool mir = k > 256;
    const int own = sel * 2056, oth = 2056 - own, row0 = 4112 + sel * 264;
    u32 o[8];
#pragma unroll
    for (int i = 0; i < 8; ++i) {
        int e = mir ? ((i ? oth + (8 - i) * 257 : row0) + 512 - k) : (own + i * 257 + k);
        o[i] = ((u32)sr[e] | ((u32)si[e] << 16)) ^ (mir ? 0x80000000u : 0u);
    }
    u4* dst = (u4*)(ws + WS_WT + col * 8192 + blk * 32);
    dst[0] = (u4){o[0], o[1], o[2], o[3]};
    dst[1] = (u4){o[4], o[5], o[6], o[7]};
    if ((tid & 255) < 8) {
        const int i = tid & 7, ed = own + i * 257, em = i ? oth + (8 - i) * 257 : row0;
        float ar = 0.5f * (bf_lo(sr[ed]) + bf_lo(sr[em])), ai = 0.5f * (bf_lo(si[ed]) - bf_lo(si[em]));
        float br = 0.5f * (bf_lo(sr[ed + 256]) + bf_lo(sr[em + 256])), bi = 0.5f * (bf_lo(si[ed + 256]) - bf_lo(si[em + 256]));
        *(f4*)(ws + WS_WSP + ((i * 4 + ((blk >> 4) & 3)) * 64 + (blk >> 6) * 16 + (blk & 15)) * 16) = (f4){0.5f * (ar + br), 0.5f * (ai + bi), 0.5f * (ar - br), 0.5f * (ai - bi)};
    }
}

DI void p1_w2(int ks, const uint16_t* __restrict__ w2, char* __restrict__ ws) {
    const int tid = threadIdx.x, w = tid >> 6, l = tid & 63, g = l >> 4, m = l & 15;
    const int P = ks / 7, q = ks - 7 * P;
    const int r0 = 224 * P + (q < 3 ? 32 * q : (q < 6 ? 16 + 32 * q : 96)), dr = q < 6 ? 16 : 112;
    u2 v[8];
#pragma unroll
    for (int j = 0; j < 8; ++j) v[j] = *(const u2*)(w2 + (r0 + (j >> 2) * dr + 4 * g + (j & 3)) * 512 + w * 64 + 4 * m);
    char* dst = ws + WS_W2T + ((w * 56 + ks) * 4 + 2 * (m >> 3) + (m & 1)) * 1024 + (g * 16 + 4 * ((m >> 1) & 3)) * 16;
#pragma unroll
    for (int i = 0; i < 4; ++i) {
        u4 o;
#pragma unroll
        for (int jj = 0; jj < 4; ++jj) o[jj] = __builtin_amdgcn_perm(v[2 * jj + 1][i >> 1], v[2 * jj][i >> 1], (i & 1) ? 0x07060302u : 0x05040100u);
        *(u4*)(dst + i * 16) = o;
    }
}

__global__ __launch_bounds__(512, 4) void k_a(const uint16_t* __restrict__ x, const uint16_t* __restrict__ wr,
                                              const uint16_t* __restrict__ wi, const uint16_t* __restrict__ w2, char* __restrict__ ws) {
    __shared__ __attribute__((aligned(16))) char smem[2 * 580 * 16];
    ST_DECL;
    if (blockIdx.x < 256) p1_main(xcd_remap(blockIdx.x), x, ws, smem);
    else if (blockIdx.x < 384) p1_wt((blockIdx.x & 7) * 16 + ((blockIdx.x >> 3) & 15), wr, wi, ws, smem);
    else p1_w2(blockIdx.x - 384, w2, ws);
    ST(2);
    ST_END(ws, 0);
}

DI void glds16(const void* sbase, u32 voff, u32 lds_dst) {
    u32 keep;
    asm volatile("s_mov_b32 %0, m0\n\ts_mov_b32 m0, %3\n\ts_nop 0\n\tglobal_load_lds_dwordx4 %1, %2\n\ts_mov_b32 m0, %0"
                 : "=&s"(keep) : "v"(voff), "s"(sbase), "s"(lds_dst) : "memory");
}
DI f2 silu2(f2 a) {
    f2 t = a * -1.4426950408889634f;
    f2 e = (f2){__builtin_amdgcn_exp2f(t[0]), __builtin_amdgcn_exp2f(t[1])} + 1.0f;
    return a * (f2){__builtin_amdgcn_rcpf(e[0]), __builtin_amdgcn_rcpf(e[1])};
}

#define W1_B1 8
#define W1_B2 6
#define W1_B3 6
#define W2_OUT 2
#define G1_DEPTH 4
#define G2_DEPTH 2
constexpr int LDS_SCR = 114688;
constexpr int SCR_X = 8448;
constexpr int SCR_S = SCR_X + 20480;
constexpr int SCR_F = SCR_S + 16384;
constexpr int LDS_B = LDS_SCR + SCR_F + 64;

DI void wait_tok(u4& t0, u4& t1, rsrc_t rw, int vo, int so) {
    if (__builtin_expect(!__all(umax(max4(t0), max4(t1)) != SENT), 0)) {
#pragma nounroll
        for (int sp = 0; sp < SPIN_MAX; ++sp) {
            FENCE();
            t0 = bld4<SC1>(rw, vo, so); t1 = bld4<SC1>(rw, vo, so + 16384);
            if (__all(umax(max4(t0), max4(t1)) != SENT)) break;
        }
    }
}
DI u4 ldw1(LAS char* wb, int kk, int ft) {
    s4 a0 = __builtin_amdgcn_ds_read_tr16_b64_v4i16((LAS s4*)(wb + kk * 32 * 224 + ft * 32));
    s4 a1 = __builtin_amdgcn_ds_read_tr16_b64_v4i16((LAS s4*)(wb + (kk * 32 + 16) * 224 + ft * 32));
    u2 x0 = __builtin_bit_cast(u2, a0), x1 = __builtin_bit_cast(u2, a1);
    return (u4){x0[0], x0[1], x1[0], x1[1]};
}
DI u2 pk4(f4 a) { return (u2){pk_b(a[0], a[1]), pk_b(a[2], a[3])}; }
DI u32 silu_pk(u32 p) { return pk_b2(silu2(bf_2(p))); }
DI u4 silu_pk4(u4 p) { return (u4){silu_pk(p[0]), silu_pk(p[1]), silu_pk(p[2]), silu_pk(p[3])}; }
DI void lds_set(LAS u32* p, u32 v) {
    asm volatile("s_waitcnt lgkmcnt(0)" ::: "memory");
    __hip_atomic_store(p, v, __ATOMIC_RELAXED, __HIP_MEMORY_SCOPE_WORKGROUP);
}
DI void lds_wait(LAS u32* p, u32 v) {
#pragma nounroll
    for (int sp = 0; sp < SPIN_MAX; ++sp) {
        if (__builtin_amdgcn_readfirstlane(__hip_atomic_load(p, __ATOMIC_RELAXED, __HIP_MEMORY_SCOPE_WORKGROUP)) >= v) break;
        __builtin_amdgcn_s_sleep(1);
    }
    FENCE();
}
DI void flag_wait(GAS u32* p) {
#pragma nounroll
    for (int sp = 0; sp < SPIN_MAX; ++sp) {
        if (__all(__hip_atomic_load(p, __ATOMIC_RELAXED, __HIP_MEMORY_SCOPE_AGENT) == 1u)) break;
        __builtin_amdgcn_s_sleep(2);
    }
    FENCE();
}

__global__ __launch_bounds__(512, 2) void k_b(const uint16_t* x, const uint16_t* w1, uint16_t* out, char* ws) {
    extern __shared__ __attribute__((aligned(16))) char smem[];
    const int tid = threadIdx.x, l = tid & 63, g = l >> 4, m = l & 15;
    const int w = __builtin_amdgcn_readfirstlane(tid >> 6);
    const int b = xcd_remap(blockIdx.x);
    const int mt1 = (b >> 6) * 4 + ((b >> 3) & 3), nt1 = ((b >> 5) & 1) * 8 + (b & 7);
    const int grp = b >> 4, th = (b >> 3) & 1, fb = b & 7;
    const rsrc_t rw = mkrs(ws, WS_END), ro = mkrs(out, 2097152);
    char* scr = smem + LDS_SCR;
    LAS u32* hw = (LAS u32*)((LAS char*)smem + LDS_SCR + SCR_F);
    const u32 lds0 = (u32)(uintptr_t)(LAS char*)smem;
    const float mf = (float)m, gf = (float)g;
    const bool aw = w < 4;
    ST_DECL;
    const int xoff = (256 * w + b) * 1024 + m * 32 + g * 8;
    u2 xl[2];
    f4 wsp[8];
    const int rq = ((b >> 3) + b) & 3;
    u32 voff0;
    int cch0;
    const char* wsrc = (const char*)w1 + nt1 * 224;
    auto w1_burst = [&](int i0, int n) {
#pragma unroll
        for (int i = i0; i < i0 + n; ++i) {
            const int q = (i / 7 + rq) & 3, j = i % 7;
            const int cj = (4 * j) % 14, rj = 18 * j + (4 * j) / 14;
            u32 vo = voff0 + rj * 3584 + cj * 16 + (cch0 + cj >= 14 ? 3584 - 224 : 0);
            glds16(wsrc + q * (128 * 3584), vo, lds0 + (w - 4) * 1024 + q * 28672 + j * 4096);
        }
    };
    if (!aw) {
        const u32 so = (l ^ ((l >> 4) & 1)) * 16;
#pragma unroll
        for (int i = 0; i < 4; ++i)
            glds16(ws + (i < 2 ? WS_U : WS_WT) + b * 8192 + (w - 4) * 2048 + (i & 1) * 1024, so, lds0 + LDS_SCR + SCR_S + (i >> 1) * 8192 + (w - 4) * 2048 + (i & 1) * 1024);
#pragma unroll
        for (int it = 0; it < 2; ++it) {
            const int ch = (tid - 256) + 256 * it;
            bst4<SC1>((u4){SENT, SENT, SENT, SENT}, rw, WS_LN2 + (b >> 1) * 16384 + (ch >> 3) * 256 + (b & 1) * 128 + (ch & 7) * 16, 0);
        }
    } else {
        if (tid < 16) hw[tid] = 0;
        if (b == 0) {
#pragma unroll
            for (int q = 0; q < 8; ++q) wsp[q] = *(const f4*)(ws + WS_WSP + ((w * 8 + q) * 64 + l) * 16);
        }
    }
    xl[0] = *(const u2*)((const char*)x + xoff); xl[1] = *(const u2*)((const char*)x + xoff + 512);
    FENCE();
    ST(2);
    if (!aw) {
        const int sl = tid - 256, row = sl / 14;
        cch0 = sl - row * 14;
        voff0 = row * 3584 + cch0 * 16;
        w1_burst(0, 2);
        WAITV(6);
        BARRIER();
        w1_burst(2, W1_B1 - 2);
    }
    u4 Gre3, Gim3, SGre, SGim, GA[2]; f2 twc[2], tws[2];
    float c16[4], s16[4];
    auto consts3 = [&]() {
        const float sg = (m & 1) ? -1.0f : 1.0f;
#pragma unroll
        for (int u = 0; u < 4; ++u) {
            const float k = 4.0f * gf + u;
            const bool k0 = u == 0 && g == 0;
            float a = k * (mf * (1.0f / 16));
            float cc = c16[u], ss = s16[u];
            Gre3[u] = pk_b(cc, -ss); Gim3[u] = pk_b(ss, cc);
            float a2 = (2.0f * k + 1.0f) * (mf * (1.0f / 32));
            u32 sp = pk_b(g < 2 ? 2.f * cc : 2.f * sg * cosr(a2), g < 2 ? -2.f * ss : -2.f * sg * sinr(a2));
            SGre[u] = k0 ? pk_b(1.0f, sg) : (g < 2 ? sp : 0u);
            SGim[u] = g < 2 ? 0u : sp;
            float a3 = a * (1.0f / 32);
            twc[u >> 1][u & 1] = cosr(a3); tws[u >> 1][u & 1] = sinr(a3);
            float a4 = a * 0.5f, c4 = 2.f * cosr(a4), s4_ = -2.f * sinr(a4);
            GA[0][u] = k0 ? pk_b(1.0f, sg) : pk_b(c4, s4_);
            GA[1][u] = k0 ? pk_b(1.0f, sg) : ((u & 1) ? pk_b(-c4, -s4_) : pk_b(c4, s4_));
        }
        PIN(Gre3); PIN(Gim3); PIN(SGre); PIN(SGim); PIN(GA[0]); PIN(GA[1]); PIN(twc[0]); PIN(twc[1]); PIN(tws[0]); PIN(tws[1]);
    };
#pragma unroll
    for (int u = 0; u < 4; ++u) { float a = (4.0f * gf + u) * (mf * (1.0f / 16)); c16[u] = cosr(a); s16[u] = sinr(a); }
    if (aw) {
        u4 Fre, Fim, Gre, Gim, N0, N1; f2 tc[2], ts[2];
#pragma unroll
        for (int u = 0; u < 4; ++u) {
            float cc = c16[u], ss = s16[u];
            Fre[u] = pk_h(0.25f * cc, 0.25f * ss); Fim[u] = pk_h(-0.25f * ss, 0.25f * cc);
            Gre[u] = pk_b(cc * (1.0f / 256), -ss * (1.0f / 256)); Gim[u] = pk_b(ss * (1.0f / 256), cc * (1.0f / 256));
            float a3 = (4.0f * gf + u) * (mf * (1.0f / 256));
            tc[u >> 1][u & 1] = cosr(a3); ts[u >> 1][u & 1] = sinr(a3);
        }
        PIN(Fre); PIN(Fim); PIN(Gre); PIN(Gim); PIN(tc[0]); PIN(tc[1]); PIN(ts[0]); PIN(ts[1]);
        BARRIER();
        ST(3);
        u2 ld[4], wl[4];
        {
            const char* sp = scr + SCR_S + (64 * g + m) * 32 + (((w >> 1) ^ (m >> 3)) * 16) + (w & 1) * 8;
#pragma unroll
            for (int q = 0; q < 4; ++q) ld[q] = *(const u2*)(sp + 512 * q);
#pragma unroll
            for (int q = 0; q < 4; ++q) wl[q] = *(const u2*)(sp + 8192 + 512 * q);
        }
        f4 Xre[2], Xim[2];
#pragma unroll
        for (int e = 0; e < 2; ++e) {
            u4 A = {ld[0][e], ld[1][e], ld[2][e], ld[3][e]};
            f4 Dre = mma_h(A, Fre, Z4), Dim = mma_h(A, Fim, Z4);
            f2 r0 = fma2(lo2(Dim), ts[0], lo2(Dre) * tc[0]), i0 = fma2(-lo2(Dre), ts[0], lo2(Dim) * tc[0]);
            f2 r1 = fma2(hi2(Dim), ts[1], hi2(Dre) * tc[1]), i1 = fma2(-hi2(Dre), ts[1], hi2(Dim) * tc[1]);
            u4 B = {pk_h(r0[0], i0[0]), pk_h(r0[1], i0[1]), pk_h(r1[0], i1[0]), pk_h(r1[1], i1[1])};
            Xre[e] = mma_h(Fre, B, Z4); Xim[e] = mma_h(Fim, B, Z4);
        }
        u4 Y[2];
#pragma unroll
        for (int e = 0; e < 2; ++e) {
            f2 wr0 = {bf_lo(wl[0][e]), bf_lo(wl[1][e])}, wi0 = {bf_hi(wl[0][e]), bf_hi(wl[1][e])};
            f2 wr1 = {bf_lo(wl[2][e]), bf_lo(wl[3][e])}, wi1 = {bf_hi(wl[2][e]), bf_hi(wl[3][e])};
            f2 r0 = fma2(-lo2(Xim[e]), wi0, lo2(Xre[e]) * wr0), i0 = fma2(lo2(Xre[e]), wi0, lo2(Xim[e]) * wr0);
            f2 r1 = fma2(-hi2(Xim[e]), wi1, hi2(Xre[e]) * wr1), i1 = fma2(hi2(Xre[e]), wi1, hi2(Xim[e]) * wr1);
            Y[e] = (u4){pk_b(r0[0], i0[0]), pk_b(r0[1], i0[1]), pk_b(r1[0], i1[0]), pk_b(r1[1], i1[1])};
        }
        if (b == 0) {
            f2* sx = (f2*)(scr + SCR_X);
#pragma unroll
            for (int e = 0; e < 2; ++e)
#pragma unroll
                for (int i = 0; i < 4; ++i) sx[2 * w + e + 8 * (m + 16 * (4 * g + i))] = (f2){Xre[e][i], Xim[e][i]};
            BARRIER();
#pragma unroll
            for (int e = 0; e < 2; ++e)
#pragma unroll
                for (int i = 0; i < 4; ++i) {
                    const int f = 2 * w + e + 8 * (m + 16 * (4 * g + i));
                    f2 xm = sx[(2048 - f) & 2047];
                    f4 ww = wsp[4 * e + i];
                    float yr = Xre[e][i] * ww[0] - Xim[e][i] * ww[1] + xm[0] * ww[2] + xm[1] * ww[3];
                    float yi = Xre[e][i] * ww[1] + Xim[e][i] * ww[0] + xm[0] * ww[3] - xm[1] * ww[2];
                    Y[e][i] = pk_b(yr, yi);
                }
        }
#pragma unroll
        for (int e = 0; e < 2; ++e) {
            f4 Dre = mma_b(Y[e], Gre, Z4), Dim = mma_b(Y[e], Gim, Z4);
            f2 r0 = fma2(-lo2(Dim), ts[0], lo2(Dre) * tc[0]), i0 = fma2(lo2(Dre), ts[0], lo2(Dim) * tc[0]);
            f2 r1 = fma2(-hi2(Dim), ts[1], hi2(Dre) * tc[1]), i1 = fma2(hi2(Dre), ts[1], hi2(Dim) * tc[1]);
            u4 A = {pk_b(r0[0], i0[0]), pk_b(r0[1], i0[1]), pk_b(r1[0], i1[0]), pk_b(r1[1], i1[1])};
            f4 Ere = mma_b(A, Gre, Z4), Eim = mma_b(A, Gim, Z4);
            u4 o;
#pragma unroll
            for (int i = 0; i < 4; ++i) o[i] = clean(pk_b(Ere[i], Eim[i]));
            *(u4*)(scr + (2 * w + e) * 1040 + m * 64 + g * 16) = o;
        }
        BARRIER();
        ST(4);
#pragma unroll
        for (int it = 0; it < 2; ++it) {
            const int ch = tid + 256 * it;
            const u32* sp = (const u32*)(scr + (ch & 1) * 4160 + (ch >> 1) * 4);
            bst4<SC1>((u4){sp[0], sp[260], sp[520], sp[780]}, rw, ch * 16, WS_V + b * 8192);
        }
        FENCE();
        consts3();
#pragma unroll
        for (int u = 0; u < 4; ++u) {
            float a1 = (float)(((4 * (g & 1) + u) * (256 * (m >> 1) + b)) & 2047) * (1.0f / 2048);
            float c1 = cosr(a1), s1 = sinr(a1);
            u32 nb = (m & 1) ? pk_b(s1, c1) : pk_b(c1, -s1);
            N0[u] = g < 2 ? nb : 0u; N1[u] = g < 2 ? 0u : nb;
        }
        PIN(N0); PIN(N1);
        WAITV(0);
        const int vo = (2 * m + (g >> 1)) * 8192 + (g & 1) * 16, vs = WS_V + w * 64 * 8192 + b * 32;
        u4 v0, v1;
#pragma nounroll
        for (int sp = 0; sp < SPIN_MAX; ++sp) {
            FENCE();
            v0 = bld4<SC1>(rw, vo, vs); v1 = bld4<SC1>(rw, vo, vs + 32 * 8192);
            if (__all(umax(max4(v0), max4(v1)) != SENT)) break;
        }
        ST(5);
        char* sz = scr + SCR_X + g * 2048 + w * 256 + m * 8;
#pragma unroll
        for (int hh = 0; hh < 2; ++hh) {
            f4 D0 = mma_b(N0, hh ? v1 : v0, Z4), D1 = mma_b(N1, hh ? v1 : v0, Z4);
            *(u2*)(sz + hh * 128) = (u2){pk_b(D0[0], D0[1]), pk_b(D1[0], D1[1])};
            *(u2*)(sz + 1024 + hh * 128) = (u2){pk_b(D0[2], D0[3]), pk_b(D1[2], D1[3])};
        }
    } else {
        consts3();
        asm volatile("s_waitcnt vmcnt(%0)" :: "n"(W1_B1) : "memory");
        if (b == 0) BARRIER();
        BARRIER();
        __builtin_amdgcn_s_sleep(3);
        w1_burst(W1_B1, W1_B2);
    }
    BARRIER();
    if (!aw) w1_burst(W1_B1 + W1_B2, W1_B3);
    {
        u4 A = *(const u4*)(scr + SCR_X + w * 1024 + m * 64 + g * 16);
        f4 Dre = mma_b(A, Gre3, Z4), Dim = mma_b(A, Gim3, Z4), Hre = mma_b(A, SGre, Z4), Him = mma_b(A, SGim, Z4);
        if (g == 0) { Dre[0] = Hre[0]; Dim[0] = Him[0]; }
        f2 r0 = fma2(-lo2(Dim), tws[0], lo2(Dre) * twc[0]), i0 = fma2(lo2(Dre), tws[0], lo2(Dim) * twc[0]);
        f2 r1 = fma2(-hi2(Dim), tws[1], hi2(Dre) * twc[1]), i1 = fma2(hi2(Dre), tws[1], hi2(Dim) * twc[1]);
        u4 A2 = {pk_b(r0[0], i0[0]), pk_b(r0[1], i0[1]), pk_b(r1[0], i1[0]), pk_b(r1[1], i1[1])};
        f2 h[4]; u32 ln[4];
#pragma unroll
        for (int t = 0; t < 2; ++t) {
            f4 Yv = mma_b(A2, GA[t], Z4);
            u32 h0 = pk_b2(bf_2(xl[t][0]) + bf_2(pk_b(Yv[0], Yv[1]))), h1 = pk_b2(bf_2(xl[t][1]) + bf_2(pk_b(Yv[2], Yv[3])));
            bst2<SC1>((u2){h0, h1}, ro, xoff + 512 * t, 0);
            h[2 * t] = bf_2(h0); h[2 * t + 1] = bf_2(h1);
        }
        layernorm8(h, ln);
#pragma unroll
        for (int t = 0; t < 2; ++t)
            *(u2*)(scr + ((8 * t + (m >> 1)) * 4 + g) * 128 + (w ^ (m >> 1)) * 16 + (m & 1) * 8) = (u2){clean(ln[2 * t]), clean(ln[2 * t + 1])};
        WAITV(2);
        BARRIER();
        u4 lo = *(const u4*)(scr + (tid ^ ((tid >> 5) & 7)) * 16);
        bst4<SC1>(lo, rw, (tid >> 3) * 256 + (tid & 7) * 16, WS_LN2 + (b >> 1) * 16384 + (b & 1) * 128);
        FENCE();
        ST(6);
    }
    const int pw = (w + 4 * th) & 7;
    GAS u32* fl = (GAS u32*)(ws + WS_FLAG);
    {
        const int tg = w & 3;
        char* xch = scr + SCR_X + tg * 5120 + l * 16;
        char* xc2 = scr + SCR_S + tg * 2048 + l * 16;
        if (aw) {
            const int toff = WS_LN2 + (mt1 * 8 + 2 * tg) * 16384;
            LAS char* wb0 = (LAS char*)smem + (4 * g + (m >> 2)) * 224 + (m & 3) * 8;
            LAS char* wb[4];
            int tq[4];
#pragma unroll
            for (int q = 0; q < 4; ++q) { const int qq = (q + rq) & 3; wb[q] = wb0 + qq * 28672; tq[q] = toff + qq * 4096; }
            u4 T[2][16], A[2][5];
            auto ldt = [&](int kk) {
#pragma unroll
                for (int tt = 0; tt < 2; ++tt) T[tt][kk] = bld4<SC1>(rw, l * 16 + (kk & 3) * 1024, tq[kk >> 2] + tt * 16384);
            };
#pragma unroll
            for (int i = 0; i < 5; ++i) A[0][i] = ldw1(wb[0], 0, i < 4 ? i : 6);
            WAITV(0);
            ST(7);
#pragma unroll
            for (int kk = 0; kk < G1_DEPTH; ++kk) { ldt(kk); SCHED(); }
            f4 acc[2][5];
#pragma unroll
            for (int tt = 0; tt < 2; ++tt)
#pragma unroll
                for (int i = 0; i < 5; ++i) acc[tt][i] = Z4;
#pragma unroll
            for (int kk = 0; kk < 16; ++kk) {
                wait_tok(T[0][kk], T[1][kk], rw, l * 16, tq[kk >> 2] + (kk & 3) * 1024);
                if (kk == 0) ST(8);
                if (kk == 7) lds_wait(hw + 5, 4u);
                if (kk + G1_DEPTH < 16) ldt(kk + G1_DEPTH);
                if (kk < 15) {
#pragma unroll
                    for (int i = 0; i < 5; ++i) A[(kk + 1) & 1][i] = ldw1(wb[(kk + 1) >> 2], (kk + 1) & 3, i < 4 ? i : 6);
                }
#pragma unroll
                for (int i = 0; i < 5; ++i)
#pragma unroll
                    for (int tt = 0; tt < 2; ++tt) acc[tt][i] = mma_b(A[kk & 1][i], T[tt][kk], acc[tt][i]);
                __builtin_amdgcn_sched_group_barrier(0x20, 2, 0);
#pragma unroll
                for (int i = 0; i < 10; ++i) {
                    __builtin_amdgcn_sched_group_barrier(0x8, 1, 0);
                    __builtin_amdgcn_sched_group_barrier(0x100, 1, 0);
                }
            }
            u4 A2[4][2];
#pragma unroll
            for (int kk = 0; kk < 3; ++kk)
#pragma unroll
                for (int i = 0; i < 2; ++i) A2[kk][i] = ldw1(wb[0], kk, 4 + i);
            SCHED();
            {
                u2 p[2][5];
#pragma unroll
                for (int tt = 0; tt < 2; ++tt)
#pragma unroll
                    for (int i = 0; i < 5; ++i) p[tt][i] = pk4(acc[tt][i]);
#pragma unroll
                for (int tt = 0; tt < 2; ++tt)
#pragma unroll
                    for (int q = 0; q < 2; ++q) *(u4*)(xch + (2 * tt + q) * 1024) = (u4){p[tt][2 * q][0], p[tt][2 * q][1], p[tt][2 * q + 1][0], p[tt][2 * q + 1][1]};
                *(u4*)(xch + 4096) = (u4){p[0][4][0], p[0][4][1], p[1][4][0], p[1][4][1]};
            }
            lds_set(hw + tg, 1u);
            ST(9);
            f4 ac2[2][2] = {{Z4, Z4}, {Z4, Z4}};
#pragma unroll
            for (int kk = 0; kk < 16; ++kk) {
                if (kk + 3 < 16) {
#pragma unroll
                    for (int i = 0; i < 2; ++i) A2[(kk + 3) & 3][i] = ldw1(wb[(kk + 3) >> 2], (kk + 3) & 3, 4 + i);
                }
#pragma unroll
                for (int i = 0; i < 2; ++i)
#pragma unroll
                    for (int tt = 0; tt < 2; ++tt) ac2[tt][i] = mma_b(A2[kk & 3][i], T[tt][kk], ac2[tt][i]);
#pragma unroll
                for (int i = 0; i < 4; ++i) {
                    __builtin_amdgcn_sched_group_barrier(0x8, 1, 0);
                    __builtin_amdgcn_sched_group_barrier(0x100, 1, 0);
                }
                SCHED();
            }
#pragma unroll
            for (int tt = 0; tt < 2; ++tt) {
                u2 p0 = pk4(ac2[tt][0]), p1 = pk4(ac2[tt][1]);
                *(u4*)(xc2 + tt * 1024) = (u4){p0[0], p0[1], p1[0], p1[1]};
            }
            lds_set(hw + tg, 2u);
            ST(10);
        } else {
            const int hoff = WS_H1 + (mt1 * 8 + 2 * tg) * (56 * 1024) + (nt1 >> 1) * 7168;
            w1_burst(W1_B1 + W1_B2 + W1_B3, 28 - W1_B1 - W1_B2 - W1_B3);
            WAITV(0);
            if (l == 0) __hip_atomic_fetch_add(hw + 5, 1u, __ATOMIC_RELAXED, __HIP_MEMORY_SCOPE_WORKGROUP);
            lds_wait(hw + tg, 1u);
            ST(9);
#pragma unroll
            for (int j = 0; j < 5; ++j) {
                u4 o = silu_pk4(*(const u4*)(xch + j * 1024));
                if (j < 4) bst4<SC1>(o, rw, l * 16, hoff + (j >> 1) * (56 * 1024) + ((nt1 & 1) * 3 + (j & 1)) * 1024);
                else {
                    bst2<SC1>((u2){o[0], o[1]}, rw, l * 8, hoff + 6144 + (nt1 & 1) * 512);
                    bst2<SC1>((u2){o[2], o[3]}, rw, l * 8, hoff + 56 * 1024 + 6144 + (nt1 & 1) * 512);
                }
            }
            lds_wait(hw + tg, 2u);
#pragma unroll
            for (int tt = 0; tt < 2; ++tt)
                bst4<SC1>(silu_pk4(*(const u4*)(xc2 + tt * 1024)), rw, l * 16, hoff + tt * (56 * 1024) + ((nt1 & 1) * 3 + 2) * 1024);
            ST(10);
            WAITV(0);
            u32 prev = 0;
            if (l == 0) prev = __hip_atomic_fetch_add(hw + 4, 1u, __ATOMIC_RELAXED, __HIP_MEMORY_SCOPE_WORKGROUP);
            if (__builtin_amdgcn_readfirstlane(prev) == 3u && l < 2)
                __hip_atomic_store(fl + (l ? 4096 + b : (mt1 * 8 + (nt1 >> 1)) * 32 + (nt1 & 1)), 1u, __ATOMIC_RELAXED, __HIP_MEMORY_SCOPE_AGENT);
            FENCE();
        }
    }
    {
        int ko[7];
#pragma unroll
        for (int ks = 0; ks < 6; ++ks) { int k = ks + (fb < 6 ? fb : fb - 5); ko[ks] = k >= 6 ? k - 6 : k; }
        ko[6] = 6;
        u4 W[7][4];
#pragma unroll
        for (int ks = 0; ks < 7; ++ks) {
#pragma unroll
            for (int t = 0; t < 4; ++t) {
                W[ks][t] = bld4<0>(rw, l * 16 + t * 1024, WS_W2T + (fb * 56 + pw * 7 + ko[ks]) * 4096);
                if (t & 1) { SCHED(); WAITV(W2_OUT); }
            }
        }
        ST(11);
        flag_wait(fl + (grp * 8 + pw) * 32 + (l & 1));
        ST(12);
        const int rot = w & 3;
        const int toff = WS_H1 + (grp * 8 + th * 4) * (56 * 1024) + pw * 7168;
        int to[4];
#pragma unroll
        for (int tt = 0; tt < 4; ++tt) to[tt] = toff + ((tt + rot) & 3) * (56 * 1024);
        u4 T[7][4];
        auto ldtok = [&](int ks) {
#pragma unroll
            for (int tt = 0; tt < 4; ++tt) {
                if (ks < 6) T[ks][tt] = bld4<SC1>(rw, l * 16, to[tt] + ko[ks] * 1024);
                else {
                    u2 a = bld2<SC1>(rw, l * 8 + 2048, to[tt] + 4096), c = bld2<SC1>(rw, l * 8 + 2560, to[tt] + 4096);
                    T[ks][tt] = (u4){a[0], a[1], c[0], c[1]};
                }
            }
            SCHED();
        };
#pragma unroll
        for (int ks = 0; ks < G2_DEPTH; ++ks) ldtok(ks);
        const int rho = grp * 128 + th * 64 + rot * 16 + m;
        const int ooff = (256 * (rho & 7) + (rho >> 3)) * 1024 + fb * 128 + g * 16;
        u4 hres[2];
        f4 acc[4][4];
#pragma unroll
        for (int tt = 0; tt < 4; ++tt)
#pragma unroll
            for (int t = 0; t < 4; ++t) acc[tt][t] = Z4;
#pragma unroll
        for (int ks = 0; ks < 7; ++ks) {
            if (ks + G2_DEPTH < 7) { PIN(T[ks][0]); ldtok(ks + G2_DEPTH); }
#pragma unroll
            for (int tt = 0; tt < 4; ++tt)
#pragma unroll
                for (int t = 0; t < 4; ++t) acc[tt][t] = mma_b(W[ks][t], T[ks][tt], acc[tt][t]);
            SCHED();
        }
        if (!aw) {
            flag_wait(fl + 4096 + grp * 16 + th * 8 + rot * 2 + (l & 1));
            hres[0] = bld4<SC1>(ro, ooff, 0); hres[1] = bld4<SC1>(ro, ooff + 64, 0);
        }
        ST(13);
        lds_wait(hw + 4, 4u);
#pragma unroll
        for (int tt = 0; tt < 4; ++tt)
            if (aw || tt > 0)
#pragma unroll
                for (int t = 0; t < 4; ++t) *(f4*)(smem + (w * 16 + ((tt + rot) & 3) * 4 + t) * 1024 + l * 16) = acc[tt][t];
        BARRIER();
        if (!aw) {
#pragma unroll
            for (int j = 1; j < 8; ++j)
#pragma unroll
                for (int t = 0; t < 4; ++t) acc[0][t] += *(const f4*)(smem + (((w + j) & 7) * 16 + rot * 4 + t) * 1024 + l * 16);
#pragma unroll
            for (int p = 0; p < 2; ++p) {
                u2 s0 = pk4(acc[0][2 * p]), s1 = pk4(acc[0][2 * p + 1]);
                u4 o = {pk_b2(bf_2(hres[p][0]) + bf_2(s0[0])), pk_b2(bf_2(hres[p][1]) + bf_2(s0[1])),
                        pk_b2(bf_2(hres[p][2]) + bf_2(s1[0])), pk_b2(bf_2(hres[p][3]) + bf_2(s1[1]))};
                bst4<0>(o, ro, ooff + 64 * p, 0);
            }
        }
    }
    ST_END(ws, 1);
}

extern "C" void kernel_launch(void* const* d_in, const int* in_sizes, int n_in, void* d_out, int out_size,
                              void* d_ws, size_t ws_size, hipStream_t stream) {
    const uint16_t* x = (const uint16_t*)d_in[0];
    const uint16_t* wr = (const uint16_t*)d_in[1];
    const uint16_t* wi = (const uint16_t*)d_in[2];
    const uint16_t* w1 = (const uint16_t*)d_in[4];
    const uint16_t* w2 = (const uint16_t*)d_in[5];
    char* ws = (char*)d_ws;
    (void)hipFuncSetAttribute((const void*)k_b, hipFuncAttributeMaxDynamicSharedMemorySize, LDS_B);
    k_a<<<440, 512, 0, stream>>>(x, wr, wi, w2, ws);
    k_b<<<256, 512, LDS_B, stream>>>(x, w1, (uint16_t*)d_out, ws);
}
```

```cpp
#include <hip/hip_runtime.h>
#include <hip/hip_bf16.h>
#include <stdint.h>

typedef _Float16 h8 __attribute__((ext_vector_type(8)));
typedef _Float16 h4 __attribute__((ext_vector_type(4)));
typedef _Float16 h2 __attribute__((ext_vector_type(2)));
typedef __bf16 b8 __attribute__((ext_vector_type(8)));
typedef __bf16 b2 __attribute__((ext_vector_type(2)));
typedef short s4 __attribute__((ext_vector_type(4)));
typedef float f4 __attribute__((ext_vector_type(4)));
typedef float f2 __attribute__((ext_vector_type(2)));
typedef unsigned u32;
typedef u32 u4 __attribute__((ext_vector_type(4)));
typedef u32 u2 __attribute__((ext_vector_type(2)));
#define LAS __attribute__((address_space(3)))
#define GAS __attribute__((address_space(1)))
#define DI __device__ __forceinline__
#define FENCE() asm volatile("" ::: "memory")
#define PIN(x) asm volatile("" : "+v"(x))
#define SCHED() __builtin_amdgcn_sched_barrier(0)
#define WAITV(n) asm volatile("s_waitcnt vmcnt(%0)" :: "n"(n) : "memory")
#define BARRIER() do { asm volatile("s_waitcnt lgkmcnt(0)" ::: "memory"); __builtin_amdgcn_s_barrier(); FENCE(); } while (0)
constexpr int SC1 = 16;
constexpr u32 SENT = 0xffffffffu;
constexpr int SPIN_MAX = 1 << 20;

DI u32 pk_h(float a, float b) { f2 v = {a, b}; h2 r = __builtin_convertvector(v, h2); return __builtin_bit_cast(u32, r); }
DI u32 pk_b(float a, float b) { f2 v = {a, b}; b2 r = __builtin_convertvector(v, b2); return __builtin_bit_cast(u32, r); }
DI float bf_lo(u32 v) { return __builtin_bit_cast(float, v << 16); }
DI float bf_hi(u32 v) { return __builtin_bit_cast(float, v & 0xffff0000u); }
DI f2 bf_2(u32 v) { return (f2){bf_lo(v), bf_hi(v)}; }
DI u32 pk_b2(f2 v) { return pk_b(v[0], v[1]); }
DI float rbf(float a) { return bf_lo(pk_b(a, a)); }
DI float cosr(float rev) { return __builtin_amdgcn_cosf(rev); }
DI float sinr(float rev) { return __builtin_amdgcn_sinf(rev); }
DI f2 lo2(f4 v) { return __builtin_shufflevector(v, v, 0, 1); }
DI f2 hi2(f4 v) { return __builtin_shufflevector(v, v, 2, 3); }
DI f2 fma2(f2 a, f2 b, f2 c) { return __builtin_elementwise_fma(a, b, c); }
DI u32 umin(u32 a, u32 b) { return a < b ? a : b; }
DI u32 umax(u32 a, u32 b) { return a > b ? a : b; }
DI u32 clean(u32 v) { return umin(v, SENT - 1); }
DI u32 max4(u4 v) { return umax(umax(umax(v[0], v[1]), v[2]), v[3]); }
#define Z4 ((f4){0, 0, 0, 0})
DI f4 mma_h(u4 a, u4 b, f4 c) { return __builtin_amdgcn_mfma_f32_16x16x32_f16(__builtin_bit_cast(h8, a), __builtin_bit_cast(h8, b), c, 0, 0, 0); }
DI f4 mma_b(u4 a, u4 b, f4 c) { return __builtin_amdgcn_mfma_f32_16x16x32_bf16(__builtin_bit_cast(b8, a), __builtin_bit_cast(b8, b), c, 0, 0, 0); }
DI f4 mma16_h(u2 a, u2 b, f4 c) { return __builtin_amdgcn_mfma_f32_16x16x16f16(__builtin_bit_cast(h4, a), __builtin_bit_cast(h4, b), c, 0, 0, 0); }
typedef __amdgpu_buffer_rsrc_t rsrc_t;
DI rsrc_t mkrs(const void* p, u32 bytes) { return __builtin_amdgcn_make_buffer_rsrc((void*)p, 0, bytes, 0x00020000); }
template <int AUX> DI u4 bld4(rsrc_t r, int v, int s) { return __builtin_amdgcn_raw_buffer_load_b128(r, v, s, AUX); }
template <int AUX> DI u2 bld2(rsrc_t r, int v, int s) { return __builtin_amdgcn_raw_buffer_load_b64(r, v, s, AUX); }
template <int AUX> DI void bst4(u4 d, rsrc_t r, int v, int s) {
    __builtin_amdgcn_raw_buffer_store_b128(d, r, v, s, AUX);
    asm volatile("s_nop 1" :: "v"(d));
}
template <int AUX> DI void bst2(u2 d, rsrc_t r, int v, int s) { __builtin_amdgcn_raw_buffer_store_b64(d, r, v, s, AUX); }

template <int CTRL> DI float dpp(float v) {
    return __builtin_bit_cast(float, __builtin_amdgcn_update_dpp(0, __builtin_bit_cast(int, v), CTRL, 0xf, 0xf, true));
}
template <bool ALL> DI float red_lanes(float v) {
    v += dpp<0xB1>(v);
    if (ALL) v += dpp<0x4E>(v);
    v += dpp<0x124>(v); v += dpp<0x128>(v);
    u32 x = __builtin_bit_cast(u32, v), y = x;
    asm("" : "+v"(y));
    auto r = __builtin_amdgcn_permlane16_swap(x, y, false, false);
    v = __builtin_bit_cast(float, (u32)r[0]) + __builtin_bit_cast(float, (u32)r[1]);
    x = __builtin_bit_cast(u32, v); y = x;
    asm("" : "+v"(y));
    auto q = __builtin_amdgcn_permlane32_swap(x, y, false, false);
    return __builtin_bit_cast(float, (u32)q[0]) + __builtin_bit_cast(float, (u32)q[1]);
}
DI float ln_inv(float var) { return __builtin_amdgcn_rcpf(rbf(__builtin_amdgcn_sqrtf(rbf(rbf(var) + 1e-5f)))); }

#define ST_DECL
#define ST(i)
#define ST_END(ws, kid)

constexpr u32 WS_UW = 0;
constexpr u32 WS_V = WS_UW + (4u << 20);
constexpr u32 WS_LN2 = WS_V + (2u << 20);
constexpr u32 WS_W2T = WS_LN2 + (2u << 20);
constexpr u32 WS_WB = WS_W2T + 1835008u;
constexpr u32 WS_FA = WS_WB + 8192u;
constexpr u32 WS_FLAG = WS_FA + 8192u;
constexpr u32 WS_EP = WS_FLAG + 20480u;
constexpr u32 WS_H1 = WS_W2T + (2u << 20);
constexpr u32 WS_END = WS_H1 + 7340032u;

#define W1_B0 4
#define W1_B1 6
#define W1_B2 5
#define W1_B3 6
#define W1_CHK ((((W1_B0 + W1_B1 + W1_B2 + W1_B3) / 7) * 4 - 2) < 0 ? 0 : (((W1_B0 + W1_B1 + W1_B2 + W1_B3) / 7) * 4 - 2))
#define W2_SLEEP 20
#define AUX_OUT 2
#define W2_OUT 2
#define W2_ATHR 3
#define W2_EARLY 3
#define G1_DEPTH 6
#define G2_DEPTH 2
constexpr int LDS_SCR = 114688;
constexpr int SCR_X = 8448;
constexpr int SCR_S = SCR_X + 20480;
constexpr int SCR_F = SCR_S + 16384;
constexpr int LDS_B = LDS_SCR + SCR_F + 64;

DI void glds16(const void* sbase, u32 voff, u32 lds_dst) {
    u32 keep;
    asm volatile("s_mov_b32 %0, m0\n\ts_mov_b32 m0, %3\n\ts_nop 0\n\tglobal_load_lds_dwordx4 %1, %2\n\ts_mov_b32 m0, %0"
                 : "=&s"(keep) : "v"(voff), "s"(sbase), "s"(lds_dst) : "memory");
}
DI f2 silu2(f2 a) {
    f2 t = a * -1.4426950408889634f;
    f2 e = (f2){__builtin_amdgcn_exp2f(t[0]), __builtin_amdgcn_exp2f(t[1])} + 1.0f;
    return a * (f2){__builtin_amdgcn_rcpf(e[0]), __builtin_amdgcn_rcpf(e[1])};
}
DI bool any_sent(u32 v) { return __builtin_amdgcn_ballot_w64(v == SENT) != 0; }
DI void wait_tok(u4& t0, u4& t1, rsrc_t rw, int vo, int so) {
    if (any_sent(umax(max4(t0), max4(t1)))) {
#pragma nounroll
        for (int sp = 0; sp < SPIN_MAX; ++sp) {
            FENCE();
            t0 = bld4<SC1>(rw, vo, so); t1 = bld4<SC1>(rw, vo, so + 16384);
            if (!any_sent(umax(max4(t0), max4(t1)))) break;
        }
    }
}
DI u4 ldw1(LAS char* wb, int kk, int ft) {
    s4 a0 = __builtin_amdgcn_ds_read_tr16_b64_v4i16((LAS s4*)(wb + kk * 32 * 224 + ft * 32));
    s4 a1 = __builtin_amdgcn_ds_read_tr16_b64_v4i16((LAS s4*)(wb + (kk * 32 + 16) * 224 + ft * 32));
    u2 x0 = __builtin_bit_cast(u2, a0), x1 = __builtin_bit_cast(u2, a1);
    return (u4){x0[0], x0[1], x1[0], x1[1]};
}
DI u2 pk4(f4 a) { return (u2){pk_b(a[0], a[1]), pk_b(a[2], a[3])}; }
DI u32 silu_pk(u32 p) { return pk_b2(silu2(bf_2(p))); }
DI u4 silu_pk4(u4 p) { return (u4){silu_pk(p[0]), silu_pk(p[1]), silu_pk(p[2]), silu_pk(p[3])}; }
DI void lds_set(LAS u32* p, u32 v) {
    asm volatile("s_waitcnt lgkmcnt(0)" ::: "memory");
    __hip_atomic_store(p, v, __ATOMIC_RELAXED, __HIP_MEMORY_SCOPE_WORKGROUP);
}
DI void lds_wait(LAS u32* p, u32 v) {
#pragma nounroll
    for (int sp = 0; sp < SPIN_MAX; ++sp) {
        if (__builtin_amdgcn_readfirstlane(__hip_atomic_load(p, __ATOMIC_RELAXED, __HIP_MEMORY_SCOPE_WORKGROUP)) >= v) break;
        __builtin_amdgcn_s_sleep(1);
    }
    FENCE();
}
DI void flag_wait(GAS u32* p, u32 tag) {
#pragma nounroll
    for (int sp = 0; sp < SPIN_MAX; ++sp) {
        if (__builtin_amdgcn_ballot_w64(__hip_atomic_load(p, __ATOMIC_RELAXED, __HIP_MEMORY_SCOPE_AGENT) != tag) == 0) break;
        __builtin_amdgcn_s_sleep(2);
    }
    FENCE();
}
#define SENT4 ((u4){SENT, SENT, SENT, SENT})

__global__ __launch_bounds__(512, 2) void k_fused(const uint16_t* x, const uint16_t* wr, const uint16_t* wi, const uint16_t* w1,
                                                  const uint16_t* w2, uint16_t* out, char* ws) {
    extern __shared__ __attribute__((aligned(16))) char smem[];
    const int tid = threadIdx.x, l = tid & 63, g = l >> 4, m = l & 15;
    const int w = __builtin_amdgcn_readfirstlane(tid >> 6);
    const int b = (blockIdx.x & 7) * 32 + (blockIdx.x >> 3);
    const int mt1 = (b >> 6) * 4 + ((b >> 3) & 3), nt1 = ((b >> 5) & 1) * 8 + (b & 7);
    const int grp = b >> 4, th = (b >> 3) & 1, fb = b & 7;
    const rsrc_t rw = mkrs(ws, WS_END), ro = mkrs(out, 2097152);
    char* scr = smem + LDS_SCR;
    LAS u32* hw = (LAS u32*)((LAS char*)smem + LDS_SCR + SCR_F);
    const u32 lds0 = (u32)(uintptr_t)(LAS char*)smem;
    const float mf = (float)m, gf = (float)g, sg = (m & 1) ? -1.0f : 1.0f;
    const bool aw = w < 4;
    ST_DECL;
    const int rq = ((b >> 3) + b) & 3;
    const int row0 = (tid & 255) / 14, cch0 = (tid & 255) - row0 * 14;
    const u32 voff0 = row0 * 3584 + cch0 * 16;
    const char* wsrc = (const char*)w1 + nt1 * 224;
    auto w1_burst = [&](int i0, int n) {
#pragma unroll
        for (int i = i0; i < i0 + n; ++i) {
            const int q = (i / 7 + rq) & 3, j = i % 7;
            const int cj = (4 * j) % 14, rj = 18 * j + (4 * j) / 14;
            u32 vo = voff0 + rj * 3584 + cj * 16 + (cch0 + cj >= 14 ? 3584 - 224 : 0);
            glds16(wsrc + q * (128 * 3584), vo, lds0 + (w - 4) * 1024 + q * 28672 + j * 4096);
        }
    };
    const u32 epoch = __hip_atomic_load((GAS u32*)(ws + WS_EP), __ATOMIC_RELAXED, __HIP_MEMORY_SCOPE_AGENT);
    FENCE();
    u32 xd[8];
    u2 w2v[8];
    const int task = 2 * b + w - 4;
    if (aw) {
        const u32* xr = (const u32*)(x + (256 * (2 * w + ((m >> 1) & 1)) + b) * 512) + 8 * g + 2 * (m >> 2) + (m & 1);
#pragma unroll
        for (int j = 0; j < 8; ++j) xd[j] = xr[32 * j];
        if (tid < 8) hw[tid] = 0;
    } else if (w < 7) {
#pragma unroll
        for (int i = 0; i < 7; ++i) {
            const int inst = umin((w - 4) * 7 + i, 19), arr = inst >= 10, part = (inst - 10 * arr) >= 5, j = inst - 10 * arr - 5 * part;
            glds16((const char*)(arr ? wi : wr) + (part ? 255 - b : b) * 4112, umin(j * 64 + l, part && b ? 289 : 256) * 16, lds0 + LDS_SCR + SCR_X + inst * 1024);
        }
        if (w < 6 && task < 448) {
            const int ks = task >> 3, P = ks / 7, q = ks - 7 * P;
            const int r0 = 224 * P + (q < 3 ? 32 * q : (q < 6 ? 16 + 32 * q : 96)), dr = q < 6 ? 16 : 112;
#pragma unroll
            for (int j = 0; j < 8; ++j) w2v[j] = *(const u2*)(w2 + (r0 + (j >> 2) * dr + 4 * g + (j & 3)) * 512 + (task & 7) * 64 + 4 * m);
        }
    } else {
#pragma unroll
        for (int i = 0; i < 16; ++i) bst4<SC1>(SENT4, rw, l * 16 + (i & 3) * 1024, WS_UW + b * 16384 + (i >> 2) * 4096);
        if (l < 2) bst4<SC1>(SENT4, rw, l * 16, WS_WB + b * 32);
    }
    FENCE();
    ST(2);
    float c16[4], s16[4];
    f2 twc[2], tws[2];
#pragma unroll
    for (int u = 0; u < 4; ++u) {
        float a = (4.0f * gf + u) * (mf * (1.0f / 16));
        c16[u] = cosr(a); s16[u] = sinr(a);
        twc[u >> 1][u & 1] = cosr(a * (1.0f / 32)); tws[u >> 1][u & 1] = sinr(a * (1.0f / 32));
    }
    u4 Gre3, Gim3, SGre, SGim, GA[2];
    auto consts3 = [&]() {
#pragma unroll
        for (int u = 0; u < 4; ++u) {
            const float k = 4.0f * gf + u;
            const bool k0 = u == 0 && g == 0;
            float cc = c16[u], ss = s16[u];
            Gre3[u] = pk_b(cc, -ss); Gim3[u] = pk_b(ss, cc);
            float a2 = (2.0f * k + 1.0f) * (mf * (1.0f / 32));
            u32 sp = pk_b(g < 2 ? 2.f * cc : 2.f * sg * cosr(a2), g < 2 ? -2.f * ss : -2.f * sg * sinr(a2));
            SGre[u] = k0 ? pk_b(1.0f, sg) : (g < 2 ? sp : 0u);
            SGim[u] = g < 2 ? 0u : sp;
            float a4 = k * (mf * (1.0f / 32)), c4 = 2.f * cosr(a4), s4_ = -2.f * sinr(a4);
            GA[0][u] = k0 ? pk_b(1.0f, sg) : pk_b(c4, s4_);
            GA[1][u] = k0 ? pk_b(1.0f, sg) : ((u & 1) ? pk_b(-c4, -s4_) : pk_b(c4, s4_));
        }
        PIN(Gre3); PIN(Gim3); PIN(SGre); PIN(SGim); PIN(GA[0]); PIN(GA[1]); PIN(twc[0]); PIN(twc[1]); PIN(tws[0]); PIN(tws[1]);
    };
    if (aw) {
        u4 FAre, FAim, Fre, Fim, Sre, Sim; u2 Mb; f2 pc[2], ps[2];
        {
            float c0[8], s0[8];
#pragma unroll
            for (int j = 0; j < 8; ++j) {
                float a = (gf + 4.0f * j) * (mf * (1.0f / 32));
                c0[j] = cosr(a); s0[j] = m ? -sinr(a) : ((g & 1) ? -1.0f : 1.0f);
            }
            const float mo = (float)(2 * (m & 7) + 1) * (1.0f / 32);
#pragma unroll
            for (int u = 0; u < 4; ++u) {
                FAre[u] = pk_h(c0[2 * u], c0[2 * u + 1]); FAim[u] = pk_h(s0[2 * u], s0[2 * u + 1]);
                float cc = c16[u], ss = s16[u];
                Fre[u] = pk_h(cc, ss); Fim[u] = pk_h(-ss, cc);
                float a2 = (4.0f * gf + u) * mo;
                u32 re = pk_h(m < 8 ? cc : cosr(a2), 0.f), im = pk_h(m < 8 ? (m ? -ss : ((u & 1) ? -1.0f : 1.0f)) : -sinr(a2), 0.f);
                Sre[u] = m < 8 ? re : re << 16; Sim[u] = m < 8 ? im : im << 16;
                pc[u & 1][u >> 1] = twc[u >> 1][u & 1]; ps[u & 1][u >> 1] = tws[u >> 1][u & 1];
            }
#pragma unroll
            for (int e = 0; e < 2; ++e) {
                float a = (float)(((m >> 1) * (256 * (2 * g + e) + b)) & 2047) * (1.0f / 2048);
                float cc = cosr(a), ss = sinr(a);
                Mb[e] = (m & 1) ? pk_h(-ss, cc) : pk_h(cc, ss);
            }
            PIN(FAre); PIN(FAim); PIN(Fre); PIN(Fim); PIN(Sre); PIN(Sim); PIN(Mb); PIN(pc[0]); PIN(pc[1]); PIN(ps[0]); PIN(ps[1]);
        }
        ST(3);
        f2 v[8];
#pragma unroll
        for (int j = 0; j < 8; ++j) v[j] = bf_2(xd[j]);
        f2 s2 = ((v[0] + v[1]) + (v[2] + v[3])) + ((v[4] + v[5]) + (v[6] + v[7]));
        const float mean = red_lanes<false>(s2[0] + s2[1]) * (1.0f / 512);
        f2 q0 = {0.f, 0.f}, q1 = {0.f, 0.f};
#pragma unroll
        for (int j = 0; j < 8; ++j) { v[j] -= mean; if (j & 1) q1 = fma2(v[j], v[j], q1); else q0 = fma2(v[j], v[j], q0); }
        q0 += q1;
        const float inv = ln_inv(red_lanes<false>(q0[0] + q0[1]) * (1.0f / 512));
        const float off = (mean - rbf(mean)) * inv;
#pragma unroll
        for (int j = 0; j < 8; ++j) v[j] = fma2(v[j], (f2){inv, inv}, (f2){off, off});
        u4 B[2];
#pragma unroll
        for (int p = 0; p < 2; ++p) {
            u4 A = {pk_h(v[0][p], v[1][p]), pk_h(v[2][p], v[3][p]), pk_h(v[4][p], v[5][p]), pk_h(v[6][p], v[7][p])};
            f4 Dre = mma_h(A, FAre, Z4), Dim = mma_h(A, FAim, Z4);
#pragma unroll
            for (int r = 0; r < 2; ++r) {
                f2 re = r ? hi2(Dre) : lo2(Dre), im = r ? hi2(Dim) : lo2(Dim);
                f2 tr = fma2(im, ps[p], re * pc[p]), ti = fma2(-re, ps[p], im * pc[p]);
                B[r][p] = pk_h(tr[0], ti[0]); B[r][2 + p] = pk_h(tr[1], ti[1]);
            }
        }
#pragma unroll
        for (int r = 0; r < 2; ++r) {
            f4 Gre = mma_h(Fre, B[r], Z4), Gim = mma_h(Fim, B[r], Z4), Hre = mma_h(Sre, B[r], Z4), Him = mma_h(Sim, B[r], Z4);
            u4 Z;
#pragma unroll
            for (int i = 0; i < 4; ++i) Z[i] = pk_h(m ? Gre[i] : Hre[i], m ? Gim[i] : Him[i]);
            *(u4*)(scr + (2 * w + r) * 1024 + m * 64 + g * 16) = Z;
        }
        BARRIER();
        u4 r0 = *(const u4*)(scr + (2 * g) * 1024 + w * 256 + m * 16);
        u4 r1 = *(const u4*)(scr + (2 * g + 1) * 1024 + w * 256 + m * 16);
#pragma unroll
        for (int t = 0; t < 4; ++t) {
            f4 D = mma16_h(Mb, (u2){r0[t], r1[t]}, Z4);
            *(u2*)(scr + SCR_S + w * 4096 + m * 256 + (((4 * t + (g >> 1)) ^ (m >> 1)) * 16) + (g & 1) * 8) =
                (u2){clean(pk_h(D[0], D[1])), clean(pk_h(D[2], D[3]))};
        }
    } else {
        if (w == 7) {
            WAITV(0);
            if (l < 8) __hip_atomic_store((GAS u32*)(ws + WS_FA) + l * 256 + b, epoch + 1u, __ATOMIC_RELAXED, __HIP_MEMORY_SCOPE_AGENT);
#pragma unroll
            for (int i = 0; i < 8; ++i) bst4<SC1>(SENT4, rw, l * 16 + (i & 3) * 1024, WS_V + b * 8192 + (i >> 2) * 4096);
#pragma unroll
            for (int i = 0; i < 8; ++i) bst4<SC1>(SENT4, rw, (l >> 3) * 256 + (l & 7) * 16 + (i & 1) * 2048, WS_LN2 + (b >> 1) * 16384 + (b & 1) * 128 + (i >> 1) * 4096);
            consts3();
        } else {
            consts3();
            WAITV(0);
            __hip_atomic_store(hw + 4 + w, epoch + 1u, __ATOMIC_RELAXED, __HIP_MEMORY_SCOPE_WORKGROUP);
        }
#pragma nounroll
        for (int sp = 0; sp < SPIN_MAX; ++sp) {
            if (__builtin_amdgcn_ballot_w64(__hip_atomic_load(hw + 8 + (l % 3), __ATOMIC_RELAXED, __HIP_MEMORY_SCOPE_WORKGROUP) != epoch + 1u) == 0) break;
            __builtin_amdgcn_s_sleep(1);
        }
        FENCE();
        ST(3);
        const uint16_t* sr = (const uint16_t*)(scr + SCR_X);
        const uint16_t* si = sr + 5120;
        const int em0 = b ? 2560 + 2056 : 0;
        const int j = (tid >> 4) & 15, col = 16 * m + j;
        const int k = m ? m + 32 * j : (j < 8 ? 32 * j : 16 + 32 * (j - 8));
        const bool mir = k > 256;
        u32 o[8];
#pragma unroll
        for (int i = 0; i < 8; ++i) {
            int e = mir ? ((i ? 2560 + (8 - i) * 257 : em0) + 512 - k) : (i * 257 + k);
            o[i] = clean(((u32)sr[e] | ((u32)si[e] << 16)) ^ (mir ? 0x80000000u : 0u));
        }
        const int sw = (col >> 3) & 7, c0 = col * 4 + 2;
        *(u4*)(scr + SCR_S + (c0 ^ sw) * 16) = (u4){o[0], o[1], o[2], o[3]};
        *(u4*)(scr + SCR_S + ((c0 + 1) ^ sw) * 16) = (u4){o[4], o[5], o[6], o[7]};
        if (w == 4) {
            if (l < 8) {
                const int ed = l * 257, em = l ? 2560 + (8 - l) * 257 : em0;
                float ar = bf_lo(sr[ed]) + bf_lo(sr[em]), ai = bf_lo(si[ed]) - bf_lo(si[em]);
                float br = bf_lo(sr[ed + 256]) + bf_lo(sr[em + 256]), bi = bf_lo(si[ed + 256]) - bf_lo(si[em + 256]);
                *(u32*)(scr + SCR_S + 32 + l * 4) = clean(pk_b(0.25f * (ar + br), 0.25f * (ai + bi)));
                o[0] = clean(pk_b(0.25f * (ar - br), 0.25f * (ai - bi)));
            }
        }
        if (w < 6 && task < 448) {
#pragma unroll
            for (int t = 0; t < 4; ++t) {
                u4 f;
#pragma unroll
                for (int jj = 0; jj < 4; ++jj) f[jj] = __builtin_amdgcn_perm(w2v[2 * jj + 1][t >> 1], w2v[2 * jj][t >> 1], (t & 1) ? 0x07060302u : 0x05040100u);
                bst4<SC1>(f, rw, l * 16 + t * 1024, WS_W2T + ((task & 7) * 56 + (task >> 3)) * 4096);
            }
        }
        w1_burst(0, W1_B0);
        if (w == 7) WAITV(W1_B0);
        BARRIER();
        if (w == 4 && l < 8) __builtin_amdgcn_raw_buffer_store_b32(o[0], rw, l * 4, WS_WB + b * 32, SC1);
    }
    BARRIER();
    ST(4);
    const u32 tag = __builtin_amdgcn_readfirstlane(epoch) + 1u;
#pragma unroll
    for (int it = 0; it < 2; ++it)
        bst4<SC1>(*(const u4*)(scr + SCR_S + it * 8192 + (tid ^ ((tid >> 5) & 7)) * 16), rw, tid * 16 + it * 8192, WS_UW + b * 16384);
    const int xoff = (256 * w + b) * 1024 + m * 32 + g * 8;
    u2 xl[2];
    xl[0] = *(const u2*)((const char*)x + xoff); xl[1] = *(const u2*)((const char*)x + xoff + 512);
    FENCE();
    if (aw) {
        u4 Fre, Fim, Gre, Gim, N0, N1; f2 tc[2], ts[2];
#pragma unroll
        for (int u = 0; u < 4; ++u) {
            float cc = c16[u], ss = s16[u];
            Fre[u] = pk_h(0.25f * cc, 0.25f * ss); Fim[u] = pk_h(-0.25f * ss, 0.25f * cc);
            Gre[u] = pk_b(cc * (1.0f / 256), -ss * (1.0f / 256)); Gim[u] = pk_b(ss * (1.0f / 256), cc * (1.0f / 256));
            float a3 = (4.0f * gf + u) * (mf * (1.0f / 256));
            tc[u >> 1][u & 1] = cosr(a3); ts[u >> 1][u & 1] = sinr(a3);
        }
        PIN(Fre); PIN(Fim); PIN(Gre); PIN(Gim); PIN(tc[0]); PIN(tc[1]); PIN(ts[0]); PIN(ts[1]);
        WAITV(0);
        BARRIER();
        ST(5);
        {
            u4 d[4], e[2] = {{0, 0, 0, 0}, {0, 0, 0, 0}};
#pragma nounroll
            for (int sp = 0; sp < SPIN_MAX; ++sp) {
                FENCE();
#pragma unroll
                for (int it = 0; it < 4; ++it) d[it] = bld4<SC1>(rw, (tid >> 2) * 16384 + (tid & 3) * 16, WS_UW + it * (64 * 16384) + b * 64);
                if (b == 0) { e[0] = bld4<SC1>(rw, tid * 16, WS_WB); e[1] = bld4<SC1>(rw, tid * 16 + 4096, WS_WB); }
                if (!any_sent(umax(umax(umax(max4(d[0]), max4(d[1])), umax(max4(d[2]), max4(d[3]))), umax(max4(e[0]), max4(e[1]))))) break;
            }
            char* sp = scr + SCR_S + (tid & 2) * 4096 + (tid >> 2) * 32 + (((tid ^ (tid >> 5)) & 1) * 16);
#pragma unroll
            for (int it = 0; it < 4; ++it) *(u4*)(sp + it * 2048) = d[it];
            if (b == 0) {
                *(u4*)(scr + (tid ^ ((tid >> 4) & 1)) * 16) = e[0]; *(u4*)(scr + 4096 + (tid ^ ((tid >> 4) & 1)) * 16) = e[1];
                if (tid == 0) __hip_atomic_store((GAS u32*)(ws + WS_EP), tag, __ATOMIC_RELAXED, __HIP_MEMORY_SCOPE_AGENT);
            }
        }
        BARRIER();
        ST(6);
        u2 ld[4], wl[4];
        const int so = (64 * g + m) * 32 + (((w >> 1) ^ (m >> 3)) * 16) + (w & 1) * 8;
#pragma unroll
        for (int q = 0; q < 4; ++q) ld[q] = *(const u2*)(scr + SCR_S + so + 512 * q);
#pragma unroll
        for (int q = 0; q < 4; ++q) wl[q] = *(const u2*)(scr + SCR_S + so + 8192 + 512 * q);
        FENCE();
        f4 Xre[2], Xim[2];
#pragma unroll
        for (int e = 0; e < 2; ++e) {
            u4 A = {ld[0][e], ld[1][e], ld[2][e], ld[3][e]};
            f4 Dre = mma_h(A, Fre, Z4), Dim = mma_h(A, Fim, Z4);
            f2 r0 = fma2(lo2(Dim), ts[0], lo2(Dre) * tc[0]), i0 = fma2(-lo2(Dre), ts[0], lo2(Dim) * tc[0]);
            f2 r1 = fma2(hi2(Dim), ts[1], hi2(Dre) * tc[1]), i1 = fma2(-hi2(Dre), ts[1], hi2(Dim) * tc[1]);
            u4 B = {pk_h(r0[0], i0[0]), pk_h(r0[1], i0[1]), pk_h(r1[0], i1[0]), pk_h(r1[1], i1[1])};
            Xre[e] = mma_h(Fre, B, Z4); Xim[e] = mma_h(Fim, B, Z4);
        }
        f2 yr[2][2], yi[2][2];
#pragma unroll
        for (int e = 0; e < 2; ++e)
#pragma unroll
            for (int hh = 0; hh < 2; ++hh) {
                f2 wre = {bf_lo(wl[2 * hh][e]), bf_lo(wl[2 * hh + 1][e])}, wim = {bf_hi(wl[2 * hh][e]), bf_hi(wl[2 * hh + 1][e])};
                f2 xr = hh ? hi2(Xre[e]) : lo2(Xre[e]), xi = hh ? hi2(Xim[e]) : lo2(Xim[e]);
                yr[e][hh] = fma2(-xi, wim, xr * wre); yi[e][hh] = fma2(xr, wim, xi * wre);
            }
        if (b == 0) {
            f2* sx = (f2*)(scr + SCR_X);
            u2 wb[4];
#pragma unroll
            for (int q = 0; q < 4; ++q) wb[q] = *(const u2*)(scr + so + 512 * q);
#pragma unroll
            for (int e = 0; e < 2; ++e)
#pragma unroll
                for (int i = 0; i < 4; ++i) sx[2 * w + e + 8 * (m + 16 * (4 * g + i))] = (f2){Xre[e][i], Xim[e][i]};
            BARRIER();
#pragma unroll
            for (int e = 0; e < 2; ++e)
#pragma unroll
                for (int i = 0; i < 4; ++i) {
                    f2 xm = sx[(2048 - (2 * w + e + 8 * (m + 16 * (4 * g + i)))) & 2047];
                    float br = bf_lo(wb[i][e]), bi = bf_hi(wb[i][e]);
                    yr[e][i >> 1][i & 1] += xm[0] * br + xm[1] * bi;
                    yi[e][i >> 1][i & 1] += xm[0] * bi - xm[1] * br;
                }
        }
#pragma unroll
        for (int e = 0; e < 2; ++e) {
            u4 Y = {pk_b(yr[e][0][0], yi[e][0][0]), pk_b(yr[e][0][1], yi[e][0][1]), pk_b(yr[e][1][0], yi[e][1][0]), pk_b(yr[e][1][1], yi[e][1][1])};
            f4 Dre = mma_b(Y, Gre, Z4), Dim = mma_b(Y, Gim, Z4);
            f2 r0 = fma2(-lo2(Dim), ts[0], lo2(Dre) * tc[0]), i0 = fma2(lo2(Dre), ts[0], lo2(Dim) * tc[0]);
            f2 r1 = fma2(-hi2(Dim), ts[1], hi2(Dre) * tc[1]), i1 = fma2(hi2(Dre), ts[1], hi2(Dim) * tc[1]);
            u4 A = {pk_b(r0[0], i0[0]), pk_b(r0[1], i0[1]), pk_b(r1[0], i1[0]), pk_b(r1[1], i1[1])};
            f4 Ere = mma_b(A, Gre, Z4), Eim = mma_b(A, Gim, Z4);
            u4 o;
#pragma unroll
            for (int i = 0; i < 4; ++i) o[i] = clean(pk_b(Ere[i], Eim[i]));
            *(u4*)(scr + (2 * w + e) * 1040 + m * 64 + g * 16) = o;
        }
        BARRIER();
        ST(7);
#pragma unroll
        for (int it = 0; it < 2; ++it) {
            const int ch = tid + 256 * it;
            const u32* sp = (const u32*)(scr + (ch & 1) * 4160 + (ch >> 1) * 4);
            bst4<SC1>((u4){sp[0], sp[260], sp[520], sp[780]}, rw, ch * 16, WS_V + b * 8192);
        }
        FENCE();
        consts3();
#pragma unroll
        for (int u = 0; u < 4; ++u) {
            float a1 = (float)(((4 * (g & 1) + u) * (256 * (m >> 1) + b)) & 2047) * (1.0f / 2048);
            float c1 = cosr(a1), s1 = sinr(a1);
            u32 nb = (m & 1) ? pk_b(s1, c1) : pk_b(c1, -s1);
            N0[u] = g < 2 ? nb : 0u; N1[u] = g < 2 ? 0u : nb;
        }
        PIN(N0); PIN(N1);
        WAITV(0);
        ST(8);
        const int vo = (2 * m + (g >> 1)) * 8192 + (g & 1) * 16, vs = WS_V + w * 64 * 8192 + b * 32;
        u4 v0, v1;
#pragma nounroll
        for (int sp = 0; sp < SPIN_MAX; ++sp) {
            FENCE();
            v0 = bld4<SC1>(rw, vo, vs); v1 = bld4<SC1>(rw, vo, vs + 32 * 8192);
            if (!any_sent(umax(max4(v0), max4(v1)))) break;
        }
        ST(9);
        char* sz = scr + SCR_X + g * 2048 + w * 256 + m * 8;
#pragma unroll
        for (int hh = 0; hh < 2; ++hh) {
            f4 D0 = mma_b(N0, hh ? v1 : v0, Z4), D1 = mma_b(N1, hh ? v1 : v0, Z4);
            *(u2*)(sz + hh * 128) = (u2){pk_b(D0[0], D0[1]), pk_b(D1[0], D1[1])};
            *(u2*)(sz + 1024 + hh * 128) = (u2){pk_b(D0[2], D0[3]), pk_b(D1[2], D1[3])};
        }
    } else {
        if (w == 7) {
#pragma nounroll
            for (int sp = 0; sp < SPIN_MAX; ++sp) {
                FENCE();
                u4 f = bld4<SC1>(rw, l * 16, WS_FA + (blockIdx.x & 7) * 1024);
                if (__builtin_amdgcn_ballot_w64(f[0] != tag || f[1] != tag || f[2] != tag || f[3] != tag) == 0) break;
                __builtin_amdgcn_s_sleep(2);
            }
        }
        BARRIER();
        BARRIER();
        w1_burst(W1_B0, W1_B1);
        WAITV(W1_B1);
        if (b == 0) BARRIER();
        BARRIER();
        __builtin_amdgcn_s_sleep(3);
        w1_burst(W1_B0 + W1_B1, W1_B2);
    }
    BARRIER();
    if (!aw) w1_burst(W1_B0 + W1_B1 + W1_B2, W1_B3);
    {
        u4 A = *(const u4*)(scr + SCR_X + w * 1024 + m * 64 + g * 16);
        f4 Dre = mma_b(A, Gre3, Z4), Dim = mma_b(A, Gim3, Z4), Hre = mma_b(A, SGre, Z4), Him = mma_b(A, SGim, Z4);
        if (g == 0) { Dre[0] = Hre[0]; Dim[0] = Him[0]; }
        f2 r0 = fma2(-lo2(Dim), tws[0], lo2(Dre) * twc[0]), i0 = fma2(lo2(Dre), tws[0], lo2(Dim) * twc[0]);
        f2 r1 = fma2(-hi2(Dim), tws[1], hi2(Dre) * twc[1]), i1 = fma2(hi2(Dre), tws[1], hi2(Dim) * twc[1]);
        u4 A2 = {pk_b(r0[0], i0[0]), pk_b(r0[1], i0[1]), pk_b(r1[0], i1[0]), pk_b(r1[1], i1[1])};
        f2 h[4];
#pragma unroll
        for (int t = 0; t < 2; ++t) {
            f4 Yv = mma_b(A2, GA[t], Z4);
            u32 h0 = pk_b2(bf_2(xl[t][0]) + bf_2(pk_b(Yv[0], Yv[1]))), h1 = pk_b2(bf_2(xl[t][1]) + bf_2(pk_b(Yv[2], Yv[3])));
            bst2<SC1>((u2){h0, h1}, ro, xoff + 512 * t, 0);
            h[2 * t] = bf_2(h0); h[2 * t + 1] = bf_2(h1);
        }
        f2 s2 = (h[0] + h[1]) + (h[2] + h[3]);
        const float mean = red_lanes<true>(s2[0] + s2[1]) * (1.0f / 512);
        f2 q2 = {0.f, 0.f};
#pragma unroll
        for (int j = 0; j < 4; ++j) { f2 d = h[j] - mean; q2 = fma2(d, d, q2); }
        const float inv = ln_inv(red_lanes<true>(q2[0] + q2[1]) * (1.0f / 512));
        const float mb = rbf(mean);
#pragma unroll
        for (int t = 0; t < 2; ++t)
            *(u2*)(scr + ((8 * t + (m >> 1)) * 4 + g) * 128 + (w ^ (m >> 1)) * 16 + (m & 1) * 8) =
                (u2){clean(pk_b2(bf_2(pk_b2(h[2 * t] - mb)) * inv)), clean(pk_b2(bf_2(pk_b2(h[2 * t + 1] - mb)) * inv))};
        WAITV(2);
        BARRIER();
        u4 lo = *(const u4*)(scr + (tid ^ ((tid >> 5) & 7)) * 16);
        bst4<SC1>(lo, rw, (tid >> 3) * 256 + (tid & 7) * 16, WS_LN2 + (b >> 1) * 16384 + (b & 1) * 128);
        FENCE();
        ST(10);
    }
    const int pw = (w + 4 * th) & 7;
    GAS u32* fl = (GAS u32*)(ws + WS_FLAG);
    int ko[7];
    {
        const int r6 = (fb < 6 ? fb : fb - 5) + 3 * (grp & 1);
#pragma unroll
        for (int ks = 0; ks < 6; ++ks) { int k = ks + r6; k = k >= 6 ? k - 6 : k; ko[ks] = k >= 6 ? k - 6 : k; }
        ko[6] = 6;
    }
    u4 W[7][4];
    auto ldw2 = [&](int k0, int k1, bool throttle) {
#pragma unroll
        for (int ks = 0; ks < 7; ++ks)
            if (ks >= k0 && ks < k1) {
#pragma unroll
                for (int t = 0; t < 4; ++t) {
                    W[ks][t] = bld4<SC1>(rw, l * 16 + t * 1024, WS_W2T + (fb * 56 + pw * 7 + ko[ks]) * 4096);
                    if (t & 1) { SCHED(); if (throttle) WAITV(W2_OUT); }
                }
            }
    };
    auto publish = [&]() {
        u32 prev = 0;
        if (l == 0) prev = __hip_atomic_fetch_add(hw + 4, 1u, __ATOMIC_RELAXED, __HIP_MEMORY_SCOPE_WORKGROUP);
        if (__builtin_amdgcn_readfirstlane(prev) == 7u && l < 2)
            __hip_atomic_store(fl + (l ? 4096 + b : (mt1 * 8 + (nt1 >> 1)) * 32 + (nt1 & 1)), tag, __ATOMIC_RELAXED, __HIP_MEMORY_SCOPE_AGENT);
        FENCE();
    };
    {
        const int tg = w & 3;
        char* xch = scr + SCR_X + tg * 5120 + l * 16;
        const int hoff = WS_H1 + (mt1 * 8 + 2 * tg) * (56 * 1024) + (nt1 >> 1) * 7168;
        if (aw) {
            const int toff = WS_LN2 + (mt1 * 8 + 2 * tg) * 16384;
            LAS char* wb0 = (LAS char*)smem + (4 * g + (m >> 2)) * 224 + (m & 3) * 8;
            LAS char* wb[4];
            int tq[4];
#pragma unroll
            for (int q = 0; q < 4; ++q) { const int qq = (q + rq) & 3; wb[q] = wb0 + qq * 28672; tq[q] = toff + qq * 4096; }
            u4 T[2][16], A[2][5];
            auto ldt = [&](int kk) {
#pragma unroll
                for (int tt = 0; tt < 2; ++tt) T[tt][kk] = bld4<SC1>(rw, l * 16 + (kk & 3) * 1024, tq[kk >> 2] + tt * 16384);
            };
#pragma unroll
            for (int i = 0; i < 5; ++i) A[0][i] = ldw1(wb[0], 0, i < 4 ? i : 6);
            WAITV(0);
            ST(11);
#pragma unroll
            for (int kk = 0; kk < G1_DEPTH; ++kk) { ldt(kk); SCHED(); }
            f4 acc[2][5];
#pragma unroll
            for (int tt = 0; tt < 2; ++tt)
#pragma unroll
                for (int i = 0; i < 5; ++i) acc[tt][i] = Z4;
#pragma unroll
            for (int kk = 0; kk < 16; ++kk) {
                if ((kk & 1) == 0) {
                    if (__builtin_expect(any_sent(umax(umax(max4(T[0][kk]), max4(T[1][kk])), umax(max4(T[0][kk + 1]), max4(T[1][kk + 1])))), 0)) {
                        wait_tok(T[0][kk], T[1][kk], rw, l * 16, tq[kk >> 2] + (kk & 3) * 1024);
                        wait_tok(T[0][kk + 1], T[1][kk + 1], rw, l * 16, tq[kk >> 2] + ((kk + 1) & 3) * 1024);
                    }
                    if (kk == 0) ST(12);
                    if (kk == W1_CHK) lds_wait(hw + 5, 4u);
                }
                if (kk + G1_DEPTH < 16) ldt(kk + G1_DEPTH);
                if (kk < 15) {
#pragma unroll
                    for (int i = 0; i < 5; ++i) A[(kk + 1) & 1][i] = ldw1(wb[(kk + 1) >> 2], (kk + 1) & 3, i < 4 ? i : 6);
                }
#pragma unroll
                for (int i = 0; i < 5; ++i)
#pragma unroll
                    for (int tt = 0; tt < 2; ++tt) acc[tt][i] = mma_b(A[kk & 1][i], T[tt][kk], acc[tt][i]);
#pragma unroll
                for (int i = 0; i < 10; ++i) {
                    __builtin_amdgcn_sched_group_barrier(0x8, 1, 0);
                    if (i == 1 || i == 5) __builtin_amdgcn_sched_group_barrier(0x20, 1, 0);
                    __builtin_amdgcn_sched_group_barrier(0x100, 1, 0);
                }
                if (kk & 1) {} else SCHED();
            }
            u4 A2[4][2];
#pragma unroll
            for (int kk = 0; kk < 3; ++kk)
#pragma unroll
                for (int i = 0; i < 2; ++i) A2[kk][i] = ldw1(wb[0], kk, 4 + i);
            SCHED();
            {
                u2 p[2][5];
#pragma unroll
                for (int tt = 0; tt < 2; ++tt)
#pragma unroll
                    for (int i = 0; i < 5; ++i) p[tt][i] = pk4(acc[tt][i]);
#pragma unroll
                for (int tt = 0; tt < 2; ++tt)
#pragma unroll
                    for (int q = 0; q < 2; ++q) *(u4*)(xch + (2 * tt + q) * 1024) = (u4){p[tt][2 * q][0], p[tt][2 * q][1], p[tt][2 * q + 1][0], p[tt][2 * q + 1][1]};
                *(u4*)(xch + 4096) = (u4){p[0][4][0], p[0][4][1], p[1][4][0], p[1][4][1]};
            }
            lds_set(hw + tg, 1u);
            ST(13);
            f4 ac2[2][2] = {{Z4, Z4}, {Z4, Z4}};
#pragma unroll
            for (int kk = 0; kk < 16; ++kk) {
                if (kk + 3 < 16) {
#pragma unroll
                    for (int i = 0; i < 2; ++i) A2[(kk + 3) & 3][i] = ldw1(wb[(kk + 3) >> 2], (kk + 3) & 3, 4 + i);
                }
#pragma unroll
                for (int i = 0; i < 2; ++i)
#pragma unroll
                    for (int tt = 0; tt < 2; ++tt) ac2[tt][i] = mma_b(A2[kk & 3][i], T[tt][kk], ac2[tt][i]);
#pragma unroll
                for (int i = 0; i < 4; ++i) {
                    __builtin_amdgcn_sched_group_barrier(0x8, 1, 0);
                    __builtin_amdgcn_sched_group_barrier(0x100, 1, 0);
                }
                SCHED();
            }
#pragma unroll
            for (int tt = 0; tt < 2; ++tt) {
                u2 p0 = pk4(ac2[tt][0]), p1 = pk4(ac2[tt][1]);
                bst4<SC1>(silu_pk4((u4){p0[0], p0[1], p1[0], p1[1]}), rw, l * 16, hoff + tt * (56 * 1024) + ((nt1 & 1) * 3 + 2) * 1024);
            }
            FENCE();
            ST(14);
            ldw2(0, 1, false);
            WAITV(4);
            publish();
            ldw2(1, W2_ATHR, true);
            ldw2(W2_ATHR, 7, false);
        } else {
            w1_burst(W1_B0 + W1_B1 + W1_B2 + W1_B3, 28 - W1_B0 - W1_B1 - W1_B2 - W1_B3);
            WAITV(0);
            if (l == 0) __hip_atomic_fetch_add(hw + 5, 1u, __ATOMIC_RELAXED, __HIP_MEMORY_SCOPE_WORKGROUP);
            __builtin_amdgcn_s_sleep(W2_SLEEP);
            ldw2(0, W2_EARLY, true);
            ST(12);
            lds_wait(hw + tg, 1u);
            ST(13);
#pragma unroll
            for (int j = 0; j < 5; ++j) {
                u4 o = silu_pk4(*(const u4*)(xch + j * 1024));
                if (j < 4) bst4<SC1>(o, rw, l * 16, hoff + (j >> 1) * (56 * 1024) + ((nt1 & 1) * 3 + (j & 1)) * 1024);
                else {
                    bst2<SC1>((u2){o[0], o[1]}, rw, l * 8, hoff + 6144 + (nt1 & 1) * 512);
                    bst2<SC1>((u2){o[2], o[3]}, rw, l * 8, hoff + 56 * 1024 + 6144 + (nt1 & 1) * 512);
                }
            }
            FENCE();
            ST(14);
            ldw2(W2_EARLY, W2_EARLY + 1, false);
            WAITV(4);
            publish();
            ldw2(W2_EARLY + 1, 7, false);
        }
    }
    {
        ST(15);
        flag_wait(fl + (grp * 8 + pw) * 32 + (l & 1), tag);
        ST(16);
        const int rot = w & 3;
        const int toff = WS_H1 + (grp * 8 + th * 4) * (56 * 1024) + pw * 7168;
        int to[4];
#pragma unroll
        for (int tt = 0; tt < 4; ++tt) to[tt] = toff + ((tt + rot) & 3) * (56 * 1024);
        u4 T[7][4];
        auto ldtok = [&](int ks) {
#pragma unroll
            for (int tt = 0; tt < 4; ++tt) {
                if (ks < 6) T[ks][tt] = bld4<SC1>(rw, l * 16, to[tt] + ko[ks] * 1024);
                else {
                    u2 a = bld2<SC1>(rw, l * 8 + 2048, to[tt] + 4096), c = bld2<SC1>(rw, l * 8 + 2560, to[tt] + 4096);
                    T[ks][tt] = (u4){a[0], a[1], c[0], c[1]};
                }
            }
            SCHED();
        };
#pragma unroll
        for (int ks = 0; ks < G2_DEPTH; ++ks) ldtok(ks);
        const int rho = grp * 128 + th * 64 + rot * 16 + m;
        const int ooff = (256 * (rho & 7) + (rho >> 3)) * 1024 + fb * 128 + g * 32;
        u4 hres[2];
        f4 acc[4][4];
#pragma unroll
        for (int tt = 0; tt < 4; ++tt)
#pragma unroll
            for (int t = 0; t < 4; ++t) acc[tt][t] = Z4;
#pragma unroll
        for (int ks = 0; ks < 7; ++ks) {
            if (ks + G2_DEPTH < 7) { PIN(T[ks][0]); ldtok(ks + G2_DEPTH); }
#pragma unroll
            for (int tt = 0; tt < 4; ++tt)
#pragma unroll
                for (int t = 0; t < 4; ++t) acc[tt][t] = mma_b(W[ks][t], T[ks][tt], acc[tt][t]);
            SCHED();
        }
        if (!aw) {
            flag_wait(fl + 4096 + grp * 16 + th * 8 + rot * 2 + (l & 1), tag);
            hres[0] = bld4<SC1>(ro, ooff, 0); hres[1] = bld4<SC1>(ro, ooff + 16, 0);
        }
        ST(17);
        lds_wait(hw + 4, 8u);
#pragma unroll
        for (int tt = 0; tt < 4; ++tt)
            if (aw || tt > 0)
#pragma unroll
                for (int t = 0; t < 4; ++t) *(f4*)(smem + (w * 16 + ((tt + rot) & 3) * 4 + t) * 1024 + l * 16) = acc[tt][t];
        BARRIER();
        if (!aw) {
#pragma unroll
            for (int j = 1; j < 8; ++j)
#pragma unroll
                for (int t = 0; t < 4; ++t) acc[0][t] += *(const f4*)(smem + (((w + j) & 7) * 16 + rot * 4 + t) * 1024 + l * 16);
#pragma unroll
            for (int p = 0; p < 2; ++p) {
                u4 o;
#pragma unroll
                for (int j = 0; j < 4; ++j) {
                    const int i = 2 * p + (j >> 1), t = 2 * (j & 1);
                    o[j] = pk_b2(bf_2(hres[p][j]) + bf_2(pk_b(acc[0][t][i], acc[0][t + 1][i])));
                }
                bst4<AUX_OUT>(o, ro, ooff + 16 * p, 0);
            }
        }
    }
    ST_END(ws, 0);
}

extern "C" void kernel_launch(void* const* d_in, const int* in_sizes, int n_in, void* d_out, int out_size,
                              void* d_ws, size_t ws_size, hipStream_t stream) {
    (void)hipFuncSetAttribute((const void*)k_fused, hipFuncAttributeMaxDynamicSharedMemorySize, LDS_B);
    k_fused<<<256, 512, LDS_B, stream>>>((const uint16_t*)d_in[0], (const uint16_t*)d_in[1], (const uint16_t*)d_in[2],
                                         (const uint16_t*)d_in[4], (const uint16_t*)d_in[5], (uint16_t*)d_out, (char*)d_ws);
}
```

```cpp
#include <hip/hip_runtime.h>
#include <hip/hip_bf16.h>
#include <stdint.h>

typedef _Float16 h8 __attribute__((ext_vector_type(8)));
typedef _Float16 h4 __attribute__((ext_vector_type(4)));
typedef _Float16 h2 __attribute__((ext_vector_type(2)));
typedef __bf16 b8 __attribute__((ext_vector_type(8)));
typedef __bf16 b2 __attribute__((ext_vector_type(2)));
typedef short s4 __attribute__((ext_vector_type(4)));
typedef float f4 __attribute__((ext_vector_type(4)));
typedef float f2 __attribute__((ext_vector_type(2)));
typedef unsigned u32;
typedef u32 u4 __attribute__((ext_vector_type(4)));
typedef u32 u2 __attribute__((ext_vector_type(2)));
#define LAS __attribute__((address_space(3)))
#define GAS __attribute__((address_space(1)))
#define DI __device__ __forceinline__
#define FENCE() asm volatile("" ::: "memory")
#define PIN(x) asm volatile("" : "+v"(x))
#define SCHED() __builtin_amdgcn_sched_barrier(0)
#define WAITV(n) asm volatile("s_waitcnt vmcnt(%0)" :: "n"(n) : "memory")
#define BARRIER() do { asm volatile("s_waitcnt lgkmcnt(0)" ::: "memory"); __builtin_amdgcn_s_barrier(); FENCE(); } while (0)
constexpr int SC1 = 16;
constexpr u32 SENT = 0xffffffffu;
constexpr int SPIN_MAX = 1 << 20;

DI u32 pk_h(float a, float b) { f2 v = {a, b}; h2 r = __builtin_convertvector(v, h2); return __builtin_bit_cast(u32, r); }
DI u32 pk_b(float a, float b) { f2 v = {a, b}; b2 r = __builtin_convertvector(v, b2); return __builtin_bit_cast(u32, r); }
DI float bf_lo(u32 v) { return __builtin_bit_cast(float, v << 16); }
DI float bf_hi(u32 v) { return __builtin_bit_cast(float, v & 0xffff0000u); }
DI f2 bf_2(u32 v) { return (f2){bf_lo(v), bf_hi(v)}; }
DI u32 pk_b2(f2 v) { return pk_b(v[0], v[1]); }
DI float rbf(float a) { return bf_lo(pk_b(a, a)); }
DI float cosr(float rev) { return __builtin_amdgcn_cosf(rev); }
DI float sinr(float rev) { return __builtin_amdgcn_sinf(rev); }
DI f2 lo2(f4 v) { return __builtin_shufflevector(v, v, 0, 1); }
DI f2 hi2(f4 v) { return __builtin_shufflevector(v, v, 2, 3); }
DI f2 fma2(f2 a, f2 b, f2 c) { return __builtin_elementwise_fma(a, b, c); }
DI u32 umin(u32 a, u32 b) { return a < b ? a : b; }
DI u32 umax(u32 a, u32 b) { return a > b ? a : b; }
DI u32 clean(u32 v) { return umin(v, SENT - 1); }
DI u32 max4(u4 v) { return umax(umax(umax(v[0], v[1]), v[2]), v[3]); }
#define Z4 ((f4){0, 0, 0, 0})
DI f4 mma_h(u4 a, u4 b, f4 c) { return __builtin_amdgcn_mfma_f32_16x16x32_f16(__builtin_bit_cast(h8, a), __builtin_bit_cast(h8, b), c, 0, 0, 0); }
DI f4 mma_b(u4 a, u4 b, f4 c) { return __builtin_amdgcn_mfma_f32_16x16x32_bf16(__builtin_bit_cast(b8, a), __builtin_bit_cast(b8, b), c, 0, 0, 0); }
DI f4 mma16_h(u2 a, u2 b, f4 c) { return __builtin_amdgcn_mfma_f32_16x16x16f16(__builtin_bit_cast(h4, a), __builtin_bit_cast(h4, b), c, 0, 0, 0); }
typedef __amdgpu_buffer_rsrc_t rsrc_t;
DI rsrc_t mkrs(const void* p, u32 bytes) { return __builtin_amdgcn_make_buffer_rsrc((void*)p, 0, bytes, 0x00020000); }
template <int AUX> DI u4 bld4(rsrc_t r, int v, int s) { return __builtin_amdgcn_raw_buffer_load_b128(r, v, s, AUX); }
template <int AUX> DI u2 bld2(rsrc_t r, int v, int s) { return __builtin_amdgcn_raw_buffer_load_b64(r, v, s, AUX); }
template <int AUX> DI void bst4(u4 d, rsrc_t r, int v, int s) {
    __builtin_amdgcn_raw_buffer_store_b128(d, r, v, s, AUX);
    asm volatile("s_nop 1" :: "v"(d));
}
template <int AUX> DI void bst2(u2 d, rsrc_t r, int v, int s) { __builtin_amdgcn_raw_buffer_store_b64(d, r, v, s, AUX); }

template <int CTRL> DI float dpp(float v) {
    return __builtin_bit_cast(float, __builtin_amdgcn_update_dpp(0, __builtin_bit_cast(int, v), CTRL, 0xf, 0xf, true));
}
template <bool ALL> DI float red_lanes(float v) {
    v += dpp<0xB1>(v);
    if (ALL) v += dpp<0x4E>(v);
    v += dpp<0x124>(v); v += dpp<0x128>(v);
    u32 x = __builtin_bit_cast(u32, v), y = x;
    asm("" : "+v"(y));
    auto r = __builtin_amdgcn_permlane16_swap(x, y, false, false);
    v = __builtin_bit_cast(float, (u32)r[0]) + __builtin_bit_cast(float, (u32)r[1]);
    x = __builtin_bit_cast(u32, v); y = x;
    asm("" : "+v"(y));
    auto q = __builtin_amdgcn_permlane32_swap(x, y, false, false);
    return __builtin_bit_cast(float, (u32)q[0]) + __builtin_bit_cast(float, (u32)q[1]);
}
DI float ln_inv(float var) { return __builtin_amdgcn_rcpf(rbf(__builtin_amdgcn_sqrtf(rbf(rbf(var) + 1e-5f)))); }

#define ST_DECL
#define ST(i)
#define ST_END(ws, kid)

constexpr u32 WS_U = 0;
constexpr u32 WS_WT = WS_U + (2u << 20);
constexpr u32 WS_V = WS_WT + (2u << 20);
constexpr u32 WS_LN2 = WS_V + (2u << 20);
constexpr u32 WS_WA = WS_LN2 + (2u << 20);
constexpr u32 WS_WB = WS_WA + 8192u;
constexpr u32 WS_FA = WS_WB + 8192u;
constexpr u32 WS_FLAG = WS_FA + 16384u;
constexpr u32 WS_EP = WS_FLAG + 20480u;
constexpr u32 WS_H1 = WS_WA + (64u << 10);
constexpr u32 WS_END = WS_H1 + 7340032u;

#define W1_B0 0
#define W1_B1 7
#define W1_B2 5
#define W1_B3 6
#define W1_CHK ((((W1_B0 + W1_B1 + W1_B2 + W1_B3) / 7) * 4 - 2) < 0 ? 0 : (((W1_B0 + W1_B1 + W1_B2 + W1_B3) / 7) * 4 - 2))
#define CLR_SLEEP 6
#define BPRIO 2
#define EXP_UCLR 0
#define POLL_SLEEP 2
#define W2_SLEEP 20
#define AUX_OUT 2
#define W2_OUT 2
#define W2_ATHR 3
#define W2_EARLY 3
#define G1_DEPTH 6
#define G2_DEPTH 2
constexpr int LDS_SCR = 114688;
constexpr int SCR_X = 8448;
constexpr int SCR_S = SCR_X + 20480;
constexpr int SCR_F = SCR_S + 16384;
constexpr int LDS_B = LDS_SCR + SCR_F + 128;

DI void glds16(const void* sbase, u32 voff, u32 lds_dst) {
    asm volatile("s_mov_b32 m0, %2\n\ts_nop 0\n\tglobal_load_lds_dwordx4 %0, %1" :: "v"(voff), "s"(sbase), "s"(lds_dst) : "memory");
}
DI void glds16x4(const void* sbase, u32 voff, u32 lds_dst) {
    asm volatile("s_mov_b32 m0, %2\n\ts_nop 0\n\tglobal_load_lds_dwordx4 %0, %1\n\tglobal_load_lds_dwordx4 %0, %1 offset:1024\n\t"
                 "global_load_lds_dwordx4 %0, %1 offset:2048\n\tglobal_load_lds_dwordx4 %0, %1 offset:3072"
                 :: "v"(voff), "s"(sbase), "s"(lds_dst) : "memory");
}
DI f2 silu2(f2 a) {
    f2 t = a * -1.4426950408889634f;
    f2 e = (f2){__builtin_amdgcn_exp2f(t[0]), __builtin_amdgcn_exp2f(t[1])} + 1.0f;
    return a * (f2){__builtin_amdgcn_rcpf(e[0]), __builtin_amdgcn_rcpf(e[1])};
}
DI bool any_sent(u32 v) { return __builtin_amdgcn_ballot_w64(v == SENT) != 0; }
DI void wait_tok(u4& t0, u4& t1, rsrc_t rw, int vo, int so) {
    if (any_sent(umax(max4(t0), max4(t1)))) {
#pragma nounroll
        for (int sp = 0; sp < SPIN_MAX; ++sp) {
            FENCE();
            t0 = bld4<SC1>(rw, vo, so); t1 = bld4<SC1>(rw, vo, so + 16384);
            if (!any_sent(umax(max4(t0), max4(t1)))) break;
        }
    }
}
DI u4 ldw1(LAS char* wb, int kk, int ft) {
    s4 a0 = __builtin_amdgcn_ds_read_tr16_b64_v4i16((LAS s4*)(wb + kk * 32 * 224 + ft * 32));
    s4 a1 = __builtin_amdgcn_ds_read_tr16_b64_v4i16((LAS s4*)(wb + (kk * 32 + 16) * 224 + ft * 32));
    u2 x0 = __builtin_bit_cast(u2, a0), x1 = __builtin_bit_cast(u2, a1);
    return (u4){x0[0], x0[1], x1[0], x1[1]};
}
DI u2 pk4(f4 a) { return (u2){pk_b(a[0], a[1]), pk_b(a[2], a[3])}; }
DI u32 silu_pk(u32 p) { return pk_b2(silu2(bf_2(p))); }
DI u4 silu_pk4(u4 p) { return (u4){silu_pk(p[0]), silu_pk(p[1]), silu_pk(p[2]), silu_pk(p[3])}; }
DI void lds_set(LAS u32* p, u32 v) {
    asm volatile("s_waitcnt lgkmcnt(0)" ::: "memory");
    __hip_atomic_store(p, v, __ATOMIC_RELAXED, __HIP_MEMORY_SCOPE_WORKGROUP);
}
DI void lds_wait(LAS u32* p, u32 v) {
#pragma nounroll
    for (int sp = 0; sp < SPIN_MAX; ++sp) {
        if (__builtin_amdgcn_readfirstlane(__hip_atomic_load(p, __ATOMIC_RELAXED, __HIP_MEMORY_SCOPE_WORKGROUP)) >= v) break;
        __builtin_amdgcn_s_sleep(1);
    }
    FENCE();
}
DI void flag_wait(GAS u32* p, u32 tag) {
#pragma nounroll
    for (int sp = 0; sp < SPIN_MAX; ++sp) {
        if (__builtin_amdgcn_ballot_w64(__hip_atomic_load(p, __ATOMIC_RELAXED, __HIP_MEMORY_SCOPE_AGENT) != tag) == 0) break;
        __builtin_amdgcn_s_sleep(2);
    }
    FENCE();
}
#define SENT4 ((u4){SENT, SENT, SENT, SENT})

__global__ __launch_bounds__(512, 2) void k_fused(const uint16_t* x, const uint16_t* wr, const uint16_t* wi, const uint16_t* w1,
                                                  const uint16_t* w2, uint16_t* out, char* ws) {
    extern __shared__ __attribute__((aligned(16))) char smem[];
    const int tid = threadIdx.x, l = tid & 63, g = l >> 4, m = l & 15;
    const int w = __builtin_amdgcn_readfirstlane(tid >> 6);
    const int b = (blockIdx.x & 7) * 32 + (blockIdx.x >> 3);
    const int mt1 = (b >> 6) * 4 + ((b >> 3) & 3), nt1 = ((b >> 5) & 1) * 8 + (b & 7);
    const int grp = b >> 4, th = (b >> 3) & 1, fb = b & 7;
    const rsrc_t rw = mkrs(ws, WS_END), ro = mkrs(out, 2097152);
    char* scr = smem + LDS_SCR;
    LAS u32* hw = (LAS u32*)((LAS char*)smem + LDS_SCR + SCR_F);
    const u32 lds0 = (u32)(uintptr_t)(LAS char*)smem;
    const float mf = (float)m, gf = (float)g, sg = (m & 1) ? -1.0f : 1.0f;
    const bool aw = w < 4;
    ST_DECL;
    const int rq = ((b >> 3) + b) & 3;
    const int row0 = (tid & 255) / 14, cch0 = (tid & 255) - row0 * 14;
    const char* wsrc = (const char*)w1 + nt1 * 224;
    u32 w1o[7];
    auto w1_burst = [&](int i0, int n) {
#pragma unroll
        for (int i = i0; i < i0 + n; ++i) {
            const int q = (i / 7 + rq) & 3, j = i % 7;
            glds16(wsrc + q * (128 * 3584), w1o[j], lds0 + (w - 4) * 1024 + q * 28672 + j * 4096);
        }
    };
    const u32 epoch = __hip_atomic_load((GAS u32*)(ws + WS_EP), __ATOMIC_RELAXED, __HIP_MEMORY_SCOPE_AGENT);
    FENCE();
    const int xoff = (256 * w + b) * 1024 + m * 32 + g * 8;
    u2 xl[2];
    float c16[4], s16[4];
    f2 twc[2], tws[2];
    u4 Gre3, Gim3, SGre, SGim, GA[2];
    auto consts0 = [&]() {
#pragma unroll
        for (int u = 0; u < 4; ++u) {
            float a = (4.0f * gf + u) * (mf * (1.0f / 16));
            c16[u] = cosr(a); s16[u] = sinr(a);
            twc[u >> 1][u & 1] = cosr(a * (1.0f / 32)); tws[u >> 1][u & 1] = sinr(a * (1.0f / 32));
        }
    };
    auto consts3 = [&]() {
#pragma unroll
        for (int u = 0; u < 4; ++u) {
            const float k = 4.0f * gf + u;
            const bool k0 = u == 0 && g == 0;
            float cc = c16[u], ss = s16[u];
            Gre3[u] = pk_b(cc, -ss); Gim3[u] = pk_b(ss, cc);
            float a2 = (2.0f * k + 1.0f) * (mf * (1.0f / 32));
            u32 sp = pk_b(g < 2 ? 2.f * cc : 2.f * sg * cosr(a2), g < 2 ? -2.f * ss : -2.f * sg * sinr(a2));
            SGre[u] = k0 ? pk_b(1.0f, sg) : (g < 2 ? sp : 0u);
            SGim[u] = g < 2 ? 0u : sp;
            float a4 = k * (mf * (1.0f / 32)), c4 = 2.f * cosr(a4), s4_ = -2.f * sinr(a4);
            GA[0][u] = k0 ? pk_b(1.0f, sg) : pk_b(c4, s4_);
            GA[1][u] = k0 ? pk_b(1.0f, sg) : ((u & 1) ? pk_b(-c4, -s4_) : pk_b(c4, s4_));
        }
        PIN(Gre3); PIN(Gim3); PIN(SGre); PIN(SGim); PIN(GA[0]); PIN(GA[1]); PIN(twc[0]); PIN(twc[1]); PIN(tws[0]); PIN(tws[1]);
    };
    u32 tag;
    if (aw) {
        u32 xd[8];
        const u32* xr = (const u32*)(x + (256 * (2 * w + ((m >> 1) & 1)) + b) * 512) + 8 * g + 2 * (m >> 2) + (m & 1);
#pragma unroll
        for (int j = 0; j < 8; ++j) xd[j] = xr[32 * j];
        if (tid < 8) hw[tid] = 0;
        FENCE();
        ST(2);
        {
            u4 FAre, FAim, Fre, Fim, Sre, Sim; u2 Mb; f2 pc[2], ps[2];
            consts0();
            float c0[8], s0[8];
#pragma unroll
            for (int j = 0; j < 8; ++j) {
                float a = (gf + 4.0f * j) * (mf * (1.0f / 32));
                c0[j] = cosr(a); s0[j] = m ? -sinr(a) : ((g & 1) ? -1.0f : 1.0f);
            }
            const float mo = (float)(2 * (m & 7) + 1) * (1.0f / 32);
#pragma unroll
            for (int u = 0; u < 4; ++u) {
                FAre[u] = pk_h(c0[2 * u], c0[2 * u + 1]); FAim[u] = pk_h(s0[2 * u], s0[2 * u + 1]);
                float cc = c16[u], ss = s16[u];
                Fre[u] = pk_h(cc, ss); Fim[u] = pk_h(-ss, cc);
                float a2 = (4.0f * gf + u) * mo;
                u32 re = pk_h(m < 8 ? cc : cosr(a2), 0.f), im = pk_h(m < 8 ? (m ? -ss : ((u & 1) ? -1.0f : 1.0f)) : -sinr(a2), 0.f);
                Sre[u] = m < 8 ? re : re << 16; Sim[u] = m < 8 ? im : im << 16;
                pc[u & 1][u >> 1] = twc[u >> 1][u & 1]; ps[u & 1][u >> 1] = tws[u >> 1][u & 1];
            }
#pragma unroll
            for (int e = 0; e < 2; ++e) {
                float a = (float)(((m >> 1) * (256 * (2 * g + e) + b)) & 2047) * (1.0f / 2048);
                float cc = cosr(a), ss = sinr(a);
                Mb[e] = (m & 1) ? pk_h(-ss, cc) : pk_h(cc, ss);
            }
            PIN(FAre); PIN(FAim); PIN(Fre); PIN(Fim); PIN(Sre); PIN(Sim); PIN(Mb); PIN(pc[0]); PIN(pc[1]); PIN(ps[0]); PIN(ps[1]);
            ST(3);
            f2 v[8];
#pragma unroll
            for (int j = 0; j < 8; ++j) v[j] = bf_2(xd[j]);
            f2 s2 = ((v[0] + v[1]) + (v[2] + v[3])) + ((v[4] + v[5]) + (v[6] + v[7]));
            const float mean = red_lanes<false>(s2[0] + s2[1]) * (1.0f / 512);
            f2 q0 = {0.f, 0.f}, q1 = {0.f, 0.f};
#pragma unroll
            for (int j = 0; j < 8; ++j) { v[j] -= mean; if (j & 1) q1 = fma2(v[j], v[j], q1); else q0 = fma2(v[j], v[j], q0); }
            q0 += q1;
            const float inv = ln_inv(red_lanes<false>(q0[0] + q0[1]) * (1.0f / 512));
            const float off = (mean - rbf(mean)) * inv;
#pragma unroll
            for (int j = 0; j < 8; ++j) v[j] = fma2(v[j], (f2){inv, inv}, (f2){off, off});
            u4 B[2];
#pragma unroll
            for (int p = 0; p < 2; ++p) {
                u4 A = {pk_h(v[0][p], v[1][p]), pk_h(v[2][p], v[3][p]), pk_h(v[4][p], v[5][p]), pk_h(v[6][p], v[7][p])};
                f4 Dre = mma_h(A, FAre, Z4), Dim = mma_h(A, FAim, Z4);
#pragma unroll
                for (int r = 0; r < 2; ++r) {
                    f2 re = r ? hi2(Dre) : lo2(Dre), im = r ? hi2(Dim) : lo2(Dim);
                    f2 tr = fma2(im, ps[p], re * pc[p]), ti = fma2(-re, ps[p], im * pc[p]);
                    B[r][p] = pk_h(tr[0], ti[0]); B[r][2 + p] = pk_h(tr[1], ti[1]);
                }
            }
#pragma unroll
            for (int r = 0; r < 2; ++r) {
                f4 Gre = mma_h(Fre, B[r], Z4), Gim = mma_h(Fim, B[r], Z4), Hre = mma_h(Sre, B[r], Z4), Him = mma_h(Sim, B[r], Z4);
                u4 Z;
#pragma unroll
                for (int i = 0; i < 4; ++i) Z[i] = pk_h(m ? Gre[i] : Hre[i], m ? Gim[i] : Him[i]);
                *(u4*)(scr + (2 * w + r) * 1024 + m * 64 + g * 16) = Z;
            }
            BARRIER();
            ST(18);
            u4 r0 = *(const u4*)(scr + (2 * g) * 1024 + w * 256 + m * 16);
            u4 r1 = *(const u4*)(scr + (2 * g + 1) * 1024 + w * 256 + m * 16);
#pragma unroll
            for (int t = 0; t < 4; ++t) {
                f4 D = mma16_h(Mb, (u2){r0[t], r1[t]}, Z4);
                *(u2*)(scr + SCR_S + w * 2048 + m * 128 + (((2 * t + (g >> 1)) ^ (m >> 1)) * 16) + (g & 1) * 8) =
                    (u2){clean(pk_h(D[0], D[1])), clean(pk_h(D[2], D[3]))};
            }
        }
        u4 uo0 = *(const u4*)(scr + SCR_S + w * 2048 + (l ^ ((l >> 4) & 7)) * 16);
        u4 uo1 = *(const u4*)(scr + SCR_S + w * 2048 + 1024 + (l ^ (((l >> 4) + 4) & 7)) * 16);
        u4 Fre, Fim, Gre, Gim, N0, N1; f2 tc[2], ts[2];
#pragma unroll
        for (int u = 0; u < 4; ++u) {
            float cc = c16[u], ss = s16[u];
            Fre[u] = pk_h(0.25f * cc, 0.25f * ss); Fim[u] = pk_h(-0.25f * ss, 0.25f * cc);
            Gre[u] = pk_b(cc * (1.0f / 256), -ss * (1.0f / 256)); Gim[u] = pk_b(ss * (1.0f / 256), cc * (1.0f / 256));
            float a3 = (4.0f * gf + u) * (mf * (1.0f / 256));
            tc[u >> 1][u & 1] = cosr(a3); ts[u >> 1][u & 1] = sinr(a3);
        }
        PIN(Fre); PIN(Fim); PIN(Gre); PIN(Gim); PIN(tc[0]); PIN(tc[1]); PIN(ts[0]); PIN(ts[1]);
        ST(19);
        BARRIER();
        ST(4);
        bst4<SC1>(uo0, rw, l * 16, WS_U + b * 8192 + w * 2048);
        bst4<SC1>(uo1, rw, l * 16 + 1024, WS_U + b * 8192 + w * 2048);
        xl[0] = *(const u2*)((const char*)x + xoff); xl[1] = *(const u2*)((const char*)x + xoff + 512);
        FENCE();
        tag = __builtin_amdgcn_readfirstlane(epoch) + 1u;
        consts3();
#pragma unroll
        for (int u = 0; u < 4; ++u) {
            float a1 = (float)(((4 * (g & 1) + u) * (256 * (m >> 1) + b)) & 2047) * (1.0f / 2048);
            float c1 = cosr(a1), s1 = sinr(a1);
            u32 nb = (m & 1) ? pk_b(s1, c1) : pk_b(c1, -s1);
            N0[u] = g < 2 ? nb : 0u; N1[u] = g < 2 ? 0u : nb;
        }
        PIN(N0); PIN(N1);
        WAITV(0);
        BARRIER();
        ST(5);
        u4 d[2], f[2], e[2];
        const int uo = (tid >> 1) * 8192 + (tid & 1) * 16;
        d[0] = bld4<SC1>(rw, uo, WS_U + b * 32); d[1] = bld4<SC1>(rw, uo, WS_U + 128 * 8192 + b * 32);
        f[0] = bld4<SC1>(rw, uo, WS_WT + b * 32); f[1] = bld4<SC1>(rw, uo, WS_WT + 128 * 8192 + b * 32);
        asm volatile("" :: "v"(f[0]), "v"(f[1]));
        if (any_sent(umax(max4(d[0]), max4(d[1])))) {
#pragma nounroll
            for (int sp = 0; sp < SPIN_MAX; ++sp) {
                __builtin_amdgcn_s_sleep(POLL_SLEEP);
                FENCE();
                d[0] = bld4<SC1>(rw, uo, WS_U + b * 32); d[1] = bld4<SC1>(rw, uo, WS_U + 128 * 8192 + b * 32);
                if (!any_sent(umax(max4(d[0]), max4(d[1])))) break;
            }
        }
        FENCE();
        f[0] = bld4<SC1>(rw, uo, WS_WT + b * 32); f[1] = bld4<SC1>(rw, uo, WS_WT + 128 * 8192 + b * 32);
        char* sp = scr + SCR_S + (tid ^ ((tid >> 4) & 1)) * 16;
        *(u4*)sp = d[0]; *(u4*)(sp + 4096) = d[1];
        BARRIER();
        if (b == 0) {
            f[0] = bld4<SC1>(rw, tid * 16, WS_WA); f[1] = bld4<SC1>(rw, tid * 16 + 4096, WS_WA);
            e[0] = bld4<SC1>(rw, tid * 16, WS_WB); e[1] = bld4<SC1>(rw, tid * 16 + 4096, WS_WB);
            if (tid == 0) __hip_atomic_store((GAS u32*)(ws + WS_EP), tag, __ATOMIC_RELAXED, __HIP_MEMORY_SCOPE_AGENT);
        }
        ST(6);
        u2 ld[4], wl[4];
        const int so = (64 * g + m) * 32 + (((w >> 1) ^ (m >> 3)) * 16) + (w & 1) * 8;
#pragma unroll
        for (int q = 0; q < 4; ++q) ld[q] = *(const u2*)(scr + SCR_S + so + 512 * q);
        FENCE();
        f4 Xre[2], Xim[2];
#pragma unroll
        for (int e = 0; e < 2; ++e) {
            u4 A = {ld[0][e], ld[1][e], ld[2][e], ld[3][e]};
            f4 Dre = mma_h(A, Fre, Z4), Dim = mma_h(A, Fim, Z4);
            f2 r0 = fma2(lo2(Dim), ts[0], lo2(Dre) * tc[0]), i0 = fma2(-lo2(Dre), ts[0], lo2(Dim) * tc[0]);
            f2 r1 = fma2(hi2(Dim), ts[1], hi2(Dre) * tc[1]), i1 = fma2(-hi2(Dre), ts[1], hi2(Dim) * tc[1]);
            u4 B = {pk_h(r0[0], i0[0]), pk_h(r0[1], i0[1]), pk_h(r1[0], i1[0]), pk_h(r1[1], i1[1])};
            Xre[e] = mma_h(Fre, B, Z4); Xim[e] = mma_h(Fim, B, Z4);
        }
        *(u4*)(sp + 8192) = f[0]; *(u4*)(sp + 12288) = f[1];
        f2* sx = (f2*)(scr + SCR_X);
        if (b == 0) {
            *(u4*)(sp - SCR_S) = e[0]; *(u4*)(sp - SCR_S + 4096) = e[1];
#pragma unroll
            for (int e = 0; e < 2; ++e)
#pragma unroll
                for (int i = 0; i < 4; ++i) sx[2 * w + e + 8 * (m + 16 * (4 * g + i))] = (f2){Xre[e][i], Xim[e][i]};
        }
        BARRIER();
        ST(20);
#pragma unroll
        for (int q = 0; q < 4; ++q) wl[q] = *(const u2*)(scr + SCR_S + so + 8192 + 512 * q);
        FENCE();
        f2 yr[2][2], yi[2][2];
#pragma unroll
        for (int e = 0; e < 2; ++e)
#pragma unroll
            for (int hh = 0; hh < 2; ++hh) {
                f2 wre = {bf_lo(wl[2 * hh][e]), bf_lo(wl[2 * hh + 1][e])}, wim = {bf_hi(wl[2 * hh][e]), bf_hi(wl[2 * hh + 1][e])};
                f2 xr = hh ? hi2(Xre[e]) : lo2(Xre[e]), xi = hh ? hi2(Xim[e]) : lo2(Xim[e]);
                yr[e][hh] = fma2(-xi, wim, xr * wre); yi[e][hh] = fma2(xr, wim, xi * wre);
            }
        if (b == 0) {
#pragma unroll
            for (int i = 0; i < 4; ++i) {
                u2 wb = *(const u2*)(scr + so + 512 * i);
#pragma unroll
                for (int e = 0; e < 2; ++e) {
                    f2 xm = sx[(2048 - (2 * w + e + 8 * (m + 16 * (4 * g + i)))) & 2047];
                    float br = bf_lo(wb[e]), bi = bf_hi(wb[e]);
                    yr[e][i >> 1][i & 1] += xm[0] * br + xm[1] * bi;
                    yi[e][i >> 1][i & 1] += xm[0] * bi - xm[1] * br;
                }
            }
        }
        char* pv = scr + SCR_S;
#pragma unroll
        for (int e = 0; e < 2; ++e) {
            u4 Y = {pk_b(yr[e][0][0], yi[e][0][0]), pk_b(yr[e][0][1], yi[e][0][1]), pk_b(yr[e][1][0], yi[e][1][0]), pk_b(yr[e][1][1], yi[e][1][1])};
            f4 Dre = mma_b(Y, Gre, Z4), Dim = mma_b(Y, Gim, Z4);
            f2 r0 = fma2(-lo2(Dim), ts[0], lo2(Dre) * tc[0]), i0 = fma2(lo2(Dre), ts[0], lo2(Dim) * tc[0]);
            f2 r1 = fma2(-hi2(Dim), ts[1], hi2(Dre) * tc[1]), i1 = fma2(hi2(Dre), ts[1], hi2(Dim) * tc[1]);
            u4 A = {pk_b(r0[0], i0[0]), pk_b(r0[1], i0[1]), pk_b(r1[0], i1[0]), pk_b(r1[1], i1[1])};
            f4 Ere = mma_b(A, Gre, Z4), Eim = mma_b(A, Gim, Z4);
            u4 o;
#pragma unroll
            for (int i = 0; i < 4; ++i) o[i] = clean(pk_b(Ere[i], Eim[i]));
            *(u4*)(pv + (2 * w + e) * 1024 + ((m * 4 + g) ^ ((w >> 1) * 4)) * 16) = o;
        }
        BARRIER();
        ST(7);
#pragma unroll
        for (int it = 0; it < 2; ++it) {
            const int ch = tid + 256 * it;
            const u32* q = (const u32*)(pv + (ch & 1) * 4096 + (((ch >> 1) * 4) ^ ((ch & 1) * 64)));
            bst4<SC1>((u4){q[0], q[256], q[512], q[768]}, rw, ch * 16, WS_V + b * 8192);
        }
        WAITV(0);
        BARRIER();
        ST(8);
        const int vo = (2 * m + (g >> 1)) * 8192 + (g & 1) * 16, vs = WS_V + w * 64 * 8192 + b * 32;
        u4 v0, v1;
#pragma nounroll
        for (int sp = 0; sp < SPIN_MAX; ++sp) {
            FENCE();
            v0 = bld4<SC1>(rw, vo, vs); v1 = bld4<SC1>(rw, vo, vs + 32 * 8192);
            if (!any_sent(umax(max4(v0), max4(v1)))) break;
            __builtin_amdgcn_s_sleep(POLL_SLEEP);
        }
        ST(9);
        char* sz = scr + SCR_X + g * 2048 + w * 256 + m * 8;
#pragma unroll
        for (int hh = 0; hh < 2; ++hh) {
            f4 D0 = mma_b(N0, hh ? v1 : v0, Z4), D1 = mma_b(N1, hh ? v1 : v0, Z4);
            *(u2*)(sz + hh * 128) = (u2){pk_b(D0[0], D0[1]), pk_b(D1[0], D1[1])};
            *(u2*)(sz + 1024 + hh * 128) = (u2){pk_b(D0[2], D0[3]), pk_b(D1[2], D1[3])};
        }
    } else {
        auto announce = [&](int which) {
            if (l < 8) __hip_atomic_store((GAS u32*)(ws + WS_FA) + which * 2048 + l * 256 + b, tag, __ATOMIC_RELAXED, __HIP_MEMORY_SCOPE_AGENT);
            FENCE();
        };
        auto all_announced = [&](int which) {
#pragma nounroll
            for (int sp = 0; sp < SPIN_MAX; ++sp) {
                FENCE();
                u4 f = bld4<SC1>(rw, l * 16, WS_FA + which * 8192 + (blockIdx.x & 7) * 1024);
                if (__builtin_amdgcn_ballot_w64(f[0] != tag || f[1] != tag || f[2] != tag || f[3] != tag) == 0) break;
                __builtin_amdgcn_s_sleep(4);
            }
        };
        const rsrc_t rr = mkrs(wr, 1052672), ri = mkrs(wi, 1052672);
        const int j = 4 * (w - 4) + g;
        const int k = m ? m + 32 * j : (j < 8 ? 32 * j : 16 + 32 * (j - 8));
        const bool mir = k > 256;
        const int e1 = mir ? ((2047 - 8 * b) * 257 + 512 - k) * 2 : ((8 * b + 1) * 257 + k) * 2, es = mir ? -514 : 514;
        const int e0 = mir ? (((2048 - 8 * b) & 2047) * 257 + 512 - k) * 2 : e1 - 514;
        u32 fr[8], fi[8], sp_[8];
#pragma unroll
        for (int i = 0; i < 8; ++i) {
            const int e = i ? e1 + (i - 1) * es : e0;
            fr[i] = __builtin_amdgcn_raw_buffer_load_b16(rr, e, 0, 0); fi[i] = __builtin_amdgcn_raw_buffer_load_b16(ri, e, 0, 0);
        }
        if (w == 7) {
            const int ed = (8 * b + (l & 7)) * 514, em = ((2048 - 8 * b - (l & 7)) & 2047) * 514;
#pragma unroll
            for (int i = 0; i < 8; ++i) sp_[i] = __builtin_amdgcn_raw_buffer_load_b16((i & 1) ? ri : rr, ((i & 2) ? em : ed) + (i >> 2) * 512, 0, 0);
        }
        FENCE();
#pragma unroll
        for (int i = 0; i < 2; ++i) bst4<SC1>(SENT4, rw, l * 16 + i * 1024, WS_U + b * 8192 + (w - 4) * 2048);
        ST(2);
#pragma unroll
        for (int jj = 0; jj < 7; ++jj) {
            const int cj_ = (4 * jj) % 14, rj = 18 * jj + (4 * jj) / 14;
            w1o[jj] = (row0 + rj) * 3584 + (cch0 + cj_) * 16 + (cch0 + cj_ >= 14 ? 3584 - 224 : 0);
            PIN(w1o[jj]);
        }
        {
            const u32 cj = mir ? 0x80000000u : 0u;
            const bool up = m >= 8;
            u4 s0, s1;
#pragma unroll
            for (int i = 0; i < 4; ++i) {
                const u32 lo = (fr[i] | (fi[i] << 16)) ^ cj, hi = (fr[4 + i] | (fi[4 + i] << 16)) ^ cj;
                const u32 got = __builtin_amdgcn_update_dpp(0u, up ? lo : hi, 0x128, 0xf, 0xf, true);
                s0[i] = up ? got : lo; s1[i] = up ? hi : got;
            }
            ST(3);
            const int wo = (m & 7) * 512 + j * 32 + (m >> 3) * 16;
            bst4<SC1>(s0, rw, wo, WS_WT + b * 8192); bst4<SC1>(s1, rw, wo + 4096, WS_WT + b * 8192);
            if (w == 7 && l < 8) {
                float ar = bf_lo(sp_[0]) + bf_lo(sp_[2]), ai = bf_lo(sp_[1]) - bf_lo(sp_[3]);
                float br = bf_lo(sp_[4]) + bf_lo(sp_[6]), bi = bf_lo(sp_[5]) - bf_lo(sp_[7]);
                __builtin_amdgcn_raw_buffer_store_b32(pk_b(0.25f * (ar + br), 0.25f * (ai + bi)), rw, l * 4, WS_WA + b * 32, SC1);
                __builtin_amdgcn_raw_buffer_store_b32(pk_b(0.25f * (ar - br), 0.25f * (ai - bi)), rw, l * 4, WS_WB + b * 32, SC1);
            }
        }
        tag = __builtin_amdgcn_readfirstlane(epoch) + 1u;
        if (w == 7) WAITV(4); else WAITV(2);
        ST(18);
        BARRIER();
        if (w == 7) announce(0);
        consts0();
        WAITV(0);
        ST(19);
        BARRIER();
        __builtin_amdgcn_s_sleep(2);
#pragma unroll
        for (int i = 0; i < 2; ++i) bst4<SC1>(SENT4, rw, l * 16 + i * 1024, WS_V + b * 8192 + (w - 4) * 2048);
        xl[0] = *(const u2*)((const char*)x + xoff); xl[1] = *(const u2*)((const char*)x + xoff + 512);
        if (w == 7) all_announced(0);
        ST(4);
        BARRIER();
        consts3();
        WAITV(0);
        ST(5);
        BARRIER();
        if (w == 7) announce(1);
#pragma unroll
        for (int i = 0; i < 2; ++i) bst4<SC1>(SENT4, rw, (l >> 3) * 256 + (l & 7) * 16 + i * 2048, WS_LN2 + (b >> 1) * 16384 + (b & 1) * 128 + (w - 4) * 4096);
        w1_burst(0, W1_B1);
        BARRIER();
        WAITV(W1_B1);
        ST(6);
        BARRIER();
        if (w == 7) all_announced(1);
        else __builtin_amdgcn_s_sleep(3);
        w1_burst(W1_B1, W1_B2);
        ST(7);
        BARRIER();
    }
    BARRIER();
    if (!aw) w1_burst(W1_B0 + W1_B1 + W1_B2, W1_B3);
    {
        u4 A = *(const u4*)(scr + SCR_X + w * 1024 + m * 64 + g * 16);
        f4 Dre = mma_b(A, Gre3, Z4), Dim = mma_b(A, Gim3, Z4), Hre = mma_b(A, SGre, Z4), Him = mma_b(A, SGim, Z4);
        if (g == 0) { Dre[0] = Hre[0]; Dim[0] = Him[0]; }
        f2 r0 = fma2(-lo2(Dim), tws[0], lo2(Dre) * twc[0]), i0 = fma2(lo2(Dre), tws[0], lo2(Dim) * twc[0]);
        f2 r1 = fma2(-hi2(Dim), tws[1], hi2(Dre) * twc[1]), i1 = fma2(hi2(Dre), tws[1], hi2(Dim) * twc[1]);
        u4 A2 = {pk_b(r0[0], i0[0]), pk_b(r0[1], i0[1]), pk_b(r1[0], i1[0]), pk_b(r1[1], i1[1])};
        f2 h[4];
#pragma unroll
        for (int t = 0; t < 2; ++t) {
            f4 Yv = mma_b(A2, GA[t], Z4);
            u32 h0 = pk_b2(bf_2(xl[t][0]) + bf_2(pk_b(Yv[0], Yv[1]))), h1 = pk_b2(bf_2(xl[t][1]) + bf_2(pk_b(Yv[2], Yv[3])));
            bst2<SC1>((u2){h0, h1}, ro, xoff + 512 * t, 0);
            h[2 * t] = bf_2(h0); h[2 * t + 1] = bf_2(h1);
        }
        f2 s2 = (h[0] + h[1]) + (h[2] + h[3]);
        const float mean = red_lanes<true>(s2[0] + s2[1]) * (1.0f / 512);
        f2 q2 = {0.f, 0.f};
#pragma unroll
        for (int j = 0; j < 4; ++j) { f2 d = h[j] - mean; q2 = fma2(d, d, q2); }
        const float inv = ln_inv(red_lanes<true>(q2[0] + q2[1]) * (1.0f / 512));
        const float mb = rbf(mean);
#pragma unroll
        for (int t = 0; t < 2; ++t)
            *(u2*)(scr + ((8 * t + (m >> 1)) * 4 + g) * 128 + (w ^ (m >> 1)) * 16 + (m & 1) * 8) =
                (u2){clean(pk_b2(bf_2(pk_b2(h[2 * t] - mb)) * inv)), clean(pk_b2(bf_2(pk_b2(h[2 * t + 1] - mb)) * inv))};
        WAITV(2);
        BARRIER();
        u4 lo = *(const u4*)(scr + (tid ^ ((tid >> 5) & 7)) * 16);
        bst4<SC1>(lo, rw, (tid >> 3) * 256 + (tid & 7) * 16, WS_LN2 + (b >> 1) * 16384 + (b & 1) * 128);
        FENCE();
        ST(10);
    }
    const int pw = (w + 4 * th) & 7;
    GAS u32* fl = (GAS u32*)(ws + WS_FLAG);
    int ko[7];
    {
        const int r6 = (fb < 6 ? fb : fb - 5) + 3 * (grp & 1);
#pragma unroll
        for (int ks = 0; ks < 6; ++ks) { int k = ks + r6; k = k >= 6 ? k - 6 : k; ko[ks] = k >= 6 ? k - 6 : k; }
        ko[6] = 6;
    }
    u4 W[7][4];
    const rsrc_t r2 = mkrs(w2, 1835008);
    auto ldw2 = [&](int k0, int k1, bool throttle) {
#pragma unroll
        for (int ks = 0; ks < 7; ++ks)
            if (ks >= k0 && ks < k1) {
                const int q = ko[ks];
                const int so = (224 * pw + (ks == 6 ? 96 : (q < 3 ? 32 * q : 16 + 32 * q))) * 1024 + fb * 128;
                const int vo = (4 * g + (m & 1) * (ks == 6 ? 112 : 16)) * 1024 + (m >> 1) * 16;
#pragma unroll
                for (int t = 0; t < 4; ++t) {
                    W[ks][t] = bld4<0>(r2, vo + t * 1024, so);
                    if (t & 1) { SCHED(); if (throttle) WAITV(W2_OUT); }
                }
            }
    };
    auto tile_w2 = [&](int ks) {
        u32 lo[4][2], hi[4][2];
        asm volatile("s_mov_b64 vcc, %16\n\ts_nop 0\n\t"
                     "v_cndmask_b32_dpp %0, %10, %8, vcc quad_perm:[1,0,3,2] row_mask:0xf bank_mask:0xf\n\t"
                     "v_cndmask_b32_dpp %1, %11, %9, vcc quad_perm:[1,0,3,2] row_mask:0xf bank_mask:0xf\n\t"
                     "v_cndmask_b32_dpp %2, %14, %12, vcc quad_perm:[1,0,3,2] row_mask:0xf bank_mask:0xf\n\t"
                     "v_cndmask_b32_dpp %3, %15, %13, vcc quad_perm:[1,0,3,2] row_mask:0xf bank_mask:0xf\n\t"
                     "s_not_b64 vcc, vcc\n\t"
                     "v_cndmask_b32_dpp %4, %8, %10, vcc quad_perm:[1,0,3,2] row_mask:0xf bank_mask:0xf\n\t"
                     "v_cndmask_b32_dpp %5, %9, %11, vcc quad_perm:[1,0,3,2] row_mask:0xf bank_mask:0xf\n\t"
                     "v_cndmask_b32_dpp %6, %12, %14, vcc quad_perm:[1,0,3,2] row_mask:0xf bank_mask:0xf\n\t"
                     "v_cndmask_b32_dpp %7, %13, %15, vcc quad_perm:[1,0,3,2] row_mask:0xf bank_mask:0xf"
                     : "=&v"(lo[0][0]), "=&v"(lo[0][1]), "=&v"(lo[1][0]), "=&v"(lo[1][1]), "=&v"(hi[0][0]), "=&v"(hi[0][1]), "=&v"(hi[1][0]), "=&v"(hi[1][1])
                     : "v"(W[ks][0][0]), "v"(W[ks][0][1]), "v"(W[ks][0][2]), "v"(W[ks][0][3]), "v"(W[ks][1][0]), "v"(W[ks][1][1]), "v"(W[ks][1][2]), "v"(W[ks][1][3]),
                       "s"(0x5555555555555555ull) : "vcc");
        asm volatile("s_mov_b64 vcc, %16\n\ts_nop 0\n\t"
                     "v_cndmask_b32_dpp %0, %10, %8, vcc quad_perm:[1,0,3,2] row_mask:0xf bank_mask:0xf\n\t"
                     "v_cndmask_b32_dpp %1, %11, %9, vcc quad_perm:[1,0,3,2] row_mask:0xf bank_mask:0xf\n\t"
                     "v_cndmask_b32_dpp %2, %14, %12, vcc quad_perm:[1,0,3,2] row_mask:0xf bank_mask:0xf\n\t"
                     "v_cndmask_b32_dpp %3, %15, %13, vcc quad_perm:[1,0,3,2] row_mask:0xf bank_mask:0xf\n\t"
                     "s_not_b64 vcc, vcc\n\t"
                     "v_cndmask_b32_dpp %4, %8, %10, vcc quad_perm:[1,0,3,2] row_mask:0xf bank_mask:0xf\n\t"
                     "v_cndmask_b32_dpp %5, %9, %11, vcc quad_perm:[1,0,3,2] row_mask:0xf bank_mask:0xf\n\t"
                     "v_cndmask_b32_dpp %6, %12, %14, vcc quad_perm:[1,0,3,2] row_mask:0xf bank_mask:0xf\n\t"
                     "v_cndmask_b32_dpp %7, %13, %15, vcc quad_perm:[1,0,3,2] row_mask:0xf bank_mask:0xf"
                     : "=&v"(lo[2][0]), "=&v"(lo[2][1]), "=&v"(lo[3][0]), "=&v"(lo[3][1]), "=&v"(hi[2][0]), "=&v"(hi[2][1]), "=&v"(hi[3][0]), "=&v"(hi[3][1])
                     : "v"(W[ks][2][0]), "v"(W[ks][2][1]), "v"(W[ks][2][2]), "v"(W[ks][2][3]), "v"(W[ks][3][0]), "v"(W[ks][3][1]), "v"(W[ks][3][2]), "v"(W[ks][3][3]),
                       "s"(0x5555555555555555ull) : "vcc");
#pragma unroll
        for (int t = 0; t < 4; ++t) {
            const u32 sel = (t & 1) ? 0x07060302u : 0x05040100u;
            W[ks][t] = (u4){__builtin_amdgcn_perm(lo[1][t >> 1], lo[0][t >> 1], sel), __builtin_amdgcn_perm(lo[3][t >> 1], lo[2][t >> 1], sel),
                            __builtin_amdgcn_perm(hi[1][t >> 1], hi[0][t >> 1], sel), __builtin_amdgcn_perm(hi[3][t >> 1], hi[2][t >> 1], sel)};
            PIN(W[ks][t]);
        }
    };
    auto publish = [&]() {
        u32 prev = 0;
        if (l == 0) prev = __hip_atomic_fetch_add(hw + 4, 1u, __ATOMIC_RELAXED, __HIP_MEMORY_SCOPE_WORKGROUP);
        if (__builtin_amdgcn_readfirstlane(prev) == 7u && l < 2)
            __hip_atomic_store(fl + (l ? 4096 + b : (mt1 * 8 + (nt1 >> 1)) * 32 + (nt1 & 1)), tag, __ATOMIC_RELAXED, __HIP_MEMORY_SCOPE_AGENT);
        FENCE();
    };
    {
        const int tg = w & 3;
        char* xch = scr + SCR_X + tg * 5120 + l * 16;
        const int hoff = WS_H1 + (mt1 * 8 + 2 * tg) * (56 * 1024) + (nt1 >> 1) * 7168;
        if (aw) {
            const int toff = WS_LN2 + (mt1 * 8 + 2 * tg) * 16384;
            LAS char* wb0 = (LAS char*)smem + (4 * g + (m >> 2)) * 224 + (m & 3) * 8;
            LAS char* wb[4];
            int tq[4];
#pragma unroll
            for (int q = 0; q < 4; ++q) { const int qq = (q + rq) & 3; wb[q] = wb0 + qq * 28672; tq[q] = toff + qq * 4096; }
            u4 T[2][16], A[2][5];
            auto ldt = [&](int kk) {
#pragma unroll
                for (int tt = 0; tt < 2; ++tt) T[tt][kk] = bld4<SC1>(rw, l * 16 + (kk & 3) * 1024, tq[kk >> 2] + tt * 16384);
            };
#pragma unroll
            for (int i = 0; i < 5; ++i) A[0][i] = ldw1(wb[0], 0, i < 4 ? i : 6);
            WAITV(0);
            ST(11);
#pragma unroll
            for (int kk = 0; kk < G1_DEPTH; ++kk) { ldt(kk); SCHED(); }
            f4 acc[2][5];
#pragma unroll
            for (int tt = 0; tt < 2; ++tt)
#pragma unroll
                for (int i = 0; i < 5; ++i) acc[tt][i] = Z4;
#pragma unroll
            for (int kk = 0; kk < 16; ++kk) {
                if ((kk & 1) == 0) {
                    if (__builtin_expect(any_sent(umax(umax(max4(T[0][kk]), max4(T[1][kk])), umax(max4(T[0][kk + 1]), max4(T[1][kk + 1])))), 0)) {
                        wait_tok(T[0][kk], T[1][kk], rw, l * 16, tq[kk >> 2] + (kk & 3) * 1024);
                        wait_tok(T[0][kk + 1], T[1][kk + 1], rw, l * 16, tq[kk >> 2] + ((kk + 1) & 3) * 1024);
                    }
                    if (kk == 0) ST(12);
                    if (kk == W1_CHK) lds_wait(hw + 5, 4u);
                }
                if (kk + G1_DEPTH < 16) ldt(kk + G1_DEPTH);
                if (kk < 15) {
#pragma unroll
                    for (int i = 0; i < 5; ++i) A[(kk + 1) & 1][i] = ldw1(wb[(kk + 1) >> 2], (kk + 1) & 3, i < 4 ? i : 6);
                }
#pragma unroll
                for (int i = 0; i < 5; ++i)
#pragma unroll
                    for (int tt = 0; tt < 2; ++tt) acc[tt][i] = mma_b(A[kk & 1][i], T[tt][kk], acc[tt][i]);
#pragma unroll
                for (int i = 0; i < 10; ++i) {
                    __builtin_amdgcn_sched_group_barrier(0x8, 1, 0);
                    if (i == 1 || i == 5) __builtin_amdgcn_sched_group_barrier(0x20, 1, 0);
                    __builtin_amdgcn_sched_group_barrier(0x100, 1, 0);
                }
                if (kk & 1) {} else SCHED();
            }
            u4 A2[4][2];
#pragma unroll
            for (int kk = 0; kk < 3; ++kk)
#pragma unroll
                for (int i = 0; i < 2; ++i) A2[kk][i] = ldw1(wb[0], kk, 4 + i);
            SCHED();
            {
                u2 p[2][5];
#pragma unroll
                for (int tt = 0; tt < 2; ++tt)
#pragma unroll
                    for (int i = 0; i < 5; ++i) p[tt][i] = pk4(acc[tt][i]);
#pragma unroll
                for (int tt = 0; tt < 2; ++tt)
#pragma unroll
                    for (int q = 0; q < 2; ++q) *(u4*)(xch + (2 * tt + q) * 1024) = (u4){p[tt][2 * q][0], p[tt][2 * q][1], p[tt][2 * q + 1][0], p[tt][2 * q + 1][1]};
                *(u4*)(xch + 4096) = (u4){p[0][4][0], p[0][4][1], p[1][4][0], p[1][4][1]};
            }
            lds_set(hw + tg, 1u);
            ST(13);
            f4 ac2[2][2] = {{Z4, Z4}, {Z4, Z4}};
#pragma unroll
            for (int kk = 0; kk < 16; ++kk) {
                if (kk + 3 < 16) {
#pragma unroll
                    for (int i = 0; i < 2; ++i) A2[(kk + 3) & 3][i] = ldw1(wb[(kk + 3) >> 2], (kk + 3) & 3, 4 + i);
                }
#pragma unroll
                for (int i = 0; i < 2; ++i)
#pragma unroll
                    for (int tt = 0; tt < 2; ++tt) ac2[tt][i] = mma_b(A2[kk & 3][i], T[tt][kk], ac2[tt][i]);
#pragma unroll
                for (int i = 0; i < 4; ++i) {
                    __builtin_amdgcn_sched_group_barrier(0x8, 1, 0);
                    __builtin_amdgcn_sched_group_barrier(0x100, 1, 0);
                }
                SCHED();
            }
#pragma unroll
            for (int tt = 0; tt < 2; ++tt) {
                u2 p0 = pk4(ac2[tt][0]), p1 = pk4(ac2[tt][1]);
                bst4<SC1>(silu_pk4((u4){p0[0], p0[1], p1[0], p1[1]}), rw, l * 16, hoff + tt * (56 * 1024) + ((nt1 & 1) * 3 + 2) * 1024);
            }
            FENCE();
            ST(14);
            ldw2(0, 1, false);
            WAITV(4);
            publish();
            ldw2(1, W2_ATHR, true);
            ldw2(W2_ATHR, 7, false);
        } else {
            w1_burst(W1_B0 + W1_B1 + W1_B2 + W1_B3, 28 - W1_B0 - W1_B1 - W1_B2 - W1_B3);
            WAITV(0);
            if (l == 0) __hip_atomic_fetch_add(hw + 5, 1u, __ATOMIC_RELAXED, __HIP_MEMORY_SCOPE_WORKGROUP);
            __builtin_amdgcn_s_sleep(W2_SLEEP);
            ldw2(0, W2_EARLY, true);
            ST(12);
            lds_wait(hw + tg, 1u);
            ST(13);
#pragma unroll
            for (int j = 0; j < 5; ++j) {
                u4 o = silu_pk4(*(const u4*)(xch + j * 1024));
                if (j < 4) bst4<SC1>(o, rw, l * 16, hoff + (j >> 1) * (56 * 1024) + ((nt1 & 1) * 3 + (j & 1)) * 1024);
                else {
                    bst2<SC1>((u2){o[0], o[1]}, rw, l * 8, hoff + 6144 + (nt1 & 1) * 512);
                    bst2<SC1>((u2){o[2], o[3]}, rw, l * 8, hoff + 56 * 1024 + 6144 + (nt1 & 1) * 512);
                }
            }
            FENCE();
            ST(14);
            ldw2(W2_EARLY, W2_EARLY + 1, false);
            WAITV(4);
            publish();
            ldw2(W2_EARLY + 1, 7, false);
        }
    }
    {
#pragma unroll
        for (int ks = 0; ks < 7; ++ks) tile_w2(ks);
        ST(15);
        flag_wait(fl + (grp * 8 + pw) * 32 + (l & 1), tag);
        ST(16);
        const int rot = w & 3;
        const int toff = WS_H1 + (grp * 8 + th * 4) * (56 * 1024) + pw * 7168;
        int to[4];
#pragma unroll
        for (int tt = 0; tt < 4; ++tt) to[tt] = toff + ((tt + rot) & 3) * (56 * 1024);
        u4 T[7][4];
        auto ldtok = [&](int ks) {
#pragma unroll
            for (int tt = 0; tt < 4; ++tt) {
                if (ks < 6) T[ks][tt] = bld4<SC1>(rw, l * 16, to[tt] + ko[ks] * 1024);
                else {
                    u2 a = bld2<SC1>(rw, l * 8 + 2048, to[tt] + 4096), c = bld2<SC1>(rw, l * 8 + 2560, to[tt] + 4096);
                    T[ks][tt] = (u4){a[0], a[1], c[0], c[1]};
                }
            }
            SCHED();
        };
#pragma unroll
        for (int ks = 0; ks < G2_DEPTH; ++ks) ldtok(ks);
        const int rho = grp * 128 + th * 64 + rot * 16 + m;
        const int ooff = (256 * (rho & 7) + (rho >> 3)) * 1024 + fb * 128 + g * 32;
        u4 hres[2];
        f4 acc[4][4];
#pragma unroll
        for (int tt = 0; tt < 4; ++tt)
#pragma unroll
            for (int t = 0; t < 4; ++t) acc[tt][t] = Z4;
#pragma unroll
        for (int ks = 0; ks < 7; ++ks) {
            if (ks + G2_DEPTH < 7) { PIN(T[ks][0]); ldtok(ks + G2_DEPTH); }
#pragma unroll
            for (int tt = 0; tt < 4; ++tt)
#pragma unroll
                for (int t = 0; t < 4; ++t) acc[tt][t] = mma_b(W[ks][t], T[ks][tt], acc[tt][t]);
            SCHED();
        }
        if (!aw) {
            flag_wait(fl + 4096 + grp * 16 + th * 8 + rot * 2 + (l & 1), tag);
            hres[0] = bld4<SC1>(ro, ooff, 0); hres[1] = bld4<SC1>(ro, ooff + 16, 0);
        }
        ST(17);
        lds_wait(hw + 4, 8u);
#pragma unroll
        for (int tt = 0; tt < 4; ++tt)
            if (aw || tt > 0)
#pragma unroll
                for (int t = 0; t < 4; ++t) *(f4*)(smem + (w * 16 + ((tt + rot) & 3) * 4 + t) * 1024 + l * 16) = acc[tt][t];
        BARRIER();
        if (!aw) {
#pragma unroll
            for (int j = 1; j < 8; ++j)
#pragma unroll
                for (int t = 0; t < 4; ++t) acc[0][t] += *(const f4*)(smem + (((w + j) & 7) * 16 + rot * 4 + t) * 1024 + l * 16);
#pragma unroll
            for (int p = 0; p < 2; ++p) {
                u4 o;
#pragma unroll
                for (int j = 0; j < 4; ++j) {
                    const int i = 2 * p + (j >> 1), t = 2 * (j & 1);
                    o[j] = pk_b2(bf_2(hres[p][j]) + bf_2(pk_b(acc[0][t][i], acc[0][t + 1][i])));
                }
                bst4<AUX_OUT>(o, ro, ooff + 16 * p, 0);
            }
        }
    }
    ST_END(ws, 0);
}

extern "C" void kernel_launch(void* const* d_in, const int* in_sizes, int n_in, void* d_out, int out_size,
                              void* d_ws, size_t ws_size, hipStream_t stream) {
    (void)hipFuncSetAttribute((const void*)k_fused, hipFuncAttributeMaxDynamicSharedMemorySize, LDS_B);
    k_fused<<<256, 512, LDS_B, stream>>>((const uint16_t*)d_in[0], (const uint16_t*)d_in[1], (const uint16_t*)d_in[2],
                                         (const uint16_t*)d_in[4], (const uint16_t*)d_in[5], (uint16_t*)d_out, (char*)d_ws);
}
```

```cpp
#include <hip/hip_runtime.h>
#include <hip/hip_bf16.h>
#include <stdint.h>

typedef _Float16 h8 __attribute__((ext_vector_type(8)));
typedef _Float16 h4 __attribute__((ext_vector_type(4)));
typedef _Float16 h2 __attribute__((ext_vector_type(2)));
typedef __bf16 b8 __attribute__((ext_vector_type(8)));
typedef __bf16 b2 __attribute__((ext_vector_type(2)));
typedef short s4 __attribute__((ext_vector_type(4)));
typedef float f4 __attribute__((ext_vector_type(4)));
typedef float f2 __attribute__((ext_vector_type(2)));
typedef unsigned u32;
typedef u32 u4 __attribute__((ext_vector_type(4)));
typedef u32 u2 __attribute__((ext_vector_type(2)));
#define LAS __attribute__((address_space(3)))
#define GAS __attribute__((address_space(1)))
#define DI __device__ __forceinline__
#define FENCE() asm volatile("" ::: "memory")
#define PIN(x) asm volatile("" : "+v"(x))
#define SCHED() __builtin_amdgcn_sched_barrier(0)
#define WAITV(n) asm volatile("s_waitcnt vmcnt(%0)" :: "n"(n) : "memory")
#define BARRIER() do { asm volatile("s_waitcnt lgkmcnt(0)" ::: "memory"); __builtin_amdgcn_s_barrier(); FENCE(); } while (0)
constexpr int SC1 = 16;
constexpr u32 SENT = 0xffffffffu;
constexpr int SPIN_MAX = 1 << 20;

DI u32 pk_h(float a, float b) { f2 v = {a, b}; h2 r = __builtin_convertvector(v, h2); return __builtin_bit_cast(u32, r); }
DI u32 pk_b(float a, float b) { f2 v = {a, b}; b2 r = __builtin_convertvector(v, b2); return __builtin_bit_cast(u32, r); }
DI float bf_lo(u32 v) { return __builtin_bit_cast(float, v << 16); }
DI float bf_hi(u32 v) { return __builtin_bit_cast(float, v & 0xffff0000u); }
DI f2 bf_2(u32 v) { return (f2){bf_lo(v), bf_hi(v)}; }
DI u32 pk_b2(f2 v) { return pk_b(v[0], v[1]); }
DI float rbf(float a) { return bf_lo(pk_b(a, a)); }
DI float cosr(float rev) { return __builtin_amdgcn_cosf(rev); }
DI float sinr(float rev) { return __builtin_amdgcn_sinf(rev); }
DI f2 lo2(f4 v) { return __builtin_shufflevector(v, v, 0, 1); }
DI f2 hi2(f4 v) { return __builtin_shufflevector(v, v, 2, 3); }
DI f2 fma2(f2 a, f2 b, f2 c) { return __builtin_elementwise_fma(a, b, c); }
DI u32 umin(u32 a, u32 b) { return a < b ? a : b; }
DI u32 umax(u32 a, u32 b) { return a > b ? a : b; }
DI u32 clean(u32 v) { return umin(v, SENT - 1); }
DI u32 max4(u4 v) { return umax(umax(umax(v[0], v[1]), v[2]), v[3]); }
#define Z4 ((f4){0, 0, 0, 0})
DI f4 mma_h(u4 a, u4 b, f4 c) { return __builtin_amdgcn_mfma_f32_16x16x32_f16(__builtin_bit_cast(h8, a), __builtin_bit_cast(h8, b), c, 0, 0, 0); }
DI f4 mma_b(u4 a, u4 b, f4 c) { return __builtin_amdgcn_mfma_f32_16x16x32_bf16(__builtin_bit_cast(b8, a), __builtin_bit_cast(b8, b), c, 0, 0, 0); }
DI f4 mma16_h(u2 a, u2 b, f4 c) { return __builtin_amdgcn_mfma_f32_16x16x16f16(__builtin_bit_cast(h4, a), __builtin_bit_cast(h4, b), c, 0, 0, 0); }
typedef __amdgpu_buffer_rsrc_t rsrc_t;
DI rsrc_t mkrs(const void* p, u32 bytes) { return __builtin_amdgcn_make_buffer_rsrc((void*)p, 0, bytes, 0x00020000); }
template <int AUX> DI u4 bld4(rsrc_t r, int v, int s) { return __builtin_amdgcn_raw_buffer_load_b128(r, v, s, AUX); }
template <int AUX> DI u2 bld2(rsrc_t r, int v, int s) { return __builtin_amdgcn_raw_buffer_load_b64(r, v, s, AUX); }
template <int AUX> DI void bst4(u4 d, rsrc_t r, int v, int s) {
    __builtin_amdgcn_raw_buffer_store_b128(d, r, v, s, AUX);
    asm volatile("s_nop 1" :: "v"(d));
}
template <int AUX> DI void bst2(u2 d, rsrc_t r, int v, int s) { __builtin_amdgcn_raw_buffer_store_b64(d, r, v, s, AUX); }

template <int CTRL> DI float dpp(float v) {
    return __builtin_bit_cast(float, __builtin_amdgcn_update_dpp(0, __builtin_bit_cast(int, v), CTRL, 0xf, 0xf, true));
}
template <bool ALL> DI float red_lanes(float v) {
    v += dpp<0xB1>(v);
    if (ALL) v += dpp<0x4E>(v);
    v += dpp<0x124>(v); v += dpp<0x128>(v);
    u32 x = __builtin_bit_cast(u32, v), y = x;
    asm("" : "+v"(y));
    auto r = __builtin_amdgcn_permlane16_swap(x, y, false, false);
    v = __builtin_bit_cast(float, (u32)r[0]) + __builtin_bit_cast(float, (u32)r[1]);
    x = __builtin_bit_cast(u32, v); y = x;
    asm("" : "+v"(y));
    auto q = __builtin_amdgcn_permlane32_swap(x, y, false, false);
    return __builtin_bit_cast(float, (u32)q[0]) + __builtin_bit_cast(float, (u32)q[1]);
}
DI float ln_inv(float var) { return __builtin_amdgcn_rcpf(rbf(__builtin_amdgcn_sqrtf(rbf(rbf(var) + 1e-5f)))); }

#define ST_DECL
#define ST(i)
#define ST_END(ws, kid)

constexpr u32 WS_U = 0;
constexpr u32 WS_WT = WS_U + (2u << 20);
constexpr u32 WS_V = WS_WT + (2u << 20);
constexpr u32 WS_LN2 = WS_V + (2u << 20);
constexpr u32 WS_WA = WS_LN2 + (2u << 20);
constexpr u32 WS_WB = WS_WA + 8192u;
constexpr u32 WS_FA = WS_WB + 8192u;
constexpr u32 WS_FLAG = WS_FA + 16384u;
constexpr u32 WS_EP = WS_FLAG + 20480u;
constexpr u32 WS_H1 = WS_WA + (64u << 10);
constexpr u32 WS_END = WS_H1 + 7340032u;

#define W1_B0 0
#define W1_B1 7
#define W1_B2 5
#define W1_B3 6
#define W1_CHK ((((W1_B0 + W1_B1 + W1_B2 + W1_B3) / 7) * 4 - 2) < 0 ? 0 : (((W1_B0 + W1_B1 + W1_B2 + W1_B3) / 7) * 4 - 2))
#define CLR_SLEEP 6
#define BPRIO 2
#define EXP_UCLR 0
#define POLL_SLEEP 2
#define BG1 6
#define BG2 12
#define BG3 17
#define BG4 23
#define BG5 29
#define BG6 35
#define BG7 41
#define BG8 47
#define A_EARLY 24
#define SILU_PRIO 0
#define EXP_HOFF 0
#define W2_SLEEP 20
#define AUX_OUT 2
#define W2_OUT 2
#define W2_ATHR 3
#define W2_EARLY 3
#define G1_DEPTH 8
#define G2_DEPTH 2
constexpr int LDS_SCR = 114688;
constexpr int SCR_X = 8448;
constexpr int SCR_S = SCR_X + 20480;
constexpr int SCR_F = SCR_S + 16384;
constexpr int LDS_B = LDS_SCR + SCR_F + 128;

DI void glds16(const void* sbase, u32 voff, u32 lds_dst) {
    asm volatile("s_mov_b32 m0, %2\n\ts_nop 0\n\tglobal_load_lds_dwordx4 %0, %1" :: "v"(voff), "s"(sbase), "s"(lds_dst) : "memory");
}
DI void glds16x4(const void* sbase, u32 voff, u32 lds_dst) {
    asm volatile("s_mov_b32 m0, %2\n\ts_nop 0\n\tglobal_load_lds_dwordx4 %0, %1\n\tglobal_load_lds_dwordx4 %0, %1 offset:1024\n\t"
                 "global_load_lds_dwordx4 %0, %1 offset:2048\n\tglobal_load_lds_dwordx4 %0, %1 offset:3072"
                 :: "v"(voff), "s"(sbase), "s"(lds_dst) : "memory");
}
DI bool any_sent(u32 v) { return __builtin_amdgcn_ballot_w64(v == SENT) != 0; }
DI void wait_tok(u4& t0, u4& t1, rsrc_t rw, int vo, int so) {
    if (any_sent(umax(max4(t0), max4(t1)))) {
#pragma nounroll
        for (int sp = 0; sp < SPIN_MAX; ++sp) {
            FENCE();
            t0 = bld4<SC1>(rw, vo, so); t1 = bld4<SC1>(rw, vo, so + 16384);
            if (!any_sent(umax(max4(t0), max4(t1)))) break;
        }
    }
}
DI u4 ldw1(LAS char* wb, int kk, int ft) {
    s4 a0 = __builtin_amdgcn_ds_read_tr16_b64_v4i16((LAS s4*)(wb + kk * 32 * 224 + ft * 32));
    s4 a1 = __builtin_amdgcn_ds_read_tr16_b64_v4i16((LAS s4*)(wb + (kk * 32 + 16) * 224 + ft * 32));
    u2 x0 = __builtin_bit_cast(u2, a0), x1 = __builtin_bit_cast(u2, a1);
    return (u4){x0[0], x0[1], x1[0], x1[1]};
}
DI u2 pk4(f4 a) { return (u2){pk_b(a[0], a[1]), pk_b(a[2], a[3])}; }
DI u2 silu_pk2(u32 p0, u32 p1) {
    u32 o0, o1; float a0, a1, a2, a3, t0, t1, t2, t3;
    asm("v_lshlrev_b32 %2, 16, %10\n\tv_and_b32 %3, 0xffff0000, %10\n\tv_lshlrev_b32 %4, 16, %11\n\tv_and_b32 %5, 0xffff0000, %11\n\t"
        "v_mul_f32 %6, %12, %2\n\tv_mul_f32 %7, %12, %3\n\tv_mul_f32 %8, %12, %4\n\tv_mul_f32 %9, %12, %5\n\t"
        "v_exp_f32 %6, %6\n\tv_exp_f32 %7, %7\n\tv_exp_f32 %8, %8\n\tv_exp_f32 %9, %9\n\t"
        "v_add_f32 %6, 1.0, %6\n\tv_add_f32 %7, 1.0, %7\n\tv_add_f32 %8, 1.0, %8\n\tv_add_f32 %9, 1.0, %9\n\t"
        "v_rcp_f32 %6, %6\n\tv_rcp_f32 %7, %7\n\tv_rcp_f32 %8, %8\n\tv_rcp_f32 %9, %9\n\t"
        "v_mul_f32 %2, %2, %6\n\tv_mul_f32 %3, %3, %7\n\tv_mul_f32 %4, %4, %8\n\tv_mul_f32 %5, %5, %9\n\t"
        "v_cvt_pk_bf16_f32 %0, %2, %3\n\tv_cvt_pk_bf16_f32 %1, %4, %5"
        : "=&v"(o0), "=&v"(o1), "=&v"(a0), "=&v"(a1), "=&v"(a2), "=&v"(a3), "=&v"(t0), "=&v"(t1), "=&v"(t2), "=&v"(t3)
        : "v"(p0), "v"(p1), "s"(-1.4426950408889634f));
    return (u2){o0, o1};
}
DI u4 silu_pk4(u4 p) { u2 a = silu_pk2(p[0], p[1]), b = silu_pk2(p[2], p[3]); return (u4){a[0], a[1], b[0], b[1]}; }
DI void lds_set(LAS u32* p, u32 v) {
    asm volatile("s_waitcnt lgkmcnt(0)" ::: "memory");
    __hip_atomic_store(p, v, __ATOMIC_RELAXED, __HIP_MEMORY_SCOPE_WORKGROUP);
}
DI void lds_wait(LAS u32* p, u32 v) {
#pragma nounroll
    for (int sp = 0; sp < SPIN_MAX; ++sp) {
        if (__builtin_amdgcn_readfirstlane(__hip_atomic_load(p, __ATOMIC_RELAXED, __HIP_MEMORY_SCOPE_WORKGROUP)) >= v) break;
        __builtin_amdgcn_s_sleep(1);
    }
    FENCE();
}
DI void flag_wait(GAS u32* p, u32 tag) {
#pragma nounroll
    for (int sp = 0; sp < SPIN_MAX; ++sp) {
        if (__builtin_amdgcn_ballot_w64(__hip_atomic_load(p, __ATOMIC_RELAXED, __HIP_MEMORY_SCOPE_AGENT) != tag) == 0) break;
        __builtin_amdgcn_s_sleep(2);
    }
    FENCE();
}
#define SENT4 ((u4){SENT, SENT, SENT, SENT})

__global__ __launch_bounds__(512, 2) void k_fused(const uint16_t* x, const uint16_t* wr, const uint16_t* wi, const uint16_t* w1,
                                                  const uint16_t* w2, uint16_t* out, char* ws) {
    extern __shared__ __attribute__((aligned(16))) char smem[];
    const int tid = threadIdx.x, l = tid & 63, g = l >> 4, m = l & 15;
    const int w = __builtin_amdgcn_readfirstlane(tid >> 6);
    const int b = (blockIdx.x & 7) * 32 + (blockIdx.x >> 3);
    const int mt1 = (b >> 6) * 4 + ((b >> 3) & 3), nt1 = ((b >> 5) & 1) * 8 + (b & 7);
    const int grp = b >> 4, th = (b >> 3) & 1, fb = b & 7;
    const rsrc_t rw = mkrs(ws, WS_END), ro = mkrs(out, 2097152);
    char* scr = smem + LDS_SCR;
    LAS u32* hw = (LAS u32*)((LAS char*)smem + LDS_SCR + SCR_F);
    const u32 lds0 = (u32)(uintptr_t)(LAS char*)smem;
    const float mf = (float)m, gf = (float)g, sg = (m & 1) ? -1.0f : 1.0f;
    const bool aw = w < 4;
    ST_DECL;
    const int rq = ((b >> 3) + b) & 3;
    const int row0 = (tid & 255) / 14, cch0 = (tid & 255) - row0 * 14;
    const char* wsrc = (const char*)w1 + nt1 * 224;
    u32 w1o[7];
    auto w1_burst = [&](int i0, int n) {
#pragma unroll
        for (int i = i0; i < i0 + n; ++i) {
            const int q = (i / 7 + rq) & 3, j = i % 7;
            glds16(wsrc + q * (128 * 3584), w1o[j], lds0 + (w - 4) * 1024 + q * 28672 + j * 4096);
        }
    };
    const u32 epoch = __hip_atomic_load((GAS u32*)(ws + WS_EP), __ATOMIC_RELAXED, __HIP_MEMORY_SCOPE_AGENT);
    FENCE();
    const int xoff = (256 * w + b) * 1024 + m * 32 + g * 8;
    u2 xl[2];
    float c16[4], s16[4];
    f2 twc[2], tws[2];
    u4 Gre3, Gim3, SGre, SGim, GA[2];
    auto consts0 = [&]() {
#pragma unroll
        for (int u = 0; u < 4; ++u) {
            float a = (4.0f * gf + u) * (mf * (1.0f / 16));
            c16[u] = cosr(a); s16[u] = sinr(a);
            twc[u >> 1][u & 1] = cosr(a * (1.0f / 32)); tws[u >> 1][u & 1] = sinr(a * (1.0f / 32));
        }
    };
    auto consts3 = [&]() {
#pragma unroll
        for (int u = 0; u < 4; ++u) {
            const float k = 4.0f * gf + u;
            const bool k0 = u == 0 && g == 0;
            float cc = c16[u], ss = s16[u];
            Gre3[u] = pk_b(cc, -ss); Gim3[u] = pk_b(ss, cc);
            float a2 = (2.0f * k + 1.0f) * (mf * (1.0f / 32));
            u32 sp = pk_b(g < 2 ? 2.f * cc : 2.f * sg * cosr(a2), g < 2 ? -2.f * ss : -2.f * sg * sinr(a2));
            SGre[u] = k0 ? pk_b(1.0f, sg) : (g < 2 ? sp : 0u);
            SGim[u] = g < 2 ? 0u : sp;
            float a4 = k * (mf * (1.0f / 32)), c4 = 2.f * cosr(a4), s4_ = -2.f * sinr(a4);
            GA[0][u] = k0 ? pk_b(1.0f, sg) : pk_b(c4, s4_);
            GA[1][u] = k0 ? pk_b(1.0f, sg) : ((u & 1) ? pk_b(-c4, -s4_) : pk_b(c4, s4_));
        }
        PIN(Gre3); PIN(Gim3); PIN(SGre); PIN(SGim); PIN(GA[0]); PIN(GA[1]); PIN(twc[0]); PIN(twc[1]); PIN(tws[0]); PIN(tws[1]);
    };
    const int pw = (w + 4 * th) & 7;
    GAS u32* fl = (GAS u32*)(ws + WS_FLAG);
    int ko[7];
    {
        const int r6 = (fb < 6 ? fb : fb - 5) + 3 * (grp & 1);
#pragma unroll
        for (int ks = 0; ks < 6; ++ks) { int k = ks + r6; k = k >= 6 ? k - 6 : k; ko[ks] = k >= 6 ? k - 6 : k; }
        ko[6] = 6;
    }
    u4 W[7][4];
    const rsrc_t r2 = mkrs(w2, 1835008);
    auto ldw2q = [&](int q0, int q1, bool throttle) {
#pragma unroll
        for (int qq = 0; qq < 28; ++qq)
            if (qq >= q0 && qq < q1) {
                const int ks = qq >> 2, t = qq & 3, q = ko[ks];
                const int so = (224 * pw + (ks == 6 ? 96 : (q < 3 ? 32 * q : 16 + 32 * q))) * 1024 + fb * 128;
                const int vo = (4 * g + (m & 1) * (ks == 6 ? 112 : 16)) * 1024 + (m >> 1) * 16;
                W[ks][t] = bld4<0>(r2, vo + t * 1024, so);
                SCHED();
                if (throttle && (t & 1)) WAITV(W2_OUT);
            }
    };
    auto bg = [&](int i0, int i1) {
        if (i0 < 28) w1_burst(i0, (i1 < 28 ? i1 : 28) - i0);
        if (i1 > 28) ldw2q((i0 > 28 ? i0 : 28) - 28, i1 - 28, false);
        FENCE();
    };
    u32 tag;
    if (aw) {
        u32 xd[8];
        const u32* xr = (const u32*)(x + (256 * (2 * w + ((m >> 1) & 1)) + b) * 512) + 8 * g + 2 * (m >> 2) + (m & 1);
#pragma unroll
        for (int j = 0; j < 8; ++j) xd[j] = xr[32 * j];
        if (tid < 8) hw[tid] = 0;
        FENCE();
        ST(2);
        {
            u4 FAre, FAim, Fre, Fim, Sre, Sim; u2 Mb; f2 pc[2], ps[2];
            consts0();
            float c0[8], s0[8];
#pragma unroll
            for (int j = 0; j < 8; ++j) {
                float a = (gf + 4.0f * j) * (mf * (1.0f / 32));
                c0[j] = cosr(a); s0[j] = m ? -sinr(a) : ((g & 1) ? -1.0f : 1.0f);
            }
            const float mo = (float)(2 * (m & 7) + 1) * (1.0f / 32);
#pragma unroll
            for (int u = 0; u < 4; ++u) {
                FAre[u] = pk_h(c0[2 * u], c0[2 * u + 1]); FAim[u] = pk_h(s0[2 * u], s0[2 * u + 1]);
                float cc = c16[u], ss = s16[u];
                Fre[u] = pk_h(cc, ss); Fim[u] = pk_h(-ss, cc);
                float a2 = (4.0f * gf + u) * mo;
                u32 re = pk_h(m < 8 ? cc : cosr(a2), 0.f), im = pk_h(m < 8 ? (m ? -ss : ((u & 1) ? -1.0f : 1.0f)) : -sinr(a2), 0.f);
                Sre[u] = m < 8 ? re : re << 16; Sim[u] = m < 8 ? im : im << 16;
                pc[u & 1][u >> 1] = twc[u >> 1][u & 1]; ps[u & 1][u >> 1] = tws[u >> 1][u & 1];
            }
#pragma unroll
            for (int e = 0; e < 2; ++e) {
                float a = (float)(((m >> 1) * (256 * (2 * g + e) + b)) & 2047) * (1.0f / 2048);
                float cc = cosr(a), ss = sinr(a);
                Mb[e] = (m & 1) ? pk_h(-ss, cc) : pk_h(cc, ss);
            }
            PIN(FAre); PIN(FAim); PIN(Fre); PIN(Fim); PIN(Sre); PIN(Sim); PIN(Mb); PIN(pc[0]); PIN(pc[1]); PIN(ps[0]); PIN(ps[1]);
            ST(3);
            f2 v[8];
#pragma unroll
            for (int j = 0; j < 8; ++j) v[j] = bf_2(xd[j]);
            f2 s2 = ((v[0] + v[1]) + (v[2] + v[3])) + ((v[4] + v[5]) + (v[6] + v[7]));
            const float mean = red_lanes<false>(s2[0] + s2[1]) * (1.0f / 512);
            f2 q0 = {0.f, 0.f}, q1 = {0.f, 0.f};
#pragma unroll
            for (int j = 0; j < 8; ++j) { v[j] -= mean; if (j & 1) q1 = fma2(v[j], v[j], q1); else q0 = fma2(v[j], v[j], q0); }
            q0 += q1;
            const float inv = ln_inv(red_lanes<false>(q0[0] + q0[1]) * (1.0f / 512));
            const float off = (mean - rbf(mean)) * inv;
#pragma unroll
            for (int j = 0; j < 8; ++j) v[j] = fma2(v[j], (f2){inv, inv}, (f2){off, off});
            u4 B[2];
#pragma unroll
            for (int p = 0; p < 2; ++p) {
                u4 A = {pk_h(v[0][p], v[1][p]), pk_h(v[2][p], v[3][p]), pk_h(v[4][p], v[5][p]), pk_h(v[6][p], v[7][p])};
                f4 Dre = mma_h(A, FAre, Z4), Dim = mma_h(A, FAim, Z4);
#pragma unroll
                for (int r = 0; r < 2; ++r) {
                    f2 re = r ? hi2(Dre) : lo2(Dre), im = r ? hi2(Dim) : lo2(Dim);
                    f2 tr = fma2(im, ps[p], re * pc[p]), ti = fma2(-re, ps[p], im * pc[p]);
                    B[r][p] = pk_h(tr[0], ti[0]); B[r][2 + p] = pk_h(tr[1], ti[1]);
                }
            }
#pragma unroll
            for (int r = 0; r < 2; ++r) {
                f4 Gre = mma_h(Fre, B[r], Z4), Gim = mma_h(Fim, B[r], Z4), Hre = mma_h(Sre, B[r], Z4), Him = mma_h(Sim, B[r], Z4);
                u4 Z;
#pragma unroll
                for (int i = 0; i < 4; ++i) Z[i] = pk_h(m ? Gre[i] : Hre[i], m ? Gim[i] : Him[i]);
                *(u4*)(scr + (2 * w + r) * 1024 + m * 64 + g * 16) = Z;
            }
            BARRIER();
            ST(18);
            u4 r0 = *(const u4*)(scr + (2 * g) * 1024 + w * 256 + m * 16);
            u4 r1 = *(const u4*)(scr + (2 * g + 1) * 1024 + w * 256 + m * 16);
#pragma unroll
            for (int t = 0; t < 4; ++t) {
                f4 D = mma16_h(Mb, (u2){r0[t], r1[t]}, Z4);
                *(u2*)(scr + SCR_S + w * 2048 + m * 128 + (((2 * t + (g >> 1)) ^ (m >> 1)) * 16) + (g & 1) * 8) =
                    (u2){clean(pk_h(D[0], D[1])), clean(pk_h(D[2], D[3]))};
            }
        }
        u4 uo0 = *(const u4*)(scr + SCR_S + w * 2048 + (l ^ ((l >> 4) & 7)) * 16);
        u4 uo1 = *(const u4*)(scr + SCR_S + w * 2048 + 1024 + (l ^ (((l >> 4) + 4) & 7)) * 16);
        u4 Fre, Fim, Gre, Gim, N0, N1; f2 tc[2], ts[2];
#pragma unroll
        for (int u = 0; u < 4; ++u) {
            float cc = c16[u], ss = s16[u];
            Fre[u] = pk_h(0.25f * cc, 0.25f * ss); Fim[u] = pk_h(-0.25f * ss, 0.25f * cc);
            Gre[u] = pk_b(cc * (1.0f / 256), -ss * (1.0f / 256)); Gim[u] = pk_b(ss * (1.0f / 256), cc * (1.0f / 256));
            float a3 = (4.0f * gf + u) * (mf * (1.0f / 256));
            tc[u >> 1][u & 1] = cosr(a3); ts[u >> 1][u & 1] = sinr(a3);
        }
        PIN(Fre); PIN(Fim); PIN(Gre); PIN(Gim); PIN(tc[0]); PIN(tc[1]); PIN(ts[0]); PIN(ts[1]);
        ST(19);
        BARRIER();
        ST(4);
        bst4<SC1>(uo0, rw, l * 16, WS_U + b * 8192 + w * 2048);
        bst4<SC1>(uo1, rw, l * 16 + 1024, WS_U + b * 8192 + w * 2048);
        xl[0] = *(const u2*)((const char*)x + xoff); xl[1] = *(const u2*)((const char*)x + xoff + 512);
        FENCE();
        tag = __builtin_amdgcn_readfirstlane(epoch) + 1u;
        consts3();
#pragma unroll
        for (int u = 0; u < 4; ++u) {
            float a1 = (float)(((4 * (g & 1) + u) * (256 * (m >> 1) + b)) & 2047) * (1.0f / 2048);
            float c1 = cosr(a1), s1 = sinr(a1);
            u32 nb = (m & 1) ? pk_b(s1, c1) : pk_b(c1, -s1);
            N0[u] = g < 2 ? nb : 0u; N1[u] = g < 2 ? 0u : nb;
        }
        PIN(N0); PIN(N1);
        WAITV(0);
        BARRIER();
        ST(5);
        u4 d[2], f[2], e[2];
        const int uo = (tid >> 1) * 8192 + (tid & 1) * 16;
        d[0] = bld4<SC1>(rw, uo, WS_U + b * 32); d[1] = bld4<SC1>(rw, uo, WS_U + 128 * 8192 + b * 32);
        f[0] = bld4<SC1>(rw, uo, WS_WT + b * 32); f[1] = bld4<SC1>(rw, uo, WS_WT + 128 * 8192 + b * 32);
        asm volatile("" :: "v"(f[0]), "v"(f[1]));
        if (any_sent(umax(max4(d[0]), max4(d[1])))) {
#pragma nounroll
            for (int sp = 0; sp < SPIN_MAX; ++sp) {
                __builtin_amdgcn_s_sleep(POLL_SLEEP);
                FENCE();
                d[0] = bld4<SC1>(rw, uo, WS_U + b * 32); d[1] = bld4<SC1>(rw, uo, WS_U + 128 * 8192 + b * 32);
                if (!any_sent(umax(max4(d[0]), max4(d[1])))) break;
            }
        }
        FENCE();
        f[0] = bld4<SC1>(rw, uo, WS_WT + b * 32); f[1] = bld4<SC1>(rw, uo, WS_WT + 128 * 8192 + b * 32);
        char* sp = scr + SCR_S + (tid ^ ((tid >> 4) & 1)) * 16;
        *(u4*)sp = d[0]; *(u4*)(sp + 4096) = d[1];
        BARRIER();
        if (b == 0) {
            f[0] = bld4<SC1>(rw, tid * 16, WS_WA); f[1] = bld4<SC1>(rw, tid * 16 + 4096, WS_WA);
            e[0] = bld4<SC1>(rw, tid * 16, WS_WB); e[1] = bld4<SC1>(rw, tid * 16 + 4096, WS_WB);
            if (tid == 0) __hip_atomic_store((GAS u32*)(ws + WS_EP), tag, __ATOMIC_RELAXED, __HIP_MEMORY_SCOPE_AGENT);
        }
        ST(6);
        u2 ld[4], wl[4];
        const int so = (64 * g + m) * 32 + (((w >> 1) ^ (m >> 3)) * 16) + (w & 1) * 8;
#pragma unroll
        for (int q = 0; q < 4; ++q) ld[q] = *(const u2*)(scr + SCR_S + so + 512 * q);
        FENCE();
        f4 Xre[2], Xim[2];
#pragma unroll
        for (int e = 0; e < 2; ++e) {
            u4 A = {ld[0][e], ld[1][e], ld[2][e], ld[3][e]};
            f4 Dre = mma_h(A, Fre, Z4), Dim = mma_h(A, Fim, Z4);
            f2 r0 = fma2(lo2(Dim), ts[0], lo2(Dre) * tc[0]), i0 = fma2(-lo2(Dre), ts[0], lo2(Dim) * tc[0]);
            f2 r1 = fma2(hi2(Dim), ts[1], hi2(Dre) * tc[1]), i1 = fma2(-hi2(Dre), ts[1], hi2(Dim) * tc[1]);
            u4 B = {pk_h(r0[0], i0[0]), pk_h(r0[1], i0[1]), pk_h(r1[0], i1[0]), pk_h(r1[1], i1[1])};
            Xre[e] = mma_h(Fre, B, Z4); Xim[e] = mma_h(Fim, B, Z4);
        }
        *(u4*)(sp + 8192) = f[0]; *(u4*)(sp + 12288) = f[1];
        f2* sx = (f2*)(scr + SCR_X);
        if (b == 0) {
            *(u4*)(sp - SCR_S) = e[0]; *(u4*)(sp - SCR_S + 4096) = e[1];
#pragma unroll
            for (int e = 0; e < 2; ++e)
#pragma unroll
                for (int i = 0; i < 4; ++i) sx[2 * w + e + 8 * (m + 16 * (4 * g + i))] = (f2){Xre[e][i], Xim[e][i]};
        }
        BARRIER();
        ST(20);
#pragma unroll
        for (int q = 0; q < 4; ++q) wl[q] = *(const u2*)(scr + SCR_S + so + 8192 + 512 * q);
        FENCE();
        f2 yr[2][2], yi[2][2];
#pragma unroll
        for (int e = 0; e < 2; ++e)
#pragma unroll
            for (int hh = 0; hh < 2; ++hh) {
                f2 wre = {bf_lo(wl[2 * hh][e]), bf_lo(wl[2 * hh + 1][e])}, wim = {bf_hi(wl[2 * hh][e]), bf_hi(wl[2 * hh + 1][e])};
                f2 xr = hh ? hi2(Xre[e]) : lo2(Xre[e]), xi = hh ? hi2(Xim[e]) : lo2(Xim[e]);
                yr[e][hh] = fma2(-xi, wim, xr * wre); yi[e][hh] = fma2(xr, wim, xi * wre);
            }
        if (b == 0) {
#pragma unroll
            for (int i = 0; i < 4; ++i) {
                u2 wb = *(const u2*)(scr + so + 512 * i);
#pragma unroll
                for (int e = 0; e < 2; ++e) {
                    f2 xm = sx[(2048 - (2 * w + e + 8 * (m + 16 * (4 * g + i)))) & 2047];
                    float br = bf_lo(wb[e]), bi = bf_hi(wb[e]);
                    yr[e][i >> 1][i & 1] += xm[0] * br + xm[1] * bi;
                    yi[e][i >> 1][i & 1] += xm[0] * bi - xm[1] * br;
                }
            }
        }
        char* pv = scr + SCR_S;
#pragma unroll
        for (int e = 0; e < 2; ++e) {
            u4 Y = {pk_b(yr[e][0][0], yi[e][0][0]), pk_b(yr[e][0][1], yi[e][0][1]), pk_b(yr[e][1][0], yi[e][1][0]), pk_b(yr[e][1][1], yi[e][1][1])};
            f4 Dre = mma_b(Y, Gre, Z4), Dim = mma_b(Y, Gim, Z4);
            f2 r0 = fma2(-lo2(Dim), ts[0], lo2(Dre) * tc[0]), i0 = fma2(lo2(Dre), ts[0], lo2(Dim) * tc[0]);
            f2 r1 = fma2(-hi2(Dim), ts[1], hi2(Dre) * tc[1]), i1 = fma2(hi2(Dre), ts[1], hi2(Dim) * tc[1]);
            u4 A = {pk_b(r0[0], i0[0]), pk_b(r0[1], i0[1]), pk_b(r1[0], i1[0]), pk_b(r1[1], i1[1])};
            f4 Ere = mma_b(A, Gre, Z4), Eim = mma_b(A, Gim, Z4);
            u4 o;
#pragma unroll
            for (int i = 0; i < 4; ++i) o[i] = clean(pk_b(Ere[i], Eim[i]));
            *(u4*)(pv + (2 * w + e) * 1024 + ((m * 4 + g) ^ ((w >> 1) * 4)) * 16) = o;
        }
        BARRIER();
        ST(7);
#pragma unroll
        for (int it = 0; it < 2; ++it) {
            const int ch = tid + 256 * it;
            const u32* q = (const u32*)(pv + (ch & 1) * 4096 + (((ch >> 1) * 4) ^ ((ch & 1) * 64)));
            bst4<SC1>((u4){q[0], q[256], q[512], q[768]}, rw, ch * 16, WS_V + b * 8192);
        }
        WAITV(0);
        BARRIER();
        ST(8);
        const int vo = (2 * m + (g >> 1)) * 8192 + (g & 1) * 16, vs = WS_V + w * 64 * 8192 + b * 32;
        u4 v0, v1;
#pragma nounroll
        for (int sp = 0; sp < SPIN_MAX; ++sp) {
            FENCE();
            v0 = bld4<SC1>(rw, vo, vs); v1 = bld4<SC1>(rw, vo, vs + 32 * 8192);
            if (!any_sent(umax(max4(v0), max4(v1)))) break;
            __builtin_amdgcn_s_sleep(POLL_SLEEP);
        }
        ST(9);
        char* sz = scr + SCR_X + g * 2048 + w * 256 + m * 8;
#pragma unroll
        for (int hh = 0; hh < 2; ++hh) {
            f4 D0 = mma_b(N0, hh ? v1 : v0, Z4), D1 = mma_b(N1, hh ? v1 : v0, Z4);
            *(u2*)(sz + hh * 128) = (u2){pk_b(D0[0], D0[1]), pk_b(D1[0], D1[1])};
            *(u2*)(sz + 1024 + hh * 128) = (u2){pk_b(D0[2], D0[3]), pk_b(D1[2], D1[3])};
        }
    } else {
        auto announce = [&](int which) {
            if (l < 8) __hip_atomic_store((GAS u32*)(ws + WS_FA) + which * 2048 + l * 256 + b, tag, __ATOMIC_RELAXED, __HIP_MEMORY_SCOPE_AGENT);
            FENCE();
        };
        auto all_announced = [&](int which) {
#pragma nounroll
            for (int sp = 0; sp < SPIN_MAX; ++sp) {
                FENCE();
                u4 f = bld4<SC1>(rw, l * 16, WS_FA + which * 8192 + (blockIdx.x & 7) * 1024);
                if (__builtin_amdgcn_ballot_w64(f[0] != tag || f[1] != tag || f[2] != tag || f[3] != tag) == 0) break;
                __builtin_amdgcn_s_sleep(4);
            }
        };
        const rsrc_t rr = mkrs(wr, 1052672), ri = mkrs(wi, 1052672);
        const int j = 4 * (w - 4) + g, i0_ = 4 * (m & 1);
        const bool mir = w >= 6;
        u32 fr[4], fi[4], sp_[8];
#pragma unroll
        for (int i = 0; i < 4; ++i) {
            const int e = mir ? ((2048 - 8 * b - i0_ - i) & 2047) * 514 + (512 - 32 * j - (m | 1)) * 2 : (8 * b + i0_ + i) * 514 + (32 * j + (m & 14)) * 2;
            fr[i] = __builtin_amdgcn_raw_buffer_load_b32(rr, e, 0, 0); fi[i] = __builtin_amdgcn_raw_buffer_load_b32(ri, e, 0, 0);
        }
        if (mir) {
            const int e = (8 * b + (l & 7)) * 514 + (16 + 32 * (4 * (w - 6) + ((l >> 3) & 3))) * 2;
            sp_[0] = __builtin_amdgcn_raw_buffer_load_b16(rr, e, 0, 0); sp_[1] = __builtin_amdgcn_raw_buffer_load_b16(ri, e, 0, 0);
        } else if (w == 5) {
            const int ed = (8 * b + (l & 7)) * 514, em = ((2048 - 8 * b - (l & 7)) & 2047) * 514;
#pragma unroll
            for (int i = 0; i < 8; ++i) sp_[i] = __builtin_amdgcn_raw_buffer_load_b16((i & 1) ? ri : rr, ((i & 2) ? em : ed) + (i >> 2) * 512, 0, 0);
        }
        FENCE();
#pragma unroll
        for (int i = 0; i < 2; ++i) bst4<SC1>(SENT4, rw, l * 16 + i * 1024, WS_U + b * 8192 + (w - 4) * 2048);
        ST(2);
#pragma unroll
        for (int jj = 0; jj < 7; ++jj) {
            const int cj_ = (4 * jj) % 14, rj = 18 * jj + (4 * jj) / 14;
            w1o[jj] = (row0 + rj) * 3584 + (cch0 + cj_) * 16 + (cch0 + cj_ >= 14 ? 3584 - 224 : 0);
            PIN(w1o[jj]);
        }
        {
            const u32 cj = mir ? 0x80000000u : 0u, se = mir ? 0x07060302u : 0x05040100u, so_ = mir ? 0x05040100u : 0x07060302u;
            u4 s0, s1;
#pragma unroll
            for (int i = 0; i < 4; ++i) { s0[i] = __builtin_amdgcn_perm(fi[i], fr[i], se) ^ cj; s1[i] = __builtin_amdgcn_perm(fi[i], fr[i], so_) ^ cj; }
            ST(3);
            const int wo = (m >> 1) * 1024 + j * 32 + (m & 1) * 16;
            bst4<SC1>(s0, rw, wo, WS_WT + b * 8192); bst4<SC1>(s1, rw, wo + 512, WS_WT + b * 8192);
            if (mir) {
                if (l < 32) __builtin_amdgcn_raw_buffer_store_b32(sp_[0] | (sp_[1] << 16), rw, l * 4, WS_WT + b * 8192 + (w - 4) * 128, SC1);
            } else if (w == 5 && l < 8) {
                float ar = bf_lo(sp_[0]) + bf_lo(sp_[2]), ai = bf_lo(sp_[1]) - bf_lo(sp_[3]);
                float br = bf_lo(sp_[4]) + bf_lo(sp_[6]), bi = bf_lo(sp_[5]) - bf_lo(sp_[7]);
                __builtin_amdgcn_raw_buffer_store_b32(pk_b(0.25f * (ar + br), 0.25f * (ai + bi)), rw, l * 4, WS_WA + b * 32, SC1);
                __builtin_amdgcn_raw_buffer_store_b32(pk_b(0.25f * (ar - br), 0.25f * (ai - bi)), rw, l * 4, WS_WB + b * 32, SC1);
            }
        }
        tag = __builtin_amdgcn_readfirstlane(epoch) + 1u;
        if (w == 4) WAITV(2); else if (w == 5) WAITV(4); else WAITV(3);
        ST(18);
        BARRIER();
        if (w == 7) announce(0);
        bg(0, BG1);
        consts0();
        WAITV(BG1);
        ST(19);
        BARRIER();
        __builtin_amdgcn_s_sleep(2);
#pragma unroll
        for (int i = 0; i < 2; ++i) bst4<SC1>(SENT4, rw, l * 16 + i * 1024, WS_V + b * 8192 + (w - 4) * 2048);
        xl[0] = *(const u2*)((const char*)x + xoff); xl[1] = *(const u2*)((const char*)x + xoff + 512);
        FENCE();
        if (w == 7) all_announced(0);
        bg(BG1, BG2);
        ST(4);
        BARRIER();
        consts3();
        bg(BG2, BG3);
        WAITV(BG3 - BG1);
        ST(5);
        BARRIER();
        if (w == 7) announce(1);
#pragma unroll
        for (int i = 0; i < 2; ++i) bst4<SC1>(SENT4, rw, (l >> 3) * 256 + (l & 7) * 16 + i * 2048, WS_LN2 + (b >> 1) * 16384 + (b & 1) * 128 + (w - 4) * 4096);
        bg(BG3, BG4);
        BARRIER();
        bg(BG4, BG5);
        WAITV(BG5 - BG3);
        ST(6);
        BARRIER();
        if (w == 7) all_announced(1);
        else __builtin_amdgcn_s_sleep(3);
        bg(BG5, BG6);
        ST(7);
        BARRIER();
        __builtin_amdgcn_s_sleep(4);
        bg(BG6, BG7);
    }
    BARRIER();
    if (!aw) bg(BG7, BG8);
    {
        u4 A = *(const u4*)(scr + SCR_X + w * 1024 + m * 64 + g * 16);
        f4 Dre = mma_b(A, Gre3, Z4), Dim = mma_b(A, Gim3, Z4), Hre = mma_b(A, SGre, Z4), Him = mma_b(A, SGim, Z4);
        if (g == 0) { Dre[0] = Hre[0]; Dim[0] = Him[0]; }
        f2 r0 = fma2(-lo2(Dim), tws[0], lo2(Dre) * twc[0]), i0 = fma2(lo2(Dre), tws[0], lo2(Dim) * twc[0]);
        f2 r1 = fma2(-hi2(Dim), tws[1], hi2(Dre) * twc[1]), i1 = fma2(hi2(Dre), tws[1], hi2(Dim) * twc[1]);
        u4 A2 = {pk_b(r0[0], i0[0]), pk_b(r0[1], i0[1]), pk_b(r1[0], i1[0]), pk_b(r1[1], i1[1])};
        f2 h[4];
#pragma unroll
        for (int t = 0; t < 2; ++t) {
            f4 Yv = mma_b(A2, GA[t], Z4);
            u32 h0 = pk_b2(bf_2(xl[t][0]) + bf_2(pk_b(Yv[0], Yv[1]))), h1 = pk_b2(bf_2(xl[t][1]) + bf_2(pk_b(Yv[2], Yv[3])));
            bst2<SC1>((u2){h0, h1}, ro, xoff + 512 * t, 0);
            h[2 * t] = bf_2(h0); h[2 * t + 1] = bf_2(h1);
        }
        f2 s2 = (h[0] + h[1]) + (h[2] + h[3]);
        const float mean = red_lanes<true>(s2[0] + s2[1]) * (1.0f / 512);
        f2 q2 = {0.f, 0.f};
#pragma unroll
        for (int j = 0; j < 4; ++j) { f2 d = h[j] - mean; q2 = fma2(d, d, q2); }
        const float inv = ln_inv(red_lanes<true>(q2[0] + q2[1]) * (1.0f / 512));
        const float mb = rbf(mean);
#pragma unroll
        for (int t = 0; t < 2; ++t)
            *(u2*)(scr + ((8 * t + (m >> 1)) * 4 + g) * 128 + (w ^ (m >> 1)) * 16 + (m & 1) * 8) =
                (u2){clean(pk_b2(bf_2(pk_b2(h[2 * t] - mb)) * inv)), clean(pk_b2(bf_2(pk_b2(h[2 * t + 1] - mb)) * inv))};
        WAITV(2 + BG8 - BG7);
        BARRIER();
        u4 lo = *(const u4*)(scr + (tid ^ ((tid >> 5) & 7)) * 16);
        bst4<SC1>(lo, rw, (tid >> 3) * 256 + (tid & 7) * 16, WS_LN2 + (b >> 1) * 16384 + (b & 1) * 128);
        FENCE();
        ST(10);
    }
    auto tile_w2 = [&](int ks) {
        u32 lo[4][2], hi[4][2];
        asm volatile("s_mov_b64 vcc, %16\n\ts_nop 0\n\t"
                     "v_cndmask_b32_dpp %0, %10, %8, vcc quad_perm:[1,0,3,2] row_mask:0xf bank_mask:0xf\n\t"
                     "v_cndmask_b32_dpp %1, %11, %9, vcc quad_perm:[1,0,3,2] row_mask:0xf bank_mask:0xf\n\t"
                     "v_cndmask_b32_dpp %2, %14, %12, vcc quad_perm:[1,0,3,2] row_mask:0xf bank_mask:0xf\n\t"
                     "v_cndmask_b32_dpp %3, %15, %13, vcc quad_perm:[1,0,3,2] row_mask:0xf bank_mask:0xf\n\t"
                     "s_not_b64 vcc, vcc\n\t"
                     "v_cndmask_b32_dpp %4, %8, %10, vcc quad_perm:[1,0,3,2] row_mask:0xf bank_mask:0xf\n\t"
                     "v_cndmask_b32_dpp %5, %9, %11, vcc quad_perm:[1,0,3,2] row_mask:0xf bank_mask:0xf\n\t"
                     "v_cndmask_b32_dpp %6, %12, %14, vcc quad_perm:[1,0,3,2] row_mask:0xf bank_mask:0xf\n\t"
                     "v_cndmask_b32_dpp %7, %13, %15, vcc quad_perm:[1,0,3,2] row_mask:0xf bank_mask:0xf"
                     : "=&v"(lo[0][0]), "=&v"(lo[0][1]), "=&v"(lo[1][0]), "=&v"(lo[1][1]), "=&v"(hi[0][0]), "=&v"(hi[0][1]), "=&v"(hi[1][0]), "=&v"(hi[1][1])
                     : "v"(W[ks][0][0]), "v"(W[ks][0][1]), "v"(W[ks][0][2]), "v"(W[ks][0][3]), "v"(W[ks][1][0]), "v"(W[ks][1][1]), "v"(W[ks][1][2]), "v"(W[ks][1][3]),
                       "s"(0x5555555555555555ull) : "vcc");
        asm volatile("s_mov_b64 vcc, %16\n\ts_nop 0\n\t"
                     "v_cndmask_b32_dpp %0, %10, %8, vcc quad_perm:[1,0,3,2] row_mask:0xf bank_mask:0xf\n\t"
                     "v_cndmask_b32_dpp %1, %11, %9, vcc quad_perm:[1,0,3,2] row_mask:0xf bank_mask:0xf\n\t"
                     "v_cndmask_b32_dpp %2, %14, %12, vcc quad_perm:[1,0,3,2] row_mask:0xf bank_mask:0xf\n\t"
                     "v_cndmask_b32_dpp %3, %15, %13, vcc quad_perm:[1,0,3,2] row_mask:0xf bank_mask:0xf\n\t"
                     "s_not_b64 vcc, vcc\n\t"
                     "v_cndmask_b32_dpp %4, %8, %10, vcc quad_perm:[1,0,3,2] row_mask:0xf bank_mask:0xf\n\t"
                     "v_cndmask_b32_dpp %5, %9, %11, vcc quad_perm:[1,0,3,2] row_mask:0xf bank_mask:0xf\n\t"
                     "v_cndmask_b32_dpp %6, %12, %14, vcc quad_perm:[1,0,3,2] row_mask:0xf bank_mask:0xf\n\t"
                     "v_cndmask_b32_dpp %7, %13, %15, vcc quad_perm:[1,0,3,2] row_mask:0xf bank_mask:0xf"
                     : "=&v"(lo[2][0]), "=&v"(lo[2][1]), "=&v"(lo[3][0]), "=&v"(lo[3][1]), "=&v"(hi[2][0]), "=&v"(hi[2][1]), "=&v"(hi[3][0]), "=&v"(hi[3][1])
                     : "v"(W[ks][2][0]), "v"(W[ks][2][1]), "v"(W[ks][2][2]), "v"(W[ks][2][3]), "v"(W[ks][3][0]), "v"(W[ks][3][1]), "v"(W[ks][3][2]), "v"(W[ks][3][3]),
                       "s"(0x5555555555555555ull) : "vcc");
#pragma unroll
        for (int t = 0; t < 4; ++t) {
            const u32 sel = (t & 1) ? 0x07060302u : 0x05040100u;
            W[ks][t] = (u4){__builtin_amdgcn_perm(lo[1][t >> 1], lo[0][t >> 1], sel), __builtin_amdgcn_perm(lo[3][t >> 1], lo[2][t >> 1], sel),
                            __builtin_amdgcn_perm(hi[1][t >> 1], hi[0][t >> 1], sel), __builtin_amdgcn_perm(hi[3][t >> 1], hi[2][t >> 1], sel)};
            PIN(W[ks][t]);
        }
    };
    auto publish = [&]() {
        u32 prev = 0;
        if (l == 0) prev = __hip_atomic_fetch_add(hw + 4, 1u, __ATOMIC_RELAXED, __HIP_MEMORY_SCOPE_WORKGROUP);
        if (__builtin_amdgcn_readfirstlane(prev) == 7u && l < 2)
            __hip_atomic_store(fl + (l ? 4096 + b : (mt1 * 8 + (nt1 >> 1)) * 32 + (nt1 & 1)), tag, __ATOMIC_RELAXED, __HIP_MEMORY_SCOPE_AGENT);
        FENCE();
    };
    {
        const int tg = w & 3;
        char* xch = scr + SCR_X + tg * 6144 + l * 16;
        const int hoff = EXP_HOFF + WS_H1 + (mt1 * 8 + 2 * tg) * (56 * 1024) + (nt1 >> 1) * 7168;
        if (aw) {
            const int toff = WS_LN2 + (mt1 * 8 + 2 * tg) * 16384;
            LAS char* wb0 = (LAS char*)smem + (4 * g + (m >> 2)) * 224 + (m & 3) * 8;
            LAS char* wb[4];
            int tq[4];
#pragma unroll
            for (int q = 0; q < 4; ++q) { const int qq = (q + rq) & 3; wb[q] = wb0 + qq * 28672; tq[q] = toff + qq * 4096; }
            u4 T[2][16], A[2][3];
            auto ldt = [&](int kk) {
#pragma unroll
                for (int tt = 0; tt < 2; ++tt) T[tt][kk] = bld4<SC1>(rw, l * 16 + (kk & 3) * 1024, tq[kk >> 2] + tt * 16384);
            };
#pragma unroll
            for (int i = 0; i < 3; ++i) A[0][i] = ldw1(wb[0], 0, i < 2 ? i : 6);
            WAITV(0);
            ST(11);
#pragma unroll
            for (int kk = 0; kk < G1_DEPTH; ++kk) { ldt(kk); SCHED(); }
            f4 acc[2][3];
#pragma unroll
            for (int tt = 0; tt < 2; ++tt)
#pragma unroll
                for (int i = 0; i < 3; ++i) acc[tt][i] = Z4;
#pragma unroll
            for (int kk = 0; kk < 16; ++kk) {
                if ((kk & 1) == 0) {
                    if (__builtin_expect(any_sent(umax(umax(max4(T[0][kk]), max4(T[1][kk])), umax(max4(T[0][kk + 1]), max4(T[1][kk + 1])))), 0)) {
                        wait_tok(T[0][kk], T[1][kk], rw, l * 16, tq[kk >> 2] + (kk & 3) * 1024);
                        wait_tok(T[0][kk + 1], T[1][kk + 1], rw, l * 16, tq[kk >> 2] + ((kk + 1) & 3) * 1024);
                    }
                    if (kk == 0) ST(12);
                }
                if (kk + G1_DEPTH < 16) ldt(kk + G1_DEPTH);
                if (kk < 15) {
#pragma unroll
                    for (int i = 0; i < 3; ++i) A[(kk + 1) & 1][i] = ldw1(wb[(kk + 1) >> 2], (kk + 1) & 3, i < 2 ? i : 6);
                }
#pragma unroll
                for (int i = 0; i < 3; ++i)
#pragma unroll
                    for (int tt = 0; tt < 2; ++tt) acc[tt][i] = mma_b(A[kk & 1][i], T[tt][kk], acc[tt][i]);
#pragma unroll
                for (int i = 0; i < 6; ++i) {
                    __builtin_amdgcn_sched_group_barrier(0x8, 1, 0);
                    if (i == 0 || i == 3) __builtin_amdgcn_sched_group_barrier(0x20, 1, 0);
                    __builtin_amdgcn_sched_group_barrier(0x100, 1, 0);
                }
                if (kk & 1) {} else SCHED();
            }
            u4 A2[4][2];
#pragma unroll
            for (int kk = 0; kk < 3; ++kk)
#pragma unroll
                for (int i = 0; i < 2; ++i) A2[kk][i] = ldw1(wb[0], kk, 2 + i);
            SCHED();
            {
                u2 p[2][3];
#pragma unroll
                for (int tt = 0; tt < 2; ++tt)
#pragma unroll
                    for (int i = 0; i < 3; ++i) p[tt][i] = pk4(acc[tt][i]);
#pragma unroll
                for (int tt = 0; tt < 2; ++tt) *(u4*)(xch + tt * 1024) = (u4){p[tt][0][0], p[tt][0][1], p[tt][1][0], p[tt][1][1]};
                *(u4*)(xch + 2048) = (u4){p[0][2][0], p[0][2][1], p[1][2][0], p[1][2][1]};
            }
            lds_set(hw + tg, 1u);
            ST(13);
#pragma unroll
            for (int ps = 0; ps < 2; ++ps) {
                f4 ac2[2][2] = {{Z4, Z4}, {Z4, Z4}};
#pragma unroll
                for (int kk = 0; kk < 16; ++kk) {
                    const int kn = kk + 3;
                    if (kn < 16 || ps == 0) {
#pragma unroll
                        for (int i = 0; i < 2; ++i) A2[kn & 3][i] = ldw1(wb[(kn & 15) >> 2], kn & 3, (kn < 16 ? 2 + 2 * ps : 4) + i);
                    }
#pragma unroll
                    for (int i = 0; i < 2; ++i)
#pragma unroll
                        for (int tt = 0; tt < 2; ++tt) ac2[tt][i] = mma_b(A2[kk & 3][i], T[tt][kk], ac2[tt][i]);
#pragma unroll
                    for (int i = 0; i < 4; ++i) {
                        __builtin_amdgcn_sched_group_barrier(0x8, 1, 0);
                        __builtin_amdgcn_sched_group_barrier(0x100, 1, 0);
                    }
                    SCHED();
                    if (ps == 1 && 2 * kk < A_EARLY) ldw2q(2 * kk, 2 * kk + 2, false);
                }
                u2 p00 = pk4(ac2[0][0]), p01 = pk4(ac2[0][1]), p10 = pk4(ac2[1][0]), p11 = pk4(ac2[1][1]);
                if (ps == 0) *(u4*)(xch + 3072) = (u4){p00[0], p00[1], p01[0], p01[1]};
                *(u4*)(xch + 4096 + ps * 1024) = (u4){p10[0], p10[1], p11[0], p11[1]};
                lds_set(hw + tg, 2u + ps);
                if (ps == 1) bst4<SC1>(silu_pk4((u4){p00[0], p00[1], p01[0], p01[1]}), rw, l * 16, hoff + ((nt1 & 1) * 3 + 2) * 1024);
                else ST(21);
            }
            FENCE();
            ST(14);
            ldw2q(A_EARLY, A_EARLY + 4, false);
            WAITV(4);
            publish();
            ldw2q(A_EARLY + 4, 28, false);
#pragma unroll
            for (int ks = 0; ks < 7; ++ks) tile_w2(ks);
        } else {
            bg(BG8, 56);
#pragma unroll
            for (int ks = 0; ks < 7; ++ks) tile_w2(ks);
            ST(12);
            lds_wait(hw + tg, 1u);
            __builtin_amdgcn_s_setprio(SILU_PRIO);
            ST(13);
#pragma unroll
            for (int tt = 0; tt < 2; ++tt) bst4<SC1>(silu_pk4(*(const u4*)(xch + tt * 1024)), rw, l * 16, hoff + tt * (56 * 1024) + (nt1 & 1) * 3072);
            {
                u4 o = silu_pk4(*(const u4*)(xch + 2048));
                bst2<SC1>((u2){o[0], o[1]}, rw, l * 8, hoff + 6144 + (nt1 & 1) * 512);
                bst2<SC1>((u2){o[2], o[3]}, rw, l * 8, hoff + 56 * 1024 + 6144 + (nt1 & 1) * 512);
            }
            ST(20);
            lds_wait(hw + tg, 2u);
            ST(21);
#pragma unroll
            for (int tt = 0; tt < 2; ++tt) bst4<SC1>(silu_pk4(*(const u4*)(xch + 3072 + tt * 1024)), rw, l * 16, hoff + tt * (56 * 1024) + ((nt1 & 1) * 3 + 1) * 1024);
            lds_wait(hw + tg, 3u);
            bst4<SC1>(silu_pk4(*(const u4*)(xch + 5120)), rw, l * 16, hoff + 56 * 1024 + ((nt1 & 1) * 3 + 2) * 1024);
            ST(14);
            __builtin_amdgcn_s_setprio(0);
            WAITV(0);
            publish();
        }
    }
    {
        ST(15);
        flag_wait(fl + (grp * 8 + pw) * 32 + (l & 1), tag);
        ST(16);
        const int rot = w & 3;
        const int toff = WS_H1 + (grp * 8 + th * 4) * (56 * 1024) + pw * 7168;
        int to[4];
#pragma unroll
        for (int tt = 0; tt < 4; ++tt) to[tt] = toff + ((tt + rot) & 3) * (56 * 1024);
        u4 T[7][4];
        auto ldtok = [&](int ks) {
#pragma unroll
            for (int tt = 0; tt < 4; ++tt) {
                if (ks < 6) T[ks][tt] = bld4<SC1>(rw, l * 16, to[tt] + ko[ks] * 1024);
                else {
                    u2 a = bld2<SC1>(rw, l * 8 + 2048, to[tt] + 4096), c = bld2<SC1>(rw, l * 8 + 2560, to[tt] + 4096);
                    T[ks][tt] = (u4){a[0], a[1], c[0], c[1]};
                }
            }
            SCHED();
        };
#pragma unroll
        for (int ks = 0; ks < G2_DEPTH; ++ks) ldtok(ks);
        const int rho = grp * 128 + th * 64 + rot * 16 + m;
        const int ooff = (256 * (rho & 7) + (rho >> 3)) * 1024 + fb * 128 + g * 32;
        u4 hres[2];
        f4 acc[4][4];
#pragma unroll
        for (int tt = 0; tt < 4; ++tt)
#pragma unroll
            for (int t = 0; t < 4; ++t) acc[tt][t] = Z4;
#pragma unroll
        for (int ks = 0; ks < 7; ++ks) {
            if (ks + G2_DEPTH < 7) { PIN(T[ks][0]); ldtok(ks + G2_DEPTH); }
#pragma unroll
            for (int tt = 0; tt < 4; ++tt)
#pragma unroll
                for (int t = 0; t < 4; ++t) acc[tt][t] = mma_b(W[ks][t], T[ks][tt], acc[tt][t]);
            SCHED();
        }
        if (!aw) {
            flag_wait(fl + 4096 + grp * 16 + th * 8 + rot * 2 + (l & 1), tag);
            hres[0] = bld4<SC1>(ro, ooff, 0); hres[1] = bld4<SC1>(ro, ooff + 16, 0);
        }
        ST(17);
        lds_wait(hw + 4, 8u);
#pragma unroll
        for (int tt = 0; tt < 4; ++tt)
            if (aw || tt > 0)
#pragma unroll
                for (int t = 0; t < 4; ++t) *(f4*)(smem + (w * 16 + ((tt + rot) & 3) * 4 + t) * 1024 + l * 16) = acc[tt][t];
        BARRIER();
        if (!aw) {
#pragma unroll
            for (int j = 1; j < 8; ++j)
#pragma unroll
                for (int t = 0; t < 4; ++t) acc[0][t] += *(const f4*)(smem + (((w + j) & 7) * 16 + rot * 4 + t) * 1024 + l * 16);
#pragma unroll
            for (int p = 0; p < 2; ++p) {
                u4 o;
#pragma unroll
                for (int j = 0; j < 4; ++j) {
                    const int i = 2 * p + (j >> 1), t = 2 * (j & 1);
                    o[j] = pk_b2(bf_2(hres[p][j]) + bf_2(pk_b(acc[0][t][i], acc[0][t + 1][i])));
                }
                bst4<AUX_OUT>(o, ro, ooff + 16 * p, 0);
            }
        }
    }
    ST_END(ws, 0);
}

extern "C" void kernel_launch(void* const* d_in, const int* in_sizes, int n_in, void* d_out, int out_size,
                              void* d_ws, size_t ws_size, hipStream_t stream) {
    (void)hipFuncSetAttribute((const void*)k_fused, hipFuncAttributeMaxDynamicSharedMemorySize, LDS_B);
    k_fused<<<256, 512, LDS_B, stream>>>((const uint16_t*)d_in[0], (const uint16_t*)d_in[1], (const uint16_t*)d_in[2],
                                         (const uint16_t*)d_in[4], (const uint16_t*)d_in[5], (uint16_t*)d_out, (char*)d_ws);
}
```

```cpp
#include <hip/hip_runtime.h>
#include <hip/hip_bf16.h>
#include <stdint.h>

typedef _Float16 h8 __attribute__((ext_vector_type(8)));
typedef _Float16 h4 __attribute__((ext_vector_type(4)));
typedef _Float16 h2 __attribute__((ext_vector_type(2)));
typedef __bf16 b8 __attribute__((ext_vector_type(8)));
typedef __bf16 b2 __attribute__((ext_vector_type(2)));
typedef short s4 __attribute__((ext_vector_type(4)));
typedef float f4 __attribute__((ext_vector_type(4)));
typedef float f2 __attribute__((ext_vector_type(2)));
typedef unsigned u32;
typedef u32 u4 __attribute__((ext_vector_type(4)));
typedef u32 u2 __attribute__((ext_vector_type(2)));
#define LAS __attribute__((address_space(3)))
#define GAS __attribute__((address_space(1)))
#define DI __device__ __forceinline__
#define FENCE() asm volatile("" ::: "memory")
#define PIN(x) asm volatile("" : "+v"(x))
#define SCHED() __builtin_amdgcn_sched_barrier(0)
#define WAITV(n) asm volatile("s_waitcnt vmcnt(%0)" :: "n"(n) : "memory")
#define BARRIER() do { asm volatile("s_waitcnt lgkmcnt(0)" ::: "memory"); __builtin_amdgcn_s_barrier(); FENCE(); } while (0)
constexpr int SC1 = 16;
constexpr u32 SENT = 0xffffffffu;
constexpr int SPIN_MAX = 1 << 20;

DI u32 pk_h(float a, float b) { f2 v = {a, b}; h2 r = __builtin_convertvector(v, h2); return __builtin_bit_cast(u32, r); }
DI u32 pk_b(float a, float b) { f2 v = {a, b}; b2 r = __builtin_convertvector(v, b2); return __builtin_bit_cast(u32, r); }
DI float bf_lo(u32 v) { return __builtin_bit_cast(float, v << 16); }
DI float bf_hi(u32 v) { return __builtin_bit_cast(float, v & 0xffff0000u); }
DI f2 bf_2(u32 v) { return (f2){bf_lo(v), bf_hi(v)}; }
DI u32 pk_b2(f2 v) { return pk_b(v[0], v[1]); }
DI float rbf(float a) { return bf_lo(pk_b(a, a)); }
DI float cosr(float rev) { return __builtin_amdgcn_cosf(rev); }
DI float sinr(float rev) { return __builtin_amdgcn_sinf(rev); }
DI f2 lo2(f4 v) { return __builtin_shufflevector(v, v, 0, 1); }
DI f2 hi2(f4 v) { return __builtin_shufflevector(v, v, 2, 3); }
DI f2 fma2(f2 a, f2 b, f2 c) { return __builtin_elementwise_fma(a, b, c); }
DI u32 umin(u32 a, u32 b) { return a < b ? a : b; }
DI u32 umax(u32 a, u32 b) { return a > b ? a : b; }
DI u32 clean(u32 v) { return umin(v, SENT - 1); }
DI u32 max4(u4 v) { return umax(umax(umax(v[0], v[1]), v[2]), v[3]); }
#define Z4 ((f4){0, 0, 0, 0})
DI f4 mma_h(u4 a, u4 b, f4 c) { return __builtin_amdgcn_mfma_f32_16x16x32_f16(__builtin_bit_cast(h8, a), __builtin_bit_cast(h8, b), c, 0, 0, 0); }
DI f4 mma_b(u4 a, u4 b, f4 c) { return __builtin_amdgcn_mfma_f32_16x16x32_bf16(__builtin_bit_cast(b8, a), __builtin_bit_cast(b8, b), c, 0, 0, 0); }
DI f4 mma16_h(u2 a, u2 b, f4 c) { return __builtin_amdgcn_mfma_f32_16x16x16f16(__builtin_bit_cast(h4, a), __builtin_bit_cast(h4, b), c, 0, 0, 0); }
typedef __amdgpu_buffer_rsrc_t rsrc_t;
DI rsrc_t mkrs(const void* p, u32 bytes) { return __builtin_amdgcn_make_buffer_rsrc((void*)p, 0, bytes, 0x00020000); }
template <int AUX> DI u4 bld4(rsrc_t r, int v, int s) { return __builtin_amdgcn_raw_buffer_load_b128(r, v, s, AUX); }
template <int AUX> DI u2 bld2(rsrc_t r, int v, int s) { return __builtin_amdgcn_raw_buffer_load_b64(r, v, s, AUX); }
template <int AUX> DI void bst4(u4 d, rsrc_t r, int v, int s) {
    __builtin_amdgcn_raw_buffer_store_b128(d, r, v, s, AUX);
    asm volatile("s_nop 1" :: "v"(d));
}
template <int AUX> DI void bst2(u2 d, rsrc_t r, int v, int s) { __builtin_amdgcn_raw_buffer_store_b64(d, r, v, s, AUX); }

template <int CTRL> DI float dpp(float v) {
    return __builtin_bit_cast(float, __builtin_amdgcn_update_dpp(0, __builtin_bit_cast(int, v), CTRL, 0xf, 0xf, true));
}
template <bool ALL> DI float red_lanes(float v) {
    v += dpp<0xB1>(v);
    if (ALL) v += dpp<0x4E>(v);
    v += dpp<0x124>(v); v += dpp<0x128>(v);
    u32 x = __builtin_bit_cast(u32, v), y = x;
    asm("" : "+v"(y));
    auto r = __builtin_amdgcn_permlane16_swap(x, y, false, false);
    v = __builtin_bit_cast(float, (u32)r[0]) + __builtin_bit_cast(float, (u32)r[1]);
    x = __builtin_bit_cast(u32, v); y = x;
    asm("" : "+v"(y));
    auto q = __builtin_amdgcn_permlane32_swap(x, y, false, false);
    return __builtin_bit_cast(float, (u32)q[0]) + __builtin_bit_cast(float, (u32)q[1]);
}
DI float ln_inv(float var) { return __builtin_amdgcn_rcpf(rbf(__builtin_amdgcn_sqrtf(rbf(rbf(var) + 1e-5f)))); }

#define ST_DECL
#define ST(i)
#define ST_END(ws, kid)

constexpr u32 WS_U = 0;
constexpr u32 WS_WT = WS_U + (2u << 20);
constexpr u32 WS_V = WS_WT + (2u << 20);
constexpr u32 WS_LN2 = WS_V + (2u << 20);
constexpr u32 WS_WA = WS_LN2 + (2u << 20);
constexpr u32 WS_WB = WS_WA + 8192u;
constexpr u32 WS_FA = WS_WB + 8192u;
constexpr u32 WS_FLAG = WS_FA + 16384u;
constexpr u32 WS_EP = WS_FLAG + 20480u;
constexpr u32 WS_H1 = WS_WA + (64u << 10);
constexpr u32 WS_END = WS_H1 + 7340032u;

#define W1_B0 0
#define W1_B1 7
#define W1_B2 5
#define W1_B3 6
#define W1_CHK ((((W1_B0 + W1_B1 + W1_B2 + W1_B3) / 7) * 4 - 2) < 0 ? 0 : (((W1_B0 + W1_B1 + W1_B2 + W1_B3) / 7) * 4 - 2))
#define CLR_SLEEP 6
#define BPRIO 2
#define EXP_UCLR 0
#define POLL_SLEEP 2
#define BG1 6
#define BG2 6
#define BG3 6
#define BG4 12
#define BG5 17
#define BG6 17
#define BG7 17
#define BG8 23
#define A_EARLY 24
#define SILU_PRIO 0
#define EXP_HOFF 0
#define B_EARLY 8
#define W2_DELAY 4
#define W2_SLEEP 8
#define AUX_OUT 2
#define W2_OUT 2
#define W2_ATHR 3
#define W2_EARLY 3
#define G1_DEPTH 8
#define G2_DEPTH 2
constexpr int LDS_SCR = 114688;
constexpr int SCR_X = 8448;
constexpr int SCR_S = SCR_X + 20480;
constexpr int SCR_F = 49024;
constexpr int XCH = 131072;
constexpr int LDS_B = 163840;

DI void glds16(const void* sbase, u32 voff, u32 lds_dst) {
    asm volatile("s_mov_b32 m0, %2\n\ts_nop 0\n\tglobal_load_lds_dwordx4 %0, %1" :: "v"(voff), "s"(sbase), "s"(lds_dst) : "memory");
}
DI void glds16x4(const void* sbase, u32 voff, u32 lds_dst) {
    asm volatile("s_mov_b32 m0, %2\n\ts_nop 0\n\tglobal_load_lds_dwordx4 %0, %1\n\tglobal_load_lds_dwordx4 %0, %1 offset:1024\n\t"
                 "global_load_lds_dwordx4 %0, %1 offset:2048\n\tglobal_load_lds_dwordx4 %0, %1 offset:3072"
                 :: "v"(voff), "s"(sbase), "s"(lds_dst) : "memory");
}
DI bool any_sent(u32 v) { return __builtin_amdgcn_ballot_w64(v == SENT) != 0; }
DI void wait_tok(u4& t0, u4& t1, rsrc_t rw, int vo, int so) {
    if (any_sent(umax(max4(t0), max4(t1)))) {
#pragma nounroll
        for (int sp = 0; sp < SPIN_MAX; ++sp) {
            FENCE();
            t0 = bld4<SC1>(rw, vo, so); t1 = bld4<SC1>(rw, vo, so + 16384);
            if (!any_sent(umax(max4(t0), max4(t1)))) break;
        }
    }
}
DI u4 ldw1(LAS char* wb, int kk, int ft) {
    s4 a0 = __builtin_amdgcn_ds_read_tr16_b64_v4i16((LAS s4*)(wb + kk * 32 * 224 + ft * 32));
    s4 a1 = __builtin_amdgcn_ds_read_tr16_b64_v4i16((LAS s4*)(wb + (kk * 32 + 16) * 224 + ft * 32));
    u2 x0 = __builtin_bit_cast(u2, a0), x1 = __builtin_bit_cast(u2, a1);
    return (u4){x0[0], x0[1], x1[0], x1[1]};
}
DI u2 pk4(f4 a) { return (u2){pk_b(a[0], a[1]), pk_b(a[2], a[3])}; }
DI u2 silu_pk2(u32 p0, u32 p1) {
    u32 o0, o1; float a0, a1, a2, a3, t0, t1, t2, t3;
    asm("v_lshlrev_b32 %2, 16, %10\n\tv_and_b32 %3, 0xffff0000, %10\n\tv_lshlrev_b32 %4, 16, %11\n\tv_and_b32 %5, 0xffff0000, %11\n\t"
        "v_mul_f32 %6, %12, %2\n\tv_mul_f32 %7, %12, %3\n\tv_mul_f32 %8, %12, %4\n\tv_mul_f32 %9, %12, %5\n\t"
        "v_exp_f32 %6, %6\n\tv_exp_f32 %7, %7\n\tv_exp_f32 %8, %8\n\tv_exp_f32 %9, %9\n\t"
        "v_add_f32 %6, 1.0, %6\n\tv_add_f32 %7, 1.0, %7\n\tv_add_f32 %8, 1.0, %8\n\tv_add_f32 %9, 1.0, %9\n\t"
        "v_rcp_f32 %6, %6\n\tv_rcp_f32 %7, %7\n\tv_rcp_f32 %8, %8\n\tv_rcp_f32 %9, %9\n\t"
        "v_mul_f32 %2, %2, %6\n\tv_mul_f32 %3, %3, %7\n\tv_mul_f32 %4, %4, %8\n\tv_mul_f32 %5, %5, %9\n\t"
        "v_cvt_pk_bf16_f32 %0, %2, %3\n\tv_cvt_pk_bf16_f32 %1, %4, %5"
        : "=&v"(o0), "=&v"(o1), "=&v"(a0), "=&v"(a1), "=&v"(a2), "=&v"(a3), "=&v"(t0), "=&v"(t1), "=&v"(t2), "=&v"(t3)
        : "v"(p0), "v"(p1), "s"(-1.4426950408889634f));
    return (u2){o0, o1};
}
DI u4 silu_pk4(u4 p) { u2 a = silu_pk2(p[0], p[1]), b = silu_pk2(p[2], p[3]); return (u4){a[0], a[1], b[0], b[1]}; }
DI void lds_set(LAS u32* p, u32 v) {
    asm volatile("s_waitcnt lgkmcnt(0)" ::: "memory");
    __hip_atomic_store(p, v, __ATOMIC_RELAXED, __HIP_MEMORY_SCOPE_WORKGROUP);
}
DI void lds_wait(LAS u32* p, u32 v) {
#pragma nounroll
    for (int sp = 0; sp < SPIN_MAX; ++sp) {
        if (__builtin_amdgcn_readfirstlane(__hip_atomic_load(p, __ATOMIC_RELAXED, __HIP_MEMORY_SCOPE_WORKGROUP)) >= v) break;
        __builtin_amdgcn_s_sleep(1);
    }
    FENCE();
}
DI void flag_wait(GAS u32* p, u32 tag) {
#pragma nounroll
    for (int sp = 0; sp < SPIN_MAX; ++sp) {
        if (__builtin_amdgcn_ballot_w64(__hip_atomic_load(p, __ATOMIC_RELAXED, __HIP_MEMORY_SCOPE_AGENT) != tag) == 0) break;
        __builtin_amdgcn_s_sleep(2);
    }
    FENCE();
}
#define SENT4 ((u4){SENT, SENT, SENT, SENT})

__global__ __launch_bounds__(512, 2) void k_fused(const uint16_t* x, const uint16_t* wr, const uint16_t* wi, const uint16_t* w1,
                                                  const uint16_t* w2, uint16_t* out, char* ws) {
    extern __shared__ __attribute__((aligned(16))) char smem[];
    const int tid = threadIdx.x, l = tid & 63, g = l >> 4, m = l & 15;
    const int w = __builtin_amdgcn_readfirstlane(tid >> 6);
    const int b = (blockIdx.x & 7) * 32 + (blockIdx.x >> 3);
    const int mt1 = (b >> 6) * 4 + ((b >> 3) & 3), nt1 = ((b >> 5) & 1) * 8 + (b & 7);
    const int grp = b >> 4, th = (b >> 3) & 1, fb = b & 7;
    const rsrc_t rw = mkrs(ws, WS_END), ro = mkrs(out, 2097152);
    char* scr = smem + LDS_SCR;
    LAS u32* hw = (LAS u32*)((LAS char*)smem + LDS_SCR + SCR_F);
    const u32 lds0 = (u32)(uintptr_t)(LAS char*)smem;
    const float mf = (float)m, gf = (float)g, sg = (m & 1) ? -1.0f : 1.0f;
    const bool aw = w < 4;
    ST_DECL;
    const int rq = ((b >> 3) + b) & 3;
    const int row0 = (tid & 255) / 14, cch0 = (tid & 255) - row0 * 14;
    const char* wsrc = (const char*)w1 + nt1 * 224;
    u32 w1o[7];
    auto w1_burst = [&](int i0, int n) {
#pragma unroll
        for (int i = i0; i < i0 + n; ++i) {
            const int q = (i / 7 + rq) & 3, j = i % 7;
            glds16(wsrc + q * (128 * 3584), w1o[j], lds0 + (w - 4) * 1024 + q * 28672 + j * 4096);
        }
    };
    const u32 epoch = __hip_atomic_load((GAS u32*)(ws + WS_EP), __ATOMIC_RELAXED, __HIP_MEMORY_SCOPE_AGENT);
    FENCE();
    const int xoff = (256 * w + b) * 1024 + m * 32 + g * 8;
    u2 xl[2];
    float c16[4], s16[4];
    f2 twc[2], tws[2];
    u4 Gre3, Gim3, SGre, SGim, GA[2];
    auto consts0 = [&]() {
#pragma unroll
        for (int u = 0; u < 4; ++u) {
            float a = (4.0f * gf + u) * (mf * (1.0f / 16));
            c16[u] = cosr(a); s16[u] = sinr(a);
            twc[u >> 1][u & 1] = cosr(a * (1.0f / 32)); tws[u >> 1][u & 1] = sinr(a * (1.0f / 32));
        }
    };
    auto consts3 = [&]() {
#pragma unroll
        for (int u = 0; u < 4; ++u) {
            const float k = 4.0f * gf + u;
            const bool k0 = u == 0 && g == 0;
            float cc = c16[u], ss = s16[u];
            Gre3[u] = pk_b(cc, -ss); Gim3[u] = pk_b(ss, cc);
            float a2 = (2.0f * k + 1.0f) * (mf * (1.0f / 32));
            u32 sp = pk_b(g < 2 ? 2.f * cc : 2.f * sg * cosr(a2), g < 2 ? -2.f * ss : -2.f * sg * sinr(a2));
            SGre[u] = k0 ? pk_b(1.0f, sg) : (g < 2 ? sp : 0u);
            SGim[u] = g < 2 ? 0u : sp;
            float a4 = k * (mf * (1.0f / 32)), c4 = 2.f * cosr(a4), s4_ = -2.f * sinr(a4);
            GA[0][u] = k0 ? pk_b(1.0f, sg) : pk_b(c4, s4_);
            GA[1][u] = k0 ? pk_b(1.0f, sg) : ((u & 1) ? pk_b(-c4, -s4_) : pk_b(c4, s4_));
        }
        PIN(Gre3); PIN(Gim3); PIN(SGre); PIN(SGim); PIN(GA[0]); PIN(GA[1]); PIN(twc[0]); PIN(twc[1]); PIN(tws[0]); PIN(tws[1]);
    };
    const int pw = (w + 4 * th) & 7;
    GAS u32* fl = (GAS u32*)(ws + WS_FLAG);
    int ko[7];
    {
        const int r6 = (fb < 6 ? fb : fb - 5) + 3 * (grp & 1);
#pragma unroll
        for (int ks = 0; ks < 6; ++ks) { int k = ks + r6; k = k >= 6 ? k - 6 : k; ko[ks] = k >= 6 ? k - 6 : k; }
        ko[6] = 6;
    }
    u4 W[7][4];
    const u32 w2vo = (l >> 3) * 1024 + (((l & 7) ^ (((l >> 4) & 1) << 2)) * 16);
    auto w2_glds = [&](int p_, int ks, u32 dst) {
        const int q = ko[ks];
        const char* src = (const char*)w2 + (224 * p_ + (ks == 6 ? 96 : (q < 3 ? 32 * q : 16 + 32 * q))) * 1024 + fb * 128;
        glds16(src, w2vo, dst); glds16(src + 8192, w2vo, dst + 1024);
        glds16(src + (ks == 6 ? 112 : 16) * 1024, w2vo, dst + 2048); glds16(src + (ks == 6 ? 120 : 24) * 1024, w2vo, dst + 3072);
    };
    LAS char* w2rd = (LAS char*)smem + (4 * g + (m >> 2)) * 128 + (((m & 3) ^ (((m >> 3) & 1) << 2)) * 16);
    auto w2_read = [&](u4 (&f)[4], int off) {
#pragma unroll
        for (int t = 0; t < 4; ++t) {
            LAS char* p = (LAS char*)((u32)(uintptr_t)w2rd ^ ((t >> 1) * 64)) + off + (t & 1) * 8;
            u2 x0 = __builtin_bit_cast(u2, __builtin_amdgcn_ds_read_tr16_b64_v4i16((LAS s4*)p));
            u2 x1 = __builtin_bit_cast(u2, __builtin_amdgcn_ds_read_tr16_b64_v4i16((LAS s4*)(p + 2048)));
            f[t] = (u4){x0[0], x0[1], x1[0], x1[1]};
        }
    };
    auto bg = [&](int i0, int i1) { w1_burst(i0, i1 - i0); };
    u32 tag;
    if (aw) {
        u32 xd[8];
        const u32* xr = (const u32*)(x + (256 * (2 * w + ((m >> 1) & 1)) + b) * 512) + 8 * g + 2 * (m >> 2) + (m & 1);
#pragma unroll
        for (int j = 0; j < 8; ++j) xd[j] = xr[32 * j];
        if (tid < 8) hw[tid] = 0;
        FENCE();
        ST(2);
        {
            u4 FAre, FAim, Fre, Fim, Sre, Sim; u2 Mb; f2 pc[2], ps[2];
            consts0();
            float c0[8], s0[8];
#pragma unroll
            for (int j = 0; j < 8; ++j) {
                float a = (gf + 4.0f * j) * (mf * (1.0f / 32));
                c0[j] = cosr(a); s0[j] = m ? -sinr(a) : ((g & 1) ? -1.0f : 1.0f);
            }
            const float mo = (float)(2 * (m & 7) + 1) * (1.0f / 32);
#pragma unroll
            for (int u = 0; u < 4; ++u) {
                FAre[u] = pk_h(c0[2 * u], c0[2 * u + 1]); FAim[u] = pk_h(s0[2 * u], s0[2 * u + 1]);
                float cc = c16[u], ss = s16[u];
                Fre[u] = pk_h(cc, ss); Fim[u] = pk_h(-ss, cc);
                float a2 = (4.0f * gf + u) * mo;
                u32 re = pk_h(m < 8 ? cc : cosr(a2), 0.f), im = pk_h(m < 8 ? (m ? -ss : ((u & 1) ? -1.0f : 1.0f)) : -sinr(a2), 0.f);
                Sre[u] = m < 8 ? re : re << 16; Sim[u] = m < 8 ? im : im << 16;
                pc[u & 1][u >> 1] = twc[u >> 1][u & 1]; ps[u & 1][u >> 1] = tws[u >> 1][u & 1];
            }
#pragma unroll
            for (int e = 0; e < 2; ++e) {
                float a = (float)(((m >> 1) * (256 * (2 * g + e) + b)) & 2047) * (1.0f / 2048);
                float cc = cosr(a), ss = sinr(a);
                Mb[e] = (m & 1) ? pk_h(-ss, cc) : pk_h(cc, ss);
            }
            PIN(FAre); PIN(FAim); PIN(Fre); PIN(Fim); PIN(Sre); PIN(Sim); PIN(Mb); PIN(pc[0]); PIN(pc[1]); PIN(ps[0]); PIN(ps[1]);
            ST(3);
            f2 v[8];
#pragma unroll
            for (int j = 0; j < 8; ++j) v[j] = bf_2(xd[j]);
            f2 s2 = ((v[0] + v[1]) + (v[2] + v[3])) + ((v[4] + v[5]) + (v[6] + v[7]));
            const float mean = red_lanes<false>(s2[0] + s2[1]) * (1.0f / 512);
            f2 q0 = {0.f, 0.f}, q1 = {0.f, 0.f};
#pragma unroll
            for (int j = 0; j < 8; ++j) { v[j] -= mean; if (j & 1) q1 = fma2(v[j], v[j], q1); else q0 = fma2(v[j], v[j], q0); }
            q0 += q1;
            const float inv = ln_inv(red_lanes<false>(q0[0] + q0[1]) * (1.0f / 512));
            const float off = (mean - rbf(mean)) * inv;
#pragma unroll
            for (int j = 0; j < 8; ++j) v[j] = fma2(v[j], (f2){inv, inv}, (f2){off, off});
            u4 B[2];
#pragma unroll
            for (int p = 0; p < 2; ++p) {
                u4 A = {pk_h(v[0][p], v[1][p]), pk_h(v[2][p], v[3][p]), pk_h(v[4][p], v[5][p]), pk_h(v[6][p], v[7][p])};
                f4 Dre = mma_h(A, FAre, Z4), Dim = mma_h(A, FAim, Z4);
#pragma unroll
                for (int r = 0; r < 2; ++r) {
                    f2 re = r ? hi2(Dre) : lo2(Dre), im = r ? hi2(Dim) : lo2(Dim);
                    f2 tr = fma2(im, ps[p], re * pc[p]), ti = fma2(-re, ps[p], im * pc[p]);
                    B[r][p] = pk_h(tr[0], ti[0]); B[r][2 + p] = pk_h(tr[1], ti[1]);
                }
            }
#pragma unroll
            for (int r = 0; r < 2; ++r) {
                f4 Gre = mma_h(Fre, B[r], Z4), Gim = mma_h(Fim, B[r], Z4), Hre = mma_h(Sre, B[r], Z4), Him = mma_h(Sim, B[r], Z4);
                u4 Z;
#pragma unroll
                for (int i = 0; i < 4; ++i) Z[i] = pk_h(m ? Gre[i] : Hre[i], m ? Gim[i] : Him[i]);
                *(u4*)(scr + (2 * w + r) * 1024 + m * 64 + g * 16) = Z;
            }
            BARRIER();
            ST(18);
            u4 r0 = *(const u4*)(scr + (2 * g) * 1024 + w * 256 + m * 16);
            u4 r1 = *(const u4*)(scr + (2 * g + 1) * 1024 + w * 256 + m * 16);
#pragma unroll
            for (int t = 0; t < 4; ++t) {
                f4 D = mma16_h(Mb, (u2){r0[t], r1[t]}, Z4);
                *(u2*)(scr + SCR_S + w * 2048 + m * 128 + (((2 * t + (g >> 1)) ^ (m >> 1)) * 16) + (g & 1) * 8) =
                    (u2){clean(pk_h(D[0], D[1])), clean(pk_h(D[2], D[3]))};
            }
        }
        u4 uo0 = *(const u4*)(scr + SCR_S + w * 2048 + (l ^ ((l >> 4) & 7)) * 16);
        u4 uo1 = *(const u4*)(scr + SCR_S + w * 2048 + 1024 + (l ^ (((l >> 4) + 4) & 7)) * 16);
        u4 Fre, Fim, Gre, Gim, N0, N1; f2 tc[2], ts[2];
#pragma unroll
        for (int u = 0; u < 4; ++u) {
            float cc = c16[u], ss = s16[u];
            Fre[u] = pk_h(0.25f * cc, 0.25f * ss); Fim[u] = pk_h(-0.25f * ss, 0.25f * cc);
            Gre[u] = pk_b(cc * (1.0f / 256), -ss * (1.0f / 256)); Gim[u] = pk_b(ss * (1.0f / 256), cc * (1.0f / 256));
            float a3 = (4.0f * gf + u) * (mf * (1.0f / 256));
            tc[u >> 1][u & 1] = cosr(a3); ts[u >> 1][u & 1] = sinr(a3);
        }
        PIN(Fre); PIN(Fim); PIN(Gre); PIN(Gim); PIN(tc[0]); PIN(tc[1]); PIN(ts[0]); PIN(ts[1]);
        ST(19);
        BARRIER();
        ST(4);
        bst4<SC1>(uo0, rw, l * 16, WS_U + b * 8192 + w * 2048);
        bst4<SC1>(uo1, rw, l * 16 + 1024, WS_U + b * 8192 + w * 2048);
        xl[0] = *(const u2*)((const char*)x + xoff); xl[1] = *(const u2*)((const char*)x + xoff + 512);
        FENCE();
        tag = __builtin_amdgcn_readfirstlane(epoch) + 1u;
        consts3();
#pragma unroll
        for (int u = 0; u < 4; ++u) {
            float a1 = (float)(((4 * (g & 1) + u) * (256 * (m >> 1) + b)) & 2047) * (1.0f / 2048);
            float c1 = cosr(a1), s1 = sinr(a1);
            u32 nb = (m & 1) ? pk_b(s1, c1) : pk_b(c1, -s1);
            N0[u] = g < 2 ? nb : 0u; N1[u] = g < 2 ? 0u : nb;
        }
        PIN(N0); PIN(N1);
        WAITV(0);
        BARRIER();
        ST(5);
        u4 d[2], f[2], e[2];
        const int uo = (tid >> 1) * 8192 + (tid & 1) * 16;
        d[0] = bld4<SC1>(rw, uo, WS_U + b * 32); d[1] = bld4<SC1>(rw, uo, WS_U + 128 * 8192 + b * 32);
        f[0] = bld4<SC1>(rw, uo, WS_WT + b * 32); f[1] = bld4<SC1>(rw, uo, WS_WT + 128 * 8192 + b * 32);
        asm volatile("" :: "v"(f[0]), "v"(f[1]));
        if (any_sent(umax(max4(d[0]), max4(d[1])))) {
#pragma nounroll
            for (int sp = 0; sp < SPIN_MAX; ++sp) {
                __builtin_amdgcn_s_sleep(POLL_SLEEP);
                FENCE();
                d[0] = bld4<SC1>(rw, uo, WS_U + b * 32); d[1] = bld4<SC1>(rw, uo, WS_U + 128 * 8192 + b * 32);
                if (!any_sent(umax(max4(d[0]), max4(d[1])))) break;
            }
        }
        FENCE();
        f[0] = bld4<SC1>(rw, uo, WS_WT + b * 32); f[1] = bld4<SC1>(rw, uo, WS_WT + 128 * 8192 + b * 32);
        char* sp = scr + SCR_S + (tid ^ ((tid >> 4) & 1)) * 16;
        *(u4*)sp = d[0]; *(u4*)(sp + 4096) = d[1];
        BARRIER();
        if (b == 0) {
            f[0] = bld4<SC1>(rw, tid * 16, WS_WA); f[1] = bld4<SC1>(rw, tid * 16 + 4096, WS_WA);
            e[0] = bld4<SC1>(rw, tid * 16, WS_WB); e[1] = bld4<SC1>(rw, tid * 16 + 4096, WS_WB);
            if (tid == 0) __hip_atomic_store((GAS u32*)(ws + WS_EP), tag, __ATOMIC_RELAXED, __HIP_MEMORY_SCOPE_AGENT);
        }
        ST(6);
        u2 ld[4], wl[4];
        const int so = (64 * g + m) * 32 + (((w >> 1) ^ (m >> 3)) * 16) + (w & 1) * 8;
#pragma unroll
        for (int q = 0; q < 4; ++q) ld[q] = *(const u2*)(scr + SCR_S + so + 512 * q);
        FENCE();
        f4 Xre[2], Xim[2];
#pragma unroll
        for (int e = 0; e < 2; ++e) {
            u4 A = {ld[0][e], ld[1][e], ld[2][e], ld[3][e]};
            f4 Dre = mma_h(A, Fre, Z4), Dim = mma_h(A, Fim, Z4);
            f2 r0 = fma2(lo2(Dim), ts[0], lo2(Dre) * tc[0]), i0 = fma2(-lo2(Dre), ts[0], lo2(Dim) * tc[0]);
            f2 r1 = fma2(hi2(Dim), ts[1], hi2(Dre) * tc[1]), i1 = fma2(-hi2(Dre), ts[1], hi2(Dim) * tc[1]);
            u4 B = {pk_h(r0[0], i0[0]), pk_h(r0[1], i0[1]), pk_h(r1[0], i1[0]), pk_h(r1[1], i1[1])};
            Xre[e] = mma_h(Fre, B, Z4); Xim[e] = mma_h(Fim, B, Z4);
        }
        *(u4*)(sp + 8192) = f[0]; *(u4*)(sp + 12288) = f[1];
        f2* sx = (f2*)(scr + SCR_X);
        if (b == 0) {
            *(u4*)(sp - SCR_S) = e[0]; *(u4*)(sp - SCR_S + 4096) = e[1];
#pragma unroll
            for (int e = 0; e < 2; ++e)
#pragma unroll
                for (int i = 0; i < 4; ++i) sx[2 * w + e + 8 * (m + 16 * (4 * g + i))] = (f2){Xre[e][i], Xim[e][i]};
        }
        BARRIER();
        ST(20);
#pragma unroll
        for (int q = 0; q < 4; ++q) wl[q] = *(const u2*)(scr + SCR_S + so + 8192 + 512 * q);
        FENCE();
        f2 yr[2][2], yi[2][2];
#pragma unroll
        for (int e = 0; e < 2; ++e)
#pragma unroll
            for (int hh = 0; hh < 2; ++hh) {
                f2 wre = {bf_lo(wl[2 * hh][e]), bf_lo(wl[2 * hh + 1][e])}, wim = {bf_hi(wl[2 * hh][e]), bf_hi(wl[2 * hh + 1][e])};
                f2 xr = hh ? hi2(Xre[e]) : lo2(Xre[e]), xi = hh ? hi2(Xim[e]) : lo2(Xim[e]);
                yr[e][hh] = fma2(-xi, wim, xr * wre); yi[e][hh] = fma2(xr, wim, xi * wre);
            }
        if (b == 0) {
#pragma unroll
            for (int i = 0; i < 4; ++i) {
                u2 wb = *(const u2*)(scr + so + 512 * i);
#pragma unroll
                for (int e = 0; e < 2; ++e) {
                    f2 xm = sx[(2048 - (2 * w + e + 8 * (m + 16 * (4 * g + i)))) & 2047];
                    float br = bf_lo(wb[e]), bi = bf_hi(wb[e]);
                    yr[e][i >> 1][i & 1] += xm[0] * br + xm[1] * bi;
                    yi[e][i >> 1][i & 1] += xm[0] * bi - xm[1] * br;
                }
            }
        }
        char* pv = scr + SCR_S;
#pragma unroll
        for (int e = 0; e < 2; ++e) {
            u4 Y = {pk_b(yr[e][0][0], yi[e][0][0]), pk_b(yr[e][0][1], yi[e][0][1]), pk_b(yr[e][1][0], yi[e][1][0]), pk_b(yr[e][1][1], yi[e][1][1])};
            f4 Dre = mma_b(Y, Gre, Z4), Dim = mma_b(Y, Gim, Z4);
            f2 r0 = fma2(-lo2(Dim), ts[0], lo2(Dre) * tc[0]), i0 = fma2(lo2(Dre), ts[0], lo2(Dim) * tc[0]);
            f2 r1 = fma2(-hi2(Dim), ts[1], hi2(Dre) * tc[1]), i1 = fma2(hi2(Dre), ts[1], hi2(Dim) * tc[1]);
            u4 A = {pk_b(r0[0], i0[0]), pk_b(r0[1], i0[1]), pk_b(r1[0], i1[0]), pk_b(r1[1], i1[1])};
            f4 Ere = mma_b(A, Gre, Z4), Eim = mma_b(A, Gim, Z4);
            u4 o;
#pragma unroll
            for (int i = 0; i < 4; ++i) o[i] = clean(pk_b(Ere[i], Eim[i]));
            *(u4*)(pv + (2 * w + e) * 1024 + ((m * 4 + g) ^ ((w >> 1) * 4)) * 16) = o;
        }
        BARRIER();
        ST(7);
#pragma unroll
        for (int it = 0; it < 2; ++it) {
            const int ch = tid + 256 * it;
            const u32* q = (const u32*)(pv + (ch & 1) * 4096 + (((ch >> 1) * 4) ^ ((ch & 1) * 64)));
            bst4<SC1>((u4){q[0], q[256], q[512], q[768]}, rw, ch * 16, WS_V + b * 8192);
        }
        WAITV(0);
        BARRIER();
        ST(8);
        const int vo = (2 * m + (g >> 1)) * 8192 + (g & 1) * 16, vs = WS_V + w * 64 * 8192 + b * 32;
        u4 v0, v1;
#pragma nounroll
        for (int sp = 0; sp < SPIN_MAX; ++sp) {
            FENCE();
            v0 = bld4<SC1>(rw, vo, vs); v1 = bld4<SC1>(rw, vo, vs + 32 * 8192);
            if (!any_sent(umax(max4(v0), max4(v1)))) break;
            __builtin_amdgcn_s_sleep(POLL_SLEEP);
        }
        ST(9);
        char* sz = scr + SCR_X + g * 2048 + w * 256 + m * 8;
#pragma unroll
        for (int hh = 0; hh < 2; ++hh) {
            f4 D0 = mma_b(N0, hh ? v1 : v0, Z4), D1 = mma_b(N1, hh ? v1 : v0, Z4);
            *(u2*)(sz + hh * 128) = (u2){pk_b(D0[0], D0[1]), pk_b(D1[0], D1[1])};
            *(u2*)(sz + 1024 + hh * 128) = (u2){pk_b(D0[2], D0[3]), pk_b(D1[2], D1[3])};
        }
    } else {
        auto announce = [&](int which) {
            if (l < 8) __hip_atomic_store((GAS u32*)(ws + WS_FA) + which * 2048 + l * 256 + b, tag, __ATOMIC_RELAXED, __HIP_MEMORY_SCOPE_AGENT);
            FENCE();
        };
        auto all_announced = [&](int which) {
#pragma nounroll
            for (int sp = 0; sp < SPIN_MAX; ++sp) {
                FENCE();
                u4 f = bld4<SC1>(rw, l * 16, WS_FA + which * 8192 + (blockIdx.x & 7) * 1024);
                if (__builtin_amdgcn_ballot_w64(f[0] != tag || f[1] != tag || f[2] != tag || f[3] != tag) == 0) break;
                __builtin_amdgcn_s_sleep(4);
            }
        };
        const rsrc_t rr = mkrs(wr, 1052672), ri = mkrs(wi, 1052672);
        const int j = 4 * (w - 4) + g, i0_ = 4 * (m & 1);
        const bool mir = w >= 6;
        u32 fr[4], fi[4], sp_[8];
#pragma unroll
        for (int i = 0; i < 4; ++i) {
            const int e = mir ? ((2048 - 8 * b - i0_ - i) & 2047) * 514 + (512 - 32 * j - (m | 1)) * 2 : (8 * b + i0_ + i) * 514 + (32 * j + (m & 14)) * 2;
            fr[i] = __builtin_amdgcn_raw_buffer_load_b32(rr, e, 0, 0); fi[i] = __builtin_amdgcn_raw_buffer_load_b32(ri, e, 0, 0);
        }
        if (mir) {
            const int e = (8 * b + (l & 7)) * 514 + (16 + 32 * (4 * (w - 6) + ((l >> 3) & 3))) * 2;
            sp_[0] = __builtin_amdgcn_raw_buffer_load_b16(rr, e, 0, 0); sp_[1] = __builtin_amdgcn_raw_buffer_load_b16(ri, e, 0, 0);
        } else if (w == 5) {
            const int ed = (8 * b + (l & 7)) * 514, em = ((2048 - 8 * b - (l & 7)) & 2047) * 514;
#pragma unroll
            for (int i = 0; i < 8; ++i) sp_[i] = __builtin_amdgcn_raw_buffer_load_b16((i & 1) ? ri : rr, ((i & 2) ? em : ed) + (i >> 2) * 512, 0, 0);
        }
        FENCE();
#pragma unroll
        for (int i = 0; i < 2; ++i) bst4<SC1>(SENT4, rw, l * 16 + i * 1024, WS_U + b * 8192 + (w - 4) * 2048);
        ST(2);
#pragma unroll
        for (int jj = 0; jj < 7; ++jj) {
            const int cj_ = (4 * jj) % 14, rj = 18 * jj + (4 * jj) / 14;
            w1o[jj] = (row0 + rj) * 3584 + (cch0 + cj_) * 16 + (cch0 + cj_ >= 14 ? 3584 - 224 : 0);
            PIN(w1o[jj]);
        }
        {
            const u32 cj = mir ? 0x80000000u : 0u, se = mir ? 0x07060302u : 0x05040100u, so_ = mir ? 0x05040100u : 0x07060302u;
            u4 s0, s1;
#pragma unroll
            for (int i = 0; i < 4; ++i) { s0[i] = __builtin_amdgcn_perm(fi[i], fr[i], se) ^ cj; s1[i] = __builtin_amdgcn_perm(fi[i], fr[i], so_) ^ cj; }
            ST(3);
            const int wo = (m >> 1) * 1024 + j * 32 + (m & 1) * 16;
            bst4<SC1>(s0, rw, wo, WS_WT + b * 8192); bst4<SC1>(s1, rw, wo + 512, WS_WT + b * 8192);
            if (mir) {
                if (l < 32) __builtin_amdgcn_raw_buffer_store_b32(sp_[0] | (sp_[1] << 16), rw, l * 4, WS_WT + b * 8192 + (w - 4) * 128, SC1);
            } else if (w == 5 && l < 8) {
                float ar = bf_lo(sp_[0]) + bf_lo(sp_[2]), ai = bf_lo(sp_[1]) - bf_lo(sp_[3]);
                float br = bf_lo(sp_[4]) + bf_lo(sp_[6]), bi = bf_lo(sp_[5]) - bf_lo(sp_[7]);
                __builtin_amdgcn_raw_buffer_store_b32(pk_b(0.25f * (ar + br), 0.25f * (ai + bi)), rw, l * 4, WS_WA + b * 32, SC1);
                __builtin_amdgcn_raw_buffer_store_b32(pk_b(0.25f * (ar - br), 0.25f * (ai - bi)), rw, l * 4, WS_WB + b * 32, SC1);
            }
        }
        tag = __builtin_amdgcn_readfirstlane(epoch) + 1u;
        if (w == 4) WAITV(2); else if (w == 5) WAITV(4); else WAITV(3);
        ST(18);
        BARRIER();
        if (w == 7) announce(0);
        bg(0, BG1);
        consts0();
        WAITV(BG1);
        ST(19);
        BARRIER();
        __builtin_amdgcn_s_sleep(2);
#pragma unroll
        for (int i = 0; i < 2; ++i) bst4<SC1>(SENT4, rw, l * 16 + i * 1024, WS_V + b * 8192 + (w - 4) * 2048);
        xl[0] = *(const u2*)((const char*)x + xoff); xl[1] = *(const u2*)((const char*)x + xoff + 512);
        FENCE();
        if (w == 7) all_announced(0);
        bg(BG1, BG2);
        ST(4);
        BARRIER();
        consts3();
        bg(BG2, BG3);
        WAITV(BG3 - BG1);
        ST(5);
        BARRIER();
        if (w == 7) announce(1);
#pragma unroll
        for (int i = 0; i < 2; ++i) bst4<SC1>(SENT4, rw, (l >> 3) * 256 + (l & 7) * 16 + i * 2048, WS_LN2 + (b >> 1) * 16384 + (b & 1) * 128 + (w - 4) * 4096);
        bg(BG3, BG4);
        BARRIER();
        bg(BG4, BG5);
        WAITV(BG5 - BG3);
        ST(6);
        BARRIER();
        if (w == 7) all_announced(1);
        else __builtin_amdgcn_s_sleep(3);
        bg(BG5, BG6);
        ST(7);
        BARRIER();
        __builtin_amdgcn_s_sleep(4);
        bg(BG6, BG7);
    }
    BARRIER();
    if (!aw) bg(BG7, BG8);
    {
        u4 A = *(const u4*)(scr + SCR_X + w * 1024 + m * 64 + g * 16);
        f4 Dre = mma_b(A, Gre3, Z4), Dim = mma_b(A, Gim3, Z4), Hre = mma_b(A, SGre, Z4), Him = mma_b(A, SGim, Z4);
        if (g == 0) { Dre[0] = Hre[0]; Dim[0] = Him[0]; }
        f2 r0 = fma2(-lo2(Dim), tws[0], lo2(Dre) * twc[0]), i0 = fma2(lo2(Dre), tws[0], lo2(Dim) * twc[0]);
        f2 r1 = fma2(-hi2(Dim), tws[1], hi2(Dre) * twc[1]), i1 = fma2(hi2(Dre), tws[1], hi2(Dim) * twc[1]);
        u4 A2 = {pk_b(r0[0], i0[0]), pk_b(r0[1], i0[1]), pk_b(r1[0], i1[0]), pk_b(r1[1], i1[1])};
        f2 h[4];
#pragma unroll
        for (int t = 0; t < 2; ++t) {
            f4 Yv = mma_b(A2, GA[t], Z4);
            u32 h0 = pk_b2(bf_2(xl[t][0]) + bf_2(pk_b(Yv[0], Yv[1]))), h1 = pk_b2(bf_2(xl[t][1]) + bf_2(pk_b(Yv[2], Yv[3])));
            bst2<SC1>((u2){h0, h1}, ro, xoff + 512 * t, 0);
            h[2 * t] = bf_2(h0); h[2 * t + 1] = bf_2(h1);
        }
        f2 s2 = (h[0] + h[1]) + (h[2] + h[3]);
        const float mean = red_lanes<true>(s2[0] + s2[1]) * (1.0f / 512);
        f2 q2 = {0.f, 0.f};
#pragma unroll
        for (int j = 0; j < 4; ++j) { f2 d = h[j] - mean; q2 = fma2(d, d, q2); }
        const float inv = ln_inv(red_lanes<true>(q2[0] + q2[1]) * (1.0f / 512));
        const float mb = rbf(mean);
#pragma unroll
        for (int t = 0; t < 2; ++t)
            *(u2*)(scr + ((8 * t + (m >> 1)) * 4 + g) * 128 + (w ^ (m >> 1)) * 16 + (m & 1) * 8) =
                (u2){clean(pk_b2(bf_2(pk_b2(h[2 * t] - mb)) * inv)), clean(pk_b2(bf_2(pk_b2(h[2 * t + 1] - mb)) * inv))};
        WAITV(2);
        BARRIER();
        u4 lo = *(const u4*)(scr + (tid ^ ((tid >> 5) & 7)) * 16);
        bst4<SC1>(lo, rw, (tid >> 3) * 256 + (tid & 7) * 16, WS_LN2 + (b >> 1) * 16384 + (b & 1) * 128);
        FENCE();
        ST(10);
    }
    auto publish = [&]() {
        u32 prev = 0;
        if (l == 0) prev = __hip_atomic_fetch_add(hw + 4, 1u, __ATOMIC_RELAXED, __HIP_MEMORY_SCOPE_WORKGROUP);
        if (__builtin_amdgcn_readfirstlane(prev) == 3u && l < 2)
            __hip_atomic_store(fl + (l ? 4096 + b : (mt1 * 8 + (nt1 >> 1)) * 32 + (nt1 & 1)), tag, __ATOMIC_RELAXED, __HIP_MEMORY_SCOPE_AGENT);
        FENCE();
    };
    {
        const int tg = w & 3;
        char* xch = smem + XCH + tg * 7168 + l * 16;
        const int hoff = EXP_HOFF + WS_H1 + (mt1 * 8 + 2 * tg) * (56 * 1024) + (nt1 >> 1) * 7168;
        if (aw) {
            const int toff = WS_LN2 + (mt1 * 8 + 2 * tg) * 16384;
            LAS char* wb0 = (LAS char*)smem + (4 * g + (m >> 2)) * 224 + (m & 3) * 8;
            LAS char* wb[4];
            int tq[4];
#pragma unroll
            for (int q = 0; q < 4; ++q) { const int qq = (q + rq) & 3; wb[q] = wb0 + qq * 28672; tq[q] = toff + qq * 4096; }
            u4 T[2][16], A[2][3];
            auto ldt = [&](int kk) {
#pragma unroll
                for (int tt = 0; tt < 2; ++tt) T[tt][kk] = bld4<SC1>(rw, l * 16 + (kk & 3) * 1024, tq[kk >> 2] + tt * 16384);
            };
#pragma unroll
            for (int i = 0; i < 3; ++i) A[0][i] = ldw1(wb[0], 0, i < 2 ? i : 6);
            WAITV(0);
            if (l == 0) __hip_atomic_fetch_add(hw + 5, 1u, __ATOMIC_RELAXED, __HIP_MEMORY_SCOPE_WORKGROUP);
            ST(11);
#pragma unroll
            for (int kk = 0; kk < G1_DEPTH; ++kk) { ldt(kk); SCHED(); }
            f4 acc[2][3];
#pragma unroll
            for (int tt = 0; tt < 2; ++tt)
#pragma unroll
                for (int i = 0; i < 3; ++i) acc[tt][i] = Z4;
#pragma unroll
            for (int kk = 0; kk < 16; ++kk) {
                if ((kk & 1) == 0) {
                    if (__builtin_expect(any_sent(umax(umax(max4(T[0][kk]), max4(T[1][kk])), umax(max4(T[0][kk + 1]), max4(T[1][kk + 1])))), 0)) {
                        wait_tok(T[0][kk], T[1][kk], rw, l * 16, tq[kk >> 2] + (kk & 3) * 1024);
                        wait_tok(T[0][kk + 1], T[1][kk + 1], rw, l * 16, tq[kk >> 2] + ((kk + 1) & 3) * 1024);
                    }
                    if (kk == 0) ST(12);
                    if (kk == (BG8 / 7) * 4 - 2) lds_wait(hw + 5, 8u);
                }
                if (kk + G1_DEPTH < 16) ldt(kk + G1_DEPTH);
                if (kk < 15) {
#pragma unroll
                    for (int i = 0; i < 3; ++i) A[(kk + 1) & 1][i] = ldw1(wb[(kk + 1) >> 2], (kk + 1) & 3, i < 2 ? i : 6);
                }
#pragma unroll
                for (int i = 0; i < 3; ++i)
#pragma unroll
                    for (int tt = 0; tt < 2; ++tt) acc[tt][i] = mma_b(A[kk & 1][i], T[tt][kk], acc[tt][i]);
#pragma unroll
                for (int i = 0; i < 6; ++i) {
                    __builtin_amdgcn_sched_group_barrier(0x8, 1, 0);
                    if (i == 0 || i == 3) __builtin_amdgcn_sched_group_barrier(0x20, 1, 0);
                    __builtin_amdgcn_sched_group_barrier(0x100, 1, 0);
                }
                if (kk & 1) {} else SCHED();
            }
            u4 A2[4][2];
#pragma unroll
            for (int kk = 0; kk < 3; ++kk)
#pragma unroll
                for (int i = 0; i < 2; ++i) A2[kk][i] = ldw1(wb[0], kk, 2 + i);
            SCHED();
            {
                u2 p[2][3];
#pragma unroll
                for (int tt = 0; tt < 2; ++tt)
#pragma unroll
                    for (int i = 0; i < 3; ++i) p[tt][i] = pk4(acc[tt][i]);
#pragma unroll
                for (int tt = 0; tt < 2; ++tt) *(u4*)(xch + tt * 1024) = (u4){p[tt][0][0], p[tt][0][1], p[tt][1][0], p[tt][1][1]};
                *(u4*)(xch + 2048) = (u4){p[0][2][0], p[0][2][1], p[1][2][0], p[1][2][1]};
            }
            lds_set(hw + tg, 1u);
            ST(13);
#pragma unroll
            for (int ps = 0; ps < 2; ++ps) {
                f4 ac2[2][2] = {{Z4, Z4}, {Z4, Z4}};
#pragma unroll
                for (int kk = 0; kk < 16; ++kk) {
                    const int kn = kk + 3;
                    if (kn < 16 || ps == 0) {
#pragma unroll
                        for (int i = 0; i < 2; ++i) A2[kn & 3][i] = ldw1(wb[(kn & 15) >> 2], kn & 3, (kn < 16 ? 2 + 2 * ps : 4) + i);
                    }
#pragma unroll
                    for (int i = 0; i < 2; ++i)
#pragma unroll
                        for (int tt = 0; tt < 2; ++tt) ac2[tt][i] = mma_b(A2[kk & 3][i], T[tt][kk], ac2[tt][i]);
#pragma unroll
                    for (int i = 0; i < 4; ++i) {
                        __builtin_amdgcn_sched_group_barrier(0x8, 1, 0);
                        __builtin_amdgcn_sched_group_barrier(0x100, 1, 0);
                    }
                    SCHED();
                }
                u2 p00 = pk4(ac2[0][0]), p01 = pk4(ac2[0][1]), p10 = pk4(ac2[1][0]), p11 = pk4(ac2[1][1]);
                *(u4*)(xch + 3072 + ps * 2048) = (u4){p00[0], p00[1], p01[0], p01[1]};
                *(u4*)(xch + 4096 + ps * 2048) = (u4){p10[0], p10[1], p11[0], p11[1]};
                lds_set(hw + tg, 2u + ps);
                if (ps == 0) ST(21);
            }
            ST(14);
            lds_wait(hw + 6, 4u);
#pragma unroll
            for (int ks = 0; ks < 4; ++ks) w2_glds(pw, ks, lds0 + tg * 16384 + ks * 4096);
#pragma unroll
            for (int ks = 3; ks < 7; ++ks) w2_glds(pw ^ 4, ks, lds0 + 65536 + tg * 16384 + (ks - 3) * 4096);
            WAITV(16);
#pragma unroll
            for (int ks = 0; ks < 4; ++ks) w2_read(W[ks], tg * 16384 + ks * 4096);
            asm volatile("s_waitcnt lgkmcnt(0)" ::: "memory");
#pragma unroll
            for (int ks = 0; ks < 4; ++ks) { PIN(W[ks][0]); PIN(W[ks][1]); PIN(W[ks][2]); PIN(W[ks][3]); }
#pragma unroll
            for (int ks = 4; ks < 7; ++ks) w2_glds(pw, ks, lds0 + tg * 16384 + (ks - 4) * 4096);
            WAITV(12);
            lds_set(hw + 8 + tg, tag);
            WAITV(0);
#pragma unroll
            for (int ks = 4; ks < 7; ++ks) w2_read(W[ks], tg * 16384 + (ks - 4) * 4096);
        } else {
            bg(BG8, 28);
            WAITV(0);
            if (l == 0) __hip_atomic_fetch_add(hw + 5, 1u, __ATOMIC_RELAXED, __HIP_MEMORY_SCOPE_WORKGROUP);
            __builtin_amdgcn_s_sleep(W2_SLEEP);
            lds_wait(hw + 5, 8u);
            auto stage_next = [&](int ks) {
                w2_read(W[ks - 1], LDS_SCR + tg * 4096);
                asm volatile("s_waitcnt lgkmcnt(0)" ::: "memory");
                PIN(W[ks - 1][0]); PIN(W[ks - 1][1]); PIN(W[ks - 1][2]); PIN(W[ks - 1][3]);
                if (ks < 3) w2_glds(pw, ks, lds0 + LDS_SCR + tg * 4096);
            };
            w2_glds(pw, 0, lds0 + LDS_SCR + tg * 4096);
            WAITV(0);
            stage_next(1);
            ST(12);
            lds_wait(hw + tg, 1u);
            __builtin_amdgcn_s_setprio(SILU_PRIO);
            ST(13);
#pragma unroll
            for (int tt = 0; tt < 2; ++tt) bst4<SC1>(silu_pk4(*(const u4*)(xch + tt * 1024)), rw, l * 16, hoff + tt * (56 * 1024) + (nt1 & 1) * 3072);
            {
                u4 o = silu_pk4(*(const u4*)(xch + 2048));
                bst2<SC1>((u2){o[0], o[1]}, rw, l * 8, hoff + 6144 + (nt1 & 1) * 512);
                bst2<SC1>((u2){o[2], o[3]}, rw, l * 8, hoff + 56 * 1024 + 6144 + (nt1 & 1) * 512);
            }
            WAITV(4);
            stage_next(2);
            ST(20);
#pragma unroll
            for (int ps = 0; ps < 2; ++ps) {
                lds_wait(hw + tg, 2u + ps);
                if (ps == 0) ST(21);
#pragma unroll
                for (int tt = 0; tt < 2; ++tt)
                    bst4<SC1>(silu_pk4(*(const u4*)(xch + 3072 + ps * 2048 + tt * 1024)), rw, l * 16, hoff + tt * (56 * 1024) + ((nt1 & 1) * 3 + 1 + ps) * 1024);
                if (ps == 0) { WAITV(2); stage_next(3); }
            }
            if (l == 0) __hip_atomic_fetch_add(hw + 6, 1u, __ATOMIC_RELAXED, __HIP_MEMORY_SCOPE_WORKGROUP);
            ST(14);
            __builtin_amdgcn_s_setprio(0);
            WAITV(0);
            publish();
#pragma nounroll
            for (int sp = 0; sp < SPIN_MAX; ++sp) {
                if (__builtin_amdgcn_readfirstlane(__hip_atomic_load(hw + 8 + tg, __ATOMIC_RELAXED, __HIP_MEMORY_SCOPE_WORKGROUP)) == tag) break;
                __builtin_amdgcn_s_sleep(1);
            }
            FENCE();
#pragma unroll
            for (int ks = 3; ks < 7; ++ks) w2_read(W[ks], 65536 + tg * 16384 + (ks - 3) * 4096);
        }
    }
    {
        ST(15);
        flag_wait(fl + (grp * 8 + pw) * 32 + (l & 1), tag);
        ST(16);
        const int rot = w & 3;
        const int toff = WS_H1 + (grp * 8 + th * 4) * (56 * 1024) + pw * 7168;
        int to[4];
#pragma unroll
        for (int tt = 0; tt < 4; ++tt) to[tt] = toff + ((tt + rot) & 3) * (56 * 1024);
        u4 T[7][4];
        auto ldtok = [&](int ks) {
#pragma unroll
            for (int tt = 0; tt < 4; ++tt) {
                if (ks < 6) T[ks][tt] = bld4<SC1>(rw, l * 16, to[tt] + ko[ks] * 1024);
                else {
                    u2 a = bld2<SC1>(rw, l * 8 + 2048, to[tt] + 4096), c = bld2<SC1>(rw, l * 8 + 2560, to[tt] + 4096);
                    T[ks][tt] = (u4){a[0], a[1], c[0], c[1]};
                }
            }
            SCHED();
        };
#pragma unroll
        for (int ks = 0; ks < G2_DEPTH; ++ks) ldtok(ks);
        const int rho = grp * 128 + th * 64 + rot * 16 + m;
        const int ooff = (256 * (rho & 7) + (rho >> 3)) * 1024 + fb * 128 + g * 16;
        u4 hres[2];
        f4 acc[4][4];
#pragma unroll
        for (int tt = 0; tt < 4; ++tt)
#pragma unroll
            for (int t = 0; t < 4; ++t) acc[tt][t] = Z4;
#pragma unroll
        for (int ks = 0; ks < 7; ++ks) {
            if (ks + G2_DEPTH < 7) { PIN(T[ks][0]); ldtok(ks + G2_DEPTH); }
#pragma unroll
            for (int tt = 0; tt < 4; ++tt)
#pragma unroll
                for (int t = 0; t < 4; ++t) acc[tt][t] = mma_b(W[ks][t], T[ks][tt], acc[tt][t]);
            SCHED();
        }
        if (!aw) {
            flag_wait(fl + 4096 + grp * 16 + th * 8 + rot * 2 + (l & 1), tag);
            hres[0] = bld4<SC1>(ro, ooff, 0); hres[1] = bld4<SC1>(ro, ooff + 64, 0);
        }
        ST(17);
#pragma unroll
        for (int tt = 0; tt < 4; ++tt)
            if (aw || tt > 0)
#pragma unroll
                for (int t = 0; t < 4; ++t) *(f4*)(smem + (w * 16 + ((tt + rot) & 3) * 4 + t) * 1024 + l * 16) = acc[tt][t];
        BARRIER();
        if (!aw) {
#pragma unroll
            for (int j = 1; j < 8; ++j)
#pragma unroll
                for (int t = 0; t < 4; ++t) acc[0][t] += *(const f4*)(smem + (((w + j) & 7) * 16 + rot * 4 + t) * 1024 + l * 16);
#pragma unroll
            for (int p = 0; p < 2; ++p) {
                u2 s0 = pk4(acc[0][2 * p]), s1 = pk4(acc[0][2 * p + 1]);
                u4 o = {pk_b2(bf_2(hres[p][0]) + bf_2(s0[0])), pk_b2(bf_2(hres[p][1]) + bf_2(s0[1])),
                        pk_b2(bf_2(hres[p][2]) + bf_2(s1[0])), pk_b2(bf_2(hres[p][3]) + bf_2(s1[1]))};
                bst4<AUX_OUT>(o, ro, ooff + 64 * p, 0);
            }
        }
    }
    ST_END(ws, 0);
}

extern "C" void kernel_launch(void* const* d_in, const int* in_sizes, int n_in, void* d_out, int out_size,
                              void* d_ws, size_t ws_size, hipStream_t stream) {
    (void)hipFuncSetAttribute((const void*)k_fused, hipFuncAttributeMaxDynamicSharedMemorySize, LDS_B);
    k_fused<<<256, 512, LDS_B, stream>>>((const uint16_t*)d_in[0], (const uint16_t*)d_in[1], (const uint16_t*)d_in[2],
                                         (const uint16_t*)d_in[4], (const uint16_t*)d_in[5], (uint16_t*)d_out, (char*)d_ws);
}
```

```cpp
#define KK 0
#include <hip/hip_runtime.h>
#include <hip/hip_bf16.h>
#include <stdint.h>

typedef _Float16 h8 __attribute__((ext_vector_type(8)));
typedef _Float16 h4 __attribute__((ext_vector_type(4)));
typedef _Float16 h2 __attribute__((ext_vector_type(2)));
typedef __bf16 b8 __attribute__((ext_vector_type(8)));
typedef __bf16 b2 __attribute__((ext_vector_type(2)));
typedef short s4 __attribute__((ext_vector_type(4)));
typedef float f4 __attribute__((ext_vector_type(4)));
typedef float f2 __attribute__((ext_vector_type(2)));
typedef unsigned u32;
typedef u32 u4 __attribute__((ext_vector_type(4)));
typedef u32 u2 __attribute__((ext_vector_type(2)));
#define LAS __attribute__((address_space(3)))
#define GAS __attribute__((address_space(1)))
#define DI __device__ __forceinline__
#define FENCE() asm volatile("" ::: "memory")
#define PIN(x) asm volatile("" : "+v"(x))
#define SCHED() __builtin_amdgcn_sched_barrier(0)
#define WAITV(n) asm volatile("s_waitcnt vmcnt(%0)" :: "n"(n) : "memory")
#define BARRIER() do { asm volatile("s_waitcnt lgkmcnt(0)" ::: "memory"); __builtin_amdgcn_s_barrier(); FENCE(); } while (0)
constexpr int SC1 = 16;
constexpr u32 SENT = 0xffffffffu;
constexpr int SPIN_MAX = 1 << 20;

DI u32 pk_h(float a, float b) { f2 v = {a, b}; h2 r = __builtin_convertvector(v, h2); return __builtin_bit_cast(u32, r); }
DI u32 pk_b(float a, float b) { f2 v = {a, b}; b2 r = __builtin_convertvector(v, b2); return __builtin_bit_cast(u32, r); }
DI float bf_lo(u32 v) { return __builtin_bit_cast(float, v << 16); }
DI float bf_hi(u32 v) { return __builtin_bit_cast(float, v & 0xffff0000u); }
DI f2 bf_2(u32 v) { return (f2){bf_lo(v), bf_hi(v)}; }
DI u32 pk_b2(f2 v) { return pk_b(v[0], v[1]); }
DI float rbf(float a) { return bf_lo(pk_b(a, a)); }
DI float cosr(float rev) { return __builtin_amdgcn_cosf(rev); }
DI float sinr(float rev) { return __builtin_amdgcn_sinf(rev); }
DI f2 lo2(f4 v) { return __builtin_shufflevector(v, v, 0, 1); }
DI f2 hi2(f4 v) { return __builtin_shufflevector(v, v, 2, 3); }
DI f2 fma2(f2 a, f2 b, f2 c) { return __builtin_elementwise_fma(a, b, c); }
DI u32 umin(u32 a, u32 b) { return a < b ? a : b; }
DI u32 umax(u32 a, u32 b) { return a > b ? a : b; }
DI u32 clean(u32 v) { return umin(v, SENT - 1); }
DI u32 max4(u4 v) { return umax(umax(umax(v[0], v[1]), v[2]), v[3]); }
#define Z4 ((f4){0, 0, 0, 0})
DI f4 mma_h(u4 a, u4 b, f4 c) { return __builtin_amdgcn_mfma_f32_16x16x32_f16(__builtin_bit_cast(h8, a), __builtin_bit_cast(h8, b), c, 0, 0, 0); }
DI f4 mma_b(u4 a, u4 b, f4 c) { return __builtin_amdgcn_mfma_f32_16x16x32_bf16(__builtin_bit_cast(b8, a), __builtin_bit_cast(b8, b), c, 0, 0, 0); }
DI f4 mma16_h(u2 a, u2 b, f4 c) { return __builtin_amdgcn_mfma_f32_16x16x16f16(__builtin_bit_cast(h4, a), __builtin_bit_cast(h4, b), c, 0, 0, 0); }
typedef __amdgpu_buffer_rsrc_t rsrc_t;
DI rsrc_t mkrs(const void* p, u32 bytes) { return __builtin_amdgcn_make_buffer_rsrc((void*)p, 0, bytes, 0x00020000); }
template <int AUX> DI u4 bld4(rsrc_t r, int v, int s) { return __builtin_amdgcn_raw_buffer_load_b128(r, v, s, AUX); }
template <int AUX> DI u2 bld2(rsrc_t r, int v, int s) { return __builtin_amdgcn_raw_buffer_load_b64(r, v, s, AUX); }
template <int AUX> DI void bst4(u4 d, rsrc_t r, int v, int s) {
    __builtin_amdgcn_raw_buffer_store_b128(d, r, v, s, AUX);
    asm volatile("s_nop 1" :: "v"(d));
}
template <int AUX> DI void bst2(u2 d, rsrc_t r, int v, int s) { __builtin_amdgcn_raw_buffer_store_b64(d, r, v, s, AUX); }

template <int CTRL> DI float dpp(float v) {
    return __builtin_bit_cast(float, __builtin_amdgcn_update_dpp(0, __builtin_bit_cast(int, v), CTRL, 0xf, 0xf, true));
}
template <bool ALL> DI float red_lanes(float v) {
    v += dpp<0xB1>(v);
    if (ALL) v += dpp<0x4E>(v);
    v += dpp<0x124>(v); v += dpp<0x128>(v);
    u32 x = __builtin_bit_cast(u32, v), y = x;
    asm("" : "+v"(y));
    auto r = __builtin_amdgcn_permlane16_swap(x, y, false, false);
    v = __builtin_bit_cast(float, (u32)r[0]) + __builtin_bit_cast(float, (u32)r[1]);
    x = __builtin_bit_cast(u32, v); y = x;
    asm("" : "+v"(y));
    auto q = __builtin_amdgcn_permlane32_swap(x, y, false, false);
    return __builtin_bit_cast(float, (u32)q[0]) + __builtin_bit_cast(float, (u32)q[1]);
}
DI float ln_inv(float var) { return __builtin_amdgcn_rcpf(rbf(__builtin_amdgcn_sqrtf(rbf(rbf(var) + 1e-5f)))); }

#define ST_DECL
#define ST(i)
#define ST_END(ws, kid)

constexpr u32 WS_U = 0;
constexpr u32 WS_WT = WS_U + (2u << 20);
constexpr u32 WS_V = WS_WT + (2u << 20);
constexpr u32 WS_LN2 = WS_V + (2u << 20);
constexpr u32 WS_WA = WS_LN2 + (2u << 20);
constexpr u32 WS_WB = WS_WA + 8192u;
constexpr u32 WS_FA = WS_WB + 8192u;
constexpr u32 WS_FLAG = WS_FA + 16384u;
constexpr u32 WS_EP = WS_FLAG + 20480u;
constexpr u32 WS_H1 = WS_WA + (64u << 10);
constexpr u32 WS_END = WS_H1 + 7340032u;

#define W1_B0 0
#define W1_B1 7
#define W1_B2 5
#define W1_B3 6
#define W1_CHK ((((W1_B0 + W1_B1 + W1_B2 + W1_B3) / 7) * 4 - 2) < 0 ? 0 : (((W1_B0 + W1_B1 + W1_B2 + W1_B3) / 7) * 4 - 2))
#define CLR_SLEEP 6
#define BPRIO 2
#define EXP_UCLR 0
#define POLL_SLEEP 2
#define BG1 6
#define BG2 6
#define BG3 6
#define BG4 12
#define BG5 17
#define BG6 17
#define BG7 17
#define BG8 23
#define A_EARLY 24
#define SILU_PRIO 0
#define EXP_HOFF 0
#define B_EARLY 8
#define W2_DELAY 4
#define W2_SLEEP 8
#define AUX_OUT 2
#define W2_OUT 2
#define W2_ATHR 3
#define W2_EARLY 3
#define G1_DEPTH 8
#define G2_DEPTH 2
constexpr int LDS_SCR = 114688;
constexpr int SCR_X = 8448;
constexpr int SCR_S = SCR_X + 20480;
constexpr int SCR_F = 49024;
constexpr int XCH = 131072;
constexpr int LDS_B = 163840;

DI void glds16(const void* sbase, u32 voff, u32 lds_dst) {
    asm volatile("s_mov_b32 m0, %2\n\ts_nop 0\n\tglobal_load_lds_dwordx4 %0, %1" :: "v"(voff), "s"(sbase), "s"(lds_dst) : "memory");
}
DI void glds16x4(const void* sbase, u32 voff, u32 lds_dst) {
    asm volatile("s_mov_b32 m0, %2\n\ts_nop 0\n\tglobal_load_lds_dwordx4 %0, %1\n\tglobal_load_lds_dwordx4 %0, %1 offset:1024\n\t"
                 "global_load_lds_dwordx4 %0, %1 offset:2048\n\tglobal_load_lds_dwordx4 %0, %1 offset:3072"
                 :: "v"(voff), "s"(sbase), "s"(lds_dst) : "memory");
}
DI bool any_sent(u32 v) { return __builtin_amdgcn_ballot_w64(v == SENT) != 0; }
DI void wait_tok(u4& t0, u4& t1, rsrc_t rw, int vo, int so) {
    if (any_sent(umax(max4(t0), max4(t1)))) {
#pragma nounroll
        for (int sp = 0; sp < SPIN_MAX; ++sp) {
            FENCE();
            t0 = bld4<SC1>(rw, vo, so); t1 = bld4<SC1>(rw, vo, so + 16384);
            if (!any_sent(umax(max4(t0), max4(t1)))) break;
        }
    }
}
DI u4 ldw1(LAS char* wb, int kk, int ft) {
    s4 a0 = __builtin_amdgcn_ds_read_tr16_b64_v4i16((LAS s4*)(wb + kk * 32 * 224 + ft * 32));
    s4 a1 = __builtin_amdgcn_ds_read_tr16_b64_v4i16((LAS s4*)(wb + (kk * 32 + 16) * 224 + ft * 32));
    u2 x0 = __builtin_bit_cast(u2, a0), x1 = __builtin_bit_cast(u2, a1);
    return (u4){x0[0], x0[1], x1[0], x1[1]};
}
DI u2 pk4(f4 a) { return (u2){pk_b(a[0], a[1]), pk_b(a[2], a[3])}; }
DI u2 silu_pk2(u32 p0, u32 p1) {
    u32 o0, o1; float a0, a1, a2, a3, t0, t1, t2, t3;
    asm("v_lshlrev_b32 %2, 16, %10\n\tv_and_b32 %3, 0xffff0000, %10\n\tv_lshlrev_b32 %4, 16, %11\n\tv_and_b32 %5, 0xffff0000, %11\n\t"
        "v_mul_f32 %6, %12, %2\n\tv_mul_f32 %7, %12, %3\n\tv_mul_f32 %8, %12, %4\n\tv_mul_f32 %9, %12, %5\n\t"
        "v_exp_f32 %6, %6\n\tv_exp_f32 %7, %7\n\tv_exp_f32 %8, %8\n\tv_exp_f32 %9, %9\n\t"
        "v_add_f32 %6, 1.0, %6\n\tv_add_f32 %7, 1.0, %7\n\tv_add_f32 %8, 1.0, %8\n\tv_add_f32 %9, 1.0, %9\n\t"
        "v_rcp_f32 %6, %6\n\tv_rcp_f32 %7, %7\n\tv_rcp_f32 %8, %8\n\tv_rcp_f32 %9, %9\n\t"
        "v_mul_f32 %2, %2, %6\n\tv_mul_f32 %3, %3, %7\n\tv_mul_f32 %4, %4, %8\n\tv_mul_f32 %5, %5, %9\n\t"
        "v_cvt_pk_bf16_f32 %0, %2, %3\n\tv_cvt_pk_bf16_f32 %1, %4, %5"
        : "=&v"(o0), "=&v"(o1), "=&v"(a0), "=&v"(a1), "=&v"(a2), "=&v"(a3), "=&v"(t0), "=&v"(t1), "=&v"(t2), "=&v"(t3)
        : "v"(p0), "v"(p1), "s"(-1.4426950408889634f));
    return (u2){o0, o1};
}
DI u4 silu_pk4(u4 p) { u2 a = silu_pk2(p[0], p[1]), b = silu_pk2(p[2], p[3]); return (u4){a[0], a[1], b[0], b[1]}; }
DI void lds_set(LAS u32* p, u32 v) {
    asm volatile("s_waitcnt lgkmcnt(0)" ::: "memory");
    __hip_atomic_store(p, v, __ATOMIC_RELAXED, __HIP_MEMORY_SCOPE_WORKGROUP);
}
DI void lds_wait(LAS u32* p, u32 v) {
#pragma nounroll
    for (int sp = 0; sp < SPIN_MAX; ++sp) {
        if (__builtin_amdgcn_readfirstlane(__hip_atomic_load(p, __ATOMIC_RELAXED, __HIP_MEMORY_SCOPE_WORKGROUP)) >= v) break;
        __builtin_amdgcn_s_sleep(1);
    }
    FENCE();
}
DI void flag_wait(GAS u32* p, u32 tag) {
#pragma nounroll
    for (int sp = 0; sp < SPIN_MAX; ++sp) {
        if (__builtin_amdgcn_ballot_w64(__hip_atomic_load(p, __ATOMIC_RELAXED, __HIP_MEMORY_SCOPE_AGENT) != tag) == 0) break;
        __builtin_amdgcn_s_sleep(2);
    }
    FENCE();
}
#define SENT4 ((u4){SENT, SENT, SENT, SENT})

__global__ __launch_bounds__(512, 2) void k_fused(const uint16_t* x, const uint16_t* wr, const uint16_t* wi, const uint16_t* w1,
                                                  const uint16_t* w2, uint16_t* out, char* ws) {
    extern __shared__ __attribute__((aligned(16))) char smem[];
    const int tid = threadIdx.x, l = tid & 63, g = l >> 4, m = l & 15;
    const int w = __builtin_amdgcn_readfirstlane(tid >> 6);
    const int b = (blockIdx.x & 7) * 32 + (blockIdx.x >> 3);
    const int mt1 = (b >> 6) * 4 + ((b >> 3) & 3), nt1 = ((b >> 5) & 1) * 8 + (b & 7);
    const int grp = b >> 4, th = (b >> 3) & 1, fb = b & 7;
    const rsrc_t rw = mkrs(ws, WS_END), ro = mkrs(out, 2097152);
    char* scr = smem + LDS_SCR;
    LAS u32* hw = (LAS u32*)((LAS char*)smem + LDS_SCR + SCR_F);
    const u32 lds0 = (u32)(uintptr_t)(LAS char*)smem;
    const float mf = (float)m, gf = (float)g, sg = (m & 1) ? -1.0f : 1.0f;
    const bool aw = w < 4;
    ST_DECL;
    const int rq = ((b >> 3) + b) & 3;
    const int row0 = (tid & 255) / 14, cch0 = (tid & 255) - row0 * 14;
    const char* wsrc = (const char*)w1 + nt1 * 224;
    u32 w1o[7];
    auto w1_burst = [&](int i0, int n) {
#pragma unroll
        for (int i = i0; i < i0 + n; ++i) {
            const int q = (i / 7 + rq) & 3, j = i % 7;
            glds16(wsrc + q * (128 * 3584), w1o[j], lds0 + (w - 4) * 1024 + q * 28672 + j * 4096);
        }
    };
    const u32 epoch = __hip_atomic_load((GAS u32*)(ws + WS_EP), __ATOMIC_RELAXED, __HIP_MEMORY_SCOPE_AGENT);
    FENCE();
    const int xoff = (256 * w + b) * 1024 + m * 32 + g * 8;
    u2 xl[2];
    float c16[4], s16[4];
    f2 twc[2], tws[2];
    u4 Gre3, Gim3, SGre, SGim, GA[2];
    auto consts0 = [&]() {
#pragma unroll
        for (int u = 0; u < 4; ++u) {
            float a = (4.0f * gf + u) * (mf * (1.0f / 16));
            c16[u] = cosr(a); s16[u] = sinr(a);
            twc[u >> 1][u & 1] = cosr(a * (1.0f / 32)); tws[u >> 1][u & 1] = sinr(a * (1.0f / 32));
        }
    };
    auto consts3 = [&]() {
#pragma unroll
        for (int u = 0; u < 4; ++u) {
            const float k = 4.0f * gf + u;
            const bool k0 = u == 0 && g == 0;
            float cc = c16[u], ss = s16[u];
            Gre3[u] = pk_b(cc, -ss); Gim3[u] = pk_b(ss, cc);
            float a2 = (2.0f * k + 1.0f) * (mf * (1.0f / 32));
            u32 sp = pk_b(g < 2 ? 2.f * cc : 2.f * sg * cosr(a2), g < 2 ? -2.f * ss : -2.f * sg * sinr(a2));
            SGre[u] = k0 ? pk_b(1.0f, sg) : (g < 2 ? sp : 0u);
            SGim[u] = g < 2 ? 0u : sp;
            float a4 = k * (mf * (1.0f / 32)), c4 = 2.f * cosr(a4), s4_ = -2.f * sinr(a4);
            GA[0][u] = k0 ? pk_b(1.0f, sg) : pk_b(c4, s4_);
            GA[1][u] = k0 ? pk_b(1.0f, sg) : ((u & 1) ? pk_b(-c4, -s4_) : pk_b(c4, s4_));
        }
        PIN(Gre3); PIN(Gim3); PIN(SGre); PIN(SGim); PIN(GA[0]); PIN(GA[1]); PIN(twc[0]); PIN(twc[1]); PIN(tws[0]); PIN(tws[1]);
    };
    const int pw = (w + 4 * th) & 7;
    GAS u32* fl = (GAS u32*)(ws + WS_FLAG);
    int ko[7];
    {
        const int r6 = (fb < 6 ? fb : fb - 5) + 3 * (grp & 1);
#pragma unroll
        for (int ks = 0; ks < 6; ++ks) { int k = ks + r6; k = k >= 6 ? k - 6 : k; ko[ks] = k >= 6 ? k - 6 : k; }
        ko[6] = 6;
    }
    u4 W[7][4];
    const u32 w2vo = (l >> 3) * 1024 + (((l & 7) ^ (((l >> 4) & 1) << 2)) * 16);
    auto w2_glds = [&](int p_, int ks, u32 dst) {
        const int q = ko[ks];
        const char* src = (const char*)w2 + (224 * p_ + (ks == 6 ? 96 : (q < 3 ? 32 * q : 16 + 32 * q))) * 1024 + fb * 128;
        glds16(src, w2vo, dst); glds16(src + 8192, w2vo, dst + 1024);
        glds16(src + (ks == 6 ? 112 : 16) * 1024, w2vo, dst + 2048); glds16(src + (ks == 6 ? 120 : 24) * 1024, w2vo, dst + 3072);
    };
    LAS char* w2rd = (LAS char*)smem + (4 * g + (m >> 2)) * 128 + (((m & 3) ^ (((m >> 3) & 1) << 2)) * 16);
    auto w2_read = [&](u4 (&f)[4], int off) {
#pragma unroll
        for (int t = 0; t < 4; ++t) {
            LAS char* p = (LAS char*)((u32)(uintptr_t)w2rd ^ ((t >> 1) * 64)) + off + (t & 1) * 8;
            u2 x0 = __builtin_bit_cast(u2, __builtin_amdgcn_ds_read_tr16_b64_v4i16((LAS s4*)p));
            u2 x1 = __builtin_bit_cast(u2, __builtin_amdgcn_ds_read_tr16_b64_v4i16((LAS s4*)(p + 2048)));
            f[t] = (u4){x0[0], x0[1], x1[0], x1[1]};
        }
    };
    auto bg = [&](int i0, int i1) { w1_burst(i0, i1 - i0); };
    u32 tag;
    if (aw) {
        u32 xd[8];
        const u32* xr = (const u32*)(x + (256 * (2 * w + ((m >> 1) & 1)) + b) * 512) + 8 * g + 2 * (m >> 2) + (m & 1);
#pragma unroll
        for (int j = 0; j < 8; ++j) xd[j] = xr[32 * j];
        if (tid < 8) hw[tid] = 0;
        FENCE();
        ST(2);
        {
            u4 FAre, FAim, Fre, Fim, Sre, Sim; u2 Mb; f2 pc[2], ps[2];
            consts0();
            float c0[8], s0[8];
#pragma unroll
            for (int j = 0; j < 8; ++j) {
                float a = (gf + 4.0f * j) * (mf * (1.0f / 32));
                c0[j] = cosr(a); s0[j] = m ? -sinr(a) : ((g & 1) ? -1.0f : 1.0f);
            }
            const float mo = (float)(2 * (m & 7) + 1) * (1.0f / 32);
#pragma unroll
            for (int u = 0; u < 4; ++u) {
                FAre[u] = pk_h(c0[2 * u], c0[2 * u + 1]); FAim[u] = pk_h(s0[2 * u], s0[2 * u + 1]);
                float cc = c16[u], ss = s16[u];
                Fre[u] = pk_h(cc, ss); Fim[u] = pk_h(-ss, cc);
                float a2 = (4.0f * gf + u) * mo;
                u32 re = pk_h(m < 8 ? cc : cosr(a2), 0.f), im = pk_h(m < 8 ? (m ? -ss : ((u & 1) ? -1.0f : 1.0f)) : -sinr(a2), 0.f);
                Sre[u] = m < 8 ? re : re << 16; Sim[u] = m < 8 ? im : im << 16;
                pc[u & 1][u >> 1] = twc[u >> 1][u & 1]; ps[u & 1][u >> 1] = tws[u >> 1][u & 1];
            }
#pragma unroll
            for (int e = 0; e < 2; ++e) {
                float a = (float)(((m >> 1) * (256 * (2 * g + e) + b)) & 2047) * (1.0f / 2048);
                float cc = cosr(a), ss = sinr(a);
                Mb[e] = (m & 1) ? pk_h(-ss, cc) : pk_h(cc, ss);
            }
            PIN(FAre); PIN(FAim); PIN(Fre); PIN(Fim); PIN(Sre); PIN(Sim); PIN(Mb); PIN(pc[0]); PIN(pc[1]); PIN(ps[0]); PIN(ps[1]);
            ST(3);
            f2 v[8];
#pragma unroll
            for (int j = 0; j < 8; ++j) v[j] = bf_2(xd[j]);
            f2 s2 = ((v[0] + v[1]) + (v[2] + v[3])) + ((v[4] + v[5]) + (v[6] + v[7]));
            const float mean = red_lanes<false>(s2[0] + s2[1]) * (1.0f / 512);
            f2 q0 = {0.f, 0.f}, q1 = {0.f, 0.f};
#pragma unroll
            for (int j = 0; j < 8; ++j) { v[j] -= mean; if (j & 1) q1 = fma2(v[j], v[j], q1); else q0 = fma2(v[j], v[j], q0); }
            q0 += q1;
            const float inv = ln_inv(red_lanes<false>(q0[0] + q0[1]) * (1.0f / 512));
            const float off = (mean - rbf(mean)) * inv;
#pragma unroll
            for (int j = 0; j < 8; ++j) v[j] = fma2(v[j], (f2){inv, inv}, (f2){off, off});
            u4 B[2];
#pragma unroll
            for (int p = 0; p < 2; ++p) {
                u4 A = {pk_h(v[0][p], v[1][p]), pk_h(v[2][p], v[3][p]), pk_h(v[4][p], v[5][p]), pk_h(v[6][p], v[7][p])};
                f4 Dre = mma_h(A, FAre, Z4), Dim = mma_h(A, FAim, Z4);
#pragma unroll
                for (int r = 0; r < 2; ++r) {
                    f2 re = r ? hi2(Dre) : lo2(Dre), im = r ? hi2(Dim) : lo2(Dim);
                    f2 tr = fma2(im, ps[p], re * pc[p]), ti = fma2(-re, ps[p], im * pc[p]);
                    B[r][p] = pk_h(tr[0], ti[0]); B[r][2 + p] = pk_h(tr[1], ti[1]);
                }
            }
#pragma unroll
            for (int r = 0; r < 2; ++r) {
                f4 Gre = mma_h(Fre, B[r], Z4), Gim = mma_h(Fim, B[r], Z4), Hre = mma_h(Sre, B[r], Z4), Him = mma_h(Sim, B[r], Z4);
                u4 Z;
#pragma unroll
                for (int i = 0; i < 4; ++i) Z[i] = pk_h(m ? Gre[i] : Hre[i], m ? Gim[i] : Him[i]);
                *(u4*)(scr + (2 * w + r) * 1024 + m * 64 + g * 16) = Z;
            }
            BARRIER();
            ST(18);
            u4 r0 = *(const u4*)(scr + (2 * g) * 1024 + w * 256 + m * 16);
            u4 r1 = *(const u4*)(scr + (2 * g + 1) * 1024 + w * 256 + m * 16);
#pragma unroll
            for (int t = 0; t < 4; ++t) {
                f4 D = mma16_h(Mb, (u2){r0[t], r1[t]}, Z4);
                *(u2*)(scr + SCR_S + w * 2048 + m * 128 + (((2 * t + (g >> 1)) ^ (m >> 1)) * 16) + (g & 1) * 8) =
                    (u2){clean(pk_h(D[0], D[1])), clean(pk_h(D[2], D[3]))};
            }
        }
        u4 uo0 = *(const u4*)(scr + SCR_S + w * 2048 + (l ^ ((l >> 4) & 7)) * 16);
        u4 uo1 = *(const u4*)(scr + SCR_S + w * 2048 + 1024 + (l ^ (((l >> 4) + 4) & 7)) * 16);
        u4 Fre, Fim, Gre, Gim, N0, N1; f2 tc[2], ts[2];
#pragma unroll
        for (int u = 0; u < 4; ++u) {
            float cc = c16[u], ss = s16[u];
            Fre[u] = pk_h(0.25f * cc, 0.25f * ss); Fim[u] = pk_h(-0.25f * ss, 0.25f * cc);
            Gre[u] = pk_b(cc * (1.0f / 256), -ss * (1.0f / 256)); Gim[u] = pk_b(ss * (1.0f / 256), cc * (1.0f / 256));
            float a3 = (4.0f * gf + u) * (mf * (1.0f / 256));
            tc[u >> 1][u & 1] = cosr(a3); ts[u >> 1][u & 1] = sinr(a3);
        }
        PIN(Fre); PIN(Fim); PIN(Gre); PIN(Gim); PIN(tc[0]); PIN(tc[1]); PIN(ts[0]); PIN(ts[1]);
        ST(19);
        BARRIER();
        ST(4);
        bst4<SC1>(uo0, rw, l * 16, WS_U + b * 8192 + w * 2048);
        bst4<SC1>(uo1, rw, l * 16 + 1024, WS_U + b * 8192 + w * 2048);
        xl[0] = *(const u2*)((const char*)x + xoff); xl[1] = *(const u2*)((const char*)x + xoff + 512);
        FENCE();
        tag = __builtin_amdgcn_readfirstlane(epoch) + 1u;
        consts3();
#pragma unroll
        for (int u = 0; u < 4; ++u) {
            float a1 = (float)(((4 * (g & 1) + u) * (256 * (m >> 1) + b)) & 2047) * (1.0f / 2048);
            float c1 = cosr(a1), s1 = sinr(a1);
            u32 nb = (m & 1) ? pk_b(s1, c1) : pk_b(c1, -s1);
            N0[u] = g < 2 ? nb : 0u; N1[u] = g < 2 ? 0u : nb;
        }
        PIN(N0); PIN(N1);
        WAITV(0);
        BARRIER();
        ST(5);
        u4 d[2], f[2], e[2];
        const int uo = (tid >> 1) * 8192 + (tid & 1) * 16;
        d[0] = bld4<SC1>(rw, uo, WS_U + b * 32); d[1] = bld4<SC1>(rw, uo, WS_U + 128 * 8192 + b * 32);
        f[0] = bld4<SC1>(rw, uo, WS_WT + b * 32); f[1] = bld4<SC1>(rw, uo, WS_WT + 128 * 8192 + b * 32);
        asm volatile("" :: "v"(f[0]), "v"(f[1]));
        if (any_sent(umax(max4(d[0]), max4(d[1])))) {
#pragma nounroll
            for (int sp = 0; sp < SPIN_MAX; ++sp) {
                __builtin_amdgcn_s_sleep(POLL_SLEEP);
                FENCE();
                d[0] = bld4<SC1>(rw, uo, WS_U + b * 32); d[1] = bld4<SC1>(rw, uo, WS_U + 128 * 8192 + b * 32);
                if (!any_sent(umax(max4(d[0]), max4(d[1])))) break;
            }
        }
        FENCE();
        f[0] = bld4<SC1>(rw, uo, WS_WT + b * 32); f[1] = bld4<SC1>(rw, uo, WS_WT + 128 * 8192 + b * 32);
        char* sp = scr + SCR_S + (tid ^ ((tid >> 4) & 1)) * 16;
        *(u4*)sp = d[0]; *(u4*)(sp + 4096) = d[1];
        BARRIER();
        if (b == 0) {
            f[0] = bld4<SC1>(rw, tid * 16, WS_WA); f[1] = bld4<SC1>(rw, tid * 16 + 4096, WS_WA);
            e[0] = bld4<SC1>(rw, tid * 16, WS_WB); e[1] = bld4<SC1>(rw, tid * 16 + 4096, WS_WB);
            if (tid == 0) __hip_atomic_store((GAS u32*)(ws + WS_EP), tag, __ATOMIC_RELAXED, __HIP_MEMORY_SCOPE_AGENT);
        }
        ST(6);
        u2 ld[4], wl[4];
        const int so = (64 * g + m) * 32 + (((w >> 1) ^ (m >> 3)) * 16) + (w & 1) * 8;
#pragma unroll
        for (int q = 0; q < 4; ++q) ld[q] = *(const u2*)(scr + SCR_S + so + 512 * q);
        FENCE();
        f4 Xre[2], Xim[2];
#pragma unroll
        for (int e = 0; e < 2; ++e) {
            u4 A = {ld[0][e], ld[1][e], ld[2][e], ld[3][e]};
            f4 Dre = mma_h(A, Fre, Z4), Dim = mma_h(A, Fim, Z4);
            f2 r0 = fma2(lo2(Dim), ts[0], lo2(Dre) * tc[0]), i0 = fma2(-lo2(Dre), ts[0], lo2(Dim) * tc[0]);
            f2 r1 = fma2(hi2(Dim), ts[1], hi2(Dre) * tc[1]), i1 = fma2(-hi2(Dre), ts[1], hi2(Dim) * tc[1]);
            u4 B = {pk_h(r0[0], i0[0]), pk_h(r0[1], i0[1]), pk_h(r1[0], i1[0]), pk_h(r1[1], i1[1])};
            Xre[e] = mma_h(Fre, B, Z4); Xim[e] = mma_h(Fim, B, Z4);
        }
        *(u4*)(sp + 8192) = f[0]; *(u4*)(sp + 12288) = f[1];
        f2* sx = (f2*)(scr + SCR_X);
        if (b == 0) {
            *(u4*)(sp - SCR_S) = e[0]; *(u4*)(sp - SCR_S + 4096) = e[1];
#pragma unroll
            for (int e = 0; e < 2; ++e)
#pragma unroll
                for (int i = 0; i < 4; ++i) sx[2 * w + e + 8 * (m + 16 * (4 * g + i))] = (f2){Xre[e][i], Xim[e][i]};
        }
        BARRIER();
        ST(20);
#pragma unroll
        for (int q = 0; q < 4; ++q) wl[q] = *(const u2*)(scr + SCR_S + so + 8192 + 512 * q);
        FENCE();
        f2 yr[2][2], yi[2][2];
#pragma unroll
        for (int e = 0; e < 2; ++e)
#pragma unroll
            for (int hh = 0; hh < 2; ++hh) {
                f2 wre = {bf_lo(wl[2 * hh][e]), bf_lo(wl[2 * hh + 1][e])}, wim = {bf_hi(wl[2 * hh][e]), bf_hi(wl[2 * hh + 1][e])};
                f2 xr = hh ? hi2(Xre[e]) : lo2(Xre[e]), xi = hh ? hi2(Xim[e]) : lo2(Xim[e]);
                yr[e][hh] = fma2(-xi, wim, xr * wre); yi[e][hh] = fma2(xr, wim, xi * wre);
            }
        if (b == 0) {
#pragma unroll
            for (int i = 0; i < 4; ++i) {
                u2 wb = *(const u2*)(scr + so + 512 * i);
#pragma unroll
                for (int e = 0; e < 2; ++e) {
                    f2 xm = sx[(2048 - (2 * w + e + 8 * (m + 16 * (4 * g + i)))) & 2047];
                    float br = bf_lo(wb[e]), bi = bf_hi(wb[e]);
                    yr[e][i >> 1][i & 1] += xm[0] * br + xm[1] * bi;
                    yi[e][i >> 1][i & 1] += xm[0] * bi - xm[1] * br;
                }
            }
        }
        char* pv = scr + SCR_S;
#pragma unroll
        for (int e = 0; e < 2; ++e) {
            u4 Y = {pk_b(yr[e][0][0], yi[e][0][0]), pk_b(yr[e][0][1], yi[e][0][1]), pk_b(yr[e][1][0], yi[e][1][0]), pk_b(yr[e][1][1], yi[e][1][1])};
            f4 Dre = mma_b(Y, Gre, Z4), Dim = mma_b(Y, Gim, Z4);
            f2 r0 = fma2(-lo2(Dim), ts[0], lo2(Dre) * tc[0]), i0 = fma2(lo2(Dre), ts[0], lo2(Dim) * tc[0]);
            f2 r1 = fma2(-hi2(Dim), ts[1], hi2(Dre) * tc[1]), i1 = fma2(hi2(Dre), ts[1], hi2(Dim) * tc[1]);
            u4 A = {pk_b(r0[0], i0[0]), pk_b(r0[1], i0[1]), pk_b(r1[0], i1[0]), pk_b(r1[1], i1[1])};
            f4 Ere = mma_b(A, Gre, Z4), Eim = mma_b(A, Gim, Z4);
            u4 o;
#pragma unroll
            for (int i = 0; i < 4; ++i) o[i] = clean(pk_b(Ere[i], Eim[i]));
            *(u4*)(pv + (2 * w + e) * 1024 + ((m * 4 + g) ^ ((w >> 1) * 4)) * 16) = o;
        }
        BARRIER();
        ST(7);
#pragma unroll
        for (int it = 0; it < 2; ++it) {
            const int ch = tid + 256 * it;
            const u32* q = (const u32*)(pv + (ch & 1) * 4096 + (((ch >> 1) * 4) ^ ((ch & 1) * 64)));
            bst4<SC1>((u4){q[0], q[256], q[512], q[768]}, rw, ch * 16, WS_V + b * 8192);
        }
        WAITV(0);
        BARRIER();
        ST(8);
        const int vo = (2 * m + (g >> 1)) * 8192 + (g & 1) * 16, vs = WS_V + w * 64 * 8192 + b * 32;
        u4 v0, v1;
#pragma nounroll
        for (int sp = 0; sp < SPIN_MAX; ++sp) {
            FENCE();
            v0 = bld4<SC1>(rw, vo, vs); v1 = bld4<SC1>(rw, vo, vs + 32 * 8192);
            if (!any_sent(umax(max4(v0), max4(v1)))) break;
            __builtin_amdgcn_s_sleep(POLL_SLEEP);
        }
        ST(9);
        char* sz = scr + SCR_X + g * 2048 + w * 256 + m * 8;
#pragma unroll
        for (int hh = 0; hh < 2; ++hh) {
            f4 D0 = mma_b(N0, hh ? v1 : v0, Z4), D1 = mma_b(N1, hh ? v1 : v0, Z4);
            *(u2*)(sz + hh * 128) = (u2){pk_b(D0[0], D0[1]), pk_b(D1[0], D1[1])};
            *(u2*)(sz + 1024 + hh * 128) = (u2){pk_b(D0[2], D0[3]), pk_b(D1[2], D1[3])};
        }
    } else {
        auto announce = [&](int which) {
            if (l < 8) __hip_atomic_store((GAS u32*)(ws + WS_FA) + which * 2048 + l * 256 + b, tag, __ATOMIC_RELAXED, __HIP_MEMORY_SCOPE_AGENT);
            FENCE();
        };
        auto all_announced = [&](int which) {
#pragma nounroll
            for (int sp = 0; sp < SPIN_MAX; ++sp) {
                FENCE();
                u4 f = bld4<SC1>(rw, l * 16, WS_FA + which * 8192 + (blockIdx.x & 7) * 1024);
                if (__builtin_amdgcn_ballot_w64(f[0] != tag || f[1] != tag || f[2] != tag || f[3] != tag) == 0) break;
                __builtin_amdgcn_s_sleep(4);
            }
        };
        const rsrc_t rr = mkrs(wr, 1052672), ri = mkrs(wi, 1052672);
        const int j = 4 * (w - 4) + g, i0_ = 4 * (m & 1);
        const bool mir = w >= 6;
        u32 fr[4], fi[4], sp_[8];
#pragma unroll
        for (int i = 0; i < 4; ++i) {
            const int e = mir ? ((2048 - 8 * b - i0_ - i) & 2047) * 514 + (512 - 32 * j - (m | 1)) * 2 : (8 * b + i0_ + i) * 514 + (32 * j + (m & 14)) * 2;
            fr[i] = __builtin_amdgcn_raw_buffer_load_b32(rr, e, 0, 0); fi[i] = __builtin_amdgcn_raw_buffer_load_b32(ri, e, 0, 0);
        }
        if (mir) {
            const int e = (8 * b + (l & 7)) * 514 + (16 + 32 * (4 * (w - 6) + ((l >> 3) & 3))) * 2;
            sp_[0] = __builtin_amdgcn_raw_buffer_load_b16(rr, e, 0, 0); sp_[1] = __builtin_amdgcn_raw_buffer_load_b16(ri, e, 0, 0);
        } else if (w == 5) {
            const int ed = (8 * b + (l & 7)) * 514, em = ((2048 - 8 * b - (l & 7)) & 2047) * 514;
#pragma unroll
            for (int i = 0; i < 8; ++i) sp_[i] = __builtin_amdgcn_raw_buffer_load_b16((i & 1) ? ri : rr, ((i & 2) ? em : ed) + (i >> 2) * 512, 0, 0);
        }
        FENCE();
#pragma unroll
        for (int i = 0; i < 2; ++i) bst4<SC1>(SENT4, rw, l * 16 + i * 1024, WS_U + b * 8192 + (w - 4) * 2048);
        ST(2);
#pragma unroll
        for (int jj = 0; jj < 7; ++jj) {
            const int cj_ = (4 * jj) % 14, rj = 18 * jj + (4 * jj) / 14;
            w1o[jj] = (row0 + rj) * 3584 + (cch0 + cj_) * 16 + (cch0 + cj_ >= 14 ? 3584 - 224 : 0);
            PIN(w1o[jj]);
        }
        {
            const u32 cj = mir ? 0x80000000u : 0u, se = mir ? 0x07060302u : 0x05040100u, so_ = mir ? 0x05040100u : 0x07060302u;
            u4 s0, s1;
#pragma unroll
            for (int i = 0; i < 4; ++i) { s0[i] = __builtin_amdgcn_perm(fi[i], fr[i], se) ^ cj; s1[i] = __builtin_amdgcn_perm(fi[i], fr[i], so_) ^ cj; }
            ST(3);
            const int wo = (m >> 1) * 1024 + j * 32 + (m & 1) * 16;
            bst4<SC1>(s0, rw, wo, WS_WT + b * 8192); bst4<SC1>(s1, rw, wo + 512, WS_WT + b * 8192);
            if (mir) {
                if (l < 32) __builtin_amdgcn_raw_buffer_store_b32(sp_[0] | (sp_[1] << 16), rw, l * 4, WS_WT + b * 8192 + (w - 4) * 128, SC1);
            } else if (w == 5 && l < 8) {
                float ar = bf_lo(sp_[0]) + bf_lo(sp_[2]), ai = bf_lo(sp_[1]) - bf_lo(sp_[3]);
                float br = bf_lo(sp_[4]) + bf_lo(sp_[6]), bi = bf_lo(sp_[5]) - bf_lo(sp_[7]);
                __builtin_amdgcn_raw_buffer_store_b32(pk_b(0.25f * (ar + br), 0.25f * (ai + bi)), rw, l * 4, WS_WA + b * 32, SC1);
                __builtin_amdgcn_raw_buffer_store_b32(pk_b(0.25f * (ar - br), 0.25f * (ai - bi)), rw, l * 4, WS_WB + b * 32, SC1);
            }
        }
        tag = __builtin_amdgcn_readfirstlane(epoch) + 1u;
        if (w == 4) WAITV(2); else if (w == 5) WAITV(4); else WAITV(3);
        ST(18);
        BARRIER();
        if (w == 7) announce(0);
        bg(0, BG1);
        consts0();
        WAITV(BG1);
        ST(19);
        BARRIER();
        __builtin_amdgcn_s_sleep(2);
#pragma unroll
        for (int i = 0; i < 2; ++i) bst4<SC1>(SENT4, rw, l * 16 + i * 1024, WS_V + b * 8192 + (w - 4) * 2048);
        xl[0] = *(const u2*)((const char*)x + xoff); xl[1] = *(const u2*)((const char*)x + xoff + 512);
        FENCE();
        if (w == 7) all_announced(0);
        bg(BG1, BG2);
        ST(4);
        BARRIER();
        consts3();
        bg(BG2, BG3);
        WAITV(BG3 - BG1);
        ST(5);
        BARRIER();
        if (w == 7) announce(1);
#pragma unroll
        for (int i = 0; i < 2; ++i) bst4<SC1>(SENT4, rw, (l >> 3) * 256 + (l & 7) * 16 + i * 2048, WS_LN2 + (b >> 1) * 16384 + (b & 1) * 128 + (w - 4) * 4096);
        bg(BG3, BG4);
        BARRIER();
        bg(BG4, BG5);
        WAITV(BG5 - BG3);
        ST(6);
        BARRIER();
        if (w == 7) all_announced(1);
        else __builtin_amdgcn_s_sleep(3);
        bg(BG5, BG6);
        ST(7);
        BARRIER();
        __builtin_amdgcn_s_sleep(4);
        bg(BG6, BG7);
    }
    BARRIER();
    if (!aw) bg(BG7, BG8);
    {
        u4 A = *(const u4*)(scr + SCR_X + w * 1024 + m * 64 + g * 16);
        f4 Dre = mma_b(A, Gre3, Z4), Dim = mma_b(A, Gim3, Z4), Hre = mma_b(A, SGre, Z4), Him = mma_b(A, SGim, Z4);
        if (g == 0) { Dre[0] = Hre[0]; Dim[0] = Him[0]; }
        f2 r0 = fma2(-lo2(Dim), tws[0], lo2(Dre) * twc[0]), i0 = fma2(lo2(Dre), tws[0], lo2(Dim) * twc[0]);
        f2 r1 = fma2(-hi2(Dim), tws[1], hi2(Dre) * twc[1]), i1 = fma2(hi2(Dre), tws[1], hi2(Dim) * twc[1]);
        u4 A2 = {pk_b(r0[0], i0[0]), pk_b(r0[1], i0[1]), pk_b(r1[0], i1[0]), pk_b(r1[1], i1[1])};
        f2 h[4];
#pragma unroll
        for (int t = 0; t < 2; ++t) {
            f4 Yv = mma_b(A2, GA[t], Z4);
            u32 h0 = pk_b2(bf_2(xl[t][0]) + bf_2(pk_b(Yv[0], Yv[1]))), h1 = pk_b2(bf_2(xl[t][1]) + bf_2(pk_b(Yv[2], Yv[3])));
            bst2<SC1>((u2){h0, h1}, ro, xoff + 512 * t, 0);
            h[2 * t] = bf_2(h0); h[2 * t + 1] = bf_2(h1);
        }
        f2 s2 = (h[0] + h[1]) + (h[2] + h[3]);
        const float mean = red_lanes<true>(s2[0] + s2[1]) * (1.0f / 512);
        f2 q2 = {0.f, 0.f};
#pragma unroll
        for (int j = 0; j < 4; ++j) { f2 d = h[j] - mean; q2 = fma2(d, d, q2); }
        const float inv = ln_inv(red_lanes<true>(q2[0] + q2[1]) * (1.0f / 512));
        const float mb = rbf(mean);
#pragma unroll
        for (int t = 0; t < 2; ++t)
            *(u2*)(scr + ((8 * t + (m >> 1)) * 4 + g) * 128 + (w ^ (m >> 1)) * 16 + (m & 1) * 8) =
                (u2){clean(pk_b2(bf_2(pk_b2(h[2 * t] - mb)) * inv)), clean(pk_b2(bf_2(pk_b2(h[2 * t + 1] - mb)) * inv))};
        WAITV(2);
        BARRIER();
        u4 lo = *(const u4*)(scr + (tid ^ ((tid >> 5) & 7)) * 16);
        bst4<SC1>(lo, rw, (tid >> 3) * 256 + (tid & 7) * 16, WS_LN2 + (b >> 1) * 16384 + (b & 1) * 128);
        FENCE();
        ST(10);
    }
    auto publish = [&]() {
        u32 prev = 0;
        if (l == 0) prev = __hip_atomic_fetch_add(hw + 4, 1u, __ATOMIC_RELAXED, __HIP_MEMORY_SCOPE_WORKGROUP);
        if (__builtin_amdgcn_readfirstlane(prev) == 3u && l < 2)
            __hip_atomic_store(fl + (l ? 4096 + b : (mt1 * 8 + (nt1 >> 1)) * 32 + (nt1 & 1)), tag, __ATOMIC_RELAXED, __HIP_MEMORY_SCOPE_AGENT);
        FENCE();
    };
    {
        const int tg = w & 3;
        char* xch = smem + XCH + tg * 7168 + l * 16;
        const int hoff = EXP_HOFF + WS_H1 + (mt1 * 8 + 2 * tg) * (56 * 1024) + (nt1 >> 1) * 7168;
        if (aw) {
            const int toff = WS_LN2 + (mt1 * 8 + 2 * tg) * 16384;
            LAS char* wb0 = (LAS char*)smem + (4 * g + (m >> 2)) * 224 + (m & 3) * 8;
            LAS char* wb[4];
            int tq[4];
#pragma unroll
            for (int q = 0; q < 4; ++q) { const int qq = (q + rq) & 3; wb[q] = wb0 + qq * 28672; tq[q] = toff + qq * 4096; }
            u4 T[2][16], A[2][3];
            auto ldt = [&](int kk) {
#pragma unroll
                for (int tt = 0; tt < 2; ++tt) T[tt][kk] = bld4<SC1>(rw, l * 16 + (kk & 3) * 1024, tq[kk >> 2] + tt * 16384);
            };
#pragma unroll
            for (int i = 0; i < 3; ++i) A[0][i] = ldw1(wb[0], 0, i < 2 ? i : 6);
            WAITV(0);
            if (l == 0) __hip_atomic_fetch_add(hw + 5, 1u, __ATOMIC_RELAXED, __HIP_MEMORY_SCOPE_WORKGROUP);
            ST(11);
#pragma unroll
            for (int kk = 0; kk < G1_DEPTH; ++kk) { ldt(kk); SCHED(); }
            f4 acc[2][3];
#pragma unroll
            for (int tt = 0; tt < 2; ++tt)
#pragma unroll
                for (int i = 0; i < 3; ++i) acc[tt][i] = Z4;
#pragma unroll
            for (int kk = 0; kk < 16; ++kk) {
                if ((kk & 1) == 0) {
                    if (__builtin_expect(any_sent(umax(umax(max4(T[0][kk]), max4(T[1][kk])), umax(max4(T[0][kk + 1]), max4(T[1][kk + 1])))), 0)) {
                        wait_tok(T[0][kk], T[1][kk], rw, l * 16, tq[kk >> 2] + (kk & 3) * 1024);
                        wait_tok(T[0][kk + 1], T[1][kk + 1], rw, l * 16, tq[kk >> 2] + ((kk + 1) & 3) * 1024);
                    }
                    if (kk == 0) ST(12);
                    if (kk == (BG8 / 7) * 4 - 2) lds_wait(hw + 5, 8u);
                }
                if (kk + G1_DEPTH < 16) ldt(kk + G1_DEPTH);
                if (kk < 15) {
#pragma unroll
                    for (int i = 0; i < 3; ++i) A[(kk + 1) & 1][i] = ldw1(wb[(kk + 1) >> 2], (kk + 1) & 3, i < 2 ? i : 6);
                }
#pragma unroll
                for (int i = 0; i < 3; ++i)
#pragma unroll
                    for (int tt = 0; tt < 2; ++tt) acc[tt][i] = mma_b(A[kk & 1][i], T[tt][kk], acc[tt][i]);
#pragma unroll
                for (int i = 0; i < 6; ++i) {
                    __builtin_amdgcn_sched_group_barrier(0x8, 1, 0);
                    if (i == 0 || i == 3) __builtin_amdgcn_sched_group_barrier(0x20, 1, 0);
                    __builtin_amdgcn_sched_group_barrier(0x100, 1, 0);
                }
                if (kk & 1) {} else SCHED();
            }
            u4 A2[4][2];
#pragma unroll
            for (int kk = 0; kk < 3; ++kk)
#pragma unroll
                for (int i = 0; i < 2; ++i) A2[kk][i] = ldw1(wb[0], kk, 2 + i);
            SCHED();
            {
                u2 p[2][3];
#pragma unroll
                for (int tt = 0; tt < 2; ++tt)
#pragma unroll
                    for (int i = 0; i < 3; ++i) p[tt][i] = pk4(acc[tt][i]);
#pragma unroll
                for (int tt = 0; tt < 2; ++tt) *(u4*)(xch + tt * 1024) = (u4){p[tt][0][0], p[tt][0][1], p[tt][1][0], p[tt][1][1]};
                *(u4*)(xch + 2048) = (u4){p[0][2][0], p[0][2][1], p[1][2][0], p[1][2][1]};
            }
            lds_set(hw + tg, 1u);
            ST(13);
#pragma unroll
            for (int ps = 0; ps < 2; ++ps) {
                f4 ac2[2][2] = {{Z4, Z4}, {Z4, Z4}};
#pragma unroll
                for (int kk = 0; kk < 16; ++kk) {
                    const int kn = kk + 3;
                    if (kn < 16 || ps == 0) {
#pragma unroll
                        for (int i = 0; i < 2; ++i) A2[kn & 3][i] = ldw1(wb[(kn & 15) >> 2], kn & 3, (kn < 16 ? 2 + 2 * ps : 4) + i);
                    }
#pragma unroll
                    for (int i = 0; i < 2; ++i)
#pragma unroll
                        for (int tt = 0; tt < 2; ++tt) ac2[tt][i] = mma_b(A2[kk & 3][i], T[tt][kk], ac2[tt][i]);
#pragma unroll
                    for (int i = 0; i < 4; ++i) {
                        __builtin_amdgcn_sched_group_barrier(0x8, 1, 0);
                        __builtin_amdgcn_sched_group_barrier(0x100, 1, 0);
                    }
                    SCHED();
                }
                u2 p00 = pk4(ac2[0][0]), p01 = pk4(ac2[0][1]), p10 = pk4(ac2[1][0]), p11 = pk4(ac2[1][1]);
                *(u4*)(xch + 3072 + ps * 2048) = (u4){p00[0], p00[1], p01[0], p01[1]};
                *(u4*)(xch + 4096 + ps * 2048) = (u4){p10[0], p10[1], p11[0], p11[1]};
                lds_set(hw + tg, 2u + ps);
                if (ps == 0) ST(21);
            }
            ST(14);
            lds_wait(hw + 6, 4u);
#pragma unroll
            for (int ks = 0; ks < 4; ++ks) w2_glds(pw, ks, lds0 + tg * 16384 + ks * 4096);
#pragma unroll
            for (int ks = 3; ks < 7; ++ks) w2_glds(pw ^ 4, ks, lds0 + 65536 + tg * 16384 + (ks - 3) * 4096);
            WAITV(16);
#pragma unroll
            for (int ks = 0; ks < 4; ++ks) w2_read(W[ks], tg * 16384 + ks * 4096);
            asm volatile("s_waitcnt lgkmcnt(0)" ::: "memory");
#pragma unroll
            for (int ks = 0; ks < 4; ++ks) { PIN(W[ks][0]); PIN(W[ks][1]); PIN(W[ks][2]); PIN(W[ks][3]); }
#pragma unroll
            for (int ks = 4; ks < 7; ++ks) w2_glds(pw, ks, lds0 + tg * 16384 + (ks - 4) * 4096);
            WAITV(12);
            lds_set(hw + 8 + tg, tag);
            WAITV(0);
#pragma unroll
            for (int ks = 4; ks < 7; ++ks) w2_read(W[ks], tg * 16384 + (ks - 4) * 4096);
        } else {
            bg(BG8, 28);
            WAITV(0);
            if (l == 0) __hip_atomic_fetch_add(hw + 5, 1u, __ATOMIC_RELAXED, __HIP_MEMORY_SCOPE_WORKGROUP);
            __builtin_amdgcn_s_sleep(W2_SLEEP);
            lds_wait(hw + 5, 8u);
            auto stage_next = [&](int ks) {
                w2_read(W[ks - 1], LDS_SCR + tg * 4096);
                asm volatile("s_waitcnt lgkmcnt(0)" ::: "memory");
                PIN(W[ks - 1][0]); PIN(W[ks - 1][1]); PIN(W[ks - 1][2]); PIN(W[ks - 1][3]);
                if (ks < 3) w2_glds(pw, ks, lds0 + LDS_SCR + tg * 4096);
            };
            w2_glds(pw, 0, lds0 + LDS_SCR + tg * 4096);
            WAITV(0);
            stage_next(1);
            ST(12);
            lds_wait(hw + tg, 1u);
            __builtin_amdgcn_s_setprio(SILU_PRIO);
            ST(13);
#pragma unroll
            for (int tt = 0; tt < 2; ++tt) bst4<SC1>(silu_pk4(*(const u4*)(xch + tt * 1024)), rw, l * 16, hoff + tt * (56 * 1024) + (nt1 & 1) * 3072);
            {
                u4 o = silu_pk4(*(const u4*)(xch + 2048));
                bst2<SC1>((u2){o[0], o[1]}, rw, l * 8, hoff + 6144 + (nt1 & 1) * 512);
                bst2<SC1>((u2){o[2], o[3]}, rw, l * 8, hoff + 56 * 1024 + 6144 + (nt1 & 1) * 512);
            }
            WAITV(4);
            stage_next(2);
            ST(20);
#pragma unroll
            for (int ps = 0; ps < 2; ++ps) {
                lds_wait(hw + tg, 2u + ps);
                if (ps == 0) ST(21);
#pragma unroll
                for (int tt = 0; tt < 2; ++tt)
                    bst4<SC1>(silu_pk4(*(const u4*)(xch + 3072 + ps * 2048 + tt * 1024)), rw, l * 16, hoff + tt * (56 * 1024) + ((nt1 & 1) * 3 + 1 + ps) * 1024);
                if (ps == 0) { WAITV(2); stage_next(3); }
            }
            if (l == 0) __hip_atomic_fetch_add(hw + 6, 1u, __ATOMIC_RELAXED, __HIP_MEMORY_SCOPE_WORKGROUP);
            ST(14);
            __builtin_amdgcn_s_setprio(0);
            WAITV(0);
            publish();
#pragma nounroll
            for (int sp = 0; sp < SPIN_MAX; ++sp) {
                if (__builtin_amdgcn_readfirstlane(__hip_atomic_load(hw + 8 + tg, __ATOMIC_RELAXED, __HIP_MEMORY_SCOPE_WORKGROUP)) == tag) break;
                __builtin_amdgcn_s_sleep(1);
            }
            FENCE();
#pragma unroll
            for (int ks = 3; ks < 7; ++ks) w2_read(W[ks], 65536 + tg * 16384 + (ks - 3) * 4096);
        }
    }
    {
        ST(15);
        flag_wait(fl + (grp * 8 + pw) * 32 + (l & 1), tag);
        ST(16);
        const int rot = w & 3;
        const int toff = WS_H1 + (grp * 8 + th * 4) * (56 * 1024) + pw * 7168;
        int to[4];
#pragma unroll
        for (int tt = 0; tt < 4; ++tt) to[tt] = toff + ((tt + rot) & 3) * (56 * 1024);
        u4 T[7][4];
        auto ldtok = [&](int ks) {
#pragma unroll
            for (int tt = 0; tt < 4; ++tt) {
                if (ks < 6) T[ks][tt] = bld4<SC1>(rw, l * 16, to[tt] + ko[ks] * 1024);
                else {
                    u2 a = bld2<SC1>(rw, l * 8 + 2048, to[tt] + 4096), c = bld2<SC1>(rw, l * 8 + 2560, to[tt] + 4096);
                    T[ks][tt] = (u4){a[0], a[1], c[0], c[1]};
                }
            }
            SCHED();
        };
#pragma unroll
        for (int ks = 0; ks < G2_DEPTH; ++ks) ldtok(ks);
        const int rho = grp * 128 + th * 64 + rot * 16 + m;
        const int ooff = (256 * (rho & 7) + (rho >> 3)) * 1024 + fb * 128 + g * 16;
        u4 hres[2];
        f4 acc[4][4];
#pragma unroll
        for (int tt = 0; tt < 4; ++tt)
#pragma unroll
            for (int t = 0; t < 4; ++t) acc[tt][t] = Z4;
#pragma unroll
        for (int ks = 0; ks < 7; ++ks) {
            if (ks + G2_DEPTH < 7) { PIN(T[ks][0]); ldtok(ks + G2_DEPTH); }
#pragma unroll
            for (int tt = 0; tt < 4; ++tt)
#pragma unroll
                for (int t = 0; t < 4; ++t) acc[tt][t] = mma_b(W[ks][t], T[ks][tt], acc[tt][t]);
            SCHED();
        }
        if (!aw) {
            flag_wait(fl + 4096 + grp * 16 + th * 8 + rot * 2 + (l & 1), tag);
            hres[0] = bld4<SC1>(ro, ooff, 0); hres[1] = bld4<SC1>(ro, ooff + 64, 0);
        }
        ST(17);
#pragma unroll
        for (int tt = 0; tt < 4; ++tt)
            if (aw || tt > 0)
#pragma unroll
                for (int t = 0; t < 4; ++t) *(f4*)(smem + (w * 16 + ((tt + rot) & 3) * 4 + t) * 1024 + l * 16) = acc[tt][t];
        BARRIER();
        if (!aw) {
#pragma unroll
            for (int j = 1; j < 8; ++j)
#pragma unroll
                for (int t = 0; t < 4; ++t) acc[0][t] += *(const f4*)(smem + (((w + j) & 7) * 16 + rot * 4 + t) * 1024 + l * 16);
#pragma unroll
            for (int p = 0; p < 2; ++p) {
                u2 s0 = pk4(acc[0][2 * p]), s1 = pk4(acc[0][2 * p + 1]);
                u4 o = {pk_b2(bf_2(hres[p][0]) + bf_2(s0[0])), pk_b2(bf_2(hres[p][1]) + bf_2(s0[1])),
                        pk_b2(bf_2(hres[p][2]) + bf_2(s1[0])), pk_b2(bf_2(hres[p][3]) + bf_2(s1[1]))};
                bst4<AUX_OUT>(o, ro, ooff + 64 * p, 0);
            }
        }
    }
    ST_END(ws, 0);
    WAITV(0);
    if ((threadIdx.x & 63) == 0) ((unsigned long long*)(ws + (64u << 20)))[blockIdx.x * 8 + w] = __builtin_amdgcn_s_memrealtime();
}
__global__ void k_tail(char* ws) {
    unsigned long long t0 = __builtin_amdgcn_s_memrealtime(), mx = 0;
    for (int i = threadIdx.x; i < 2048; i += 64) { unsigned long long v = ((unsigned long long*)(ws + (64u << 20)))[i]; mx = v > mx ? v : mx; }
    for (int o = 32; o; o >>= 1) { unsigned long long v = __shfl_xor(mx, o); mx = v > mx ? v : mx; }
    unsigned long long gap = t0 - mx; if (gap > 3000) gap = 3000;
    unsigned long long e = __builtin_amdgcn_s_memrealtime() + KK * gap;
    while (__builtin_amdgcn_s_memrealtime() < e) __builtin_amdgcn_s_sleep(1);
}

extern "C" void kernel_launch(void* const* d_in, const int* in_sizes, int n_in, void* d_out, int out_size,
                              void* d_ws, size_t ws_size, hipStream_t stream) {
    (void)hipFuncSetAttribute((const void*)k_fused, hipFuncAttributeMaxDynamicSharedMemorySize, LDS_B);
    k_fused<<<256, 512, LDS_B, stream>>>((const uint16_t*)d_in[0], (const uint16_t*)d_in[1], (const uint16_t*)d_in[2],
                                         (const uint16_t*)d_in[4], (const uint16_t*)d_in[5], (uint16_t*)d_out, (char*)d_ws);
    k_tail<<<1, 64, 0, stream>>>((char*)d_ws);
}
```

```cpp
#include <hip/hip_runtime.h>
#include <hip/hip_bf16.h>
#include <stdint.h>

typedef _Float16 h8 __attribute__((ext_vector_type(8)));
typedef _Float16 h4 __attribute__((ext_vector_type(4)));
typedef _Float16 h2 __attribute__((ext_vector_type(2)));
typedef __bf16 b8 __attribute__((ext_vector_type(8)));
typedef __bf16 b2 __attribute__((ext_vector_type(2)));
typedef short s4 __attribute__((ext_vector_type(4)));
typedef float f4 __attribute__((ext_vector_type(4)));
typedef float f2 __attribute__((ext_vector_type(2)));
typedef unsigned u32;
typedef u32 u4 __attribute__((ext_vector_type(4)));
typedef u32 u2 __attribute__((ext_vector_type(2)));
#define LAS __attribute__((address_space(3)))
#define GAS __attribute__((address_space(1)))
#define DI __device__ __forceinline__
#define FENCE() asm volatile("" ::: "memory")
#define PIN(x) asm volatile("" : "+v"(x))
#define SCHED() __builtin_amdgcn_sched_barrier(0)
#define WAITV(n) asm volatile("s_waitcnt vmcnt(%0)" :: "n"(n) : "memory")
#define BARRIER() do { asm volatile("s_waitcnt lgkmcnt(0)" ::: "memory"); __builtin_amdgcn_s_barrier(); FENCE(); } while (0)
constexpr int SC1 = 16;
constexpr u32 SENT = 0xffffffffu;
constexpr int SPIN_MAX = 1 << 20;

DI u32 pk_h(float a, float b) { f2 v = {a, b}; h2 r = __builtin_convertvector(v, h2); return __builtin_bit_cast(u32, r); }
DI u32 pk_b(float a, float b) { f2 v = {a, b}; b2 r = __builtin_convertvector(v, b2); return __builtin_bit_cast(u32, r); }
DI float bf_lo(u32 v) { return __builtin_bit_cast(float, v << 16); }
DI float bf_hi(u32 v) { return __builtin_bit_cast(float, v & 0xffff0000u); }
DI f2 bf_2(u32 v) { return (f2){bf_lo(v), bf_hi(v)}; }
DI u32 pk_b2(f2 v) { return pk_b(v[0], v[1]); }
DI float rbf(float a) { return bf_lo(pk_b(a, a)); }
DI float cosr(float rev) { return __builtin_amdgcn_cosf(rev); }
DI float sinr(float rev) { return __builtin_amdgcn_sinf(rev); }
DI f2 lo2(f4 v) { return __builtin_shufflevector(v, v, 0, 1); }
DI f2 hi2(f4 v) { return __builtin_shufflevector(v, v, 2, 3); }
DI f2 fma2(f2 a, f2 b, f2 c) { return __builtin_elementwise_fma(a, b, c); }
DI u32 umin(u32 a, u32 b) { return a < b ? a : b; }
DI u32 umax(u32 a, u32 b) { return a > b ? a : b; }
DI u32 clean(u32 v) { return umin(v, SENT - 1); }
DI u32 max4(u4 v) { return umax(umax(umax(v[0], v[1]), v[2]), v[3]); }
#define Z4 ((f4){0, 0, 0, 0})
DI f4 mma_h(u4 a, u4 b, f4 c) { return __builtin_amdgcn_mfma_f32_16x16x32_f16(__builtin_bit_cast(h8, a), __builtin_bit_cast(h8, b), c, 0, 0, 0); }
DI f4 mma_b(u4 a, u4 b, f4 c) { return __builtin_amdgcn_mfma_f32_16x16x32_bf16(__builtin_bit_cast(b8, a), __builtin_bit_cast(b8, b), c, 0, 0, 0); }
DI f4 mma16_h(u2 a, u2 b, f4 c) { return __builtin_amdgcn_mfma_f32_16x16x16f16(__builtin_bit_cast(h4, a), __builtin_bit_cast(h4, b), c, 0, 0, 0); }
typedef __amdgpu_buffer_rsrc_t rsrc_t;
DI rsrc_t mkrs(const void* p, u32 bytes) { return __builtin_amdgcn_make_buffer_rsrc((void*)p, 0, bytes, 0x00020000); }
template <int AUX> DI u4 bld4(rsrc_t r, int v, int s) { return __builtin_amdgcn_raw_buffer_load_b128(r, v, s, AUX); }
template <int AUX> DI u2 bld2(rsrc_t r, int v, int s) { return __builtin_amdgcn_raw_buffer_load_b64(r, v, s, AUX); }
template <int AUX> DI void bst4(u4 d, rsrc_t r, int v, int s) {
    __builtin_amdgcn_raw_buffer_store_b128(d, r, v, s, AUX);
    asm volatile("s_nop 1" :: "v"(d));
}
template <int AUX> DI void bst2(u2 d, rsrc_t r, int v, int s) { __builtin_amdgcn_raw_buffer_store_b64(d, r, v, s, AUX); }

template <int CTRL> DI float dpp(float v) {
    return __builtin_bit_cast(float, __builtin_amdgcn_update_dpp(0, __builtin_bit_cast(int, v), CTRL, 0xf, 0xf, true));
}
template <bool ALL> DI float red_lanes(float v) {
    v += dpp<0xB1>(v);
    if (ALL) v += dpp<0x4E>(v);
    v += dpp<0x124>(v); v += dpp<0x128>(v);
    u32 x = __builtin_bit_cast(u32, v), y = x;
    asm("" : "+v"(y));
    auto r = __builtin_amdgcn_permlane16_swap(x, y, false, false);
    v = __builtin_bit_cast(float, (u32)r[0]) + __builtin_bit_cast(float, (u32)r[1]);
    x = __builtin_bit_cast(u32, v); y = x;
    asm("" : "+v"(y));
    auto q = __builtin_amdgcn_permlane32_swap(x, y, false, false);
    return __builtin_bit_cast(float, (u32)q[0]) + __builtin_bit_cast(float, (u32)q[1]);
}
DI float ln_inv(float var) { return __builtin_amdgcn_rcpf(rbf(__builtin_amdgcn_sqrtf(rbf(rbf(var) + 1e-5f)))); }

#define ST_DECL
#define ST(i)
#define ST_END(ws, kid)

constexpr u32 WS_U = 0;
constexpr u32 WS_WT = WS_U + (2u << 20);
constexpr u32 WS_V = WS_WT + (2u << 20);
constexpr u32 WS_LN2 = WS_V + (2u << 20);
constexpr u32 WS_WA = WS_LN2 + (2u << 20);
constexpr u32 WS_WB = WS_WA + 8192u;
constexpr u32 WS_FA = WS_WB + 8192u;
constexpr u32 WS_FLAG = WS_FA + 16384u;
constexpr u32 WS_EP = WS_FLAG + 32768u;
constexpr u32 WS_H1 = WS_WA + (128u << 10);
constexpr u32 WS_END = WS_H1 + 7340032u;

#define W1_B0 0
#define W1_B1 7
#define W1_B2 5
#define W1_B3 6
#define W1_CHK ((((W1_B0 + W1_B1 + W1_B2 + W1_B3) / 7) * 4 - 2) < 0 ? 0 : (((W1_B0 + W1_B1 + W1_B2 + W1_B3) / 7) * 4 - 2))
#define CLR_SLEEP 6
#define BPRIO 2
#define EXP_UCLR 0
#define POLL_SLEEP 2
#define BG1 0
#define BG2 4
#define BG3 4
#define BG4 10
#define BG5 15
#define BG6 15
#define BG7 15
#define BG8 21
#define A_EARLY 24
#define SILU_PRIO 0
#define EXP_HOFF 0
#define B_EARLY 8
#define W2_DELAY 4
#define A_GENTLE 3
#define EXP_AUX SC1
#define FLAG_SLEEP 8
#define B_PREFLAG 8
#define B_R 5
#define W2_SLEEP 8
#define AUX_OUT 2
#define W2_OUT 2
#define W2_ATHR 3
#define W2_EARLY 3
#define G1_DEPTH 6
#define G2_DEPTH 3
constexpr int LDS_SCR = 114688;
constexpr int SCR_X = 8448;
constexpr int SCR_S = SCR_X + 20480;
constexpr int SCR_F = 49024;
constexpr int XCH = 147456;
constexpr int LDS_B = 163840;

DI void glds16(const void* sbase, u32 voff, u32 lds_dst) {
    asm volatile("s_mov_b32 m0, %2\n\ts_nop 0\n\tglobal_load_lds_dwordx4 %0, %1" :: "v"(voff), "s"(sbase), "s"(lds_dst) : "memory");
}
DI void glds16x4(const void* sbase, u32 voff, u32 lds_dst) {
    asm volatile("s_mov_b32 m0, %2\n\ts_nop 0\n\tglobal_load_lds_dwordx4 %0, %1\n\tglobal_load_lds_dwordx4 %0, %1 offset:1024\n\t"
                 "global_load_lds_dwordx4 %0, %1 offset:2048\n\tglobal_load_lds_dwordx4 %0, %1 offset:3072"
                 :: "v"(voff), "s"(sbase), "s"(lds_dst) : "memory");
}
DI bool any_sent(u32 v) { return __builtin_amdgcn_ballot_w64(v == SENT) != 0; }
DI void wait_tok(u4& t0, u4& t1, rsrc_t rw, int vo, int so) {
    if (any_sent(umax(max4(t0), max4(t1)))) {
#pragma nounroll
        for (int sp = 0; sp < SPIN_MAX; ++sp) {
            FENCE();
            t0 = bld4<SC1>(rw, vo, so); t1 = bld4<SC1>(rw, vo, so + 16384);
            if (!any_sent(umax(max4(t0), max4(t1)))) break;
        }
    }
}
DI u4 ldw1(LAS char* wb, int kk, int ft) {
    s4 a0 = __builtin_amdgcn_ds_read_tr16_b64_v4i16((LAS s4*)(wb + kk * 32 * 224 + ft * 32));
    s4 a1 = __builtin_amdgcn_ds_read_tr16_b64_v4i16((LAS s4*)(wb + (kk * 32 + 16) * 224 + ft * 32));
    u2 x0 = __builtin_bit_cast(u2, a0), x1 = __builtin_bit_cast(u2, a1);
    return (u4){x0[0], x0[1], x1[0], x1[1]};
}
DI u2 pk4(f4 a) { return (u2){pk_b(a[0], a[1]), pk_b(a[2], a[3])}; }
DI u2 silu_pk2(u32 p0, u32 p1) {
    u32 o0, o1; float a0, a1, a2, a3, t0, t1, t2, t3;
    asm("v_lshlrev_b32 %2, 16, %10\n\tv_and_b32 %3, 0xffff0000, %10\n\tv_lshlrev_b32 %4, 16, %11\n\tv_and_b32 %5, 0xffff0000, %11\n\t"
        "v_mul_f32 %6, %12, %2\n\tv_mul_f32 %7, %12, %3\n\tv_mul_f32 %8, %12, %4\n\tv_mul_f32 %9, %12, %5\n\t"
        "v_exp_f32 %6, %6\n\tv_exp_f32 %7, %7\n\tv_exp_f32 %8, %8\n\tv_exp_f32 %9, %9\n\t"
        "v_add_f32 %6, 1.0, %6\n\tv_add_f32 %7, 1.0, %7\n\tv_add_f32 %8, 1.0, %8\n\tv_add_f32 %9, 1.0, %9\n\t"
        "v_rcp_f32 %6, %6\n\tv_rcp_f32 %7, %7\n\tv_rcp_f32 %8, %8\n\tv_rcp_f32 %9, %9\n\t"
        "v_mul_f32 %2, %2, %6\n\tv_mul_f32 %3, %3, %7\n\tv_mul_f32 %4, %4, %8\n\tv_mul_f32 %5, %5, %9\n\t"
        "v_cvt_pk_bf16_f32 %0, %2, %3\n\tv_cvt_pk_bf16_f32 %1, %4, %5"
        : "=&v"(o0), "=&v"(o1), "=&v"(a0), "=&v"(a1), "=&v"(a2), "=&v"(a3), "=&v"(t0), "=&v"(t1), "=&v"(t2), "=&v"(t3)
        : "v"(p0), "v"(p1), "s"(-1.4426950408889634f));
    return (u2){o0, o1};
}
DI u4 silu_pk4(u4 p) { u2 a = silu_pk2(p[0], p[1]), b = silu_pk2(p[2], p[3]); return (u4){a[0], a[1], b[0], b[1]}; }
DI void lds_set(LAS u32* p, u32 v) {
    asm volatile("s_waitcnt lgkmcnt(0)" ::: "memory");
    __hip_atomic_store(p, v, __ATOMIC_RELAXED, __HIP_MEMORY_SCOPE_WORKGROUP);
}
DI void lds_wait(LAS u32* p, u32 v) {
#pragma nounroll
    for (int sp = 0; sp < SPIN_MAX; ++sp) {
        if (__builtin_amdgcn_readfirstlane(__hip_atomic_load(p, __ATOMIC_RELAXED, __HIP_MEMORY_SCOPE_WORKGROUP)) >= v) break;
        __builtin_amdgcn_s_sleep(1);
    }
    FENCE();
}
DI void flag_wait(GAS u32* p, u32 tag) {
#pragma nounroll
    for (int sp = 0; sp < SPIN_MAX; ++sp) {
        if (__builtin_amdgcn_ballot_w64(__hip_atomic_load(p, __ATOMIC_RELAXED, __HIP_MEMORY_SCOPE_AGENT) != tag) == 0) break;
        __builtin_amdgcn_s_sleep(FLAG_SLEEP);
    }
    FENCE();
}
#define SENT4 ((u4){SENT, SENT, SENT, SENT})

__global__ __launch_bounds__(512, 2) void k_fused(const uint16_t* x, const uint16_t* wr, const uint16_t* wi, const uint16_t* w1,
                                                  const uint16_t* w2, uint16_t* out, char* ws) {
    extern __shared__ __attribute__((aligned(16))) char smem[];
    const int tid = threadIdx.x, l = tid & 63, g = l >> 4, m = l & 15;
    const int w = __builtin_amdgcn_readfirstlane(tid >> 6);
    const int b = (blockIdx.x & 7) * 32 + (blockIdx.x >> 3);
    const int mt1 = (b >> 6) * 4 + ((b >> 3) & 3), nt1 = ((b >> 5) & 1) * 8 + (b & 7);
    const int grp = b >> 4, th = (b >> 3) & 1, fb = b & 7;
    const rsrc_t rw = mkrs(ws, WS_END), ro = mkrs(out, 2097152);
    char* scr = smem + LDS_SCR;
    LAS u32* hw = (LAS u32*)((LAS char*)smem + LDS_SCR + SCR_F);
    const u32 lds0 = (u32)(uintptr_t)(LAS char*)smem;
    const float mf = (float)m, gf = (float)g, sg = (m & 1) ? -1.0f : 1.0f;
    const bool aw = w < 4;
    ST_DECL;
    const int rq = ((b >> 3) + b) & 3;
    const int row0 = (tid & 255) / 14, cch0 = (tid & 255) - row0 * 14;
    const char* wsrc = (const char*)w1 + nt1 * 224;
    u32 w1o[7];
    auto w1_burst = [&](int i0, int n) {
#pragma unroll
        for (int i = i0; i < i0 + n; ++i) {
            const int q = (i / 7 + rq) & 3, j = i % 7;
            glds16(wsrc + q * (128 * 3584), w1o[j], lds0 + (w - 4) * 1024 + q * 28672 + j * 4096);
        }
    };
    const u32 epoch = __hip_atomic_load((GAS u32*)(ws + WS_EP), __ATOMIC_RELAXED, __HIP_MEMORY_SCOPE_AGENT);
    FENCE();
    const int xoff = (256 * w + b) * 1024 + m * 32 + g * 8;
    u2 xl[2];
    float c16[4], s16[4];
    f2 twc[2], tws[2];
    u4 Gre3, Gim3, SGre, SGim, GA[2];
    auto consts0 = [&]() {
#pragma unroll
        for (int u = 0; u < 4; ++u) {
            float a = (4.0f * gf + u) * (mf * (1.0f / 16));
            c16[u] = cosr(a); s16[u] = sinr(a);
            twc[u >> 1][u & 1] = cosr(a * (1.0f / 32)); tws[u >> 1][u & 1] = sinr(a * (1.0f / 32));
        }
    };
    auto consts3 = [&]() {
#pragma unroll
        for (int u = 0; u < 4; ++u) {
            const float k = 4.0f * gf + u;
            const bool k0 = u == 0 && g == 0;
            float cc = c16[u], ss = s16[u];
            Gre3[u] = pk_b(cc, -ss); Gim3[u] = pk_b(ss, cc);
            float a2 = (2.0f * k + 1.0f) * (mf * (1.0f / 32));
            u32 sp = pk_b(g < 2 ? 2.f * cc : 2.f * sg * cosr(a2), g < 2 ? -2.f * ss : -2.f * sg * sinr(a2));
            SGre[u] = k0 ? pk_b(1.0f, sg) : (g < 2 ? sp : 0u);
            SGim[u] = g < 2 ? 0u : sp;
            float a4 = k * (mf * (1.0f / 32)), c4 = 2.f * cosr(a4), s4_ = -2.f * sinr(a4);
            GA[0][u] = k0 ? pk_b(1.0f, sg) : pk_b(c4, s4_);
            GA[1][u] = k0 ? pk_b(1.0f, sg) : ((u & 1) ? pk_b(-c4, -s4_) : pk_b(c4, s4_));
        }
        PIN(Gre3); PIN(Gim3); PIN(SGre); PIN(SGim); PIN(GA[0]); PIN(GA[1]); PIN(twc[0]); PIN(twc[1]); PIN(tws[0]); PIN(tws[1]);
    };
    const int pw = (w + 4 * th) & 7;
    GAS u32* fl = (GAS u32*)(ws + WS_FLAG);
    int ko[7];
    {
        const int r6 = (fb < 6 ? fb : fb - 5) + 3 * (grp & 1);
#pragma unroll
        for (int ks = 0; ks < 6; ++ks) { int k = ks + r6; k = k >= 6 ? k - 6 : k; ko[ks] = k >= 6 ? k - 6 : k; }
        ko[6] = 6;
    }
    u4 W[7][4];
    const u32 w2vo = (l >> 3) * 1024 + (((l & 7) ^ (((l >> 4) & 1) << 2)) * 16);
    auto w2_glds = [&](int p_, int ks, u32 dst) {
        const int q = ko[ks];
        const char* src = (const char*)w2 + (224 * p_ + (ks == 6 ? 96 : (q < 3 ? 32 * q : 16 + 32 * q))) * 1024 + fb * 128;
        glds16(src, w2vo, dst); glds16(src + 8192, w2vo, dst + 1024);
        glds16(src + (ks == 6 ? 112 : 16) * 1024, w2vo, dst + 2048); glds16(src + (ks == 6 ? 120 : 24) * 1024, w2vo, dst + 3072);
    };
    LAS char* w2rd = (LAS char*)smem + (4 * g + (m >> 2)) * 128 + (((m & 3) ^ (((m >> 3) & 1) << 2)) * 16);
    auto w2_read = [&](u4 (&f)[4], int off) {
#pragma unroll
        for (int t = 0; t < 4; ++t) {
            LAS char* p = (LAS char*)((u32)(uintptr_t)w2rd ^ ((t >> 1) * 64)) + off + (t & 1) * 8;
            u2 x0 = __builtin_bit_cast(u2, __builtin_amdgcn_ds_read_tr16_b64_v4i16((LAS s4*)p));
            u2 x1 = __builtin_bit_cast(u2, __builtin_amdgcn_ds_read_tr16_b64_v4i16((LAS s4*)(p + 2048)));
            f[t] = (u4){x0[0], x0[1], x1[0], x1[1]};
        }
    };
    auto bg = [&](int i0, int i1) { w1_burst(i0, i1 - i0); };
    u32 tag;
    if (aw) {
        u32 xd[8];
        const u32* xr = (const u32*)(x + (256 * (2 * w + ((m >> 1) & 1)) + b) * 512) + 8 * g + 2 * (m >> 2) + (m & 1);
#pragma unroll
        for (int j = 0; j < 8; ++j) xd[j] = xr[32 * j];
        if (tid < 8) hw[tid] = 0;
        FENCE();
        ST(2);
        {
            u4 FAre, FAim, Fre, Fim, Sre, Sim; u2 Mb; f2 pc[2], ps[2];
            consts0();
            float c0[8], s0[8];
#pragma unroll
            for (int j = 0; j < 8; ++j) {
                float a = (gf + 4.0f * j) * (mf * (1.0f / 32));
                c0[j] = cosr(a); s0[j] = m ? -sinr(a) : ((g & 1) ? -1.0f : 1.0f);
            }
            const float mo = (float)(2 * (m & 7) + 1) * (1.0f / 32);
#pragma unroll
            for (int u = 0; u < 4; ++u) {
                FAre[u] = pk_h(c0[2 * u], c0[2 * u + 1]); FAim[u] = pk_h(s0[2 * u], s0[2 * u + 1]);
                float cc = c16[u], ss = s16[u];
                Fre[u] = pk_h(cc, ss); Fim[u] = pk_h(-ss, cc);
                float a2 = (4.0f * gf + u) * mo;
                u32 re = pk_h(m < 8 ? cc : cosr(a2), 0.f), im = pk_h(m < 8 ? (m ? -ss : ((u & 1) ? -1.0f : 1.0f)) : -sinr(a2), 0.f);
                Sre[u] = m < 8 ? re : re << 16; Sim[u] = m < 8 ? im : im << 16;
                pc[u & 1][u >> 1] = twc[u >> 1][u & 1]; ps[u & 1][u >> 1] = tws[u >> 1][u & 1];
            }
#pragma unroll
            for (int e = 0; e < 2; ++e) {
                float a = (float)(((m >> 1) * (256 * (2 * g + e) + b)) & 2047) * (1.0f / 2048);
                float cc = cosr(a), ss = sinr(a);
                Mb[e] = (m & 1) ? pk_h(-ss, cc) : pk_h(cc, ss);
            }
            PIN(FAre); PIN(FAim); PIN(Fre); PIN(Fim); PIN(Sre); PIN(Sim); PIN(Mb); PIN(pc[0]); PIN(pc[1]); PIN(ps[0]); PIN(ps[1]);
            ST(3);
            f2 v[8];
#pragma unroll
            for (int j = 0; j < 8; ++j) v[j] = bf_2(xd[j]);
            f2 s2 = ((v[0] + v[1]) + (v[2] + v[3])) + ((v[4] + v[5]) + (v[6] + v[7]));
            const float mean = red_lanes<false>(s2[0] + s2[1]) * (1.0f / 512);
            f2 q0 = {0.f, 0.f}, q1 = {0.f, 0.f};
#pragma unroll
            for (int j = 0; j < 8; ++j) { v[j] -= mean; if (j & 1) q1 = fma2(v[j], v[j], q1); else q0 = fma2(v[j], v[j], q0); }
            q0 += q1;
            const float inv = ln_inv(red_lanes<false>(q0[0] + q0[1]) * (1.0f / 512));
            const float off = (mean - rbf(mean)) * inv;
#pragma unroll
            for (int j = 0; j < 8; ++j) v[j] = fma2(v[j], (f2){inv, inv}, (f2){off, off});
            u4 B[2];
#pragma unroll
            for (int p = 0; p < 2; ++p) {
                u4 A = {pk_h(v[0][p], v[1][p]), pk_h(v[2][p], v[3][p]), pk_h(v[4][p], v[5][p]), pk_h(v[6][p], v[7][p])};
                f4 Dre = mma_h(A, FAre, Z4), Dim = mma_h(A, FAim, Z4);
#pragma unroll
                for (int r = 0; r < 2; ++r) {
                    f2 re = r ? hi2(Dre) : lo2(Dre), im = r ? hi2(Dim) : lo2(Dim);
                    f2 tr = fma2(im, ps[p], re * pc[p]), ti = fma2(-re, ps[p], im * pc[p]);
                    B[r][p] = pk_h(tr[0], ti[0]); B[r][2 + p] = pk_h(tr[1], ti[1]);
                }
            }
#pragma unroll
            for (int r = 0; r < 2; ++r) {
                f4 Gre = mma_h(Fre, B[r], Z4), Gim = mma_h(Fim, B[r], Z4), Hre = mma_h(Sre, B[r], Z4), Him = mma_h(Sim, B[r], Z4);
                u4 Z;
#pragma unroll
                for (int i = 0; i < 4; ++i) Z[i] = pk_h(m ? Gre[i] : Hre[i], m ? Gim[i] : Him[i]);
                *(u4*)(scr + (2 * w + r) * 1024 + m * 64 + g * 16) = Z;
            }
            BARRIER();
            ST(18);
            u4 r0 = *(const u4*)(scr + (2 * g) * 1024 + w * 256 + m * 16);
            u4 r1 = *(const u4*)(scr + (2 * g + 1) * 1024 + w * 256 + m * 16);
#pragma unroll
            for (int t = 0; t < 4; ++t) {
                f4 D = mma16_h(Mb, (u2){r0[t], r1[t]}, Z4);
                *(u2*)(scr + SCR_S + w * 2048 + m * 128 + (((2 * t + (g >> 1)) ^ (m >> 1)) * 16) + (g & 1) * 8) =
                    (u2){clean(pk_h(D[0], D[1])), clean(pk_h(D[2], D[3]))};
            }
        }
        u4 uo0 = *(const u4*)(scr + SCR_S + w * 2048 + (l ^ ((l >> 4) & 7)) * 16);
        u4 uo1 = *(const u4*)(scr + SCR_S + w * 2048 + 1024 + (l ^ (((l >> 4) + 4) & 7)) * 16);
        u4 Fre, Fim, Gre, Gim, N0, N1; f2 tc[2], ts[2];
#pragma unroll
        for (int u = 0; u < 4; ++u) {
            float cc = c16[u], ss = s16[u];
            Fre[u] = pk_h(0.25f * cc, 0.25f * ss); Fim[u] = pk_h(-0.25f * ss, 0.25f * cc);
            Gre[u] = pk_b(cc * (1.0f / 256), -ss * (1.0f / 256)); Gim[u] = pk_b(ss * (1.0f / 256), cc * (1.0f / 256));
            float a3 = (4.0f * gf + u) * (mf * (1.0f / 256));
            tc[u >> 1][u & 1] = cosr(a3); ts[u >> 1][u & 1] = sinr(a3);
        }
        PIN(Fre); PIN(Fim); PIN(Gre); PIN(Gim); PIN(tc[0]); PIN(tc[1]); PIN(ts[0]); PIN(ts[1]);
        ST(19);
        BARRIER();
        ST(4);
        bst4<SC1>(uo0, rw, l * 16, WS_U + b * 8192 + w * 2048);
        bst4<SC1>(uo1, rw, l * 16 + 1024, WS_U + b * 8192 + w * 2048);
        xl[0] = *(const u2*)((const char*)x + xoff); xl[1] = *(const u2*)((const char*)x + xoff + 512);
        FENCE();
        tag = __builtin_amdgcn_readfirstlane(epoch) + 1u;
        consts3();
#pragma unroll
        for (int u = 0; u < 4; ++u) {
            float a1 = (float)(((4 * (g & 1) + u) * (256 * (m >> 1) + b)) & 2047) * (1.0f / 2048);
            float c1 = cosr(a1), s1 = sinr(a1);
            u32 nb = (m & 1) ? pk_b(s1, c1) : pk_b(c1, -s1);
            N0[u] = g < 2 ? nb : 0u; N1[u] = g < 2 ? 0u : nb;
        }
        PIN(N0); PIN(N1);
        WAITV(0);
        BARRIER();
        ST(5);
        u4 d[2], f[2], e[2];
        const int uo = (tid >> 1) * 8192 + (tid & 1) * 16;
        d[0] = bld4<SC1>(rw, uo, WS_U + b * 32); d[1] = bld4<SC1>(rw, uo, WS_U + 128 * 8192 + b * 32);
        f[0] = bld4<SC1>(rw, uo, WS_WT + b * 32); f[1] = bld4<SC1>(rw, uo, WS_WT + 128 * 8192 + b * 32);
        asm volatile("" :: "v"(f[0]), "v"(f[1]));
        if (any_sent(umax(max4(d[0]), max4(d[1])))) {
#pragma nounroll
            for (int sp = 0; sp < SPIN_MAX; ++sp) {
                __builtin_amdgcn_s_sleep(POLL_SLEEP);
                FENCE();
                d[0] = bld4<SC1>(rw, uo, WS_U + b * 32); d[1] = bld4<SC1>(rw, uo, WS_U + 128 * 8192 + b * 32);
                if (!any_sent(umax(max4(d[0]), max4(d[1])))) break;
            }
        }
        FENCE();
        f[0] = bld4<SC1>(rw, uo, WS_WT + b * 32); f[1] = bld4<SC1>(rw, uo, WS_WT + 128 * 8192 + b * 32);
        char* sp = scr + SCR_S + (tid ^ ((tid >> 4) & 1)) * 16;
        *(u4*)sp = d[0]; *(u4*)(sp + 4096) = d[1];
        BARRIER();
        if (b == 0) {
            f[0] = bld4<SC1>(rw, tid * 16, WS_WA); f[1] = bld4<SC1>(rw, tid * 16 + 4096, WS_WA);
            e[0] = bld4<SC1>(rw, tid * 16, WS_WB); e[1] = bld4<SC1>(rw, tid * 16 + 4096, WS_WB);
            if (tid == 0) __hip_atomic_store((GAS u32*)(ws + WS_EP), tag, __ATOMIC_RELAXED, __HIP_MEMORY_SCOPE_AGENT);
        }
        ST(6);
        u2 ld[4], wl[4];
        const int so = (64 * g + m) * 32 + (((w >> 1) ^ (m >> 3)) * 16) + (w & 1) * 8;
#pragma unroll
        for (int q = 0; q < 4; ++q) ld[q] = *(const u2*)(scr + SCR_S + so + 512 * q);
        FENCE();
        f4 Xre[2], Xim[2];
#pragma unroll
        for (int e = 0; e < 2; ++e) {
            u4 A = {ld[0][e], ld[1][e], ld[2][e], ld[3][e]};
            f4 Dre = mma_h(A, Fre, Z4), Dim = mma_h(A, Fim, Z4);
            f2 r0 = fma2(lo2(Dim), ts[0], lo2(Dre) * tc[0]), i0 = fma2(-lo2(Dre), ts[0], lo2(Dim) * tc[0]);
            f2 r1 = fma2(hi2(Dim), ts[1], hi2(Dre) * tc[1]), i1 = fma2(-hi2(Dre), ts[1], hi2(Dim) * tc[1]);
            u4 B = {pk_h(r0[0], i0[0]), pk_h(r0[1], i0[1]), pk_h(r1[0], i1[0]), pk_h(r1[1], i1[1])};
            Xre[e] = mma_h(Fre, B, Z4); Xim[e] = mma_h(Fim, B, Z4);
        }
        *(u4*)(sp + 8192) = f[0]; *(u4*)(sp + 12288) = f[1];
        f2* sx = (f2*)(scr + SCR_X);
        if (b == 0) {
            *(u4*)(sp - SCR_S) = e[0]; *(u4*)(sp - SCR_S + 4096) = e[1];
#pragma unroll
            for (int e = 0; e < 2; ++e)
#pragma unroll
                for (int i = 0; i < 4; ++i) sx[2 * w + e + 8 * (m + 16 * (4 * g + i))] = (f2){Xre[e][i], Xim[e][i]};
        }
        BARRIER();
        ST(20);
#pragma unroll
        for (int q = 0; q < 4; ++q) wl[q] = *(const u2*)(scr + SCR_S + so + 8192 + 512 * q);
        FENCE();
        f2 yr[2][2], yi[2][2];
#pragma unroll
        for (int e = 0; e < 2; ++e)
#pragma unroll
            for (int hh = 0; hh < 2; ++hh) {
                f2 wre = {bf_lo(wl[2 * hh][e]), bf_lo(wl[2 * hh + 1][e])}, wim = {bf_hi(wl[2 * hh][e]), bf_hi(wl[2 * hh + 1][e])};
                f2 xr = hh ? hi2(Xre[e]) : lo2(Xre[e]), xi = hh ? hi2(Xim[e]) : lo2(Xim[e]);
                yr[e][hh] = fma2(-xi, wim, xr * wre); yi[e][hh] = fma2(xr, wim, xi * wre);
            }
        if (b == 0) {
#pragma unroll
            for (int i = 0; i < 4; ++i) {
                u2 wb = *(const u2*)(scr + so + 512 * i);
#pragma unroll
                for (int e = 0; e < 2; ++e) {
                    f2 xm = sx[(2048 - (2 * w + e + 8 * (m + 16 * (4 * g + i)))) & 2047];
                    float br = bf_lo(wb[e]), bi = bf_hi(wb[e]);
                    yr[e][i >> 1][i & 1] += xm[0] * br + xm[1] * bi;
                    yi[e][i >> 1][i & 1] += xm[0] * bi - xm[1] * br;
                }
            }
        }
        char* pv = scr + SCR_S;
#pragma unroll
        for (int e = 0; e < 2; ++e) {
            u4 Y = {pk_b(yr[e][0][0], yi[e][0][0]), pk_b(yr[e][0][1], yi[e][0][1]), pk_b(yr[e][1][0], yi[e][1][0]), pk_b(yr[e][1][1], yi[e][1][1])};
            f4 Dre = mma_b(Y, Gre, Z4), Dim = mma_b(Y, Gim, Z4);
            f2 r0 = fma2(-lo2(Dim), ts[0], lo2(Dre) * tc[0]), i0 = fma2(lo2(Dre), ts[0], lo2(Dim) * tc[0]);
            f2 r1 = fma2(-hi2(Dim), ts[1], hi2(Dre) * tc[1]), i1 = fma2(hi2(Dre), ts[1], hi2(Dim) * tc[1]);
            u4 A = {pk_b(r0[0], i0[0]), pk_b(r0[1], i0[1]), pk_b(r1[0], i1[0]), pk_b(r1[1], i1[1])};
            f4 Ere = mma_b(A, Gre, Z4), Eim = mma_b(A, Gim, Z4);
            u4 o;
#pragma unroll
            for (int i = 0; i < 4; ++i) o[i] = clean(pk_b(Ere[i], Eim[i]));
            *(u4*)(pv + (2 * w + e) * 1024 + ((m * 4 + g) ^ ((w >> 1) * 4)) * 16) = o;
        }
        BARRIER();
        ST(7);
#pragma unroll
        for (int it = 0; it < 2; ++it) {
            const int ch = tid + 256 * it;
            const u32* q = (const u32*)(pv + (ch & 1) * 4096 + (((ch >> 1) * 4) ^ ((ch & 1) * 64)));
            bst4<SC1>((u4){q[0], q[256], q[512], q[768]}, rw, ch * 16, WS_V + b * 8192);
        }
        WAITV(0);
        BARRIER();
        ST(8);
        const int vo = (2 * m + (g >> 1)) * 8192 + (g & 1) * 16, vs = WS_V + w * 64 * 8192 + b * 32;
        u4 v0, v1;
#pragma nounroll
        for (int sp = 0; sp < SPIN_MAX; ++sp) {
            FENCE();
            v0 = bld4<SC1>(rw, vo, vs); v1 = bld4<SC1>(rw, vo, vs + 32 * 8192);
            if (!any_sent(umax(max4(v0), max4(v1)))) break;
            __builtin_amdgcn_s_sleep(POLL_SLEEP);
        }
        ST(9);
        char* sz = scr + SCR_X + g * 2048 + w * 256 + m * 8;
#pragma unroll
        for (int hh = 0; hh < 2; ++hh) {
            f4 D0 = mma_b(N0, hh ? v1 : v0, Z4), D1 = mma_b(N1, hh ? v1 : v0, Z4);
            *(u2*)(sz + hh * 128) = (u2){pk_b(D0[0], D0[1]), pk_b(D1[0], D1[1])};
            *(u2*)(sz + 1024 + hh * 128) = (u2){pk_b(D0[2], D0[3]), pk_b(D1[2], D1[3])};
        }
    } else {
        auto announce = [&](int which) {
            if (l < 8) __hip_atomic_store((GAS u32*)(ws + WS_FA) + which * 2048 + l * 256 + b, tag, __ATOMIC_RELAXED, __HIP_MEMORY_SCOPE_AGENT);
            FENCE();
        };
        auto all_announced = [&](int which) {
#pragma nounroll
            for (int sp = 0; sp < SPIN_MAX; ++sp) {
                FENCE();
                u4 f = bld4<SC1>(rw, l * 16, WS_FA + which * 8192 + (blockIdx.x & 7) * 1024);
                if (__builtin_amdgcn_ballot_w64(f[0] != tag || f[1] != tag || f[2] != tag || f[3] != tag) == 0) break;
                __builtin_amdgcn_s_sleep(4);
            }
        };
        const rsrc_t rr = mkrs(wr, 1052672), ri = mkrs(wi, 1052672);
        const int j = 4 * (w - 4) + g, i0_ = 4 * (m & 1);
        const bool mir = w >= 6;
        u32 fr[4], fi[4], sp_[8];
#pragma unroll
        for (int i = 0; i < 4; ++i) {
            const int e = mir ? ((2048 - 8 * b - i0_ - i) & 2047) * 514 + (512 - 32 * j - (m | 1)) * 2 : (8 * b + i0_ + i) * 514 + (32 * j + (m & 14)) * 2;
            fr[i] = __builtin_amdgcn_raw_buffer_load_b32(rr, e, 0, 0); fi[i] = __builtin_amdgcn_raw_buffer_load_b32(ri, e, 0, 0);
        }
        if (mir) {
            const int e = (8 * b + (l & 7)) * 514 + (16 + 32 * (4 * (w - 6) + ((l >> 3) & 3))) * 2;
            sp_[0] = __builtin_amdgcn_raw_buffer_load_b16(rr, e, 0, 0); sp_[1] = __builtin_amdgcn_raw_buffer_load_b16(ri, e, 0, 0);
        } else if (w == 5) {
            const int ed = (8 * b + (l & 7)) * 514, em = ((2048 - 8 * b - (l & 7)) & 2047) * 514;
#pragma unroll
            for (int i = 0; i < 8; ++i) sp_[i] = __builtin_amdgcn_raw_buffer_load_b16((i & 1) ? ri : rr, ((i & 2) ? em : ed) + (i >> 2) * 512, 0, 0);
        }
        FENCE();
#pragma unroll
        for (int i = 0; i < 2; ++i) bst4<SC1>(SENT4, rw, l * 16 + i * 1024, WS_U + b * 8192 + (w - 4) * 2048);
        ST(2);
#pragma unroll
        for (int jj = 0; jj < 7; ++jj) {
            const int cj_ = (4 * jj) % 14, rj = 18 * jj + (4 * jj) / 14;
            w1o[jj] = (row0 + rj) * 3584 + (cch0 + cj_) * 16 + (cch0 + cj_ >= 14 ? 3584 - 224 : 0);
            PIN(w1o[jj]);
        }
        {
            const u32 cj = mir ? 0x80000000u : 0u, se = mir ? 0x07060302u : 0x05040100u, so_ = mir ? 0x05040100u : 0x07060302u;
            u4 s0, s1;
#pragma unroll
            for (int i = 0; i < 4; ++i) { s0[i] = __builtin_amdgcn_perm(fi[i], fr[i], se) ^ cj; s1[i] = __builtin_amdgcn_perm(fi[i], fr[i], so_) ^ cj; }
            ST(3);
            const int wo = (m >> 1) * 1024 + j * 32 + (m & 1) * 16;
            bst4<SC1>(s0, rw, wo, WS_WT + b * 8192); bst4<SC1>(s1, rw, wo + 512, WS_WT + b * 8192);
            if (mir) {
                if (l < 32) __builtin_amdgcn_raw_buffer_store_b32(sp_[0] | (sp_[1] << 16), rw, l * 4, WS_WT + b * 8192 + (w - 4) * 128, SC1);
            } else if (w == 5 && l < 8) {
                float ar = bf_lo(sp_[0]) + bf_lo(sp_[2]), ai = bf_lo(sp_[1]) - bf_lo(sp_[3]);
                float br = bf_lo(sp_[4]) + bf_lo(sp_[6]), bi = bf_lo(sp_[5]) - bf_lo(sp_[7]);
                __builtin_amdgcn_raw_buffer_store_b32(pk_b(0.25f * (ar + br), 0.25f * (ai + bi)), rw, l * 4, WS_WA + b * 32, SC1);
                __builtin_amdgcn_raw_buffer_store_b32(pk_b(0.25f * (ar - br), 0.25f * (ai - bi)), rw, l * 4, WS_WB + b * 32, SC1);
            }
        }
        tag = __builtin_amdgcn_readfirstlane(epoch) + 1u;
        if (w == 4) WAITV(2); else if (w == 5) WAITV(4); else WAITV(3);
        ST(18);
        BARRIER();
        if (w == 7) announce(0);
        bg(0, BG1);
        WAITV(BG1);
        ST(19);
        BARRIER();
        __builtin_amdgcn_s_sleep(2);
#pragma unroll
        for (int i = 0; i < 2; ++i) bst4<SC1>(SENT4, rw, l * 16 + i * 1024, WS_V + b * 8192 + (w - 4) * 2048);
        xl[0] = *(const u2*)((const char*)x + xoff); xl[1] = *(const u2*)((const char*)x + xoff + 512);
        FENCE();
        if (w == 7) all_announced(0);
        bg(BG1, BG2);
        ST(4);
        BARRIER();
        consts0();
        consts3();
        bg(BG2, BG3);
        WAITV(BG3 - BG1);
        ST(5);
        BARRIER();
        if (w == 7) announce(1);
#pragma unroll
        for (int i = 0; i < 2; ++i) bst4<SC1>(SENT4, rw, (l >> 3) * 256 + (l & 7) * 16 + i * 2048, WS_LN2 + (b >> 1) * 16384 + (b & 1) * 128 + (w - 4) * 4096);
        bg(BG3, BG4);
        BARRIER();
        bg(BG4, BG5);
        WAITV(BG5 - BG3);
        ST(6);
        BARRIER();
        if (w == 7) all_announced(1);
        else __builtin_amdgcn_s_sleep(3);
        bg(BG5, BG6);
        ST(7);
        BARRIER();
        __builtin_amdgcn_s_sleep(4);
        bg(BG6, BG7);
    }
    BARRIER();
    if (!aw) bg(BG7, BG8);
    {
        u4 A = *(const u4*)(scr + SCR_X + w * 1024 + m * 64 + g * 16);
        f4 Dre = mma_b(A, Gre3, Z4), Dim = mma_b(A, Gim3, Z4), Hre = mma_b(A, SGre, Z4), Him = mma_b(A, SGim, Z4);
        if (g == 0) { Dre[0] = Hre[0]; Dim[0] = Him[0]; }
        f2 r0 = fma2(-lo2(Dim), tws[0], lo2(Dre) * twc[0]), i0 = fma2(lo2(Dre), tws[0], lo2(Dim) * twc[0]);
        f2 r1 = fma2(-hi2(Dim), tws[1], hi2(Dre) * twc[1]), i1 = fma2(hi2(Dre), tws[1], hi2(Dim) * twc[1]);
        u4 A2 = {pk_b(r0[0], i0[0]), pk_b(r0[1], i0[1]), pk_b(r1[0], i1[0]), pk_b(r1[1], i1[1])};
        f2 h[4];
#pragma unroll
        for (int t = 0; t < 2; ++t) {
            f4 Yv = mma_b(A2, GA[t], Z4);
            u32 h0 = pk_b2(bf_2(xl[t][0]) + bf_2(pk_b(Yv[0], Yv[1]))), h1 = pk_b2(bf_2(xl[t][1]) + bf_2(pk_b(Yv[2], Yv[3])));
            bst2<SC1>((u2){h0, h1}, ro, xoff + 512 * t, 0);
            h[2 * t] = bf_2(h0); h[2 * t + 1] = bf_2(h1);
        }
        f2 s2 = (h[0] + h[1]) + (h[2] + h[3]);
        const float mean = red_lanes<true>(s2[0] + s2[1]) * (1.0f / 512);
        f2 q2 = {0.f, 0.f};
#pragma unroll
        for (int j = 0; j < 4; ++j) { f2 d = h[j] - mean; q2 = fma2(d, d, q2); }
        const float inv = ln_inv(red_lanes<true>(q2[0] + q2[1]) * (1.0f / 512));
        const float mb = rbf(mean);
#pragma unroll
        for (int t = 0; t < 2; ++t)
            *(u2*)(scr + ((8 * t + (m >> 1)) * 4 + g) * 128 + (w ^ (m >> 1)) * 16 + (m & 1) * 8) =
                (u2){clean(pk_b2(bf_2(pk_b2(h[2 * t] - mb)) * inv)), clean(pk_b2(bf_2(pk_b2(h[2 * t + 1] - mb)) * inv))};
        WAITV(2);
        BARRIER();
        u4 lo = *(const u4*)(scr + (tid ^ ((tid >> 5) & 7)) * 16);
        bst4<SC1>(lo, rw, (tid >> 3) * 256 + (tid & 7) * 16, WS_LN2 + (b >> 1) * 16384 + (b & 1) * 128);
        FENCE();
        ST(10);
    }
    auto stage_out = [&](int r) {
        w2_read(W[r], LDS_SCR + (w & 3) * 8192 + (r & 1) * 4096);
        asm volatile("s_waitcnt lgkmcnt(0)" ::: "memory");
        PIN(W[r][0]); PIN(W[r][1]); PIN(W[r][2]); PIN(W[r][3]);
        if (r + 2 < 7) w2_glds(pw, r + 2, lds0 + LDS_SCR + (w & 3) * 8192 + (r & 1) * 4096);
    };
    auto publish = [&]() {
        u32 prev = 0;
        if (l == 0) prev = __hip_atomic_fetch_add(hw + 4, 1u, __ATOMIC_RELAXED, __HIP_MEMORY_SCOPE_WORKGROUP);
        if (__builtin_amdgcn_readfirstlane(prev) == 7u && l < 2)
            __hip_atomic_store(fl + (l ? 4096 + (b >> 1) * 32 + (b & 1) : (mt1 * 8 + (nt1 >> 1)) * 32 + (nt1 & 1)), tag, __ATOMIC_RELAXED, __HIP_MEMORY_SCOPE_AGENT);
        FENCE();
    };
    {
        const int tg = w & 3;
        char* xch = smem + XCH + tg * 3584;
        const int hoff = EXP_HOFF + WS_H1 + (mt1 * 8 + 2 * tg) * (56 * 1024) + (nt1 >> 1) * 7168;
        if (aw) {
            const int toff = WS_LN2 + (mt1 * 8 + 2 * tg) * 16384;
            LAS char* wb0 = (LAS char*)smem + (4 * g + (m >> 2)) * 224 + (m & 3) * 8;
            LAS char* wb[4];
            int tq[4];
#pragma unroll
            for (int q = 0; q < 4; ++q) { const int qq = (q + rq) & 3; wb[q] = wb0 + qq * 28672; tq[q] = toff + qq * 4096; }
            u4 T[2][16], A[2][7];
            auto ldt = [&](int kk) {
#pragma unroll
                for (int tt = 0; tt < 2; ++tt) T[tt][kk] = bld4<SC1>(rw, l * 16 + (kk & 3) * 1024, tq[kk >> 2] + tt * 16384);
            };
#pragma unroll
            for (int i = 0; i < 7; ++i) A[0][i] = ldw1(wb[0], 0, i);
            WAITV(0);
            if (l == 0) __hip_atomic_fetch_add(hw + 5, 1u, __ATOMIC_RELAXED, __HIP_MEMORY_SCOPE_WORKGROUP);
            ST(11);
#pragma unroll
            for (int kk = 0; kk < G1_DEPTH; ++kk) { ldt(kk); SCHED(); }
            f4 acc[2][7];
#pragma unroll
            for (int tt = 0; tt < 2; ++tt)
#pragma unroll
                for (int i = 0; i < 7; ++i) acc[tt][i] = Z4;
#pragma unroll
            for (int kk = 0; kk < 16; ++kk) {
                if ((kk & 1) == 0) {
                    if (__builtin_expect(any_sent(umax(umax(max4(T[0][kk]), max4(T[1][kk])), umax(max4(T[0][kk + 1]), max4(T[1][kk + 1])))), 0)) {
                        wait_tok(T[0][kk], T[1][kk], rw, l * 16, tq[kk >> 2] + (kk & 3) * 1024);
                        wait_tok(T[0][kk + 1], T[1][kk + 1], rw, l * 16, tq[kk >> 2] + ((kk + 1) & 3) * 1024);
                    }
                    if (kk == 0) ST(12);
                    if (kk == (BG8 / 7) * 4 - 2) lds_wait(hw + 5, 8u);
                }
                if (kk + G1_DEPTH < 16) ldt(kk + G1_DEPTH);
                if (kk < 15) {
#pragma unroll
                    for (int i = 0; i < 7; ++i) A[(kk + 1) & 1][i] = ldw1(wb[(kk + 1) >> 2], (kk + 1) & 3, i);
                }
#pragma unroll
                for (int i = 0; i < 7; ++i)
#pragma unroll
                    for (int tt = 0; tt < 2; ++tt) acc[tt][i] = mma_b(A[kk & 1][i], T[tt][kk], acc[tt][i]);
#pragma unroll
                for (int i = 0; i < 14; ++i) {
                    __builtin_amdgcn_sched_group_barrier(0x8, 1, 0);
                    if (i == 1 || i == 7) __builtin_amdgcn_sched_group_barrier(0x20, 1, 0);
                    __builtin_amdgcn_sched_group_barrier(0x100, 1, 0);
                }
                if (kk & 1) {} else SCHED();
            }
#pragma unroll
            for (int q = 0; q < 3; ++q) {
                u2 p0 = pk4(acc[1][2 * q]), p1 = pk4(acc[1][2 * q + 1]);
                *(u4*)(xch + q * 1024 + l * 16) = (u4){p0[0], p0[1], p1[0], p1[1]};
            }
            *(u2*)(xch + 3072 + l * 8) = pk4(acc[1][6]);
            lds_set(hw + tg, 1u);
            ST(13);
#pragma unroll
            for (int q = 0; q < 3; ++q) {
                u2 p0 = pk4(acc[0][2 * q]), p1 = pk4(acc[0][2 * q + 1]);
                bst4<SC1>(silu_pk4((u4){p0[0], p0[1], p1[0], p1[1]}), rw, l * 16, hoff + ((nt1 & 1) * 3 + q) * 1024);
            }
            { u2 p = pk4(acc[0][6]); bst2<SC1>(silu_pk2(p[0], p[1]), rw, l * 8, hoff + 6144 + (nt1 & 1) * 512); }
        } else {
            bg(BG8, 28);
            WAITV(0);
            if (l == 0) __hip_atomic_fetch_add(hw + 5, 1u, __ATOMIC_RELAXED, __HIP_MEMORY_SCOPE_WORKGROUP);
            __builtin_amdgcn_s_sleep(W2_SLEEP);
            lds_wait(hw + 5, 8u);
            w2_glds(pw, 0, lds0 + LDS_SCR + tg * 8192);
            w2_glds(pw, 1, lds0 + LDS_SCR + tg * 8192 + 4096);
#pragma unroll
            for (int r = 0; r < B_R; ++r) { WAITV(4); stage_out(r); }
            ST(12);
            lds_wait(hw + tg, 1u);
            ST(13);
#pragma unroll
            for (int q = 0; q < 3; ++q)
                bst4<SC1>(silu_pk4(*(const u4*)(xch + q * 1024 + l * 16)), rw, l * 16, hoff + 56 * 1024 + ((nt1 & 1) * 3 + q) * 1024);
            { u2 p = *(const u2*)(xch + 3072 + l * 8); bst2<SC1>(silu_pk2(p[0], p[1]), rw, l * 8, hoff + 56 * 1024 + 6144 + (nt1 & 1) * 512); }
        }
        if (l == 0) __hip_atomic_fetch_add(hw + 6, 1u, __ATOMIC_RELAXED, __HIP_MEMORY_SCOPE_WORKGROUP);
        ST(14);
        WAITV(0);
        publish();
        ST(21);
        if (aw) {
            lds_wait(hw + 6, 8u);
#pragma unroll
            for (int ks = 0; ks < 7; ++ks) w2_glds(pw, ks, lds0 + tg * 28672 + ks * 4096);
        } else {
            stage_out(B_R);
#pragma unroll
            for (int r = B_R + 1; r < 7; ++r) { if (r + 1 < 7) WAITV(4); else WAITV(0); stage_out(r); }
            __builtin_amdgcn_s_sleep(B_PREFLAG);
        }
    }
    {
        ST(15);
        const int rot = w & 3;
        flag_wait(fl + ((l & 2) && !aw ? 4096 + (grp * 8 + th * 4 + rot) * 32 : (grp * 8 + pw) * 32) + (l & 1), tag);
        ST(16);
        const int toff = WS_H1 + (grp * 8 + th * 4) * (56 * 1024) + pw * 7168;
        int to[4];
#pragma unroll
        for (int tt = 0; tt < 4; ++tt) to[tt] = toff + ((tt + rot) & 3) * (56 * 1024);
        u4 T[7][4];
        auto ldtok = [&](int ks) {
#pragma unroll
            for (int tt = 0; tt < 4; ++tt) {
                if (ks < 6) T[ks][tt] = bld4<EXP_AUX>(rw, l * 16, to[tt] + ko[ks] * 1024);
                else {
                    u2 a = bld2<SC1>(rw, l * 8 + 2048, to[tt] + 4096), c = bld2<SC1>(rw, l * 8 + 2560, to[tt] + 4096);
                    T[ks][tt] = (u4){a[0], a[1], c[0], c[1]};
                }
            }
            SCHED();
        };
#pragma unroll
        for (int ks = 0; ks < G2_DEPTH; ++ks) ldtok(ks);
        if (aw) {
#pragma unroll
            for (int ks = 0; ks < 7; ++ks) w2_read(W[ks], rot * 28672 + ks * 4096);
            asm volatile("s_waitcnt lgkmcnt(0)" ::: "memory");
            if (l == 0) __hip_atomic_fetch_add(hw + 7, 1u, __ATOMIC_RELAXED, __HIP_MEMORY_SCOPE_WORKGROUP);
        }
        const int rho = grp * 128 + th * 64 + rot * 16 + m;
        const int ooff = (256 * (rho & 7) + (rho >> 3)) * 1024 + fb * 128 + g * 16;
        u4 hres[2];
        hres[0] = bld4<SC1>(ro, ooff, 0); hres[1] = bld4<SC1>(ro, ooff + 64, 0);
        SCHED();
        f4 acc[4][4];
#pragma unroll
        for (int tt = 0; tt < 4; ++tt)
#pragma unroll
            for (int t = 0; t < 4; ++t) acc[tt][t] = Z4;
#pragma unroll
        for (int ks = 0; ks < 7; ++ks) {
            if (ks + G2_DEPTH < 7) { PIN(T[ks][0]); ldtok(ks + G2_DEPTH); }
#pragma unroll
            for (int tt = 0; tt < 4; ++tt)
#pragma unroll
                for (int t = 0; t < 4; ++t) {
                    acc[tt][t] = mma_b(W[ks][t], T[ks][tt], acc[tt][t]);
                }
            SCHED();
        }
        ST(17);
        lds_wait(hw + 7, 4u);
#pragma unroll
        for (int tt = 0; tt < 4; ++tt)
            if (aw || tt > 0)
#pragma unroll
                for (int t = 0; t < 4; ++t) *(f4*)(smem + (w * 16 + ((tt + rot) & 3) * 4 + t) * 1024 + l * 16) = acc[tt][t];
        BARRIER();
        if (!aw) {
#pragma unroll
            for (int j = 1; j < 8; ++j)
#pragma unroll
                for (int t = 0; t < 4; ++t) acc[0][t] += *(const f4*)(smem + (((w + j) & 7) * 16 + rot * 4 + t) * 1024 + l * 16);
#pragma unroll
            for (int p = 0; p < 2; ++p) {
                u2 s0 = pk4(acc[0][2 * p]), s1 = pk4(acc[0][2 * p + 1]);
                u4 o = {pk_b2(bf_2(hres[p][0]) + bf_2(s0[0])), pk_b2(bf_2(hres[p][1]) + bf_2(s0[1])),
                        pk_b2(bf_2(hres[p][2]) + bf_2(s1[0])), pk_b2(bf_2(hres[p][3]) + bf_2(s1[1]))};
                bst4<AUX_OUT>(o, ro, ooff + 64 * p, 0);
            }
        }
    }
    ST_END(ws, 0);
}

extern "C" void kernel_launch(void* const* d_in, const int* in_sizes, int n_in, void* d_out, int out_size,
                              void* d_ws, size_t ws_size, hipStream_t stream) {
    (void)hipFuncSetAttribute((const void*)k_fused, hipFuncAttributeMaxDynamicSharedMemorySize, LDS_B);
    k_fused<<<256, 512, LDS_B, stream>>>((const uint16_t*)d_in[0], (const uint16_t*)d_in[1], (const uint16_t*)d_in[2],
                                         (const uint16_t*)d_in[4], (const uint16_t*)d_in[5], (uint16_t*)d_out, (char*)d_ws);
}
```

```cpp
#define G1_DEPTH 12
#include <hip/hip_runtime.h>
#include <hip/hip_bf16.h>
#include <stdint.h>

typedef _Float16 h8 __attribute__((ext_vector_type(8)));
typedef _Float16 h4 __attribute__((ext_vector_type(4)));
typedef _Float16 h2 __attribute__((ext_vector_type(2)));
typedef __bf16 b8 __attribute__((ext_vector_type(8)));
typedef __bf16 b2 __attribute__((ext_vector_type(2)));
typedef short s4 __attribute__((ext_vector_type(4)));
typedef float f4 __attribute__((ext_vector_type(4)));
typedef float f2 __attribute__((ext_vector_type(2)));
typedef unsigned u32;
typedef u32 u4 __attribute__((ext_vector_type(4)));
typedef u32 u2 __attribute__((ext_vector_type(2)));
#define LAS __attribute__((address_space(3)))
#define GAS __attribute__((address_space(1)))
#define DI __device__ __forceinline__
#define FENCE() asm volatile("" ::: "memory")
#define PIN(x) asm volatile("" : "+v"(x))
#define SCHED() __builtin_amdgcn_sched_barrier(0)
#define WAITV(n) asm volatile("s_waitcnt vmcnt(%0)" :: "n"(n) : "memory")
#define BARRIER() do { asm volatile("s_waitcnt lgkmcnt(0)" ::: "memory"); __builtin_amdgcn_s_barrier(); FENCE(); } while (0)
constexpr int SC1 = 16;
constexpr u32 SENT = 0xffffffffu;
constexpr int SPIN_MAX = 1 << 20;

DI u32 pk_h(float a, float b) { f2 v = {a, b}; h2 r = __builtin_convertvector(v, h2); return __builtin_bit_cast(u32, r); }
DI u32 pk_b(float a, float b) { f2 v = {a, b}; b2 r = __builtin_convertvector(v, b2); return __builtin_bit_cast(u32, r); }
DI float bf_lo(u32 v) { return __builtin_bit_cast(float, v << 16); }
DI float bf_hi(u32 v) { return __builtin_bit_cast(float, v & 0xffff0000u); }
DI f2 bf_2(u32 v) { return (f2){bf_lo(v), bf_hi(v)}; }
DI u32 pk_b2(f2 v) { return pk_b(v[0], v[1]); }
DI float rbf(float a) { return bf_lo(pk_b(a, a)); }
DI float cosr(float rev) { return __builtin_amdgcn_cosf(rev); }
DI float sinr(float rev) { return __builtin_amdgcn_sinf(rev); }
DI f2 lo2(f4 v) { return __builtin_shufflevector(v, v, 0, 1); }
DI f2 hi2(f4 v) { return __builtin_shufflevector(v, v, 2, 3); }
DI f2 fma2(f2 a, f2 b, f2 c) { return __builtin_elementwise_fma(a, b, c); }
DI u32 umin(u32 a, u32 b) { return a < b ? a : b; }
DI u32 umax(u32 a, u32 b) { return a > b ? a : b; }
DI u32 clean(u32 v) { return umin(v, SENT - 1); }
DI u32 max4(u4 v) { return umax(umax(umax(v[0], v[1]), v[2]), v[3]); }
#define Z4 ((f4){0, 0, 0, 0})
DI f4 mma_h(u4 a, u4 b, f4 c) { return __builtin_amdgcn_mfma_f32_16x16x32_f16(__builtin_bit_cast(h8, a), __builtin_bit_cast(h8, b), c, 0, 0, 0); }
DI f4 mma_b(u4 a, u4 b, f4 c) { return __builtin_amdgcn_mfma_f32_16x16x32_bf16(__builtin_bit_cast(b8, a), __builtin_bit_cast(b8, b), c, 0, 0, 0); }
DI f4 mma16_h(u2 a, u2 b, f4 c) { return __builtin_amdgcn_mfma_f32_16x16x16f16(__builtin_bit_cast(h4, a), __builtin_bit_cast(h4, b), c, 0, 0, 0); }
typedef __amdgpu_buffer_rsrc_t rsrc_t;
DI rsrc_t mkrs(const void* p, u32 bytes) { return __builtin_amdgcn_make_buffer_rsrc((void*)p, 0, bytes, 0x00020000); }
template <int AUX> DI u4 bld4(rsrc_t r, int v, int s) { return __builtin_amdgcn_raw_buffer_load_b128(r, v, s, AUX); }
template <int AUX> DI u2 bld2(rsrc_t r, int v, int s) { return __builtin_amdgcn_raw_buffer_load_b64(r, v, s, AUX); }
template <int AUX> DI void bst4(u4 d, rsrc_t r, int v, int s) {
    __builtin_amdgcn_raw_buffer_store_b128(d, r, v, s, AUX);
    asm volatile("s_nop 1" :: "v"(d));
}
template <int AUX> DI void bst2(u2 d, rsrc_t r, int v, int s) { __builtin_amdgcn_raw_buffer_store_b64(d, r, v, s, AUX); }

template <int CTRL> DI float dpp(float v) {
    return __builtin_bit_cast(float, __builtin_amdgcn_update_dpp(0, __builtin_bit_cast(int, v), CTRL, 0xf, 0xf, true));
}
template <bool ALL> DI float red_lanes(float v) {
    v += dpp<0xB1>(v);
    if (ALL) v += dpp<0x4E>(v);
    v += dpp<0x124>(v); v += dpp<0x128>(v);
    u32 x = __builtin_bit_cast(u32, v), y = x;
    asm("" : "+v"(y));
    auto r = __builtin_amdgcn_permlane16_swap(x, y, false, false);
    v = __builtin_bit_cast(float, (u32)r[0]) + __builtin_bit_cast(float, (u32)r[1]);
    x = __builtin_bit_cast(u32, v); y = x;
    asm("" : "+v"(y));
    auto q = __builtin_amdgcn_permlane32_swap(x, y, false, false);
    return __builtin_bit_cast(float, (u32)q[0]) + __builtin_bit_cast(float, (u32)q[1]);
}
DI float ln_inv(float var) { return __builtin_amdgcn_rcpf(rbf(__builtin_amdgcn_sqrtf(rbf(rbf(var) + 1e-5f)))); }

#define ST_DECL
#define ST(i)
#define ST_END(ws, kid)

constexpr u32 WS_U = 0;
constexpr u32 WS_WT = WS_U + (2u << 20);
constexpr u32 WS_V = WS_WT + (2u << 20);
constexpr u32 WS_LN2 = WS_V + (2u << 20);
constexpr u32 WS_WA = WS_LN2 + (2u << 20);
constexpr u32 WS_WB = WS_WA + 8192u;
constexpr u32 WS_FA = WS_WB + 8192u;
constexpr u32 WS_FLAG = WS_FA + 16384u;
constexpr u32 WS_EP = WS_FLAG + 32768u;
constexpr u32 WS_H1 = WS_WA + (128u << 10);
constexpr u32 WS_END = WS_H1 + 7340032u;

#define W1_B0 0
#define W1_B1 7
#define W1_B2 5
#define W1_B3 6
#define W1_CHK ((((W1_B0 + W1_B1 + W1_B2 + W1_B3) / 7) * 4 - 2) < 0 ? 0 : (((W1_B0 + W1_B1 + W1_B2 + W1_B3) / 7) * 4 - 2))
#define CLR_SLEEP 6
#define BPRIO 2
#define EXP_UCLR 0
#define POLL_SLEEP 2
#define BG1 0
#define BG2 4
#define BG3 4
#define BG4 10
#define BG5 15
#define BG6 15
#define BG7 15
#define BG8 21
#define A_EARLY 24
#define SILU_PRIO 0
#define EXP_HOFF 0
#define B_EARLY 8
#define W2_DELAY 4
#define A_GENTLE 3
#define EXP_AUX SC1
#define FLAG_SLEEP 2
#define B_PREFLAG 8
#define B_R 5
#define G2_PRIO 0
#define EXP_ROT 1
#define B_EARLY_BAR 0
#define FLOOD_W 4
#define W2_SLEEP 8
#define AUX_OUT 2
#define W2_OUT 2
#define W2_ATHR 3
#define W2_EARLY 3
#define G2_DEPTH 3
constexpr int LDS_SCR = 114688;
constexpr int SCR_X = 8448;
constexpr int SCR_S = SCR_X + 20480;
constexpr int SCR_F = 49024;
constexpr int XCH = 147456;
constexpr int LDS_B = 163840;

DI void glds16(const void* sbase, u32 voff, u32 lds_dst) {
    asm volatile("s_mov_b32 m0, %2\n\ts_nop 0\n\tglobal_load_lds_dwordx4 %0, %1" :: "v"(voff), "s"(sbase), "s"(lds_dst) : "memory");
}
DI void glds16x4(const void* sbase, u32 voff, u32 lds_dst) {
    asm volatile("s_mov_b32 m0, %2\n\ts_nop 0\n\tglobal_load_lds_dwordx4 %0, %1\n\tglobal_load_lds_dwordx4 %0, %1 offset:1024\n\t"
                 "global_load_lds_dwordx4 %0, %1 offset:2048\n\tglobal_load_lds_dwordx4 %0, %1 offset:3072"
                 :: "v"(voff), "s"(sbase), "s"(lds_dst) : "memory");
}
DI bool any_sent(u32 v) { return __builtin_amdgcn_ballot_w64(v == SENT) != 0; }
DI void wait_tok(u4& t0, u4& t1, rsrc_t rw, int vo, int so) {
    if (any_sent(umax(max4(t0), max4(t1)))) {
#pragma nounroll
        for (int sp = 0; sp < SPIN_MAX; ++sp) {
            FENCE();
            t0 = bld4<SC1>(rw, vo, so); t1 = bld4<SC1>(rw, vo, so + 16384);
            if (!any_sent(umax(max4(t0), max4(t1)))) break;
        }
    }
}
DI u4 ldw1(LAS char* wb, int kk, int ft) {
    s4 a0 = __builtin_amdgcn_ds_read_tr16_b64_v4i16((LAS s4*)(wb + kk * 32 * 224 + ft * 32));
    s4 a1 = __builtin_amdgcn_ds_read_tr16_b64_v4i16((LAS s4*)(wb + (kk * 32 + 16) * 224 + ft * 32));
    u2 x0 = __builtin_bit_cast(u2, a0), x1 = __builtin_bit_cast(u2, a1);
    return (u4){x0[0], x0[1], x1[0], x1[1]};
}
DI u2 pk4(f4 a) { return (u2){pk_b(a[0], a[1]), pk_b(a[2], a[3])}; }
DI u2 silu_pk2(u32 p0, u32 p1) {
    u32 o0, o1; float a0, a1, a2, a3, t0, t1, t2, t3;
    asm("v_lshlrev_b32 %2, 16, %10\n\tv_and_b32 %3, 0xffff0000, %10\n\tv_lshlrev_b32 %4, 16, %11\n\tv_and_b32 %5, 0xffff0000, %11\n\t"
        "v_mul_f32 %6, %12, %2\n\tv_mul_f32 %7, %12, %3\n\tv_mul_f32 %8, %12, %4\n\tv_mul_f32 %9, %12, %5\n\t"
        "v_exp_f32 %6, %6\n\tv_exp_f32 %7, %7\n\tv_exp_f32 %8, %8\n\tv_exp_f32 %9, %9\n\t"
        "v_add_f32 %6, 1.0, %6\n\tv_add_f32 %7, 1.0, %7\n\tv_add_f32 %8, 1.0, %8\n\tv_add_f32 %9, 1.0, %9\n\t"
        "v_rcp_f32 %6, %6\n\tv_rcp_f32 %7, %7\n\tv_rcp_f32 %8, %8\n\tv_rcp_f32 %9, %9\n\t"
        "v_mul_f32 %2, %2, %6\n\tv_mul_f32 %3, %3, %7\n\tv_mul_f32 %4, %4, %8\n\tv_mul_f32 %5, %5, %9\n\t"
        "v_cvt_pk_bf16_f32 %0, %2, %3\n\tv_cvt_pk_bf16_f32 %1, %4, %5"
        : "=&v"(o0), "=&v"(o1), "=&v"(a0), "=&v"(a1), "=&v"(a2), "=&v"(a3), "=&v"(t0), "=&v"(t1), "=&v"(t2), "=&v"(t3)
        : "v"(p0), "v"(p1), "s"(-1.4426950408889634f));
    return (u2){o0, o1};
}
DI u4 silu_pk4(u4 p) { u2 a = silu_pk2(p[0], p[1]), b = silu_pk2(p[2], p[3]); return (u4){a[0], a[1], b[0], b[1]}; }
DI void lds_set(LAS u32* p, u32 v) {
    asm volatile("s_waitcnt lgkmcnt(0)" ::: "memory");
    __hip_atomic_store(p, v, __ATOMIC_RELAXED, __HIP_MEMORY_SCOPE_WORKGROUP);
}
DI void lds_wait(LAS u32* p, u32 v) {
#pragma nounroll
    for (int sp = 0; sp < SPIN_MAX; ++sp) {
        if (__builtin_amdgcn_readfirstlane(__hip_atomic_load(p, __ATOMIC_RELAXED, __HIP_MEMORY_SCOPE_WORKGROUP)) >= v) break;
        __builtin_amdgcn_s_sleep(1);
    }
    FENCE();
}
DI void flag_wait(GAS u32* p, u32 tag) {
#pragma nounroll
    for (int sp = 0; sp < SPIN_MAX; ++sp) {
        if (__builtin_amdgcn_ballot_w64(__hip_atomic_load(p, __ATOMIC_RELAXED, __HIP_MEMORY_SCOPE_AGENT) != tag) == 0) break;
        __builtin_amdgcn_s_sleep(FLAG_SLEEP);
    }
    FENCE();
}
#define SENT4 ((u4){SENT, SENT, SENT, SENT})

__global__ __launch_bounds__(512, 2) void k_fused(const uint16_t* x, const uint16_t* wr, const uint16_t* wi, const uint16_t* w1,
                                                  const uint16_t* w2, uint16_t* out, char* ws) {
    extern __shared__ __attribute__((aligned(16))) char smem[];
    const int tid = threadIdx.x, l = tid & 63, g = l >> 4, m = l & 15;
    const int w = __builtin_amdgcn_readfirstlane(tid >> 6);
    const int b = (blockIdx.x & 7) * 32 + (blockIdx.x >> 3);
    const int mt1 = (b >> 6) * 4 + ((b >> 3) & 3), nt1 = ((b >> 5) & 1) * 8 + (b & 7);
    const int grp = b >> 4, th = (b >> 3) & 1, fb = b & 7;
    const rsrc_t rw = mkrs(ws, WS_END), ro = mkrs(out, 2097152);
    char* scr = smem + LDS_SCR;
    LAS u32* hw = (LAS u32*)((LAS char*)smem + LDS_SCR + SCR_F);
    const u32 lds0 = (u32)(uintptr_t)(LAS char*)smem;
    const float mf = (float)m, gf = (float)g, sg = (m & 1) ? -1.0f : 1.0f;
    const bool aw = w < 4;
    ST_DECL;
    const int rq = ((b >> 3) + b) & 3;
    const int row0 = (tid & 255) / 14, cch0 = (tid & 255) - row0 * 14;
    const char* wsrc = (const char*)w1 + nt1 * 224;
    u32 w1o[7];
    auto w1_burst = [&](int i0, int n) {
#pragma unroll
        for (int i = i0; i < i0 + n; ++i) {
            const int q = (i / 7 + rq) & 3, j = i % 7;
            glds16(wsrc + q * (128 * 3584), w1o[j], lds0 + (w - 4) * 1024 + q * 28672 + j * 4096);
        }
    };
    const u32 epoch = __hip_atomic_load((GAS u32*)(ws + WS_EP), __ATOMIC_RELAXED, __HIP_MEMORY_SCOPE_AGENT);
    FENCE();
    const int xoff = (256 * w + b) * 1024 + m * 32 + g * 8;
    u2 xl[2];
    float c16[4], s16[4];
    f2 twc[2], tws[2];
    u4 Gre3, Gim3, SGre, SGim, GA[2];
    auto consts0 = [&]() {
#pragma unroll
        for (int u = 0; u < 4; ++u) {
            float a = (4.0f * gf + u) * (mf * (1.0f / 16));
            c16[u] = cosr(a); s16[u] = sinr(a);
            twc[u >> 1][u & 1] = cosr(a * (1.0f / 32)); tws[u >> 1][u & 1] = sinr(a * (1.0f / 32));
        }
    };
    auto consts3 = [&]() {
#pragma unroll
        for (int u = 0; u < 4; ++u) {
            const float k = 4.0f * gf + u;
            const bool k0 = u == 0 && g == 0;
            float cc = c16[u], ss = s16[u];
            Gre3[u] = pk_b(cc, -ss); Gim3[u] = pk_b(ss, cc);
            float a2 = (2.0f * k + 1.0f) * (mf * (1.0f / 32));
            u32 sp = pk_b(g < 2 ? 2.f * cc : 2.f * sg * cosr(a2), g < 2 ? -2.f * ss : -2.f * sg * sinr(a2));
            SGre[u] = k0 ? pk_b(1.0f, sg) : (g < 2 ? sp : 0u);
            SGim[u] = g < 2 ? 0u : sp;
            float a4 = k * (mf * (1.0f / 32)), c4 = 2.f * cosr(a4), s4_ = -2.f * sinr(a4);
            GA[0][u] = k0 ? pk_b(1.0f, sg) : pk_b(c4, s4_);
            GA[1][u] = k0 ? pk_b(1.0f, sg) : ((u & 1) ? pk_b(-c4, -s4_) : pk_b(c4, s4_));
        }
        PIN(Gre3); PIN(Gim3); PIN(SGre); PIN(SGim); PIN(GA[0]); PIN(GA[1]); PIN(twc[0]); PIN(twc[1]); PIN(tws[0]); PIN(tws[1]);
    };
    const int pw = (w + 4 * th) & 7;
    GAS u32* fl = (GAS u32*)(ws + WS_FLAG);
    int ko[7];
    {
        const int r6 = EXP_ROT * ((fb < 6 ? fb : fb - 5) + 3 * (grp & 1));
#pragma unroll
        for (int ks = 0; ks < 6; ++ks) { int k = ks + r6; k = k >= 6 ? k - 6 : k; ko[ks] = k >= 6 ? k - 6 : k; }
        ko[6] = 6;
    }
    u4 W[7][4];
    const u32 w2vo = (l >> 3) * 1024 + (((l & 7) ^ (((l >> 4) & 1) << 2)) * 16);
    auto w2_glds = [&](int p_, int ks, u32 dst) {
        const int q = ko[ks];
        const char* src = (const char*)w2 + (224 * p_ + (ks == 6 ? 96 : (q < 3 ? 32 * q : 16 + 32 * q))) * 1024 + fb * 128;
        glds16(src, w2vo, dst); glds16(src + 8192, w2vo, dst + 1024);
        glds16(src + (ks == 6 ? 112 : 16) * 1024, w2vo, dst + 2048); glds16(src + (ks == 6 ? 120 : 24) * 1024, w2vo, dst + 3072);
    };
    LAS char* w2rd = (LAS char*)smem + (4 * g + (m >> 2)) * 128 + (((m & 3) ^ (((m >> 3) & 1) << 2)) * 16);
    auto w2_read = [&](u4 (&f)[4], int off) {
#pragma unroll
        for (int t = 0; t < 4; ++t) {
            LAS char* p = (LAS char*)((u32)(uintptr_t)w2rd ^ ((t >> 1) * 64)) + off + (t & 1) * 8;
            u2 x0 = __builtin_bit_cast(u2, __builtin_amdgcn_ds_read_tr16_b64_v4i16((LAS s4*)p));
            u2 x1 = __builtin_bit_cast(u2, __builtin_amdgcn_ds_read_tr16_b64_v4i16((LAS s4*)(p + 2048)));
            f[t] = (u4){x0[0], x0[1], x1[0], x1[1]};
        }
    };
    auto bg = [&](int i0, int i1) { w1_burst(i0, i1 - i0); };
    u32 tag;
    if (aw) {
        u32 xd[8];
        const u32* xr = (const u32*)(x + (256 * (2 * w + ((m >> 1) & 1)) + b) * 512) + 8 * g + 2 * (m >> 2) + (m & 1);
#pragma unroll
        for (int j = 0; j < 8; ++j) xd[j] = xr[32 * j];
        if (tid < 8) hw[tid] = 0;
        FENCE();
        ST(2);
        {
            u4 FAre, FAim, Fre, Fim, Sre, Sim; u2 Mb; f2 pc[2], ps[2];
            consts0();
            float c0[8], s0[8];
#pragma unroll
            for (int j = 0; j < 8; ++j) {
                float a = (gf + 4.0f * j) * (mf * (1.0f / 32));
                c0[j] = cosr(a); s0[j] = m ? -sinr(a) : ((g & 1) ? -1.0f : 1.0f);
            }
            const float mo = (float)(2 * (m & 7) + 1) * (1.0f / 32);
#pragma unroll
            for (int u = 0; u < 4; ++u) {
                FAre[u] = pk_h(c0[2 * u], c0[2 * u + 1]); FAim[u] = pk_h(s0[2 * u], s0[2 * u + 1]);
                float cc = c16[u], ss = s16[u];
                Fre[u] = pk_h(cc, ss); Fim[u] = pk_h(-ss, cc);
                float a2 = (4.0f * gf + u) * mo;
                u32 re = pk_h(m < 8 ? cc : cosr(a2), 0.f), im = pk_h(m < 8 ? (m ? -ss : ((u & 1) ? -1.0f : 1.0f)) : -sinr(a2), 0.f);
                Sre[u] = m < 8 ? re : re << 16; Sim[u] = m < 8 ? im : im << 16;
                pc[u & 1][u >> 1] = twc[u >> 1][u & 1]; ps[u & 1][u >> 1] = tws[u >> 1][u & 1];
            }
#pragma unroll
            for (int e = 0; e < 2; ++e) {
                float a = (float)(((m >> 1) * (256 * (2 * g + e) + b)) & 2047) * (1.0f / 2048);
                float cc = cosr(a), ss = sinr(a);
                Mb[e] = (m & 1) ? pk_h(-ss, cc) : pk_h(cc, ss);
            }
            PIN(FAre); PIN(FAim); PIN(Fre); PIN(Fim); PIN(Sre); PIN(Sim); PIN(Mb); PIN(pc[0]); PIN(pc[1]); PIN(ps[0]); PIN(ps[1]);
            ST(3);
            f2 v[8];
#pragma unroll
            for (int j = 0; j < 8; ++j) v[j] = bf_2(xd[j]);
            f2 s2 = ((v[0] + v[1]) + (v[2] + v[3])) + ((v[4] + v[5]) + (v[6] + v[7]));
            const float mean = red_lanes<false>(s2[0] + s2[1]) * (1.0f / 512);
            f2 q0 = {0.f, 0.f}, q1 = {0.f, 0.f};
#pragma unroll
            for (int j = 0; j < 8; ++j) { v[j] -= mean; if (j & 1) q1 = fma2(v[j], v[j], q1); else q0 = fma2(v[j], v[j], q0); }
            q0 += q1;
            const float inv = ln_inv(red_lanes<false>(q0[0] + q0[1]) * (1.0f / 512));
            const float off = (mean - rbf(mean)) * inv;
#pragma unroll
            for (int j = 0; j < 8; ++j) v[j] = fma2(v[j], (f2){inv, inv}, (f2){off, off});
            u4 B[2];
#pragma unroll
            for (int p = 0; p < 2; ++p) {
                u4 A = {pk_h(v[0][p], v[1][p]), pk_h(v[2][p], v[3][p]), pk_h(v[4][p], v[5][p]), pk_h(v[6][p], v[7][p])};
                f4 Dre = mma_h(A, FAre, Z4), Dim = mma_h(A, FAim, Z4);
#pragma unroll
                for (int r = 0; r < 2; ++r) {
                    f2 re = r ? hi2(Dre) : lo2(Dre), im = r ? hi2(Dim) : lo2(Dim);
                    f2 tr = fma2(im, ps[p], re * pc[p]), ti = fma2(-re, ps[p], im * pc[p]);
                    B[r][p] = pk_h(tr[0], ti[0]); B[r][2 + p] = pk_h(tr[1], ti[1]);
                }
            }
#pragma unroll
            for (int r = 0; r < 2; ++r) {
                f4 Gre = mma_h(Fre, B[r], Z4), Gim = mma_h(Fim, B[r], Z4), Hre = mma_h(Sre, B[r], Z4), Him = mma_h(Sim, B[r], Z4);
                u4 Z;
#pragma unroll
                for (int i = 0; i < 4; ++i) Z[i] = pk_h(m ? Gre[i] : Hre[i], m ? Gim[i] : Him[i]);
                *(u4*)(scr + (2 * w + r) * 1024 + m * 64 + g * 16) = Z;
            }
            BARRIER();
            ST(18);
            u4 r0 = *(const u4*)(scr + (2 * g) * 1024 + w * 256 + m * 16);
            u4 r1 = *(const u4*)(scr + (2 * g + 1) * 1024 + w * 256 + m * 16);
#pragma unroll
            for (int t = 0; t < 4; ++t) {
                f4 D = mma16_h(Mb, (u2){r0[t], r1[t]}, Z4);
                *(u2*)(scr + SCR_S + w * 2048 + m * 128 + (((2 * t + (g >> 1)) ^ (m >> 1)) * 16) + (g & 1) * 8) =
                    (u2){clean(pk_h(D[0], D[1])), clean(pk_h(D[2], D[3]))};
            }
        }
        u4 uo0 = *(const u4*)(scr + SCR_S + w * 2048 + (l ^ ((l >> 4) & 7)) * 16);
        u4 uo1 = *(const u4*)(scr + SCR_S + w * 2048 + 1024 + (l ^ (((l >> 4) + 4) & 7)) * 16);
        u4 Fre, Fim, Gre, Gim, N0, N1; f2 tc[2], ts[2];
#pragma unroll
        for (int u = 0; u < 4; ++u) {
            float cc = c16[u], ss = s16[u];
            Fre[u] = pk_h(0.25f * cc, 0.25f * ss); Fim[u] = pk_h(-0.25f * ss, 0.25f * cc);
            Gre[u] = pk_b(cc * (1.0f / 256), -ss * (1.0f / 256)); Gim[u] = pk_b(ss * (1.0f / 256), cc * (1.0f / 256));
            float a3 = (4.0f * gf + u) * (mf * (1.0f / 256));
            tc[u >> 1][u & 1] = cosr(a3); ts[u >> 1][u & 1] = sinr(a3);
        }
        PIN(Fre); PIN(Fim); PIN(Gre); PIN(Gim); PIN(tc[0]); PIN(tc[1]); PIN(ts[0]); PIN(ts[1]);
        ST(19);
        BARRIER();
        ST(4);
        bst4<SC1>(uo0, rw, l * 16, WS_U + b * 8192 + w * 2048);
        bst4<SC1>(uo1, rw, l * 16 + 1024, WS_U + b * 8192 + w * 2048);
        xl[0] = *(const u2*)((const char*)x + xoff); xl[1] = *(const u2*)((const char*)x + xoff + 512);
        FENCE();
        tag = __builtin_amdgcn_readfirstlane(epoch) + 1u;
        consts3();
#pragma unroll
        for (int u = 0; u < 4; ++u) {
            float a1 = (float)(((4 * (g & 1) + u) * (256 * (m >> 1) + b)) & 2047) * (1.0f / 2048);
            float c1 = cosr(a1), s1 = sinr(a1);
            u32 nb = (m & 1) ? pk_b(s1, c1) : pk_b(c1, -s1);
            N0[u] = g < 2 ? nb : 0u; N1[u] = g < 2 ? 0u : nb;
        }
        PIN(N0); PIN(N1);
        WAITV(0);
        BARRIER();
        ST(5);
        u4 d[2], f[2], e[2];
        const int uo = (tid >> 1) * 8192 + (tid & 1) * 16;
        d[0] = bld4<SC1>(rw, uo, WS_U + b * 32); d[1] = bld4<SC1>(rw, uo, WS_U + 128 * 8192 + b * 32);
        const u32 fd0 = __builtin_amdgcn_raw_buffer_load_b32(rw, uo, WS_WT + b * 32, SC1);
        const u32 fd1 = __builtin_amdgcn_raw_buffer_load_b32(rw, uo, WS_WT + 128 * 8192 + b * 32, SC1);
        FENCE();
        if (any_sent(umax(max4(d[0]), max4(d[1])))) {
#pragma nounroll
            for (int sp = 0; sp < SPIN_MAX; ++sp) {
                __builtin_amdgcn_s_sleep(POLL_SLEEP);
                FENCE();
                d[0] = bld4<SC1>(rw, uo, WS_U + b * 32); d[1] = bld4<SC1>(rw, uo, WS_U + 128 * 8192 + b * 32);
                if (!any_sent(umax(max4(d[0]), max4(d[1])))) break;
            }
        }
        FENCE();
        f[0] = bld4<SC1>(rw, uo, WS_WT + b * 32); f[1] = bld4<SC1>(rw, uo, WS_WT + 128 * 8192 + b * 32);
        char* sp = scr + SCR_S + (tid ^ ((tid >> 4) & 1)) * 16;
        *(u4*)sp = d[0]; *(u4*)(sp + 4096) = d[1];
        BARRIER();
        if (b == 0) {
            f[0] = bld4<SC1>(rw, tid * 16, WS_WA); f[1] = bld4<SC1>(rw, tid * 16 + 4096, WS_WA);
            e[0] = bld4<SC1>(rw, tid * 16, WS_WB); e[1] = bld4<SC1>(rw, tid * 16 + 4096, WS_WB);
            if (tid == 0) __hip_atomic_store((GAS u32*)(ws + WS_EP), tag, __ATOMIC_RELAXED, __HIP_MEMORY_SCOPE_AGENT);
        }
        ST(6);
        u2 ld[4], wl[4];
        const int so = (64 * g + m) * 32 + (((w >> 1) ^ (m >> 3)) * 16) + (w & 1) * 8;
#pragma unroll
        for (int q = 0; q < 4; ++q) ld[q] = *(const u2*)(scr + SCR_S + so + 512 * q);
        FENCE();
        f4 Xre[2], Xim[2];
#pragma unroll
        for (int e = 0; e < 2; ++e) {
            u4 A = {ld[0][e], ld[1][e], ld[2][e], ld[3][e]};
            f4 Dre = mma_h(A, Fre, Z4), Dim = mma_h(A, Fim, Z4);
            f2 r0 = fma2(lo2(Dim), ts[0], lo2(Dre) * tc[0]), i0 = fma2(-lo2(Dre), ts[0], lo2(Dim) * tc[0]);
            f2 r1 = fma2(hi2(Dim), ts[1], hi2(Dre) * tc[1]), i1 = fma2(-hi2(Dre), ts[1], hi2(Dim) * tc[1]);
            u4 B = {pk_h(r0[0], i0[0]), pk_h(r0[1], i0[1]), pk_h(r1[0], i1[0]), pk_h(r1[1], i1[1])};
            Xre[e] = mma_h(Fre, B, Z4); Xim[e] = mma_h(Fim, B, Z4);
        }
        *(u4*)(sp + 8192) = f[0]; *(u4*)(sp + 12288) = f[1];
        asm volatile("" :: "v"(fd0), "v"(fd1));
        f2* sx = (f2*)(scr + SCR_X);
        if (b == 0) {
            *(u4*)(sp - SCR_S) = e[0]; *(u4*)(sp - SCR_S + 4096) = e[1];
#pragma unroll
            for (int e = 0; e < 2; ++e)
#pragma unroll
                for (int i = 0; i < 4; ++i) sx[2 * w + e + 8 * (m + 16 * (4 * g + i))] = (f2){Xre[e][i], Xim[e][i]};
        }
        BARRIER();
        ST(20);
#pragma unroll
        for (int q = 0; q < 4; ++q) wl[q] = *(const u2*)(scr + SCR_S + so + 8192 + 512 * q);
        FENCE();
        f2 yr[2][2], yi[2][2];
#pragma unroll
        for (int e = 0; e < 2; ++e)
#pragma unroll
            for (int hh = 0; hh < 2; ++hh) {
                f2 wre = {bf_lo(wl[2 * hh][e]), bf_lo(wl[2 * hh + 1][e])}, wim = {bf_hi(wl[2 * hh][e]), bf_hi(wl[2 * hh + 1][e])};
                f2 xr = hh ? hi2(Xre[e]) : lo2(Xre[e]), xi = hh ? hi2(Xim[e]) : lo2(Xim[e]);
                yr[e][hh] = fma2(-xi, wim, xr * wre); yi[e][hh] = fma2(xr, wim, xi * wre);
            }
        if (b == 0) {
#pragma unroll
            for (int i = 0; i < 4; ++i) {
                u2 wb = *(const u2*)(scr + so + 512 * i);
#pragma unroll
                for (int e = 0; e < 2; ++e) {
                    f2 xm = sx[(2048 - (2 * w + e + 8 * (m + 16 * (4 * g + i)))) & 2047];
                    float br = bf_lo(wb[e]), bi = bf_hi(wb[e]);
                    yr[e][i >> 1][i & 1] += xm[0] * br + xm[1] * bi;
                    yi[e][i >> 1][i & 1] += xm[0] * bi - xm[1] * br;
                }
            }
        }
        char* pv = scr + SCR_S;
#pragma unroll
        for (int e = 0; e < 2; ++e) {
            u4 Y = {pk_b(yr[e][0][0], yi[e][0][0]), pk_b(yr[e][0][1], yi[e][0][1]), pk_b(yr[e][1][0], yi[e][1][0]), pk_b(yr[e][1][1], yi[e][1][1])};
            f4 Dre = mma_b(Y, Gre, Z4), Dim = mma_b(Y, Gim, Z4);
            f2 r0 = fma2(-lo2(Dim), ts[0], lo2(Dre) * tc[0]), i0 = fma2(lo2(Dre), ts[0], lo2(Dim) * tc[0]);
            f2 r1 = fma2(-hi2(Dim), ts[1], hi2(Dre) * tc[1]), i1 = fma2(hi2(Dre), ts[1], hi2(Dim) * tc[1]);
            u4 A = {pk_b(r0[0], i0[0]), pk_b(r0[1], i0[1]), pk_b(r1[0], i1[0]), pk_b(r1[1], i1[1])};
            f4 Ere = mma_b(A, Gre, Z4), Eim = mma_b(A, Gim, Z4);
            u4 o;
#pragma unroll
            for (int i = 0; i < 4; ++i) o[i] = clean(pk_b(Ere[i], Eim[i]));
            *(u4*)(pv + (2 * w + e) * 1024 + ((m * 4 + g) ^ ((w >> 1) * 4)) * 16) = o;
        }
        BARRIER();
        ST(7);
#pragma unroll
        for (int it = 0; it < 2; ++it) {
            const int ch = tid + 256 * it;
            const u32* q = (const u32*)(pv + (ch & 1) * 4096 + (((ch >> 1) * 4) ^ ((ch & 1) * 64)));
            bst4<SC1>((u4){q[0], q[256], q[512], q[768]}, rw, ch * 16, WS_V + b * 8192);
        }
        WAITV(0);
        BARRIER();
        ST(8);
        const int vo = (2 * m + (g >> 1)) * 8192 + (g & 1) * 16, vs = WS_V + w * 64 * 8192 + b * 32;
        u4 v0, v1;
#pragma nounroll
        for (int sp = 0; sp < SPIN_MAX; ++sp) {
            FENCE();
            v0 = bld4<SC1>(rw, vo, vs); v1 = bld4<SC1>(rw, vo, vs + 32 * 8192);
            if (!any_sent(umax(max4(v0), max4(v1)))) break;
            __builtin_amdgcn_s_sleep(POLL_SLEEP);
        }
        ST(9);
        char* sz = scr + SCR_X + g * 2048 + w * 256 + m * 8;
#pragma unroll
        for (int hh = 0; hh < 2; ++hh) {
            f4 D0 = mma_b(N0, hh ? v1 : v0, Z4), D1 = mma_b(N1, hh ? v1 : v0, Z4);
            *(u2*)(sz + hh * 128) = (u2){pk_b(D0[0], D0[1]), pk_b(D1[0], D1[1])};
            *(u2*)(sz + 1024 + hh * 128) = (u2){pk_b(D0[2], D0[3]), pk_b(D1[2], D1[3])};
        }
    } else {
        auto announce = [&](int which) {
            if (l < 8) __hip_atomic_store((GAS u32*)(ws + WS_FA) + which * 2048 + l * 256 + b, tag, __ATOMIC_RELAXED, __HIP_MEMORY_SCOPE_AGENT);
            FENCE();
        };
        auto all_announced = [&](int which) {
#pragma nounroll
            for (int sp = 0; sp < SPIN_MAX; ++sp) {
                FENCE();
                u4 f = bld4<SC1>(rw, l * 16, WS_FA + which * 8192 + (blockIdx.x & 7) * 1024);
                if (__builtin_amdgcn_ballot_w64(f[0] != tag || f[1] != tag || f[2] != tag || f[3] != tag) == 0) break;
                __builtin_amdgcn_s_sleep(4);
            }
        };
        const rsrc_t rr = mkrs(wr, 1052672), ri = mkrs(wi, 1052672);
        const int j7 = l >> 3, p4 = (l >> 1) & 3, ee = l & 1;
        u2 fr[4], fi[4]; u32 sr_, si_;
        if (w == 4) {
            const int e = ee * 4 * 514 + (32 * j7 + 4 * p4) * 2;
#pragma unroll
            for (int i = 0; i < 4; ++i) { fr[i] = bld2<0>(rr, e + i * 514, 8 * b * 514); fi[i] = bld2<0>(ri, e + i * 514, 8 * b * 514); }
        } else if (w == 5) {
            const int e = (2045 - 8 * b) * 514 + 253 * 2 - ee * 4 * 514 - (32 * j7 + 4 * p4) * 2;
#pragma unroll
            for (int i = 1; i < 4; ++i) { fr[i] = bld2<0>(rr, e + (3 - i) * 514, 0); fi[i] = bld2<0>(ri, e + (3 - i) * 514, 0); }
            const int e0 = e + 3 * 514 - (b == 0 && !ee ? 2048 * 514 : 0);
            fr[0] = bld2<0>(rr, e0, 0); fi[0] = bld2<0>(ri, e0, 0);
            const int e1 = (8 * b + (l & 7)) * 514 + (16 + 32 * j7) * 2;
            sr_ = __builtin_amdgcn_raw_buffer_load_b16(rr, e1, 0, 0); si_ = __builtin_amdgcn_raw_buffer_load_b16(ri, e1, 0, 0);
        } else if (w == 6) {
            const int c = j7 & 3, f = 8 * b + (l & 7);
            const int e = ((c & 1) ? (2048 - f) & 2047 : f) * 514 + (c >> 1) * 512;
            sr_ = __builtin_amdgcn_raw_buffer_load_b16(rr, e, 0, 0); si_ = __builtin_amdgcn_raw_buffer_load_b16(ri, e, 0, 0);
        }
        FENCE();
        ST(2);
#pragma unroll
        for (int jj = 0; jj < 7; ++jj) {
            const int cj_ = (4 * jj) % 14, rj = 18 * jj + (4 * jj) / 14;
            w1o[jj] = (row0 + rj) * 3584 + (cch0 + cj_) * 16 + (cch0 + cj_ >= 14 ? 3584 - 224 : 0);
            PIN(w1o[jj]);
        }
        if (w == 7) {
#pragma unroll
            for (int i = 0; i < 8; ++i) bst4<SC1>(SENT4, rw, l * 16 + (i & 3) * 1024, WS_U + b * 8192 + (i >> 2) * 4096);
        }
        BARRIER();
        if (w < 6) {
            const int wo = p4 * 2048 + j7 * 32 + ee * 16, wso = WS_WT + b * 8192 + (w - 4) * 256;
#pragma unroll
            for (int c = 0; c < 4; ++c) {
                u4 o;
#pragma unroll
                for (int i = 0; i < 4; ++i)
                    o[i] = w == 4 ? __builtin_amdgcn_perm(fi[i][c >> 1], fr[i][c >> 1], (c & 1) ? 0x07060302u : 0x05040100u)
                                  : __builtin_amdgcn_perm(fi[i][1 - (c >> 1)], fr[i][1 - (c >> 1)], (c & 1) ? 0x05040100u : 0x07060302u) ^ 0x80000000u;
                bst4<SC1>(o, rw, wo + c * 512, wso);
            }
            if (w == 5) __builtin_amdgcn_raw_buffer_store_b32(sr_ | (si_ << 16), rw, l * 4, wso, SC1);
        } else if (w == 6) {
            float re = bf_lo(sr_), im = bf_lo(si_);
            re += dpp<0x128>(re); im -= dpp<0x128>(im);
            u32 a = __builtin_bit_cast(u32, re), a2 = a, c = __builtin_bit_cast(u32, im), c2 = c;
            asm("" : "+v"(a2), "+v"(c2));
            auto pr = __builtin_amdgcn_permlane16_swap(a, a2, false, false);
            auto pi = __builtin_amdgcn_permlane16_swap(c, c2, false, false);
            const float ar = __builtin_bit_cast(float, (u32)pr[0]), br = __builtin_bit_cast(float, (u32)pr[1]);
            const float ai = __builtin_bit_cast(float, (u32)pi[0]), bi = __builtin_bit_cast(float, (u32)pi[1]);
            if (l < 8) {
                __builtin_amdgcn_raw_buffer_store_b32(pk_b(0.25f * (ar + br), 0.25f * (ai + bi)), rw, l * 4, WS_WA + b * 32, SC1);
                __builtin_amdgcn_raw_buffer_store_b32(pk_b(0.25f * (ar - br), 0.25f * (ai - bi)), rw, l * 4, WS_WB + b * 32, SC1);
            }
        }
        ST(18);
        tag = __builtin_amdgcn_readfirstlane(epoch) + 1u;
        WAITV(0);
        if (w == 7) announce(0);
        ST(19);
        BARRIER();
        if (w == 7) all_announced(0);
        else __builtin_amdgcn_s_sleep(2);
        xl[0] = *(const u2*)((const char*)x + xoff); xl[1] = *(const u2*)((const char*)x + xoff + 512);
        FENCE();
        bg(0, BG2);
        ST(4);
        BARRIER();
        __builtin_amdgcn_s_sleep(2);
#pragma unroll
        for (int i = 0; i < 2; ++i) bst4<SC1>(SENT4, rw, l * 16 + i * 1024, WS_V + b * 8192 + (w - 4) * 2048);
        FENCE();
        consts0();
        consts3();
        bg(BG2, BG3);
        WAITV(BG3 - BG2);
        ST(5);
        BARRIER();
        if (w == 7) announce(1);
#pragma unroll
        for (int i = 0; i < 2; ++i) bst4<SC1>(SENT4, rw, (l >> 3) * 256 + (l & 7) * 16 + i * 2048, WS_LN2 + (b >> 1) * 16384 + (b & 1) * 128 + (w - 4) * 4096);
        bg(BG3, BG4);
        BARRIER();
        bg(BG4, BG5);
        WAITV(BG5 - BG3);
        ST(6);
        BARRIER();
        if (w == 7) all_announced(1);
        else __builtin_amdgcn_s_sleep(3);
        bg(BG5, BG6);
        ST(7);
        BARRIER();
        __builtin_amdgcn_s_sleep(4);
        bg(BG6, BG7);
    }
    BARRIER();
    if (!aw) bg(BG7, BG8);
    {
        u4 A = *(const u4*)(scr + SCR_X + w * 1024 + m * 64 + g * 16);
        f4 Dre = mma_b(A, Gre3, Z4), Dim = mma_b(A, Gim3, Z4), Hre = mma_b(A, SGre, Z4), Him = mma_b(A, SGim, Z4);
        if (g == 0) { Dre[0] = Hre[0]; Dim[0] = Him[0]; }
        f2 r0 = fma2(-lo2(Dim), tws[0], lo2(Dre) * twc[0]), i0 = fma2(lo2(Dre), tws[0], lo2(Dim) * twc[0]);
        f2 r1 = fma2(-hi2(Dim), tws[1], hi2(Dre) * twc[1]), i1 = fma2(hi2(Dre), tws[1], hi2(Dim) * twc[1]);
        u4 A2 = {pk_b(r0[0], i0[0]), pk_b(r0[1], i0[1]), pk_b(r1[0], i1[0]), pk_b(r1[1], i1[1])};
        f2 h[4]; u2 hp[2];
#pragma unroll
        for (int t = 0; t < 2; ++t) {
            f4 Yv = mma_b(A2, GA[t], Z4);
            u32 h0 = pk_b2(bf_2(xl[t][0]) + bf_2(pk_b(Yv[0], Yv[1]))), h1 = pk_b2(bf_2(xl[t][1]) + bf_2(pk_b(Yv[2], Yv[3])));
            hp[t] = (u2){h0, h1};
            h[2 * t] = bf_2(h0); h[2 * t + 1] = bf_2(h1);
        }
        f2 s2 = (h[0] + h[1]) + (h[2] + h[3]);
        const float mean = red_lanes<true>(s2[0] + s2[1]) * (1.0f / 512);
        f2 q2 = {0.f, 0.f};
#pragma unroll
        for (int j = 0; j < 4; ++j) { f2 d = h[j] - mean; q2 = fma2(d, d, q2); }
        const float inv = ln_inv(red_lanes<true>(q2[0] + q2[1]) * (1.0f / 512));
        const float mb = rbf(mean);
#pragma unroll
        for (int t = 0; t < 2; ++t)
            *(u2*)(scr + ((8 * t + (m >> 1)) * 4 + g) * 128 + (w ^ (m >> 1)) * 16 + (m & 1) * 8) =
                (u2){clean(pk_b2(bf_2(pk_b2(h[2 * t] - mb)) * inv)), clean(pk_b2(bf_2(pk_b2(h[2 * t + 1] - mb)) * inv))};
        WAITV(0);
        BARRIER();
        u4 lo = *(const u4*)(scr + (tid ^ ((tid >> 5) & 7)) * 16);
        bst4<SC1>(lo, rw, (tid >> 3) * 256 + (tid & 7) * 16, WS_LN2 + (b >> 1) * 16384 + (b & 1) * 128);
        FENCE();
        if (aw) WAITV(0); else __builtin_amdgcn_s_sleep(8);
        bst2<SC1>(hp[0], ro, xoff, 0); bst2<SC1>(hp[1], ro, xoff + 512, 0);
        FENCE();
        ST(10);
    }
    auto stage_out = [&](int r) {
        w2_read(W[r], LDS_SCR + (w & 3) * 8192 + (r & 1) * 4096);
        asm volatile("s_waitcnt lgkmcnt(0)" ::: "memory");
        PIN(W[r][0]); PIN(W[r][1]); PIN(W[r][2]); PIN(W[r][3]);
        if (r + 2 < 7) w2_glds(pw, r + 2, lds0 + LDS_SCR + (w & 3) * 8192 + (r & 1) * 4096);
    };
    auto publish = [&]() {
        u32 prev = 0;
        if (l == 0) prev = __hip_atomic_fetch_add(hw + 4, 1u, __ATOMIC_RELAXED, __HIP_MEMORY_SCOPE_WORKGROUP);
        if (__builtin_amdgcn_readfirstlane(prev) == 7u && l < 2)
            __hip_atomic_store(fl + (l ? 4096 + (b >> 1) * 32 + (b & 1) : (mt1 * 8 + (nt1 >> 1)) * 32 + (nt1 & 1)), tag, __ATOMIC_RELAXED, __HIP_MEMORY_SCOPE_AGENT);
        FENCE();
    };
    {
        const int tg = w & 3;
        char* xch = smem + XCH + tg * 3584;
        const int hoff = EXP_HOFF + WS_H1 + (mt1 * 8 + 2 * tg) * (56 * 1024) + (nt1 >> 1) * 7168;
        if (aw) {
            const int toff = WS_LN2 + (mt1 * 8 + 2 * tg) * 16384;
            LAS char* wb0 = (LAS char*)smem + (4 * g + (m >> 2)) * 224 + (m & 3) * 8;
            LAS char* wb[4];
            int tq[4];
#pragma unroll
            for (int q = 0; q < 4; ++q) { const int qq = (q + rq) & 3; wb[q] = wb0 + qq * 28672; tq[q] = toff + qq * 4096; }
            u4 T[2][16], A[2][7];
            auto ldt = [&](int kk) {
#pragma unroll
                for (int tt = 0; tt < 2; ++tt) T[tt][kk] = bld4<SC1>(rw, l * 16 + (kk & 3) * 1024, tq[kk >> 2] + tt * 16384);
            };
#pragma unroll
            for (int i = 0; i < 7; ++i) A[0][i] = ldw1(wb[0], 0, i);
            if (l == 0) __hip_atomic_fetch_add(hw + 5, 1u, __ATOMIC_RELAXED, __HIP_MEMORY_SCOPE_WORKGROUP);
            ST(11);
#pragma unroll
            for (int kk = 0; kk < G1_DEPTH; ++kk) { ldt(kk); SCHED(); }
            f4 acc[2][7];
#pragma unroll
            for (int tt = 0; tt < 2; ++tt)
#pragma unroll
                for (int i = 0; i < 7; ++i) acc[tt][i] = Z4;
#pragma unroll
            for (int kk = 0; kk < 16; ++kk) {
                if ((kk & 1) == 0) {
                    if (__builtin_expect(any_sent(umax(umax(max4(T[0][kk]), max4(T[1][kk])), umax(max4(T[0][kk + 1]), max4(T[1][kk + 1])))), 0)) {
                        wait_tok(T[0][kk], T[1][kk], rw, l * 16, tq[kk >> 2] + (kk & 3) * 1024);
                        wait_tok(T[0][kk + 1], T[1][kk + 1], rw, l * 16, tq[kk >> 2] + ((kk + 1) & 3) * 1024);
                    }
                    if (kk == 0) ST(12);
                    if (kk == (BG8 / 7) * 4 - 2) lds_wait(hw + 5, 8u);
                }
                if (kk + G1_DEPTH < 16) ldt(kk + G1_DEPTH);
                if (kk < 15) {
#pragma unroll
                    for (int i = 0; i < 7; ++i) A[(kk + 1) & 1][i] = ldw1(wb[(kk + 1) >> 2], (kk + 1) & 3, i);
                }
#pragma unroll
                for (int i = 0; i < 7; ++i)
#pragma unroll
                    for (int tt = 0; tt < 2; ++tt) acc[tt][i] = mma_b(A[kk & 1][i], T[tt][kk], acc[tt][i]);
#pragma unroll
                for (int i = 0; i < 14; ++i) {
                    __builtin_amdgcn_sched_group_barrier(0x8, 1, 0);
                    if (i == 1 || i == 7) __builtin_amdgcn_sched_group_barrier(0x20, 1, 0);
                    __builtin_amdgcn_sched_group_barrier(0x100, 1, 0);
                    if ((kk & 1) && i >= 3) __builtin_amdgcn_sched_group_barrier(0x2, 1, 0);
                }
                if (kk & 1) {} else SCHED();
            }
#pragma unroll
            for (int q = 0; q < 3; ++q) {
                u2 p0 = pk4(acc[1][2 * q]), p1 = pk4(acc[1][2 * q + 1]);
                *(u4*)(xch + q * 1024 + l * 16) = (u4){p0[0], p0[1], p1[0], p1[1]};
            }
            *(u2*)(xch + 3072 + l * 8) = pk4(acc[1][6]);
            lds_set(hw + tg, 1u);
            ST(13);
#pragma unroll
            for (int q = 0; q < 3; ++q) {
                u2 p0 = pk4(acc[0][2 * q]), p1 = pk4(acc[0][2 * q + 1]);
                bst4<SC1>(silu_pk4((u4){p0[0], p0[1], p1[0], p1[1]}), rw, l * 16, hoff + ((nt1 & 1) * 3 + q) * 1024);
            }
            { u2 p = pk4(acc[0][6]); bst2<SC1>(silu_pk2(p[0], p[1]), rw, l * 8, hoff + 6144 + (nt1 & 1) * 512); }
        } else {
            bg(BG8, 28);
            WAITV(0);
            if (l == 0) __hip_atomic_fetch_add(hw + 5, 1u, __ATOMIC_RELAXED, __HIP_MEMORY_SCOPE_WORKGROUP);
            __builtin_amdgcn_s_sleep(W2_SLEEP);
            lds_wait(hw + 5, 8u);
            w2_glds(pw, 0, lds0 + LDS_SCR + tg * 8192);
            w2_glds(pw, 1, lds0 + LDS_SCR + tg * 8192 + 4096);
#pragma unroll
            for (int r = 0; r < B_R; ++r) { WAITV(4); stage_out(r); }
            ST(12);
            lds_wait(hw + tg, 1u);
            ST(13);
#pragma unroll
            for (int q = 0; q < 3; ++q)
                bst4<SC1>(silu_pk4(*(const u4*)(xch + q * 1024 + l * 16)), rw, l * 16, hoff + 56 * 1024 + ((nt1 & 1) * 3 + q) * 1024);
            { u2 p = *(const u2*)(xch + 3072 + l * 8); bst2<SC1>(silu_pk2(p[0], p[1]), rw, l * 8, hoff + 56 * 1024 + 6144 + (nt1 & 1) * 512); }
        }
        if (l == 0) __hip_atomic_fetch_add(hw + 6, 1u, __ATOMIC_RELAXED, __HIP_MEMORY_SCOPE_WORKGROUP);
        ST(14);
        WAITV(0);
        publish();
        ST(21);
        if (aw) {
            lds_wait(hw + FLOOD_W, 8u);
#pragma unroll
            for (int ks = 0; ks < 7; ++ks) w2_glds(pw, ks, lds0 + tg * 28672 + ks * 4096);
        } else {
            stage_out(B_R);
#pragma unroll
            for (int r = B_R + 1; r < 7; ++r) { if (r + 1 < 7) WAITV(4); else WAITV(0); stage_out(r); }
        }
    }
    {
        ST(15);
        const int rot = w & 3;
        if (aw) {
            flag_wait(fl + ((l & 4) ? 4096 + (grp * 8 + th * 4 + rot) * 32 : (grp * 8 + (pw ^ ((l & 2) * 2))) * 32) + (l & 1), tag);
            lds_set(hw + 8 + rot, tag);
        } else {
#pragma nounroll
            for (int sp = 0; sp < SPIN_MAX; ++sp) {
                if (__builtin_amdgcn_readfirstlane(__hip_atomic_load(hw + 8 + rot, __ATOMIC_RELAXED, __HIP_MEMORY_SCOPE_WORKGROUP)) == tag) break;
                __builtin_amdgcn_s_sleep(1);
            }
            FENCE();
        }
        ST(16);
        const int toff = WS_H1 + (grp * 8 + th * 4) * (56 * 1024) + pw * 7168;
        int to[4];
#pragma unroll
        for (int tt = 0; tt < 4; ++tt) to[tt] = toff + ((tt + rot) & 3) * (56 * 1024);
        u4 T[7][4];
        auto ldtok = [&](int ks) {
#pragma unroll
            for (int tt = 0; tt < 4; ++tt) {
                if (ks < 6) T[ks][tt] = bld4<EXP_AUX>(rw, l * 16, to[tt] + ko[ks] * 1024);
                else {
                    u2 a = bld2<SC1>(rw, l * 8 + 2048, to[tt] + 4096), c = bld2<SC1>(rw, l * 8 + 2560, to[tt] + 4096);
                    T[ks][tt] = (u4){a[0], a[1], c[0], c[1]};
                }
            }
            SCHED();
        };
#pragma unroll
        for (int ks = 0; ks < G2_DEPTH; ++ks) ldtok(ks);
        if (aw) {
#pragma unroll
            for (int ks = 0; ks < 7; ++ks) w2_read(W[ks], rot * 28672 + ks * 4096);
            asm volatile("s_waitcnt lgkmcnt(0)" ::: "memory");
            if (l == 0) __hip_atomic_fetch_add(hw + 7, 1u, __ATOMIC_RELAXED, __HIP_MEMORY_SCOPE_WORKGROUP);
        }
        const int rho = grp * 128 + th * 64 + rot * 16 + m;
        const int ooff = (256 * (rho & 7) + (rho >> 3)) * 1024 + fb * 128 + (w >> 2) * 64 + g * 16;
        const u4 hres = bld4<SC1>(ro, ooff, 0);
        SCHED();
        f4 acc[4][4];
#pragma unroll
        for (int tt = 0; tt < 4; ++tt)
#pragma unroll
            for (int t = 0; t < 4; ++t) acc[tt][t] = Z4;
#pragma unroll
        for (int ks = 0; ks < 7; ++ks) {
            if (ks + G2_DEPTH < 7) { PIN(T[ks][0]); ldtok(ks + G2_DEPTH); }
#pragma unroll
            for (int tt = 0; tt < 4; ++tt)
#pragma unroll
                for (int t = 0; t < 4; ++t) {
                    acc[tt][t] = mma_b(W[ks][t], T[ks][tt], acc[tt][t]);
                }
            SCHED();
        }
        ST(17);
        lds_wait(hw + 7, 4u);
        char* dp = smem + w * 16384 + l * 16;
#pragma unroll
        for (int tt = 1; tt < 4; ++tt)
#pragma unroll
            for (int t = 0; t < 4; ++t) *(f4*)(dp + (((tt + rot) & 3) * 4 + t) * 1024) = acc[tt][t];
        f4 s0, s1;
        if (aw) { *(f4*)(dp + (rot * 4 + 2) * 1024) = acc[0][2]; *(f4*)(dp + (rot * 4 + 3) * 1024) = acc[0][3]; s0 = acc[0][0]; s1 = acc[0][1]; }
        else { *(f4*)(dp + (rot * 4) * 1024) = acc[0][0]; *(f4*)(dp + (rot * 4 + 1) * 1024) = acc[0][1]; s0 = acc[0][2]; s1 = acc[0][3]; }
        BARRIER();
        f4 r0[7], r1[7];
#pragma unroll
        for (int j = 1; j < 8; ++j) {
            const char* rp = smem + (((w + j) & 7) * 16 + rot * 4 + (w >> 2) * 2) * 1024 + l * 16;
            r0[j - 1] = *(const f4*)rp; r1[j - 1] = *(const f4*)(rp + 1024);
        }
        SCHED();
        s0 = ((s0 + r0[0]) + (r0[1] + r0[2])) + ((r0[3] + r0[4]) + (r0[5] + r0[6]));
        s1 = ((s1 + r1[0]) + (r1[1] + r1[2])) + ((r1[3] + r1[4]) + (r1[5] + r1[6]));
        u2 p0 = pk4(s0), p1 = pk4(s1);
        bst4<AUX_OUT>((u4){pk_b2(bf_2(hres[0]) + bf_2(p0[0])), pk_b2(bf_2(hres[1]) + bf_2(p0[1])),
                           pk_b2(bf_2(hres[2]) + bf_2(p1[0])), pk_b2(bf_2(hres[3]) + bf_2(p1[1]))}, ro, ooff, 0);
    }
    ST_END(ws, 0);
}

extern "C" void kernel_launch(void* const* d_in, const int* in_sizes, int n_in, void* d_out, int out_size,
                              void* d_ws, size_t ws_size, hipStream_t stream) {
    (void)hipFuncSetAttribute((const void*)k_fused, hipFuncAttributeMaxDynamicSharedMemorySize, LDS_B);
    k_fused<<<256, 512, LDS_B, stream>>>((const uint16_t*)d_in[0], (const uint16_t*)d_in[1], (const uint16_t*)d_in[2],
                                         (const uint16_t*)d_in[4], (const uint16_t*)d_in[5], (uint16_t*)d_out, (char*)d_ws);
}
```

```cpp
#define DIAG_PT 2
#include <hip/hip_runtime.h>
#include <hip/hip_bf16.h>
#include <stdint.h>

typedef _Float16 h8 __attribute__((ext_vector_type(8)));
typedef _Float16 h4 __attribute__((ext_vector_type(4)));
typedef _Float16 h2 __attribute__((ext_vector_type(2)));
typedef __bf16 b8 __attribute__((ext_vector_type(8)));
typedef __bf16 b2 __attribute__((ext_vector_type(2)));
typedef short s4 __attribute__((ext_vector_type(4)));
typedef float f4 __attribute__((ext_vector_type(4)));
typedef float f2 __attribute__((ext_vector_type(2)));
typedef unsigned u32;
typedef u32 u4 __attribute__((ext_vector_type(4)));
typedef u32 u2 __attribute__((ext_vector_type(2)));
#define LAS __attribute__((address_space(3)))
#define GAS __attribute__((address_space(1)))
#define DI __device__ __forceinline__
#define FENCE() asm volatile("" ::: "memory")
#define PIN(x) asm volatile("" : "+v"(x))
#define SCHED() __builtin_amdgcn_sched_barrier(0)
#define WAITV(n) asm volatile("s_waitcnt vmcnt(%0)" :: "n"(n) : "memory")
#define BARRIER() do { asm volatile("s_waitcnt lgkmcnt(0)" ::: "memory"); __builtin_amdgcn_s_barrier(); FENCE(); } while (0)
constexpr int SC1 = 16;
constexpr u32 SENT = 0xffffffffu;
constexpr int SPIN_MAX = 1 << 20;

DI u32 pk_h(float a, float b) { f2 v = {a, b}; h2 r = __builtin_convertvector(v, h2); return __builtin_bit_cast(u32, r); }
DI u32 pk_b(float a, float b) { f2 v = {a, b}; b2 r = __builtin_convertvector(v, b2); return __builtin_bit_cast(u32, r); }
DI float bf_lo(u32 v) { return __builtin_bit_cast(float, v << 16); }
DI float bf_hi(u32 v) { return __builtin_bit_cast(float, v & 0xffff0000u); }
DI f2 bf_2(u32 v) { return (f2){bf_lo(v), bf_hi(v)}; }
DI u32 pk_b2(f2 v) { return pk_b(v[0], v[1]); }
DI float rbf(float a) { return bf_lo(pk_b(a, a)); }
DI float cosr(float rev) { return __builtin_amdgcn_cosf(rev); }
DI float sinr(float rev) { return __builtin_amdgcn_sinf(rev); }
DI f2 lo2(f4 v) { return __builtin_shufflevector(v, v, 0, 1); }
DI f2 hi2(f4 v) { return __builtin_shufflevector(v, v, 2, 3); }
DI f2 fma2(f2 a, f2 b, f2 c) { return __builtin_elementwise_fma(a, b, c); }
DI u32 umin(u32 a, u32 b) { return a < b ? a : b; }
DI u32 umax(u32 a, u32 b) { return a > b ? a : b; }
DI u32 clean(u32 v) { return umin(v, SENT - 1); }
DI u32 max4(u4 v) { return umax(umax(umax(v[0], v[1]), v[2]), v[3]); }
#define Z4 ((f4){0, 0, 0, 0})
DI f4 mma_h(u4 a, u4 b, f4 c) { return __builtin_amdgcn_mfma_f32_16x16x32_f16(__builtin_bit_cast(h8, a), __builtin_bit_cast(h8, b), c, 0, 0, 0); }
DI f4 mma_b(u4 a, u4 b, f4 c) { return __builtin_amdgcn_mfma_f32_16x16x32_bf16(__builtin_bit_cast(b8, a), __builtin_bit_cast(b8, b), c, 0, 0, 0); }
DI f4 mma16_h(u2 a, u2 b, f4 c) { return __builtin_amdgcn_mfma_f32_16x16x16f16(__builtin_bit_cast(h4, a), __builtin_bit_cast(h4, b), c, 0, 0, 0); }
typedef __amdgpu_buffer_rsrc_t rsrc_t;
DI rsrc_t mkrs(const void* p, u32 bytes) { return __builtin_amdgcn_make_buffer_rsrc((void*)p, 0, bytes, 0x00020000); }
template <int AUX> DI u4 bld4(rsrc_t r, int v, int s) { return __builtin_amdgcn_raw_buffer_load_b128(r, v, s, AUX); }
template <int AUX> DI u2 bld2(rsrc_t r, int v, int s) { return __builtin_amdgcn_raw_buffer_load_b64(r, v, s, AUX); }
template <int AUX> DI void bst4(u4 d, rsrc_t r, int v, int s) {
    __builtin_amdgcn_raw_buffer_store_b128(d, r, v, s, AUX);
    asm volatile("s_nop 1" :: "v"(d));
}
template <int AUX> DI void bst2(u2 d, rsrc_t r, int v, int s) { __builtin_amdgcn_raw_buffer_store_b64(d, r, v, s, AUX); }

template <int CTRL> DI float dpp(float v) {
    return __builtin_bit_cast(float, __builtin_amdgcn_update_dpp(0, __builtin_bit_cast(int, v), CTRL, 0xf, 0xf, true));
}
template <bool ALL> DI float red_lanes(float v) {
    v += dpp<0xB1>(v);
    if (ALL) v += dpp<0x4E>(v);
    v += dpp<0x124>(v); v += dpp<0x128>(v);
    u32 x = __builtin_bit_cast(u32, v), y = x;
    asm("" : "+v"(y));
    auto r = __builtin_amdgcn_permlane16_swap(x, y, false, false);
    v = __builtin_bit_cast(float, (u32)r[0]) + __builtin_bit_cast(float, (u32)r[1]);
    x = __builtin_bit_cast(u32, v); y = x;
    asm("" : "+v"(y));
    auto q = __builtin_amdgcn_permlane32_swap(x, y, false, false);
    return __builtin_bit_cast(float, (u32)q[0]) + __builtin_bit_cast(float, (u32)q[1]);
}
DI float ln_inv(float var) { return __builtin_amdgcn_rcpf(rbf(__builtin_amdgcn_sqrtf(rbf(rbf(var) + 1e-5f)))); }

#define ST_DECL unsigned long long dg0_ = __builtin_amdgcn_s_memrealtime(), dg1_ = dg0_
#define ST(i) do { if (i == DIAG_PT) dg1_ = __builtin_amdgcn_s_memrealtime(); } while (0)
#define ST_END(ws, kid) do { WAITV(0); ST(22); unsigned long long e_ = __builtin_amdgcn_s_memrealtime() + (dg1_ - dg0_); if (aw) while (__builtin_amdgcn_s_memrealtime() < e_) __builtin_amdgcn_s_sleep(1); } while (0)

constexpr u32 WS_U = 0;
constexpr u32 WS_WT = WS_U + (2u << 20);
constexpr u32 WS_V = WS_WT + (2u << 20);
constexpr u32 WS_LN2 = WS_V + (2u << 20);
constexpr u32 WS_WA = WS_LN2 + (2u << 20);
constexpr u32 WS_WB = WS_WA + 8192u;
constexpr u32 WS_FA = WS_WB + 8192u;
constexpr u32 WS_FLAG = WS_FA + 16384u;
constexpr u32 WS_EP = WS_FLAG + 32768u;
constexpr u32 WS_H1 = WS_WA + (128u << 10);
constexpr u32 WS_END = WS_H1 + 7340032u;

#define W1_B0 0
#define W1_B1 7
#define W1_B2 5
#define W1_B3 6
#define W1_CHK ((((W1_B0 + W1_B1 + W1_B2 + W1_B3) / 7) * 4 - 2) < 0 ? 0 : (((W1_B0 + W1_B1 + W1_B2 + W1_B3) / 7) * 4 - 2))
#define CLR_SLEEP 6
#define BPRIO 2
#define EXP_UCLR 0
#define POLL_SLEEP 2
#define BG1 0
#define BG2 4
#define BG3 4
#define BG4 10
#define BG5 15
#define BG6 15
#define BG7 15
#define BG8 21
#define A_EARLY 24
#define SILU_PRIO 0
#define EXP_HOFF 0
#define B_EARLY 8
#define W2_DELAY 4
#define A_GENTLE 3
#define EXP_AUX SC1
#define FLAG_SLEEP 2
#define B_PREFLAG 8
#define B_R 5
#define G2_PRIO 0
#define EXP_ROT 1
#define B_EARLY_BAR 0
#define FLOOD_W 4
#define W2_SLEEP 8
#define AUX_OUT 2
#define W2_OUT 2
#define W2_ATHR 3
#define W2_EARLY 3
#define G1_DEPTH 10
#define G2_DEPTH 3
constexpr int LDS_SCR = 114688;
constexpr int SCR_X = 8448;
constexpr int SCR_S = SCR_X + 20480;
constexpr int SCR_F = 49024;
constexpr int XCH = 147456;
constexpr int LDS_B = 163840;

DI void glds16(const void* sbase, u32 voff, u32 lds_dst) {
    asm volatile("s_mov_b32 m0, %2\n\ts_nop 0\n\tglobal_load_lds_dwordx4 %0, %1" :: "v"(voff), "s"(sbase), "s"(lds_dst) : "memory");
}
DI void glds16x4(const void* sbase, u32 voff, u32 lds_dst) {
    asm volatile("s_mov_b32 m0, %2\n\ts_nop 0\n\tglobal_load_lds_dwordx4 %0, %1\n\tglobal_load_lds_dwordx4 %0, %1 offset:1024\n\t"
                 "global_load_lds_dwordx4 %0, %1 offset:2048\n\tglobal_load_lds_dwordx4 %0, %1 offset:3072"
                 :: "v"(voff), "s"(sbase), "s"(lds_dst) : "memory");
}
DI bool any_sent(u32 v) { return __builtin_amdgcn_ballot_w64(v == SENT) != 0; }
DI void wait_tok(u4& t0, u4& t1, rsrc_t rw, int vo, int so) {
    if (any_sent(umax(max4(t0), max4(t1)))) {
#pragma nounroll
        for (int sp = 0; sp < SPIN_MAX; ++sp) {
            FENCE();
            t0 = bld4<SC1>(rw, vo, so); t1 = bld4<SC1>(rw, vo, so + 16384);
            if (!any_sent(umax(max4(t0), max4(t1)))) break;
        }
    }
}
DI u4 ldw1(LAS char* wb, int kk, int ft) {
    s4 a0 = __builtin_amdgcn_ds_read_tr16_b64_v4i16((LAS s4*)(wb + kk * 32 * 224 + ft * 32));
    s4 a1 = __builtin_amdgcn_ds_read_tr16_b64_v4i16((LAS s4*)(wb + (kk * 32 + 16) * 224 + ft * 32));
    u2 x0 = __builtin_bit_cast(u2, a0), x1 = __builtin_bit_cast(u2, a1);
    return (u4){x0[0], x0[1], x1[0], x1[1]};
}
DI u2 pk4(f4 a) { return (u2){pk_b(a[0], a[1]), pk_b(a[2], a[3])}; }
DI u2 silu_pk2(u32 p0, u32 p1) {
    u32 o0, o1; float a0, a1, a2, a3, t0, t1, t2, t3;
    asm("v_lshlrev_b32 %2, 16, %10\n\tv_and_b32 %3, 0xffff0000, %10\n\tv_lshlrev_b32 %4, 16, %11\n\tv_and_b32 %5, 0xffff0000, %11\n\t"
        "v_mul_f32 %6, %12, %2\n\tv_mul_f32 %7, %12, %3\n\tv_mul_f32 %8, %12, %4\n\tv_mul_f32 %9, %12, %5\n\t"
        "v_exp_f32 %6, %6\n\tv_exp_f32 %7, %7\n\tv_exp_f32 %8, %8\n\tv_exp_f32 %9, %9\n\t"
        "v_add_f32 %6, 1.0, %6\n\tv_add_f32 %7, 1.0, %7\n\tv_add_f32 %8, 1.0, %8\n\tv_add_f32 %9, 1.0, %9\n\t"
        "v_rcp_f32 %6, %6\n\tv_rcp_f32 %7, %7\n\tv_rcp_f32 %8, %8\n\tv_rcp_f32 %9, %9\n\t"
        "v_mul_f32 %2, %2, %6\n\tv_mul_f32 %3, %3, %7\n\tv_mul_f32 %4, %4, %8\n\tv_mul_f32 %5, %5, %9\n\t"
        "v_cvt_pk_bf16_f32 %0, %2, %3\n\tv_cvt_pk_bf16_f32 %1, %4, %5"
        : "=&v"(o0), "=&v"(o1), "=&v"(a0), "=&v"(a1), "=&v"(a2), "=&v"(a3), "=&v"(t0), "=&v"(t1), "=&v"(t2), "=&v"(t3)
        : "v"(p0), "v"(p1), "s"(-1.4426950408889634f));
    return (u2){o0, o1};
}
DI u4 silu_pk4(u4 p) { u2 a = silu_pk2(p[0], p[1]), b = silu_pk2(p[2], p[3]); return (u4){a[0], a[1], b[0], b[1]}; }
DI void lds_set(LAS u32* p, u32 v) {
    asm volatile("s_waitcnt lgkmcnt(0)" ::: "memory");
    __hip_atomic_store(p, v, __ATOMIC_RELAXED, __HIP_MEMORY_SCOPE_WORKGROUP);
}
DI void lds_wait(LAS u32* p, u32 v) {
#pragma nounroll
    for (int sp = 0; sp < SPIN_MAX; ++sp) {
        if (__builtin_amdgcn_readfirstlane(__hip_atomic_load(p, __ATOMIC_RELAXED, __HIP_MEMORY_SCOPE_WORKGROUP)) >= v) break;
        __builtin_amdgcn_s_sleep(1);
    }
    FENCE();
}
DI void flag_wait(GAS u32* p, u32 tag) {
#pragma nounroll
    for (int sp = 0; sp < SPIN_MAX; ++sp) {
        if (__builtin_amdgcn_ballot_w64(__hip_atomic_load(p, __ATOMIC_RELAXED, __HIP_MEMORY_SCOPE_AGENT) != tag) == 0) break;
        __builtin_amdgcn_s_sleep(FLAG_SLEEP);
    }
    FENCE();
}
#define SENT4 ((u4){SENT, SENT, SENT, SENT})

__global__ __launch_bounds__(512, 2) void k_fused(const uint16_t* x, const uint16_t* wr, const uint16_t* wi, const uint16_t* w1,
                                                  const uint16_t* w2, uint16_t* out, char* ws) {
    extern __shared__ __attribute__((aligned(16))) char smem[];
    const int tid = threadIdx.x, l = tid & 63, g = l >> 4, m = l & 15;
    const int w = __builtin_amdgcn_readfirstlane(tid >> 6);
    const int b = (blockIdx.x & 7) * 32 + (blockIdx.x >> 3);
    const int mt1 = (b >> 6) * 4 + ((b >> 3) & 3), nt1 = ((b >> 5) & 1) * 8 + (b & 7);
    const int grp = b >> 4, th = (b >> 3) & 1, fb = b & 7;
    const rsrc_t rw = mkrs(ws, WS_END), ro = mkrs(out, 2097152);
    char* scr = smem + LDS_SCR;
    LAS u32* hw = (LAS u32*)((LAS char*)smem + LDS_SCR + SCR_F);
    const u32 lds0 = (u32)(uintptr_t)(LAS char*)smem;
    const float mf = (float)m, gf = (float)g, sg = (m & 1) ? -1.0f : 1.0f;
    const bool aw = w < 4;
    ST_DECL;
    const int rq = ((b >> 3) + b) & 3;
    const int row0 = (tid & 255) / 14, cch0 = (tid & 255) - row0 * 14;
    const char* wsrc = (const char*)w1 + nt1 * 224;
    u32 w1o[7];
    auto w1_burst = [&](int i0, int n) {
#pragma unroll
        for (int i = i0; i < i0 + n; ++i) {
            const int q = (i / 7 + rq) & 3, j = i % 7;
            glds16(wsrc + q * (128 * 3584), w1o[j], lds0 + (w - 4) * 1024 + q * 28672 + j * 4096);
        }
    };
    const u32 epoch = __hip_atomic_load((GAS u32*)(ws + WS_EP), __ATOMIC_RELAXED, __HIP_MEMORY_SCOPE_AGENT);
    FENCE();
    const int xoff = (256 * w + b) * 1024 + m * 32 + g * 8;
    u2 xl[2];
    float c16[4], s16[4];
    f2 twc[2], tws[2];
    u4 Gre3, Gim3, SGre, SGim, GA[2];
    auto consts0 = [&]() {
#pragma unroll
        for (int u = 0; u < 4; ++u) {
            float a = (4.0f * gf + u) * (mf * (1.0f / 16));
            c16[u] = cosr(a); s16[u] = sinr(a);
            twc[u >> 1][u & 1] = cosr(a * (1.0f / 32)); tws[u >> 1][u & 1] = sinr(a * (1.0f / 32));
        }
    };
    auto consts3 = [&]() {
#pragma unroll
        for (int u = 0; u < 4; ++u) {
            const float k = 4.0f * gf + u;
            const bool k0 = u == 0 && g == 0;
            float cc = c16[u], ss = s16[u];
            Gre3[u] = pk_b(cc, -ss); Gim3[u] = pk_b(ss, cc);
            float a2 = (2.0f * k + 1.0f) * (mf * (1.0f / 32));
            u32 sp = pk_b(g < 2 ? 2.f * cc : 2.f * sg * cosr(a2), g < 2 ? -2.f * ss : -2.f * sg * sinr(a2));
            SGre[u] = k0 ? pk_b(1.0f, sg) : (g < 2 ? sp : 0u);
            SGim[u] = g < 2 ? 0u : sp;
            float a4 = k * (mf * (1.0f / 32)), c4 = 2.f * cosr(a4), s4_ = -2.f * sinr(a4);
            GA[0][u] = k0 ? pk_b(1.0f, sg) : pk_b(c4, s4_);
            GA[1][u] = k0 ? pk_b(1.0f, sg) : ((u & 1) ? pk_b(-c4, -s4_) : pk_b(c4, s4_));
        }
        PIN(Gre3); PIN(Gim3); PIN(SGre); PIN(SGim); PIN(GA[0]); PIN(GA[1]); PIN(twc[0]); PIN(twc[1]); PIN(tws[0]); PIN(tws[1]);
    };
    const int pw = (w + 4 * th) & 7;
    GAS u32* fl = (GAS u32*)(ws + WS_FLAG);
    int ko[7];
    {
        const int r6 = EXP_ROT * ((fb < 6 ? fb : fb - 5) + 3 * (grp & 1));
#pragma unroll
        for (int ks = 0; ks < 6; ++ks) { int k = ks + r6; k = k >= 6 ? k - 6 : k; ko[ks] = k >= 6 ? k - 6 : k; }
        ko[6] = 6;
    }
    u4 W[7][4];
    const u32 w2vo = (l >> 3) * 1024 + (((l & 7) ^ (((l >> 4) & 1) << 2)) * 16);
    auto w2_glds = [&](int p_, int ks, u32 dst) {
        const int q = ko[ks];
        const char* src = (const char*)w2 + (224 * p_ + (ks == 6 ? 96 : (q < 3 ? 32 * q : 16 + 32 * q))) * 1024 + fb * 128;
        glds16(src, w2vo, dst); glds16(src + 8192, w2vo, dst + 1024);
        glds16(src + (ks == 6 ? 112 : 16) * 1024, w2vo, dst + 2048); glds16(src + (ks == 6 ? 120 : 24) * 1024, w2vo, dst + 3072);
    };
    LAS char* w2rd = (LAS char*)smem + (4 * g + (m >> 2)) * 128 + (((m & 3) ^ (((m >> 3) & 1) << 2)) * 16);
    auto w2_read = [&](u4 (&f)[4], int off) {
#pragma unroll
        for (int t = 0; t < 4; ++t) {
            LAS char* p = (LAS char*)((u32)(uintptr_t)w2rd ^ ((t >> 1) * 64)) + off + (t & 1) * 8;
            u2 x0 = __builtin_bit_cast(u2, __builtin_amdgcn_ds_read_tr16_b64_v4i16((LAS s4*)p));
            u2 x1 = __builtin_bit_cast(u2, __builtin_amdgcn_ds_read_tr16_b64_v4i16((LAS s4*)(p + 2048)));
            f[t] = (u4){x0[0], x0[1], x1[0], x1[1]};
        }
    };
    auto bg = [&](int i0, int i1) { w1_burst(i0, i1 - i0); };
    u32 tag;
    if (aw) {
        u32 xd[8];
        const u32* xr = (const u32*)(x + (256 * (2 * w + ((m >> 1) & 1)) + b) * 512) + 8 * g + 2 * (m >> 2) + (m & 1);
#pragma unroll
        for (int j = 0; j < 8; ++j) xd[j] = xr[32 * j];
        if (tid < 8) hw[tid] = 0;
        FENCE();
        ST(2);
        {
            u4 FAre, FAim, Fre, Fim, Sre, Sim; u2 Mb; f2 pc[2], ps[2];
            consts0();
            float c0[8], s0[8];
#pragma unroll
            for (int j = 0; j < 8; ++j) {
                float a = (gf + 4.0f * j) * (mf * (1.0f / 32));
                c0[j] = cosr(a); s0[j] = m ? -sinr(a) : ((g & 1) ? -1.0f : 1.0f);
            }
            const float mo = (float)(2 * (m & 7) + 1) * (1.0f / 32);
#pragma unroll
            for (int u = 0; u < 4; ++u) {
                FAre[u] = pk_h(c0[2 * u], c0[2 * u + 1]); FAim[u] = pk_h(s0[2 * u], s0[2 * u + 1]);
                float cc = c16[u], ss = s16[u];
                Fre[u] = pk_h(cc, ss); Fim[u] = pk_h(-ss, cc);
                float a2 = (4.0f * gf + u) * mo;
                u32 re = pk_h(m < 8 ? cc : cosr(a2), 0.f), im = pk_h(m < 8 ? (m ? -ss : ((u & 1) ? -1.0f : 1.0f)) : -sinr(a2), 0.f);
                Sre[u] = m < 8 ? re : re << 16; Sim[u] = m < 8 ? im : im << 16;
                pc[u & 1][u >> 1] = twc[u >> 1][u & 1]; ps[u & 1][u >> 1] = tws[u >> 1][u & 1];
            }
#pragma unroll
            for (int e = 0; e < 2; ++e) {
                float a = (float)(((m >> 1) * (256 * (2 * g + e) + b)) & 2047) * (1.0f / 2048);
                float cc = cosr(a), ss = sinr(a);
                Mb[e] = (m & 1) ? pk_h(-ss, cc) : pk_h(cc, ss);
            }
            PIN(FAre); PIN(FAim); PIN(Fre); PIN(Fim); PIN(Sre); PIN(Sim); PIN(Mb); PIN(pc[0]); PIN(pc[1]); PIN(ps[0]); PIN(ps[1]);
            ST(3);
            f2 v[8];
#pragma unroll
            for (int j = 0; j < 8; ++j) v[j] = bf_2(xd[j]);
            f2 s2 = ((v[0] + v[1]) + (v[2] + v[3])) + ((v[4] + v[5]) + (v[6] + v[7]));
            const float mean = red_lanes<false>(s2[0] + s2[1]) * (1.0f / 512);
            f2 q0 = {0.f, 0.f}, q1 = {0.f, 0.f};
#pragma unroll
            for (int j = 0; j < 8; ++j) { v[j] -= mean; if (j & 1) q1 = fma2(v[j], v[j], q1); else q0 = fma2(v[j], v[j], q0); }
            q0 += q1;
            const float inv = ln_inv(red_lanes<false>(q0[0] + q0[1]) * (1.0f / 512));
            const float off = (mean - rbf(mean)) * inv;
#pragma unroll
            for (int j = 0; j < 8; ++j) v[j] = fma2(v[j], (f2){inv, inv}, (f2){off, off});
            u4 B[2];
#pragma unroll
            for (int p = 0; p < 2; ++p) {
                u4 A = {pk_h(v[0][p], v[1][p]), pk_h(v[2][p], v[3][p]), pk_h(v[4][p], v[5][p]), pk_h(v[6][p], v[7][p])};
                f4 Dre = mma_h(A, FAre, Z4), Dim = mma_h(A, FAim, Z4);
#pragma unroll
                for (int r = 0; r < 2; ++r) {
                    f2 re = r ? hi2(Dre) : lo2(Dre), im = r ? hi2(Dim) : lo2(Dim);
                    f2 tr = fma2(im, ps[p], re * pc[p]), ti = fma2(-re, ps[p], im * pc[p]);
                    B[r][p] = pk_h(tr[0], ti[0]); B[r][2 + p] = pk_h(tr[1], ti[1]);
                }
            }
#pragma unroll
            for (int r = 0; r < 2; ++r) {
                f4 Gre = mma_h(Fre, B[r], Z4), Gim = mma_h(Fim, B[r], Z4), Hre = mma_h(Sre, B[r], Z4), Him = mma_h(Sim, B[r], Z4);
                u4 Z;
#pragma unroll
                for (int i = 0; i < 4; ++i) Z[i] = pk_h(m ? Gre[i] : Hre[i], m ? Gim[i] : Him[i]);
                *(u4*)(scr + (2 * w + r) * 1024 + m * 64 + g * 16) = Z;
            }
            BARRIER();
            ST(18);
            u4 r0 = *(const u4*)(scr + (2 * g) * 1024 + w * 256 + m * 16);
            u4 r1 = *(const u4*)(scr + (2 * g + 1) * 1024 + w * 256 + m * 16);
#pragma unroll
            for (int t = 0; t < 4; ++t) {
                f4 D = mma16_h(Mb, (u2){r0[t], r1[t]}, Z4);
                *(u2*)(scr + SCR_S + w * 2048 + m * 128 + (((2 * t + (g >> 1)) ^ (m >> 1)) * 16) + (g & 1) * 8) =
                    (u2){clean(pk_h(D[0], D[1])), clean(pk_h(D[2], D[3]))};
            }
        }
        u4 uo0 = *(const u4*)(scr + SCR_S + w * 2048 + (l ^ ((l >> 4) & 7)) * 16);
        u4 uo1 = *(const u4*)(scr + SCR_S + w * 2048 + 1024 + (l ^ (((l >> 4) + 4) & 7)) * 16);
        u4 Fre, Fim, Gre, Gim, N0, N1; f2 tc[2], ts[2];
#pragma unroll
        for (int u = 0; u < 4; ++u) {
            float cc = c16[u], ss = s16[u];
            Fre[u] = pk_h(0.25f * cc, 0.25f * ss); Fim[u] = pk_h(-0.25f * ss, 0.25f * cc);
            Gre[u] = pk_b(cc * (1.0f / 256), -ss * (1.0f / 256)); Gim[u] = pk_b(ss * (1.0f / 256), cc * (1.0f / 256));
            float a3 = (4.0f * gf + u) * (mf * (1.0f / 256));
            tc[u >> 1][u & 1] = cosr(a3); ts[u >> 1][u & 1] = sinr(a3);
        }
        PIN(Fre); PIN(Fim); PIN(Gre); PIN(Gim); PIN(tc[0]); PIN(tc[1]); PIN(ts[0]); PIN(ts[1]);
        ST(19);
        BARRIER();
        ST(4);
        bst4<SC1>(uo0, rw, l * 16, WS_U + b * 8192 + w * 2048);
        bst4<SC1>(uo1, rw, l * 16 + 1024, WS_U + b * 8192 + w * 2048);
        xl[0] = *(const u2*)((const char*)x + xoff); xl[1] = *(const u2*)((const char*)x + xoff + 512);
        FENCE();
        tag = __builtin_amdgcn_readfirstlane(epoch) + 1u;
        consts3();
#pragma unroll
        for (int u = 0; u < 4; ++u) {
            float a1 = (float)(((4 * (g & 1) + u) * (256 * (m >> 1) + b)) & 2047) * (1.0f / 2048);
            float c1 = cosr(a1), s1 = sinr(a1);
            u32 nb = (m & 1) ? pk_b(s1, c1) : pk_b(c1, -s1);
            N0[u] = g < 2 ? nb : 0u; N1[u] = g < 2 ? 0u : nb;
        }
        PIN(N0); PIN(N1);
        WAITV(0);
        BARRIER();
        ST(5);
        u4 d[2], f[2], e[2];
        const int uo = (tid >> 1) * 8192 + (tid & 1) * 16;
        d[0] = bld4<SC1>(rw, uo, WS_U + b * 32); d[1] = bld4<SC1>(rw, uo, WS_U + 128 * 8192 + b * 32);
        const u32 fd0 = __builtin_amdgcn_raw_buffer_load_b32(rw, uo, WS_WT + b * 32, SC1);
        const u32 fd1 = __builtin_amdgcn_raw_buffer_load_b32(rw, uo, WS_WT + 128 * 8192 + b * 32, SC1);
        FENCE();
        if (any_sent(umax(max4(d[0]), max4(d[1])))) {
#pragma nounroll
            for (int sp = 0; sp < SPIN_MAX; ++sp) {
                __builtin_amdgcn_s_sleep(POLL_SLEEP);
                FENCE();
                d[0] = bld4<SC1>(rw, uo, WS_U + b * 32); d[1] = bld4<SC1>(rw, uo, WS_U + 128 * 8192 + b * 32);
                if (!any_sent(umax(max4(d[0]), max4(d[1])))) break;
            }
        }
        FENCE();
        f[0] = bld4<SC1>(rw, uo, WS_WT + b * 32); f[1] = bld4<SC1>(rw, uo, WS_WT + 128 * 8192 + b * 32);
        char* sp = scr + SCR_S + (tid ^ ((tid >> 4) & 1)) * 16;
        *(u4*)sp = d[0]; *(u4*)(sp + 4096) = d[1];
        BARRIER();
        if (b == 0) {
            f[0] = bld4<SC1>(rw, tid * 16, WS_WA); f[1] = bld4<SC1>(rw, tid * 16 + 4096, WS_WA);
            e[0] = bld4<SC1>(rw, tid * 16, WS_WB); e[1] = bld4<SC1>(rw, tid * 16 + 4096, WS_WB);
            if (tid == 0) __hip_atomic_store((GAS u32*)(ws + WS_EP), tag, __ATOMIC_RELAXED, __HIP_MEMORY_SCOPE_AGENT);
        }
        ST(6);
        u2 ld[4], wl[4];
        const int so = (64 * g + m) * 32 + (((w >> 1) ^ (m >> 3)) * 16) + (w & 1) * 8;
#pragma unroll
        for (int q = 0; q < 4; ++q) ld[q] = *(const u2*)(scr + SCR_S + so + 512 * q);
        FENCE();
        f4 Xre[2], Xim[2];
#pragma unroll
        for (int e = 0; e < 2; ++e) {
            u4 A = {ld[0][e], ld[1][e], ld[2][e], ld[3][e]};
            f4 Dre = mma_h(A, Fre, Z4), Dim = mma_h(A, Fim, Z4);
            f2 r0 = fma2(lo2(Dim), ts[0], lo2(Dre) * tc[0]), i0 = fma2(-lo2(Dre), ts[0], lo2(Dim) * tc[0]);
            f2 r1 = fma2(hi2(Dim), ts[1], hi2(Dre) * tc[1]), i1 = fma2(-hi2(Dre), ts[1], hi2(Dim) * tc[1]);
            u4 B = {pk_h(r0[0], i0[0]), pk_h(r0[1], i0[1]), pk_h(r1[0], i1[0]), pk_h(r1[1], i1[1])};
            Xre[e] = mma_h(Fre, B, Z4); Xim[e] = mma_h(Fim, B, Z4);
        }
        *(u4*)(sp + 8192) = f[0]; *(u4*)(sp + 12288) = f[1];
        asm volatile("" :: "v"(fd0), "v"(fd1));
        f2* sx = (f2*)(scr + SCR_X);
        if (b == 0) {
            *(u4*)(sp - SCR_S) = e[0]; *(u4*)(sp - SCR_S + 4096) = e[1];
#pragma unroll
            for (int e = 0; e < 2; ++e)
#pragma unroll
                for (int i = 0; i < 4; ++i) sx[2 * w + e + 8 * (m + 16 * (4 * g + i))] = (f2){Xre[e][i], Xim[e][i]};
        }
        BARRIER();
        ST(20);
#pragma unroll
        for (int q = 0; q < 4; ++q) wl[q] = *(const u2*)(scr + SCR_S + so + 8192 + 512 * q);
        FENCE();
        f2 yr[2][2], yi[2][2];
#pragma unroll
        for (int e = 0; e < 2; ++e)
#pragma unroll
            for (int hh = 0; hh < 2; ++hh) {
                f2 wre = {bf_lo(wl[2 * hh][e]), bf_lo(wl[2 * hh + 1][e])}, wim = {bf_hi(wl[2 * hh][e]), bf_hi(wl[2 * hh + 1][e])};
                f2 xr = hh ? hi2(Xre[e]) : lo2(Xre[e]), xi = hh ? hi2(Xim[e]) : lo2(Xim[e]);
                yr[e][hh] = fma2(-xi, wim, xr * wre); yi[e][hh] = fma2(xr, wim, xi * wre);
            }
        if (b == 0) {
#pragma unroll
            for (int i = 0; i < 4; ++i) {
                u2 wb = *(const u2*)(scr + so + 512 * i);
#pragma unroll
                for (int e = 0; e < 2; ++e) {
                    f2 xm = sx[(2048 - (2 * w + e + 8 * (m + 16 * (4 * g + i)))) & 2047];
                    float br = bf_lo(wb[e]), bi = bf_hi(wb[e]);
                    yr[e][i >> 1][i & 1] += xm[0] * br + xm[1] * bi;
                    yi[e][i >> 1][i & 1] += xm[0] * bi - xm[1] * br;
                }
            }
        }
        char* pv = scr + SCR_S;
#pragma unroll
        for (int e = 0; e < 2; ++e) {
            u4 Y = {pk_b(yr[e][0][0], yi[e][0][0]), pk_b(yr[e][0][1], yi[e][0][1]), pk_b(yr[e][1][0], yi[e][1][0]), pk_b(yr[e][1][1], yi[e][1][1])};
            f4 Dre = mma_b(Y, Gre, Z4), Dim = mma_b(Y, Gim, Z4);
            f2 r0 = fma2(-lo2(Dim), ts[0], lo2(Dre) * tc[0]), i0 = fma2(lo2(Dre), ts[0], lo2(Dim) * tc[0]);
            f2 r1 = fma2(-hi2(Dim), ts[1], hi2(Dre) * tc[1]), i1 = fma2(hi2(Dre), ts[1], hi2(Dim) * tc[1]);
            u4 A = {pk_b(r0[0], i0[0]), pk_b(r0[1], i0[1]), pk_b(r1[0], i1[0]), pk_b(r1[1], i1[1])};
            f4 Ere = mma_b(A, Gre, Z4), Eim = mma_b(A, Gim, Z4);
            u4 o;
#pragma unroll
            for (int i = 0; i < 4; ++i) o[i] = clean(pk_b(Ere[i], Eim[i]));
            *(u4*)(pv + (2 * w + e) * 1024 + ((m * 4 + g) ^ ((w >> 1) * 4)) * 16) = o;
        }
        BARRIER();
        ST(7);
#pragma unroll
        for (int it = 0; it < 2; ++it) {
            const int ch = tid + 256 * it;
            const u32* q = (const u32*)(pv + (ch & 1) * 4096 + (((ch >> 1) * 4) ^ ((ch & 1) * 64)));
            bst4<SC1>((u4){q[0], q[256], q[512], q[768]}, rw, ch * 16, WS_V + b * 8192);
        }
        WAITV(0);
        BARRIER();
        ST(8);
        const int vo = (2 * m + (g >> 1)) * 8192 + (g & 1) * 16, vs = WS_V + w * 64 * 8192 + b * 32;
        u4 v0, v1;
#pragma nounroll
        for (int sp = 0; sp < SPIN_MAX; ++sp) {
            FENCE();
            v0 = bld4<SC1>(rw, vo, vs); v1 = bld4<SC1>(rw, vo, vs + 32 * 8192);
            if (!any_sent(umax(max4(v0), max4(v1)))) break;
            __builtin_amdgcn_s_sleep(POLL_SLEEP);
        }
        ST(9);
        char* sz = scr + SCR_X + g * 2048 + w * 256 + m * 8;
#pragma unroll
        for (int hh = 0; hh < 2; ++hh) {
            f4 D0 = mma_b(N0, hh ? v1 : v0, Z4), D1 = mma_b(N1, hh ? v1 : v0, Z4);
            *(u2*)(sz + hh * 128) = (u2){pk_b(D0[0], D0[1]), pk_b(D1[0], D1[1])};
            *(u2*)(sz + 1024 + hh * 128) = (u2){pk_b(D0[2], D0[3]), pk_b(D1[2], D1[3])};
        }
    } else {
        auto announce = [&](int which) {
            if (l < 8) __hip_atomic_store((GAS u32*)(ws + WS_FA) + which * 2048 + l * 256 + b, tag, __ATOMIC_RELAXED, __HIP_MEMORY_SCOPE_AGENT);
            FENCE();
        };
        auto all_announced = [&](int which) {
#pragma nounroll
            for (int sp = 0; sp < SPIN_MAX; ++sp) {
                FENCE();
                u4 f = bld4<SC1>(rw, l * 16, WS_FA + which * 8192 + (blockIdx.x & 7) * 1024);
                if (__builtin_amdgcn_ballot_w64(f[0] != tag || f[1] != tag || f[2] != tag || f[3] != tag) == 0) break;
                __builtin_amdgcn_s_sleep(4);
            }
        };
        const rsrc_t rr = mkrs(wr, 1052672), ri = mkrs(wi, 1052672);
        const int j7 = l >> 3, p4 = (l >> 1) & 3, ee = l & 1;
        u2 fr[4], fi[4]; u32 sr_, si_;
        if (w == 4) {
            const int e = ee * 4 * 514 + (32 * j7 + 4 * p4) * 2;
#pragma unroll
            for (int i = 0; i < 4; ++i) { fr[i] = bld2<0>(rr, e + i * 514, 8 * b * 514); fi[i] = bld2<0>(ri, e + i * 514, 8 * b * 514); }
        } else if (w == 5) {
            const int e = (2045 - 8 * b) * 514 + 253 * 2 - ee * 4 * 514 - (32 * j7 + 4 * p4) * 2;
#pragma unroll
            for (int i = 1; i < 4; ++i) { fr[i] = bld2<0>(rr, e + (3 - i) * 514, 0); fi[i] = bld2<0>(ri, e + (3 - i) * 514, 0); }
            const int e0 = e + 3 * 514 - (b == 0 && !ee ? 2048 * 514 : 0);
            fr[0] = bld2<0>(rr, e0, 0); fi[0] = bld2<0>(ri, e0, 0);
            const int e1 = (8 * b + (l & 7)) * 514 + (16 + 32 * j7) * 2;
            sr_ = __builtin_amdgcn_raw_buffer_load_b16(rr, e1, 0, 0); si_ = __builtin_amdgcn_raw_buffer_load_b16(ri, e1, 0, 0);
        } else if (w == 6) {
            const int c = j7 & 3, f = 8 * b + (l & 7);
            const int e = ((c & 1) ? (2048 - f) & 2047 : f) * 514 + (c >> 1) * 512;
            sr_ = __builtin_amdgcn_raw_buffer_load_b16(rr, e, 0, 0); si_ = __builtin_amdgcn_raw_buffer_load_b16(ri, e, 0, 0);
        }
        FENCE();
        ST(2);
#pragma unroll
        for (int jj = 0; jj < 7; ++jj) {
            const int cj_ = (4 * jj) % 14, rj = 18 * jj + (4 * jj) / 14;
            w1o[jj] = (row0 + rj) * 3584 + (cch0 + cj_) * 16 + (cch0 + cj_ >= 14 ? 3584 - 224 : 0);
            PIN(w1o[jj]);
        }
        if (w == 7) {
#pragma unroll
            for (int i = 0; i < 8; ++i) bst4<SC1>(SENT4, rw, l * 16 + (i & 3) * 1024, WS_U + b * 8192 + (i >> 2) * 4096);
        }
        BARRIER();
        if (w < 6) {
            const int wo = p4 * 2048 + j7 * 32 + ee * 16, wso = WS_WT + b * 8192 + (w - 4) * 256;
#pragma unroll
            for (int c = 0; c < 4; ++c) {
                u4 o;
#pragma unroll
                for (int i = 0; i < 4; ++i)
                    o[i] = w == 4 ? __builtin_amdgcn_perm(fi[i][c >> 1], fr[i][c >> 1], (c & 1) ? 0x07060302u : 0x05040100u)
                                  : __builtin_amdgcn_perm(fi[i][1 - (c >> 1)], fr[i][1 - (c >> 1)], (c & 1) ? 0x05040100u : 0x07060302u) ^ 0x80000000u;
                bst4<SC1>(o, rw, wo + c * 512, wso);
            }
            if (w == 5) __builtin_amdgcn_raw_buffer_store_b32(sr_ | (si_ << 16), rw, l * 4, wso, SC1);
        } else if (w == 6) {
            float re = bf_lo(sr_), im = bf_lo(si_);
            re += dpp<0x128>(re); im -= dpp<0x128>(im);
            u32 a = __builtin_bit_cast(u32, re), a2 = a, c = __builtin_bit_cast(u32, im), c2 = c;
            asm("" : "+v"(a2), "+v"(c2));
            auto pr = __builtin_amdgcn_permlane16_swap(a, a2, false, false);
            auto pi = __builtin_amdgcn_permlane16_swap(c, c2, false, false);
            const float ar = __builtin_bit_cast(float, (u32)pr[0]), br = __builtin_bit_cast(float, (u32)pr[1]);
            const float ai = __builtin_bit_cast(float, (u32)pi[0]), bi = __builtin_bit_cast(float, (u32)pi[1]);
            if (l < 8) {
                __builtin_amdgcn_raw_buffer_store_b32(pk_b(0.25f * (ar + br), 0.25f * (ai + bi)), rw, l * 4, WS_WA + b * 32, SC1);
                __builtin_amdgcn_raw_buffer_store_b32(pk_b(0.25f * (ar - br), 0.25f * (ai - bi)), rw, l * 4, WS_WB + b * 32, SC1);
            }
        }
        ST(18);
        tag = __builtin_amdgcn_readfirstlane(epoch) + 1u;
        WAITV(0);
        if (w == 7) announce(0);
        ST(19);
        BARRIER();
        if (w == 7) all_announced(0);
        else __builtin_amdgcn_s_sleep(2);
        xl[0] = *(const u2*)((const char*)x + xoff); xl[1] = *(const u2*)((const char*)x + xoff + 512);
        FENCE();
        bg(0, BG2);
        ST(4);
        BARRIER();
        __builtin_amdgcn_s_sleep(2);
#pragma unroll
        for (int i = 0; i < 2; ++i) bst4<SC1>(SENT4, rw, l * 16 + i * 1024, WS_V + b * 8192 + (w - 4) * 2048);
        FENCE();
        consts0();
        consts3();
        bg(BG2, BG3);
        WAITV(BG3 - BG2);
        ST(5);
        BARRIER();
        if (w == 7) announce(1);
#pragma unroll
        for (int i = 0; i < 2; ++i) bst4<SC1>(SENT4, rw, (l >> 3) * 256 + (l & 7) * 16 + i * 2048, WS_LN2 + (b >> 1) * 16384 + (b & 1) * 128 + (w - 4) * 4096);
        bg(BG3, BG4);
        BARRIER();
        bg(BG4, BG5);
        WAITV(BG5 - BG3);
        ST(6);
        BARRIER();
        if (w == 7) all_announced(1);
        else __builtin_amdgcn_s_sleep(3);
        bg(BG5, BG6);
        ST(7);
        BARRIER();
        __builtin_amdgcn_s_sleep(4);
        bg(BG6, BG7);
    }
    BARRIER();
    if (!aw) bg(BG7, BG8);
    {
        u4 A = *(const u4*)(scr + SCR_X + w * 1024 + m * 64 + g * 16);
        f4 Dre = mma_b(A, Gre3, Z4), Dim = mma_b(A, Gim3, Z4), Hre = mma_b(A, SGre, Z4), Him = mma_b(A, SGim, Z4);
        if (g == 0) { Dre[0] = Hre[0]; Dim[0] = Him[0]; }
        f2 r0 = fma2(-lo2(Dim), tws[0], lo2(Dre) * twc[0]), i0 = fma2(lo2(Dre), tws[0], lo2(Dim) * twc[0]);
        f2 r1 = fma2(-hi2(Dim), tws[1], hi2(Dre) * twc[1]), i1 = fma2(hi2(Dre), tws[1], hi2(Dim) * twc[1]);
        u4 A2 = {pk_b(r0[0], i0[0]), pk_b(r0[1], i0[1]), pk_b(r1[0], i1[0]), pk_b(r1[1], i1[1])};
        f2 h[4]; u2 hp[2];
#pragma unroll
        for (int t = 0; t < 2; ++t) {
            f4 Yv = mma_b(A2, GA[t], Z4);
            u32 h0 = pk_b2(bf_2(xl[t][0]) + bf_2(pk_b(Yv[0], Yv[1]))), h1 = pk_b2(bf_2(xl[t][1]) + bf_2(pk_b(Yv[2], Yv[3])));
            hp[t] = (u2){h0, h1};
            h[2 * t] = bf_2(h0); h[2 * t + 1] = bf_2(h1);
        }
        f2 s2 = (h[0] + h[1]) + (h[2] + h[3]);
        const float mean = red_lanes<true>(s2[0] + s2[1]) * (1.0f / 512);
        f2 q2 = {0.f, 0.f};
#pragma unroll
        for (int j = 0; j < 4; ++j) { f2 d = h[j] - mean; q2 = fma2(d, d, q2); }
        const float inv = ln_inv(red_lanes<true>(q2[0] + q2[1]) * (1.0f / 512));
        const float mb = rbf(mean);
#pragma unroll
        for (int t = 0; t < 2; ++t)
            *(u2*)(scr + ((8 * t + (m >> 1)) * 4 + g) * 128 + (w ^ (m >> 1)) * 16 + (m & 1) * 8) =
                (u2){clean(pk_b2(bf_2(pk_b2(h[2 * t] - mb)) * inv)), clean(pk_b2(bf_2(pk_b2(h[2 * t + 1] - mb)) * inv))};
        WAITV(0);
        BARRIER();
        u4 lo = *(const u4*)(scr + (tid ^ ((tid >> 5) & 7)) * 16);
        bst4<SC1>(lo, rw, (tid >> 3) * 256 + (tid & 7) * 16, WS_LN2 + (b >> 1) * 16384 + (b & 1) * 128);
        FENCE();
        if (aw) WAITV(0); else __builtin_amdgcn_s_sleep(8);
        bst2<SC1>(hp[0], ro, xoff, 0); bst2<SC1>(hp[1], ro, xoff + 512, 0);
        FENCE();
        ST(10);
    }
    auto stage_out = [&](int r) {
        w2_read(W[r], LDS_SCR + (w & 3) * 8192 + (r & 1) * 4096);
        asm volatile("s_waitcnt lgkmcnt(0)" ::: "memory");
        PIN(W[r][0]); PIN(W[r][1]); PIN(W[r][2]); PIN(W[r][3]);
        if (r + 2 < 7) w2_glds(pw, r + 2, lds0 + LDS_SCR + (w & 3) * 8192 + (r & 1) * 4096);
    };
    auto publish = [&]() {
        u32 prev = 0;
        if (l == 0) prev = __hip_atomic_fetch_add(hw + 4, 1u, __ATOMIC_RELAXED, __HIP_MEMORY_SCOPE_WORKGROUP);
        if (__builtin_amdgcn_readfirstlane(prev) == 7u && l < 2)
            __hip_atomic_store(fl + (l ? 4096 + (b >> 1) * 32 + (b & 1) : (mt1 * 8 + (nt1 >> 1)) * 32 + (nt1 & 1)), tag, __ATOMIC_RELAXED, __HIP_MEMORY_SCOPE_AGENT);
        FENCE();
    };
    {
        const int tg = w & 3;
        char* xch = smem + XCH + tg * 3584;
        const int hoff = EXP_HOFF + WS_H1 + (mt1 * 8 + 2 * tg) * (56 * 1024) + (nt1 >> 1) * 7168;
        if (aw) {
            const int toff = WS_LN2 + (mt1 * 8 + 2 * tg) * 16384;
            LAS char* wb0 = (LAS char*)smem + (4 * g + (m >> 2)) * 224 + (m & 3) * 8;
            LAS char* wb[4];
            int tq[4];
#pragma unroll
            for (int q = 0; q < 4; ++q) { const int qq = (q + rq) & 3; wb[q] = wb0 + qq * 28672; tq[q] = toff + qq * 4096; }
            u4 T[2][16], A[2][7];
            auto ldt = [&](int kk) {
#pragma unroll
                for (int tt = 0; tt < 2; ++tt) T[tt][kk] = bld4<SC1>(rw, l * 16 + (kk & 3) * 1024, tq[kk >> 2] + tt * 16384);
            };
#pragma unroll
            for (int i = 0; i < 7; ++i) A[0][i] = ldw1(wb[0], 0, i);
            if (l == 0) __hip_atomic_fetch_add(hw + 5, 1u, __ATOMIC_RELAXED, __HIP_MEMORY_SCOPE_WORKGROUP);
            ST(11);
#pragma unroll
            for (int kk = 0; kk < G1_DEPTH; ++kk) { ldt(kk); SCHED(); }
            f4 acc[2][7];
#pragma unroll
            for (int tt = 0; tt < 2; ++tt)
#pragma unroll
                for (int i = 0; i < 7; ++i) acc[tt][i] = Z4;
#pragma unroll
            for (int kk = 0; kk < 16; ++kk) {
                if ((kk & 1) == 0) {
                    if (__builtin_expect(any_sent(umax(umax(max4(T[0][kk]), max4(T[1][kk])), umax(max4(T[0][kk + 1]), max4(T[1][kk + 1])))), 0)) {
                        wait_tok(T[0][kk], T[1][kk], rw, l * 16, tq[kk >> 2] + (kk & 3) * 1024);
                        wait_tok(T[0][kk + 1], T[1][kk + 1], rw, l * 16, tq[kk >> 2] + ((kk + 1) & 3) * 1024);
                    }
                    if (kk == 0) ST(12);
                    if (kk == (BG8 / 7) * 4 - 2) lds_wait(hw + 5, 8u);
                }
                if (kk + G1_DEPTH < 16) ldt(kk + G1_DEPTH);
                if (kk < 15) {
#pragma unroll
                    for (int i = 0; i < 7; ++i) A[(kk + 1) & 1][i] = ldw1(wb[(kk + 1) >> 2], (kk + 1) & 3, i);
                }
#pragma unroll
                for (int i = 0; i < 7; ++i)
#pragma unroll
                    for (int tt = 0; tt < 2; ++tt) acc[tt][i] = mma_b(A[kk & 1][i], T[tt][kk], acc[tt][i]);
#pragma unroll
                for (int i = 0; i < 14; ++i) {
                    __builtin_amdgcn_sched_group_barrier(0x8, 1, 0);
                    if (i == 1 || i == 7) __builtin_amdgcn_sched_group_barrier(0x20, 1, 0);
                    __builtin_amdgcn_sched_group_barrier(0x100, 1, 0);
                    if ((kk & 1) && i >= 3) __builtin_amdgcn_sched_group_barrier(0x2, 1, 0);
                }
                if (kk & 1) {} else SCHED();
            }
#pragma unroll
            for (int q = 0; q < 3; ++q) {
                u2 p0 = pk4(acc[1][2 * q]), p1 = pk4(acc[1][2 * q + 1]);
                *(u4*)(xch + q * 1024 + l * 16) = (u4){p0[0], p0[1], p1[0], p1[1]};
            }
            *(u2*)(xch + 3072 + l * 8) = pk4(acc[1][6]);
            lds_set(hw + tg, 1u);
            ST(13);
#pragma unroll
            for (int q = 0; q < 3; ++q) {
                u2 p0 = pk4(acc[0][2 * q]), p1 = pk4(acc[0][2 * q + 1]);
                bst4<SC1>(silu_pk4((u4){p0[0], p0[1], p1[0], p1[1]}), rw, l * 16, hoff + ((nt1 & 1) * 3 + q) * 1024);
            }
            { u2 p = pk4(acc[0][6]); bst2<SC1>(silu_pk2(p[0], p[1]), rw, l * 8, hoff + 6144 + (nt1 & 1) * 512); }
        } else {
            bg(BG8, 28);
            WAITV(0);
            if (l == 0) __hip_atomic_fetch_add(hw + 5, 1u, __ATOMIC_RELAXED, __HIP_MEMORY_SCOPE_WORKGROUP);
            __builtin_amdgcn_s_sleep(W2_SLEEP);
            lds_wait(hw + 5, 8u);
            w2_glds(pw, 0, lds0 + LDS_SCR + tg * 8192);
            w2_glds(pw, 1, lds0 + LDS_SCR + tg * 8192 + 4096);
#pragma unroll
            for (int r = 0; r < B_R; ++r) { WAITV(4); stage_out(r); }
            ST(12);
            lds_wait(hw + tg, 1u);
            ST(13);
#pragma unroll
            for (int q = 0; q < 3; ++q)
                bst4<SC1>(silu_pk4(*(const u4*)(xch + q * 1024 + l * 16)), rw, l * 16, hoff + 56 * 1024 + ((nt1 & 1) * 3 + q) * 1024);
            { u2 p = *(const u2*)(xch + 3072 + l * 8); bst2<SC1>(silu_pk2(p[0], p[1]), rw, l * 8, hoff + 56 * 1024 + 6144 + (nt1 & 1) * 512); }
        }
        if (l == 0) __hip_atomic_fetch_add(hw + 6, 1u, __ATOMIC_RELAXED, __HIP_MEMORY_SCOPE_WORKGROUP);
        ST(14);
        WAITV(0);
        publish();
        ST(21);
        if (aw) {
            lds_wait(hw + FLOOD_W, 8u);
#pragma unroll
            for (int ks = 0; ks < 7; ++ks) w2_glds(pw, ks, lds0 + tg * 28672 + ks * 4096);
        } else {
            stage_out(B_R);
#pragma unroll
            for (int r = B_R + 1; r < 7; ++r) { if (r + 1 < 7) WAITV(4); else WAITV(0); stage_out(r); }
        }
    }
    {
        ST(15);
        const int rot = w & 3;
        if (aw) {
            flag_wait(fl + ((l & 4) ? 4096 + (grp * 8 + th * 4 + rot) * 32 : (grp * 8 + (pw ^ ((l & 2) * 2))) * 32) + (l & 1), tag);
            lds_set(hw + 8 + rot, tag);
        } else {
#pragma nounroll
            for (int sp = 0; sp < SPIN_MAX; ++sp) {
                if (__builtin_amdgcn_readfirstlane(__hip_atomic_load(hw + 8 + rot, __ATOMIC_RELAXED, __HIP_MEMORY_SCOPE_WORKGROUP)) == tag) break;
                __builtin_amdgcn_s_sleep(1);
            }
            FENCE();
        }
        ST(16);
        const int toff = WS_H1 + (grp * 8 + th * 4) * (56 * 1024) + pw * 7168;
        int to[4];
#pragma unroll
        for (int tt = 0; tt < 4; ++tt) to[tt] = toff + ((tt + rot) & 3) * (56 * 1024);
        u4 T[7][4];
        auto ldtok = [&](int ks) {
#pragma unroll
            for (int tt = 0; tt < 4; ++tt) {
                if (ks < 6) T[ks][tt] = bld4<EXP_AUX>(rw, l * 16, to[tt] + ko[ks] * 1024);
                else {
                    u2 a = bld2<SC1>(rw, l * 8 + 2048, to[tt] + 4096), c = bld2<SC1>(rw, l * 8 + 2560, to[tt] + 4096);
                    T[ks][tt] = (u4){a[0], a[1], c[0], c[1]};
                }
            }
            SCHED();
        };
#pragma unroll
        for (int ks = 0; ks < G2_DEPTH; ++ks) ldtok(ks);
        if (aw) {
#pragma unroll
            for (int ks = 0; ks < 7; ++ks) w2_read(W[ks], rot * 28672 + ks * 4096);
            asm volatile("s_waitcnt lgkmcnt(0)" ::: "memory");
            if (l == 0) __hip_atomic_fetch_add(hw + 7, 1u, __ATOMIC_RELAXED, __HIP_MEMORY_SCOPE_WORKGROUP);
        }
        const int rho = grp * 128 + th * 64 + rot * 16 + m;
        const int ooff = (256 * (rho & 7) + (rho >> 3)) * 1024 + fb * 128 + (w >> 2) * 64 + g * 16;
        const u4 hres = bld4<SC1>(ro, ooff, 0);
        SCHED();
        f4 acc[4][4];
#pragma unroll
        for (int tt = 0; tt < 4; ++tt)
#pragma unroll
            for (int t = 0; t < 4; ++t) acc[tt][t] = Z4;
#pragma unroll
        for (int ks = 0; ks < 7; ++ks) {
            if (ks + G2_DEPTH < 7) { PIN(T[ks][0]); ldtok(ks + G2_DEPTH); }
#pragma unroll
            for (int tt = 0; tt < 4; ++tt)
#pragma unroll
                for (int t = 0; t < 4; ++t) {
                    acc[tt][t] = mma_b(W[ks][t], T[ks][tt], acc[tt][t]);
                }
            SCHED();
        }
        ST(17);
        lds_wait(hw + 7, 4u);
        char* dp = smem + w * 16384 + l * 16;
#pragma unroll
        for (int tt = 1; tt < 4; ++tt)
#pragma unroll
            for (int t = 0; t < 4; ++t) *(f4*)(dp + (((tt + rot) & 3) * 4 + t) * 1024) = acc[tt][t];
        f4 s0, s1;
        if (aw) { *(f4*)(dp + (rot * 4 + 2) * 1024) = acc[0][2]; *(f4*)(dp + (rot * 4 + 3) * 1024) = acc[0][3]; s0 = acc[0][0]; s1 = acc[0][1]; }
        else { *(f4*)(dp + (rot * 4) * 1024) = acc[0][0]; *(f4*)(dp + (rot * 4 + 1) * 1024) = acc[0][1]; s0 = acc[0][2]; s1 = acc[0][3]; }
        BARRIER();
        f4 r0[7], r1[7];
#pragma unroll
        for (int j = 1; j < 8; ++j) {
            const char* rp = smem + (((w + j) & 7) * 16 + rot * 4 + (w >> 2) * 2) * 1024 + l * 16;
            r0[j - 1] = *(const f4*)rp; r1[j - 1] = *(const f4*)(rp + 1024);
        }
        SCHED();
        s0 = ((s0 + r0[0]) + (r0[1] + r0[2])) + ((r0[3] + r0[4]) + (r0[5] + r0[6]));
        s1 = ((s1 + r1[0]) + (r1[1] + r1[2])) + ((r1[3] + r1[4]) + (r1[5] + r1[6]));
        u2 p0 = pk4(s0), p1 = pk4(s1);
        bst4<AUX_OUT>((u4){pk_b2(bf_2(hres[0]) + bf_2(p0[0])), pk_b2(bf_2(hres[1]) + bf_2(p0[1])),
                           pk_b2(bf_2(hres[2]) + bf_2(p1[0])), pk_b2(bf_2(hres[3]) + bf_2(p1[1]))}, ro, ooff, 0);
    }
    ST_END(ws, 0);
}

extern "C" void kernel_launch(void* const* d_in, const int* in_sizes, int n_in, void* d_out, int out_size,
                              void* d_ws, size_t ws_size, hipStream_t stream) {
    (void)hipFuncSetAttribute((const void*)k_fused, hipFuncAttributeMaxDynamicSharedMemorySize, LDS_B);
    k_fused<<<256, 512, LDS_B, stream>>>((const uint16_t*)d_in[0], (const uint16_t*)d_in[1], (const uint16_t*)d_in[2],
                                         (const uint16_t*)d_in[4], (const uint16_t*)d_in[5], (uint16_t*)d_out, (char*)d_ws);
}
```

```cpp
#define CODE_PF 9
#include <hip/hip_runtime.h>
#include <hip/hip_bf16.h>
#include <stdint.h>

typedef _Float16 h8 __attribute__((ext_vector_type(8)));
typedef _Float16 h4 __attribute__((ext_vector_type(4)));
typedef _Float16 h2 __attribute__((ext_vector_type(2)));
typedef __bf16 b8 __attribute__((ext_vector_type(8)));
typedef __bf16 b2 __attribute__((ext_vector_type(2)));
typedef short s4 __attribute__((ext_vector_type(4)));
typedef float f4 __attribute__((ext_vector_type(4)));
typedef float f2 __attribute__((ext_vector_type(2)));
typedef unsigned u32;
typedef u32 u4 __attribute__((ext_vector_type(4)));
typedef u32 u2 __attribute__((ext_vector_type(2)));
#define LAS __attribute__((address_space(3)))
#define GAS __attribute__((address_space(1)))
#define DI __device__ __forceinline__
#define FENCE() asm volatile("" ::: "memory")
#define PIN(x) asm volatile("" : "+v"(x))
#define SCHED() __builtin_amdgcn_sched_barrier(0)
#define WAITV(n) asm volatile("s_waitcnt vmcnt(%0)" :: "n"(n) : "memory")
#define BARRIER() do { asm volatile("s_waitcnt lgkmcnt(0)" ::: "memory"); __builtin_amdgcn_s_barrier(); FENCE(); } while (0)
constexpr int SC1 = 16;
constexpr u32 SENT = 0xffffffffu;
constexpr int SPIN_MAX = 1 << 20;

DI u32 pk_h(float a, float b) { f2 v = {a, b}; h2 r = __builtin_convertvector(v, h2); return __builtin_bit_cast(u32, r); }
DI u32 pk_b(float a, float b) { f2 v = {a, b}; b2 r = __builtin_convertvector(v, b2); return __builtin_bit_cast(u32, r); }
DI float bf_lo(u32 v) { return __builtin_bit_cast(float, v << 16); }
DI float bf_hi(u32 v) { return __builtin_bit_cast(float, v & 0xffff0000u); }
DI f2 bf_2(u32 v) { return (f2){bf_lo(v), bf_hi(v)}; }
DI u32 pk_b2(f2 v) { return pk_b(v[0], v[1]); }
DI float rbf(float a) { return bf_lo(pk_b(a, a)); }
DI float cosr(float rev) { return __builtin_amdgcn_cosf(rev); }
DI float sinr(float rev) { return __builtin_amdgcn_sinf(rev); }
DI f2 lo2(f4 v) { return __builtin_shufflevector(v, v, 0, 1); }
DI f2 hi2(f4 v) { return __builtin_shufflevector(v, v, 2, 3); }
DI f2 fma2(f2 a, f2 b, f2 c) { return __builtin_elementwise_fma(a, b, c); }
DI u32 umin(u32 a, u32 b) { return a < b ? a : b; }
DI u32 umax(u32 a, u32 b) { return a > b ? a : b; }
DI u32 clean(u32 v) { return umin(v, SENT - 1); }
DI u32 max4(u4 v) { return umax(umax(umax(v[0], v[1]), v[2]), v[3]); }
#define Z4 ((f4){0, 0, 0, 0})
DI f4 mma_h(u4 a, u4 b, f4 c) { return __builtin_amdgcn_mfma_f32_16x16x32_f16(__builtin_bit_cast(h8, a), __builtin_bit_cast(h8, b), c, 0, 0, 0); }
DI f4 mma_b(u4 a, u4 b, f4 c) { return __builtin_amdgcn_mfma_f32_16x16x32_bf16(__builtin_bit_cast(b8, a), __builtin_bit_cast(b8, b), c, 0, 0, 0); }
DI f4 mma16_h(u2 a, u2 b, f4 c) { return __builtin_amdgcn_mfma_f32_16x16x16f16(__builtin_bit_cast(h4, a), __builtin_bit_cast(h4, b), c, 0, 0, 0); }
typedef __amdgpu_buffer_rsrc_t rsrc_t;
DI rsrc_t mkrs(const void* p, u32 bytes) { return __builtin_amdgcn_make_buffer_rsrc((void*)p, 0, bytes, 0x00020000); }
template <int AUX> DI u4 bld4(rsrc_t r, int v, int s) { return __builtin_amdgcn_raw_buffer_load_b128(r, v, s, AUX); }
template <int AUX> DI u2 bld2(rsrc_t r, int v, int s) { return __builtin_amdgcn_raw_buffer_load_b64(r, v, s, AUX); }
template <int AUX> DI void bst4(u4 d, rsrc_t r, int v, int s) {
    __builtin_amdgcn_raw_buffer_store_b128(d, r, v, s, AUX);
    asm volatile("s_nop 1" :: "v"(d));
}
template <int AUX> DI void bst2(u2 d, rsrc_t r, int v, int s) { __builtin_amdgcn_raw_buffer_store_b64(d, r, v, s, AUX); }

template <int CTRL> DI float dpp(float v) {
    return __builtin_bit_cast(float, __builtin_amdgcn_update_dpp(0, __builtin_bit_cast(int, v), CTRL, 0xf, 0xf, true));
}
template <bool ALL> DI float red_lanes(float v) {
    v += dpp<0xB1>(v);
    if (ALL) v += dpp<0x4E>(v);
    v += dpp<0x124>(v); v += dpp<0x128>(v);
    u32 x = __builtin_bit_cast(u32, v), y = x;
    asm("" : "+v"(y));
    auto r = __builtin_amdgcn_permlane16_swap(x, y, false, false);
    v = __builtin_bit_cast(float, (u32)r[0]) + __builtin_bit_cast(float, (u32)r[1]);
    x = __builtin_bit_cast(u32, v); y = x;
    asm("" : "+v"(y));
    auto q = __builtin_amdgcn_permlane32_swap(x, y, false, false);
    return __builtin_bit_cast(float, (u32)q[0]) + __builtin_bit_cast(float, (u32)q[1]);
}
DI float ln_inv(float var) { return __builtin_amdgcn_rcpf(rbf(__builtin_amdgcn_sqrtf(rbf(rbf(var) + 1e-5f)))); }

#define ST_DECL
#define ST(i)
#define ST_END(ws, kid)

constexpr u32 WS_U = 0;
constexpr u32 WS_WT = WS_U + (2u << 20);
constexpr u32 WS_V = WS_WT + (2u << 20);
constexpr u32 WS_LN2 = WS_V + (2u << 20);
constexpr u32 WS_WA = WS_LN2 + (2u << 20);
constexpr u32 WS_WB = WS_WA + 8192u;
constexpr u32 WS_FA = WS_WB + 8192u;
constexpr u32 WS_FLAG = WS_FA + 16384u;
constexpr u32 WS_EP = WS_FLAG + 32768u;
constexpr u32 WS_H1 = WS_WA + (128u << 10);
constexpr u32 WS_END = WS_H1 + 7340032u;

#define W1_B0 0
#define W1_B1 7
#define W1_B2 5
#define W1_B3 6
#define W1_CHK ((((W1_B0 + W1_B1 + W1_B2 + W1_B3) / 7) * 4 - 2) < 0 ? 0 : (((W1_B0 + W1_B1 + W1_B2 + W1_B3) / 7) * 4 - 2))
#define CLR_SLEEP 6
#define BPRIO 2
#define EXP_UCLR 0
#define POLL_SLEEP 1
#define BG1 0
#define BG2 4
#define BG3 4
#define BG4 10
#define BG5 15
#define BG6 21
#define BG7 21
#define BG8 21
#define A_EARLY 24
#define SILU_PRIO 0
#define EXP_HOFF 0
#define B_EARLY 8
#define W2_DELAY 4
#define A_GENTLE 3
#define EXP_AUX SC1
#define FLAG_SLEEP 2
#define B_PREFLAG 8
#define B_R 5
#define G2_PRIO 0
#define EXP_ROT 1
#define B_EARLY_BAR 0
#define FLOOD_W 4
#define W2_SLEEP 8
#define AUX_OUT 2
#define W2_OUT 2
#define W2_ATHR 3
#define W2_EARLY 3
#define G1_DEPTH 10
#define G2_DEPTH 3
constexpr int LDS_SCR = 114688;
constexpr int SCR_X = 8448;
constexpr int SCR_S = SCR_X + 20480;
constexpr int SCR_F = 49024;
constexpr int XCH = 147456;
constexpr int LDS_B = 163840;

DI void glds16(const void* sbase, u32 voff, u32 lds_dst) {
    asm volatile("s_mov_b32 m0, %2\n\ts_nop 0\n\tglobal_load_lds_dwordx4 %0, %1" :: "v"(voff), "s"(sbase), "s"(lds_dst) : "memory");
}
DI void glds16x4(const void* sbase, u32 voff, u32 lds_dst) {
    asm volatile("s_mov_b32 m0, %2\n\ts_nop 0\n\tglobal_load_lds_dwordx4 %0, %1\n\tglobal_load_lds_dwordx4 %0, %1 offset:1024\n\t"
                 "global_load_lds_dwordx4 %0, %1 offset:2048\n\tglobal_load_lds_dwordx4 %0, %1 offset:3072"
                 :: "v"(voff), "s"(sbase), "s"(lds_dst) : "memory");
}
DI bool any_sent(u32 v) { return __builtin_amdgcn_ballot_w64(v == SENT) != 0; }
DI void poll2(u4& a, u4& c, rsrc_t rw, int vo, int sa, int sc) {
#pragma nounroll
    for (int sp = 0; sp < SPIN_MAX; ++sp) {
        const bool ba = max4(a) == SENT, bc = max4(c) == SENT;
        if (__builtin_amdgcn_ballot_w64(ba || bc) == 0) break;
        __builtin_amdgcn_s_sleep(POLL_SLEEP);
        FENCE();
        if (ba) a = bld4<SC1>(rw, vo, sa);
        if (bc) c = bld4<SC1>(rw, vo, sc);
    }
}
DI void wait_tok(u4& t0, u4& t1, rsrc_t rw, int vo, int so) {
    if (any_sent(umax(max4(t0), max4(t1)))) {
#pragma nounroll
        for (int sp = 0; sp < SPIN_MAX; ++sp) {
            FENCE();
            t0 = bld4<SC1>(rw, vo, so); t1 = bld4<SC1>(rw, vo, so + 16384);
            if (!any_sent(umax(max4(t0), max4(t1)))) break;
        }
    }
}
DI u4 ldw1(LAS char* wb, int kk, int ft) {
    s4 a0 = __builtin_amdgcn_ds_read_tr16_b64_v4i16((LAS s4*)(wb + kk * 32 * 224 + ft * 32));
    s4 a1 = __builtin_amdgcn_ds_read_tr16_b64_v4i16((LAS s4*)(wb + (kk * 32 + 16) * 224 + ft * 32));
    u2 x0 = __builtin_bit_cast(u2, a0), x1 = __builtin_bit_cast(u2, a1);
    return (u4){x0[0], x0[1], x1[0], x1[1]};
}
DI u2 pk4(f4 a) { return (u2){pk_b(a[0], a[1]), pk_b(a[2], a[3])}; }
DI u2 silu_pk2(u32 p0, u32 p1) {
    u32 o0, o1; float a0, a1, a2, a3, t0, t1, t2, t3;
    asm("v_lshlrev_b32 %2, 16, %10\n\tv_and_b32 %3, 0xffff0000, %10\n\tv_lshlrev_b32 %4, 16, %11\n\tv_and_b32 %5, 0xffff0000, %11\n\t"
        "v_mul_f32 %6, %12, %2\n\tv_mul_f32 %7, %12, %3\n\tv_mul_f32 %8, %12, %4\n\tv_mul_f32 %9, %12, %5\n\t"
        "v_exp_f32 %6, %6\n\tv_exp_f32 %7, %7\n\tv_exp_f32 %8, %8\n\tv_exp_f32 %9, %9\n\t"
        "v_add_f32 %6, 1.0, %6\n\tv_add_f32 %7, 1.0, %7\n\tv_add_f32 %8, 1.0, %8\n\tv_add_f32 %9, 1.0, %9\n\t"
        "v_rcp_f32 %6, %6\n\tv_rcp_f32 %7, %7\n\tv_rcp_f32 %8, %8\n\tv_rcp_f32 %9, %9\n\t"
        "v_mul_f32 %2, %2, %6\n\tv_mul_f32 %3, %3, %7\n\tv_mul_f32 %4, %4, %8\n\tv_mul_f32 %5, %5, %9\n\t"
        "v_cvt_pk_bf16_f32 %0, %2, %3\n\tv_cvt_pk_bf16_f32 %1, %4, %5"
        : "=&v"(o0), "=&v"(o1), "=&v"(a0), "=&v"(a1), "=&v"(a2), "=&v"(a3), "=&v"(t0), "=&v"(t1), "=&v"(t2), "=&v"(t3)
        : "v"(p0), "v"(p1), "s"(-1.4426950408889634f));
    return (u2){o0, o1};
}
DI u4 silu_pk4(u4 p) { u2 a = silu_pk2(p[0], p[1]), b = silu_pk2(p[2], p[3]); return (u4){a[0], a[1], b[0], b[1]}; }
DI void lds_set(LAS u32* p, u32 v) {
    asm volatile("s_waitcnt lgkmcnt(0)" ::: "memory");
    __hip_atomic_store(p, v, __ATOMIC_RELAXED, __HIP_MEMORY_SCOPE_WORKGROUP);
}
DI void lds_wait(LAS u32* p, u32 v) {
#pragma nounroll
    for (int sp = 0; sp < SPIN_MAX; ++sp) {
        if (__builtin_amdgcn_readfirstlane(__hip_atomic_load(p, __ATOMIC_RELAXED, __HIP_MEMORY_SCOPE_WORKGROUP)) >= v) break;
        __builtin_amdgcn_s_sleep(1);
    }
    FENCE();
}
DI void flag_wait(GAS u32* p, u32 tag) {
#pragma nounroll
    for (int sp = 0; sp < SPIN_MAX; ++sp) {
        if (__builtin_amdgcn_ballot_w64(__hip_atomic_load(p, __ATOMIC_RELAXED, __HIP_MEMORY_SCOPE_AGENT) != tag) == 0) break;
        __builtin_amdgcn_s_sleep(FLAG_SLEEP);
    }
    FENCE();
}
#define SENT4 ((u4){SENT, SENT, SENT, SENT})

__global__ __launch_bounds__(512, 2) void k_fused(const uint16_t* x, const uint16_t* wr, const uint16_t* wi, const uint16_t* w1,
                                                  const uint16_t* w2, uint16_t* out, char* ws) {
    extern __shared__ __attribute__((aligned(16))) char smem[];
    const int tid = threadIdx.x, l = tid & 63, g = l >> 4, m = l & 15;
    const int w = __builtin_amdgcn_readfirstlane(tid >> 6);
    const int b = (blockIdx.x & 7) * 32 + (blockIdx.x >> 3);
    const int mt1 = (b >> 6) * 4 + ((b >> 3) & 3), nt1 = ((b >> 5) & 1) * 8 + (b & 7);
    const int grp = b >> 4, th = (b >> 3) & 1, fb = b & 7;
    const rsrc_t rw = mkrs(ws, WS_END), ro = mkrs(out, 2097152);
    char* scr = smem + LDS_SCR;
    LAS u32* hw = (LAS u32*)((LAS char*)smem + LDS_SCR + SCR_F);
    const u32 lds0 = (u32)(uintptr_t)(LAS char*)smem;
    const float mf = (float)m, gf = (float)g, sg = (m & 1) ? -1.0f : 1.0f;
    const bool aw = w < 4;
    ST_DECL;
    unsigned long long pc0;
    asm volatile("s_getpc_b64 %0" : "=s"(pc0));
    const int rq = ((b >> 3) + b) & 3;
    const int row0 = (tid & 255) / 14, cch0 = (tid & 255) - row0 * 14;
    const char* wsrc = (const char*)w1 + nt1 * 224;
    u32 w1o[7];
    auto w1_burst = [&](int i0, int n) {
#pragma unroll
        for (int i = i0; i < i0 + n; ++i) {
            const int q = (i / 7 + rq) & 3, j = i % 7;
            glds16(wsrc + q * (128 * 3584), w1o[j], lds0 + (w - 4) * 1024 + q * 28672 + j * 4096);
        }
    };
    const u32 epoch = __hip_atomic_load((GAS u32*)(ws + WS_EP), __ATOMIC_RELAXED, __HIP_MEMORY_SCOPE_AGENT);
    FENCE();
    const int xoff = (256 * w + b) * 1024 + m * 32 + g * 8;
    u2 xl[2];
    float c16[4], s16[4];
    f2 twc[2], tws[2];
    u4 Gre3, Gim3, SGre, SGim, GA[2];
    auto consts0 = [&]() {
#pragma unroll
        for (int u = 0; u < 4; ++u) {
            float a = (4.0f * gf + u) * (mf * (1.0f / 16));
            c16[u] = cosr(a); s16[u] = sinr(a);
            twc[u >> 1][u & 1] = cosr(a * (1.0f / 32)); tws[u >> 1][u & 1] = sinr(a * (1.0f / 32));
        }
    };
    auto consts3 = [&]() {
#pragma unroll
        for (int u = 0; u < 4; ++u) {
            const float k = 4.0f * gf + u;
            const bool k0 = u == 0 && g == 0;
            float cc = c16[u], ss = s16[u];
            Gre3[u] = pk_b(cc, -ss); Gim3[u] = pk_b(ss, cc);
            float a2 = (2.0f * k + 1.0f) * (mf * (1.0f / 32));
            u32 sp = pk_b(g < 2 ? 2.f * cc : 2.f * sg * cosr(a2), g < 2 ? -2.f * ss : -2.f * sg * sinr(a2));
            SGre[u] = k0 ? pk_b(1.0f, sg) : (g < 2 ? sp : 0u);
            SGim[u] = g < 2 ? 0u : sp;
            float a4 = k * (mf * (1.0f / 32)), c4 = 2.f * cosr(a4), s4_ = -2.f * sinr(a4);
            GA[0][u] = k0 ? pk_b(1.0f, sg) : pk_b(c4, s4_);
            GA[1][u] = k0 ? pk_b(1.0f, sg) : ((u & 1) ? pk_b(-c4, -s4_) : pk_b(c4, s4_));
        }
        PIN(Gre3); PIN(Gim3); PIN(SGre); PIN(SGim); PIN(GA[0]); PIN(GA[1]); PIN(twc[0]); PIN(twc[1]); PIN(tws[0]); PIN(tws[1]);
    };
    const int pw = (w + 4 * th) & 7;
    GAS u32* fl = (GAS u32*)(ws + WS_FLAG);
    int ko[7];
    {
        const int r6 = EXP_ROT * ((fb < 6 ? fb : fb - 5) + 3 * (grp & 1));
#pragma unroll
        for (int ks = 0; ks < 6; ++ks) { int k = ks + r6; k = k >= 6 ? k - 6 : k; ko[ks] = k >= 6 ? k - 6 : k; }
        ko[6] = 6;
    }
    u4 W[7][4];
    const u32 w2vo = (l >> 3) * 1024 + (((l & 7) ^ (((l >> 4) & 1) << 2)) * 16);
    auto w2_glds = [&](int p_, int ks, u32 dst) {
        const int q = ko[ks];
        const char* src = (const char*)w2 + (224 * p_ + (ks == 6 ? 96 : (q < 3 ? 32 * q : 16 + 32 * q))) * 1024 + fb * 128;
        glds16(src, w2vo, dst); glds16(src + 8192, w2vo, dst + 1024);
        glds16(src + (ks == 6 ? 112 : 16) * 1024, w2vo, dst + 2048); glds16(src + (ks == 6 ? 120 : 24) * 1024, w2vo, dst + 3072);
    };
    LAS char* w2rd = (LAS char*)smem + (4 * g + (m >> 2)) * 128 + (((m & 3) ^ (((m >> 3) & 1) << 2)) * 16);
    auto w2_read = [&](u4 (&f)[4], int off) {
#pragma unroll
        for (int t = 0; t < 4; ++t) {
            LAS char* p = (LAS char*)((u32)(uintptr_t)w2rd ^ ((t >> 1) * 64)) + off + (t & 1) * 8;
            u2 x0 = __builtin_bit_cast(u2, __builtin_amdgcn_ds_read_tr16_b64_v4i16((LAS s4*)p));
            u2 x1 = __builtin_bit_cast(u2, __builtin_amdgcn_ds_read_tr16_b64_v4i16((LAS s4*)(p + 2048)));
            f[t] = (u4){x0[0], x0[1], x1[0], x1[1]};
        }
    };
    auto bg = [&](int i0, int i1) { w1_burst(i0, i1 - i0); };
    u32 tag;
    if (aw) {
        u32 xd[8];
        const u32* xr = (const u32*)(x + (256 * (2 * w + ((m >> 1) & 1)) + b) * 512) + 8 * g + 2 * (m >> 2) + (m & 1);
#pragma unroll
        for (int j = 0; j < 8; ++j) xd[j] = xr[32 * j];
        if (tid < 8) hw[tid] = 0;
        FENCE();
        ST(2);
        {
            u4 FAre, FAim, Fre, Fim, Sre, Sim; u2 Mb; f2 pc[2], ps[2];
            consts0();
            float c0[8], s0[8];
#pragma unroll
            for (int j = 0; j < 8; ++j) {
                float a = (gf + 4.0f * j) * (mf * (1.0f / 32));
                c0[j] = cosr(a); s0[j] = m ? -sinr(a) : ((g & 1) ? -1.0f : 1.0f);
            }
            const float mo = (float)(2 * (m & 7) + 1) * (1.0f / 32);
#pragma unroll
            for (int u = 0; u < 4; ++u) {
                FAre[u] = pk_h(c0[2 * u], c0[2 * u + 1]); FAim[u] = pk_h(s0[2 * u], s0[2 * u + 1]);
                float cc = c16[u], ss = s16[u];
                Fre[u] = pk_h(cc, ss); Fim[u] = pk_h(-ss, cc);
                float a2 = (4.0f * gf + u) * mo;
                u32 re = pk_h(m < 8 ? cc : cosr(a2), 0.f), im = pk_h(m < 8 ? (m ? -ss : ((u & 1) ? -1.0f : 1.0f)) : -sinr(a2), 0.f);
                Sre[u] = m < 8 ? re : re << 16; Sim[u] = m < 8 ? im : im << 16;
                pc[u & 1][u >> 1] = twc[u >> 1][u & 1]; ps[u & 1][u >> 1] = tws[u >> 1][u & 1];
            }
#pragma unroll
            for (int e = 0; e < 2; ++e) {
                float a = (float)(((m >> 1) * (256 * (2 * g + e) + b)) & 2047) * (1.0f / 2048);
                float cc = cosr(a), ss = sinr(a);
                Mb[e] = (m & 1) ? pk_h(-ss, cc) : pk_h(cc, ss);
            }
            PIN(FAre); PIN(FAim); PIN(Fre); PIN(Fim); PIN(Sre); PIN(Sim); PIN(Mb); PIN(pc[0]); PIN(pc[1]); PIN(ps[0]); PIN(ps[1]);
            ST(3);
            f2 v[8];
#pragma unroll
            for (int j = 0; j < 8; ++j) v[j] = bf_2(xd[j]);
            f2 s2 = ((v[0] + v[1]) + (v[2] + v[3])) + ((v[4] + v[5]) + (v[6] + v[7]));
            const float mean = red_lanes<false>(s2[0] + s2[1]) * (1.0f / 512);
            f2 q0 = {0.f, 0.f}, q1 = {0.f, 0.f};
#pragma unroll
            for (int j = 0; j < 8; ++j) { v[j] -= mean; if (j & 1) q1 = fma2(v[j], v[j], q1); else q0 = fma2(v[j], v[j], q0); }
            q0 += q1;
            const float inv = ln_inv(red_lanes<false>(q0[0] + q0[1]) * (1.0f / 512));
            const float off = (mean - rbf(mean)) * inv;
#pragma unroll
            for (int j = 0; j < 8; ++j) v[j] = fma2(v[j], (f2){inv, inv}, (f2){off, off});
            u4 B[2];
#pragma unroll
            for (int p = 0; p < 2; ++p) {
                u4 A = {pk_h(v[0][p], v[1][p]), pk_h(v[2][p], v[3][p]), pk_h(v[4][p], v[5][p]), pk_h(v[6][p], v[7][p])};
                f4 Dre = mma_h(A, FAre, Z4), Dim = mma_h(A, FAim, Z4);
#pragma unroll
                for (int r = 0; r < 2; ++r) {
                    f2 re = r ? hi2(Dre) : lo2(Dre), im = r ? hi2(Dim) : lo2(Dim);
                    f2 tr = fma2(im, ps[p], re * pc[p]), ti = fma2(-re, ps[p], im * pc[p]);
                    B[r][p] = pk_h(tr[0], ti[0]); B[r][2 + p] = pk_h(tr[1], ti[1]);
                }
            }
#pragma unroll
            for (int r = 0; r < 2; ++r) {
                f4 Gre = mma_h(Fre, B[r], Z4), Gim = mma_h(Fim, B[r], Z4), Hre = mma_h(Sre, B[r], Z4), Him = mma_h(Sim, B[r], Z4);
                u4 Z;
#pragma unroll
                for (int i = 0; i < 4; ++i) Z[i] = pk_h(m ? Gre[i] : Hre[i], m ? Gim[i] : Him[i]);
                *(u4*)(scr + (2 * w + r) * 1024 + m * 64 + g * 16) = Z;
            }
            BARRIER();
            ST(18);
            u4 r0 = *(const u4*)(scr + (2 * g) * 1024 + w * 256 + m * 16);
            u4 r1 = *(const u4*)(scr + (2 * g + 1) * 1024 + w * 256 + m * 16);
#pragma unroll
            for (int t = 0; t < 4; ++t) {
                f4 D = mma16_h(Mb, (u2){r0[t], r1[t]}, Z4);
                *(u2*)(scr + SCR_S + w * 2048 + m * 128 + (((2 * t + (g >> 1)) ^ (m >> 1)) * 16) + (g & 1) * 8) =
                    (u2){clean(pk_h(D[0], D[1])), clean(pk_h(D[2], D[3]))};
            }
        }
        u4 uo0 = *(const u4*)(scr + SCR_S + w * 2048 + (l ^ ((l >> 4) & 7)) * 16);
        u4 uo1 = *(const u4*)(scr + SCR_S + w * 2048 + 1024 + (l ^ (((l >> 4) + 4) & 7)) * 16);
        u4 Fre, Fim, Gre, Gim, N0, N1; f2 tc[2], ts[2];
#pragma unroll
        for (int u = 0; u < 4; ++u) {
            float cc = c16[u], ss = s16[u];
            Fre[u] = pk_h(0.25f * cc, 0.25f * ss); Fim[u] = pk_h(-0.25f * ss, 0.25f * cc);
            Gre[u] = pk_b(cc * (1.0f / 256), -ss * (1.0f / 256)); Gim[u] = pk_b(ss * (1.0f / 256), cc * (1.0f / 256));
            float a3 = (4.0f * gf + u) * (mf * (1.0f / 256));
            tc[u >> 1][u & 1] = cosr(a3); ts[u >> 1][u & 1] = sinr(a3);
        }
        PIN(Fre); PIN(Fim); PIN(Gre); PIN(Gim); PIN(tc[0]); PIN(tc[1]); PIN(ts[0]); PIN(ts[1]);
        ST(19);
        BARRIER();
        ST(4);
        bst4<SC1>(uo0, rw, l * 16, WS_U + b * 8192 + w * 2048);
        bst4<SC1>(uo1, rw, l * 16 + 1024, WS_U + b * 8192 + w * 2048);
        xl[0] = *(const u2*)((const char*)x + xoff); xl[1] = *(const u2*)((const char*)x + xoff + 512);
        FENCE();
        tag = __builtin_amdgcn_readfirstlane(epoch) + 1u;
        consts3();
#pragma unroll
        for (int u = 0; u < 4; ++u) {
            float a1 = (float)(((4 * (g & 1) + u) * (256 * (m >> 1) + b)) & 2047) * (1.0f / 2048);
            float c1 = cosr(a1), s1 = sinr(a1);
            u32 nb = (m & 1) ? pk_b(s1, c1) : pk_b(c1, -s1);
            N0[u] = g < 2 ? nb : 0u; N1[u] = g < 2 ? 0u : nb;
        }
        PIN(N0); PIN(N1);
        WAITV(0);
        BARRIER();
        ST(5);
        u4 d[2], f[2], e[2];
        const int uo = (tid >> 1) * 8192 + (tid & 1) * 16;
        d[0] = bld4<SC1>(rw, uo, WS_U + b * 32); d[1] = bld4<SC1>(rw, uo, WS_U + 128 * 8192 + b * 32);
        const u32 fd0 = __builtin_amdgcn_raw_buffer_load_b32(rw, uo, WS_WT + b * 32, SC1);
        const u32 fd1 = __builtin_amdgcn_raw_buffer_load_b32(rw, uo, WS_WT + 128 * 8192 + b * 32, SC1);
        FENCE();
        poll2(d[0], d[1], rw, uo, WS_U + b * 32, WS_U + 128 * 8192 + b * 32);
        FENCE();
        f[0] = bld4<SC1>(rw, uo, WS_WT + b * 32); f[1] = bld4<SC1>(rw, uo, WS_WT + 128 * 8192 + b * 32);
        char* sp = scr + SCR_S + (tid ^ ((tid >> 4) & 1)) * 16;
        *(u4*)sp = d[0]; *(u4*)(sp + 4096) = d[1];
        BARRIER();
        if (b == 0) {
            f[0] = bld4<SC1>(rw, tid * 16, WS_WA); f[1] = bld4<SC1>(rw, tid * 16 + 4096, WS_WA);
            e[0] = bld4<SC1>(rw, tid * 16, WS_WB); e[1] = bld4<SC1>(rw, tid * 16 + 4096, WS_WB);
            if (tid == 0) __hip_atomic_store((GAS u32*)(ws + WS_EP), tag, __ATOMIC_RELAXED, __HIP_MEMORY_SCOPE_AGENT);
        }
        ST(6);
        u2 ld[4], wl[4];
        const int so = (64 * g + m) * 32 + (((w >> 1) ^ (m >> 3)) * 16) + (w & 1) * 8;
#pragma unroll
        for (int q = 0; q < 4; ++q) ld[q] = *(const u2*)(scr + SCR_S + so + 512 * q);
        FENCE();
        f4 Xre[2], Xim[2];
#pragma unroll
        for (int e = 0; e < 2; ++e) {
            u4 A = {ld[0][e], ld[1][e], ld[2][e], ld[3][e]};
            f4 Dre = mma_h(A, Fre, Z4), Dim = mma_h(A, Fim, Z4);
            f2 r0 = fma2(lo2(Dim), ts[0], lo2(Dre) * tc[0]), i0 = fma2(-lo2(Dre), ts[0], lo2(Dim) * tc[0]);
            f2 r1 = fma2(hi2(Dim), ts[1], hi2(Dre) * tc[1]), i1 = fma2(-hi2(Dre), ts[1], hi2(Dim) * tc[1]);
            u4 B = {pk_h(r0[0], i0[0]), pk_h(r0[1], i0[1]), pk_h(r1[0], i1[0]), pk_h(r1[1], i1[1])};
            Xre[e] = mma_h(Fre, B, Z4); Xim[e] = mma_h(Fim, B, Z4);
        }
        *(u4*)(sp + 8192) = f[0]; *(u4*)(sp + 12288) = f[1];
        asm volatile("" :: "v"(fd0), "v"(fd1));
        f2* sx = (f2*)(scr + SCR_X);
        if (b == 0) {
            *(u4*)(sp - SCR_S) = e[0]; *(u4*)(sp - SCR_S + 4096) = e[1];
#pragma unroll
            for (int e = 0; e < 2; ++e)
#pragma unroll
                for (int i = 0; i < 4; ++i) sx[2 * w + e + 8 * (m + 16 * (4 * g + i))] = (f2){Xre[e][i], Xim[e][i]};
        }
        BARRIER();
        ST(20);
#pragma unroll
        for (int q = 0; q < 4; ++q) wl[q] = *(const u2*)(scr + SCR_S + so + 8192 + 512 * q);
        FENCE();
        f2 yr[2][2], yi[2][2];
#pragma unroll
        for (int e = 0; e < 2; ++e)
#pragma unroll
            for (int hh = 0; hh < 2; ++hh) {
                f2 wre = {bf_lo(wl[2 * hh][e]), bf_lo(wl[2 * hh + 1][e])}, wim = {bf_hi(wl[2 * hh][e]), bf_hi(wl[2 * hh + 1][e])};
                f2 xr = hh ? hi2(Xre[e]) : lo2(Xre[e]), xi = hh ? hi2(Xim[e]) : lo2(Xim[e]);
                yr[e][hh] = fma2(-xi, wim, xr * wre); yi[e][hh] = fma2(xr, wim, xi * wre);
            }
        if (b == 0) {
#pragma unroll
            for (int i = 0; i < 4; ++i) {
                u2 wb = *(const u2*)(scr + so + 512 * i);
#pragma unroll
                for (int e = 0; e < 2; ++e) {
                    f2 xm = sx[(2048 - (2 * w + e + 8 * (m + 16 * (4 * g + i)))) & 2047];
                    float br = bf_lo(wb[e]), bi = bf_hi(wb[e]);
                    yr[e][i >> 1][i & 1] += xm[0] * br + xm[1] * bi;
                    yi[e][i >> 1][i & 1] += xm[0] * bi - xm[1] * br;
                }
            }
        }
        char* pv = scr + SCR_S;
#pragma unroll
        for (int e = 0; e < 2; ++e) {
            u4 Y = {pk_b(yr[e][0][0], yi[e][0][0]), pk_b(yr[e][0][1], yi[e][0][1]), pk_b(yr[e][1][0], yi[e][1][0]), pk_b(yr[e][1][1], yi[e][1][1])};
            f4 Dre = mma_b(Y, Gre, Z4), Dim = mma_b(Y, Gim, Z4);
            f2 r0 = fma2(-lo2(Dim), ts[0], lo2(Dre) * tc[0]), i0 = fma2(lo2(Dre), ts[0], lo2(Dim) * tc[0]);
            f2 r1 = fma2(-hi2(Dim), ts[1], hi2(Dre) * tc[1]), i1 = fma2(hi2(Dre), ts[1], hi2(Dim) * tc[1]);
            u4 A = {pk_b(r0[0], i0[0]), pk_b(r0[1], i0[1]), pk_b(r1[0], i1[0]), pk_b(r1[1], i1[1])};
            f4 Ere = mma_b(A, Gre, Z4), Eim = mma_b(A, Gim, Z4);
            u4 o;
#pragma unroll
            for (int i = 0; i < 4; ++i) o[i] = clean(pk_b(Ere[i], Eim[i]));
            *(u4*)(pv + (2 * w + e) * 1024 + ((m * 4 + g) ^ ((w >> 1) * 4)) * 16) = o;
        }
        BARRIER();
        ST(7);
#pragma unroll
        for (int it = 0; it < 2; ++it) {
            const int ch = tid + 256 * it;
            const u32* q = (const u32*)(pv + (ch & 1) * 4096 + (((ch >> 1) * 4) ^ ((ch & 1) * 64)));
            bst4<SC1>((u4){q[0], q[256], q[512], q[768]}, rw, ch * 16, WS_V + b * 8192);
        }
        WAITV(0);
        BARRIER();
        ST(8);
        const int vo = (2 * m + (g >> 1)) * 8192 + (g & 1) * 16, vs = WS_V + w * 64 * 8192 + b * 32;
        u4 v0 = bld4<SC1>(rw, vo, vs), v1 = bld4<SC1>(rw, vo, vs + 32 * 8192);
        poll2(v0, v1, rw, vo, vs, vs + 32 * 8192);
        ST(9);
        char* sz = scr + SCR_X + g * 2048 + w * 256 + m * 8;
#pragma unroll
        for (int hh = 0; hh < 2; ++hh) {
            f4 D0 = mma_b(N0, hh ? v1 : v0, Z4), D1 = mma_b(N1, hh ? v1 : v0, Z4);
            *(u2*)(sz + hh * 128) = (u2){pk_b(D0[0], D0[1]), pk_b(D1[0], D1[1])};
            *(u2*)(sz + 1024 + hh * 128) = (u2){pk_b(D0[2], D0[3]), pk_b(D1[2], D1[3])};
        }
    } else {
        auto announce = [&](int which) {
            if (l < 8) __hip_atomic_store((GAS u32*)(ws + WS_FA) + which * 2048 + l * 256 + b, tag, __ATOMIC_RELAXED, __HIP_MEMORY_SCOPE_AGENT);
            FENCE();
        };
        auto all_announced = [&](int which) {
#pragma nounroll
            for (int sp = 0; sp < SPIN_MAX; ++sp) {
                FENCE();
                u4 f = bld4<SC1>(rw, l * 16, WS_FA + which * 8192 + (blockIdx.x & 7) * 1024);
                if (__builtin_amdgcn_ballot_w64(f[0] != tag || f[1] != tag || f[2] != tag || f[3] != tag) == 0) break;
                __builtin_amdgcn_s_sleep(4);
            }
        };
        const rsrc_t rr = mkrs(wr, 1052672), ri = mkrs(wi, 1052672);
        const int j7 = l >> 3, p4 = (l >> 1) & 3, ee = l & 1;
        u2 fr[4], fi[4]; u32 sr_, si_;
        if (w == 4) {
            const int e = ee * 4 * 514 + (32 * j7 + 4 * p4) * 2;
#pragma unroll
            for (int i = 0; i < 4; ++i) { fr[i] = bld2<0>(rr, e + i * 514, 8 * b * 514); fi[i] = bld2<0>(ri, e + i * 514, 8 * b * 514); }
        } else if (w == 5) {
            const int e = (2045 - 8 * b) * 514 + 253 * 2 - ee * 4 * 514 - (32 * j7 + 4 * p4) * 2;
#pragma unroll
            for (int i = 1; i < 4; ++i) { fr[i] = bld2<0>(rr, e + (3 - i) * 514, 0); fi[i] = bld2<0>(ri, e + (3 - i) * 514, 0); }
            const int e0 = e + 3 * 514 - (b == 0 && !ee ? 2048 * 514 : 0);
            fr[0] = bld2<0>(rr, e0, 0); fi[0] = bld2<0>(ri, e0, 0);
            const int e1 = (8 * b + (l & 7)) * 514 + (16 + 32 * j7) * 2;
            sr_ = __builtin_amdgcn_raw_buffer_load_b16(rr, e1, 0, 0); si_ = __builtin_amdgcn_raw_buffer_load_b16(ri, e1, 0, 0);
        } else if (w == 6) {
            const int c = j7 & 3, f = 8 * b + (l & 7);
            const int e = ((c & 1) ? (2048 - f) & 2047 : f) * 514 + (c >> 1) * 512;
            sr_ = __builtin_amdgcn_raw_buffer_load_b16(rr, e, 0, 0); si_ = __builtin_amdgcn_raw_buffer_load_b16(ri, e, 0, 0);
        }
        FENCE();
        ST(2);
#pragma unroll
        for (int jj = 0; jj < 7; ++jj) {
            const int cj_ = (4 * jj) % 14, rj = 18 * jj + (4 * jj) / 14;
            w1o[jj] = (row0 + rj) * 3584 + (cch0 + cj_) * 16 + (cch0 + cj_ >= 14 ? 3584 - 224 : 0);
            PIN(w1o[jj]);
        }
        u32 cpf = 0;
        if (w == 6 && l < CODE_PF) cpf = *(const volatile GAS u32*)(pc0 + ((b & 31) * CODE_PF + l) * 128);
        if (w == 7) {
#pragma unroll
            for (int i = 0; i < 8; ++i) bst4<SC1>(SENT4, rw, l * 16 + (i & 3) * 1024, WS_U + b * 8192 + (i >> 2) * 4096);
        }
        BARRIER();
        if (w < 6) {
            const int wo = p4 * 2048 + j7 * 32 + ee * 16, wso = WS_WT + b * 8192 + (w - 4) * 256;
#pragma unroll
            for (int c = 0; c < 4; ++c) {
                u4 o;
#pragma unroll
                for (int i = 0; i < 4; ++i)
                    o[i] = w == 4 ? __builtin_amdgcn_perm(fi[i][c >> 1], fr[i][c >> 1], (c & 1) ? 0x07060302u : 0x05040100u)
                                  : __builtin_amdgcn_perm(fi[i][1 - (c >> 1)], fr[i][1 - (c >> 1)], (c & 1) ? 0x05040100u : 0x07060302u) ^ 0x80000000u;
                bst4<SC1>(o, rw, wo + c * 512, wso);
            }
            if (w == 5) __builtin_amdgcn_raw_buffer_store_b32(sr_ | (si_ << 16), rw, l * 4, wso, SC1);
        } else if (w == 6) {
            float re = bf_lo(sr_), im = bf_lo(si_);
            re += dpp<0x128>(re); im -= dpp<0x128>(im);
            u32 a = __builtin_bit_cast(u32, re), a2 = a, c = __builtin_bit_cast(u32, im), c2 = c;
            asm("" : "+v"(a2), "+v"(c2));
            auto pr = __builtin_amdgcn_permlane16_swap(a, a2, false, false);
            auto pi = __builtin_amdgcn_permlane16_swap(c, c2, false, false);
            const float ar = __builtin_bit_cast(float, (u32)pr[0]), br = __builtin_bit_cast(float, (u32)pr[1]);
            const float ai = __builtin_bit_cast(float, (u32)pi[0]), bi = __builtin_bit_cast(float, (u32)pi[1]);
            if (l < 8) {
                __builtin_amdgcn_raw_buffer_store_b32(pk_b(0.25f * (ar + br), 0.25f * (ai + bi)), rw, l * 4, WS_WA + b * 32, SC1);
                __builtin_amdgcn_raw_buffer_store_b32(pk_b(0.25f * (ar - br), 0.25f * (ai - bi)), rw, l * 4, WS_WB + b * 32, SC1);
            }
        }
        ST(18);
        tag = __builtin_amdgcn_readfirstlane(epoch) + 1u;
        WAITV(0);
        asm volatile("" :: "v"(cpf));
        if (w == 7) announce(0);
        ST(19);
        BARRIER();
        if (w == 7) all_announced(0);
        else __builtin_amdgcn_s_sleep(2);
        xl[0] = *(const u2*)((const char*)x + xoff); xl[1] = *(const u2*)((const char*)x + xoff + 512);
        FENCE();
        bg(0, BG2);
        ST(4);
        BARRIER();
        __builtin_amdgcn_s_sleep(2);
#pragma unroll
        for (int i = 0; i < 2; ++i) bst4<SC1>(SENT4, rw, l * 16 + i * 1024, WS_V + b * 8192 + (w - 4) * 2048);
        FENCE();
        consts0();
        consts3();
        bg(BG2, BG3);
        WAITV(BG3 - BG2);
        ST(5);
        BARRIER();
        if (w == 7) announce(1);
#pragma unroll
        for (int i = 0; i < 2; ++i) bst4<SC1>(SENT4, rw, (l >> 3) * 256 + (l & 7) * 16 + i * 2048, WS_LN2 + (b >> 1) * 16384 + (b & 1) * 128 + (w - 4) * 4096);
        bg(BG3, BG4);
        BARRIER();
        bg(BG4, BG5);
        WAITV(BG5 - BG3);
        ST(6);
        BARRIER();
        if (w == 7) all_announced(1);
        else __builtin_amdgcn_s_sleep(3);
        bg(BG5, BG6);
        ST(7);
        BARRIER();
        __builtin_amdgcn_s_sleep(4);
        bg(BG6, BG7);
    }
    BARRIER();
    if (!aw) bg(BG7, BG8);
    {
        u4 A = *(const u4*)(scr + SCR_X + w * 1024 + m * 64 + g * 16);
        f4 Dre = mma_b(A, Gre3, Z4), Dim = mma_b(A, Gim3, Z4), Hre = mma_b(A, SGre, Z4), Him = mma_b(A, SGim, Z4);
        if (g == 0) { Dre[0] = Hre[0]; Dim[0] = Him[0]; }
        f2 r0 = fma2(-lo2(Dim), tws[0], lo2(Dre) * twc[0]), i0 = fma2(lo2(Dre), tws[0], lo2(Dim) * twc[0]);
        f2 r1 = fma2(-hi2(Dim), tws[1], hi2(Dre) * twc[1]), i1 = fma2(hi2(Dre), tws[1], hi2(Dim) * twc[1]);
        u4 A2 = {pk_b(r0[0], i0[0]), pk_b(r0[1], i0[1]), pk_b(r1[0], i1[0]), pk_b(r1[1], i1[1])};
        f2 h[4]; u2 hp[2];
#pragma unroll
        for (int t = 0; t < 2; ++t) {
            f4 Yv = mma_b(A2, GA[t], Z4);
            u32 h0 = pk_b2(bf_2(xl[t][0]) + bf_2(pk_b(Yv[0], Yv[1]))), h1 = pk_b2(bf_2(xl[t][1]) + bf_2(pk_b(Yv[2], Yv[3])));
            hp[t] = (u2){h0, h1};
            h[2 * t] = bf_2(h0); h[2 * t + 1] = bf_2(h1);
        }
        f2 s2 = (h[0] + h[1]) + (h[2] + h[3]);
        const float mean = red_lanes<true>(s2[0] + s2[1]) * (1.0f / 512);
        f2 q2 = {0.f, 0.f};
#pragma unroll
        for (int j = 0; j < 4; ++j) { f2 d = h[j] - mean; q2 = fma2(d, d, q2); }
        const float inv = ln_inv(red_lanes<true>(q2[0] + q2[1]) * (1.0f / 512));
        const float mb = rbf(mean);
#pragma unroll
        for (int t = 0; t < 2; ++t)
            *(u2*)(scr + ((8 * t + (m >> 1)) * 4 + g) * 128 + (w ^ (m >> 1)) * 16 + (m & 1) * 8) =
                (u2){clean(pk_b2(bf_2(pk_b2(h[2 * t] - mb)) * inv)), clean(pk_b2(bf_2(pk_b2(h[2 * t + 1] - mb)) * inv))};
        WAITV(0);
        BARRIER();
        u4 lo = *(const u4*)(scr + (tid ^ ((tid >> 5) & 7)) * 16);
        bst4<SC1>(lo, rw, (tid >> 3) * 256 + (tid & 7) * 16, WS_LN2 + (b >> 1) * 16384 + (b & 1) * 128);
        FENCE();
        if (aw) WAITV(0); else __builtin_amdgcn_s_sleep(8);
        bst2<SC1>(hp[0], ro, xoff, 0); bst2<SC1>(hp[1], ro, xoff + 512, 0);
        FENCE();
        ST(10);
    }
    auto stage_out = [&](int r) {
        w2_read(W[r], LDS_SCR + (w & 3) * 8192 + (r & 1) * 4096);
        asm volatile("s_waitcnt lgkmcnt(0)" ::: "memory");
        PIN(W[r][0]); PIN(W[r][1]); PIN(W[r][2]); PIN(W[r][3]);
        if (r + 2 < 7) w2_glds(pw, r + 2, lds0 + LDS_SCR + (w & 3) * 8192 + (r & 1) * 4096);
    };
    auto publish = [&]() {
        u32 prev = 0;
        if (l == 0) prev = __hip_atomic_fetch_add(hw + 4, 1u, __ATOMIC_RELAXED, __HIP_MEMORY_SCOPE_WORKGROUP);
        if (__builtin_amdgcn_readfirstlane(prev) == 7u && l < 2)
            __hip_atomic_store(fl + (l ? 4096 + (b >> 1) * 32 + (b & 1) : (mt1 * 8 + (nt1 >> 1)) * 32 + (nt1 & 1)), tag, __ATOMIC_RELAXED, __HIP_MEMORY_SCOPE_AGENT);
        FENCE();
    };
    {
        const int tg = w & 3;
        char* xch = smem + XCH + tg * 3584;
        const int hoff = EXP_HOFF + WS_H1 + (mt1 * 8 + 2 * tg) * (56 * 1024) + (nt1 >> 1) * 7168;
        if (aw) {
            const int toff = WS_LN2 + (mt1 * 8 + 2 * tg) * 16384;
            LAS char* wb0 = (LAS char*)smem + (4 * g + (m >> 2)) * 224 + (m & 3) * 8;
            LAS char* wb[4];
            int tq[4];
#pragma unroll
            for (int q = 0; q < 4; ++q) { const int qq = (q + rq) & 3; wb[q] = wb0 + qq * 28672; tq[q] = toff + qq * 4096; }
            u4 T[2][16], A[2][7];
            auto ldt = [&](int kk) {
#pragma unroll
                for (int tt = 0; tt < 2; ++tt) T[tt][kk] = bld4<SC1>(rw, l * 16 + (kk & 3) * 1024, tq[kk >> 2] + tt * 16384);
            };
#pragma unroll
            for (int i = 0; i < 7; ++i) A[0][i] = ldw1(wb[0], 0, i);
            if (l == 0) __hip_atomic_fetch_add(hw + 5, 1u, __ATOMIC_RELAXED, __HIP_MEMORY_SCOPE_WORKGROUP);
            ST(11);
#pragma unroll
            for (int kk = 0; kk < G1_DEPTH; ++kk) { ldt(kk); SCHED(); }
            f4 acc[2][7];
#pragma unroll
            for (int tt = 0; tt < 2; ++tt)
#pragma unroll
                for (int i = 0; i < 7; ++i) acc[tt][i] = Z4;
#pragma unroll
            for (int kk = 0; kk < 16; ++kk) {
                if ((kk & 1) == 0) {
                    if (__builtin_expect(any_sent(umax(umax(max4(T[0][kk]), max4(T[1][kk])), umax(max4(T[0][kk + 1]), max4(T[1][kk + 1])))), 0)) {
                        wait_tok(T[0][kk], T[1][kk], rw, l * 16, tq[kk >> 2] + (kk & 3) * 1024);
                        wait_tok(T[0][kk + 1], T[1][kk + 1], rw, l * 16, tq[kk >> 2] + ((kk + 1) & 3) * 1024);
                    }
                    if (kk == 0) ST(12);
                    if (kk == (BG8 / 7) * 4 - 2) lds_wait(hw + 5, 8u);
                }
                if (kk + G1_DEPTH < 16) ldt(kk + G1_DEPTH);
                if (kk < 15) {
#pragma unroll
                    for (int i = 0; i < 7; ++i) A[(kk + 1) & 1][i] = ldw1(wb[(kk + 1) >> 2], (kk + 1) & 3, i);
                }
#pragma unroll
                for (int i = 0; i < 7; ++i)
#pragma unroll
                    for (int tt = 0; tt < 2; ++tt) acc[tt][i] = mma_b(A[kk & 1][i], T[tt][kk], acc[tt][i]);
#pragma unroll
                for (int i = 0; i < 14; ++i) {
                    __builtin_amdgcn_sched_group_barrier(0x8, 1, 0);
                    if (i == 1 || i == 7) __builtin_amdgcn_sched_group_barrier(0x20, 1, 0);
                    __builtin_amdgcn_sched_group_barrier(0x100, 1, 0);
                    if ((kk & 1) && i >= 3) __builtin_amdgcn_sched_group_barrier(0x2, 1, 0);
                }
                if (kk & 1) {} else SCHED();
            }
#pragma unroll
            for (int q = 0; q < 3; ++q) {
                u2 p0 = pk4(acc[1][2 * q]), p1 = pk4(acc[1][2 * q + 1]);
                *(u4*)(xch + q * 1024 + l * 16) = (u4){p0[0], p0[1], p1[0], p1[1]};
            }
            *(u2*)(xch + 3072 + l * 8) = pk4(acc[1][6]);
            lds_set(hw + tg, 1u);
            ST(13);
#pragma unroll
            for (int q = 0; q < 3; ++q) {
                u2 p0 = pk4(acc[0][2 * q]), p1 = pk4(acc[0][2 * q + 1]);
                bst4<SC1>(silu_pk4((u4){p0[0], p0[1], p1[0], p1[1]}), rw, l * 16, hoff + ((nt1 & 1) * 3 + q) * 1024);
            }
            { u2 p = pk4(acc[0][6]); bst2<SC1>(silu_pk2(p[0], p[1]), rw, l * 8, hoff + 6144 + (nt1 & 1) * 512); }
        } else {
            bg(BG8, 28);
            WAITV(0);
            if (l == 0) __hip_atomic_fetch_add(hw + 5, 1u, __ATOMIC_RELAXED, __HIP_MEMORY_SCOPE_WORKGROUP);
            __builtin_amdgcn_s_sleep(W2_SLEEP);
            lds_wait(hw + 5, 8u);
            w2_glds(pw, 0, lds0 + LDS_SCR + tg * 8192);
            w2_glds(pw, 1, lds0 + LDS_SCR + tg * 8192 + 4096);
#pragma unroll
            for (int r = 0; r < B_R; ++r) { WAITV(4); stage_out(r); }
            ST(12);
            lds_wait(hw + tg, 1u);
            ST(13);
#pragma unroll
            for (int q = 0; q < 3; ++q)
                bst4<SC1>(silu_pk4(*(const u4*)(xch + q * 1024 + l * 16)), rw, l * 16, hoff + 56 * 1024 + ((nt1 & 1) * 3 + q) * 1024);
            { u2 p = *(const u2*)(xch + 3072 + l * 8); bst2<SC1>(silu_pk2(p[0], p[1]), rw, l * 8, hoff + 56 * 1024 + 6144 + (nt1 & 1) * 512); }
        }
        if (l == 0) __hip_atomic_fetch_add(hw + 6, 1u, __ATOMIC_RELAXED, __HIP_MEMORY_SCOPE_WORKGROUP);
        ST(14);
        WAITV(0);
        publish();
        ST(21);
        if (aw) {
            lds_wait(hw + FLOOD_W, 8u);
#pragma unroll
            for (int ks = 0; ks < 7; ++ks) w2_glds(pw, ks, lds0 + tg * 28672 + ks * 4096);
        } else {
            stage_out(B_R);
#pragma unroll
            for (int r = B_R + 1; r < 7; ++r) { if (r + 1 < 7) WAITV(4); else WAITV(0); stage_out(r); }
        }
    }
    {
        ST(15);
        const int rot = w & 3;
        if (aw) {
            flag_wait(fl + ((l & 4) ? 4096 + (grp * 8 + th * 4 + rot) * 32 : (grp * 8 + (pw ^ ((l & 2) * 2))) * 32) + (l & 1), tag);
            lds_set(hw + 8 + rot, tag);
        } else {
#pragma nounroll
            for (int sp = 0; sp < SPIN_MAX; ++sp) {
                if (__builtin_amdgcn_readfirstlane(__hip_atomic_load(hw + 8 + rot, __ATOMIC_RELAXED, __HIP_MEMORY_SCOPE_WORKGROUP)) == tag) break;
                __builtin_amdgcn_s_sleep(1);
            }
            FENCE();
        }
        ST(16);
        const int toff = WS_H1 + (grp * 8 + th * 4) * (56 * 1024) + pw * 7168;
        int to[4];
#pragma unroll
        for (int tt = 0; tt < 4; ++tt) to[tt] = toff + ((tt + rot) & 3) * (56 * 1024);
        u4 T[7][4];
        auto ldtok = [&](int ks) {
#pragma unroll
            for (int tt = 0; tt < 4; ++tt) {
                if (ks < 6) T[ks][tt] = bld4<EXP_AUX>(rw, l * 16, to[tt] + ko[ks] * 1024);
                else {
                    u2 a = bld2<SC1>(rw, l * 8 + 2048, to[tt] + 4096), c = bld2<SC1>(rw, l * 8 + 2560, to[tt] + 4096);
                    T[ks][tt] = (u4){a[0], a[1], c[0], c[1]};
                }
            }
            SCHED();
        };
#pragma unroll
        for (int ks = 0; ks < G2_DEPTH; ++ks) ldtok(ks);
        if (aw) {
#pragma unroll
            for (int ks = 0; ks < 7; ++ks) w2_read(W[ks], rot * 28672 + ks * 4096);
            asm volatile("s_waitcnt lgkmcnt(0)" ::: "memory");
            if (l == 0) __hip_atomic_fetch_add(hw + 7, 1u, __ATOMIC_RELAXED, __HIP_MEMORY_SCOPE_WORKGROUP);
        }
        const int rho = grp * 128 + th * 64 + rot * 16 + m;
        const int ooff = (256 * (rho & 7) + (rho >> 3)) * 1024 + fb * 128 + (w >> 2) * 64 + g * 16;
        const u4 hres = bld4<SC1>(ro, ooff, 0);
        SCHED();
        f4 acc[4][4];
#pragma unroll
        for (int tt = 0; tt < 4; ++tt)
#pragma unroll
            for (int t = 0; t < 4; ++t) acc[tt][t] = Z4;
#pragma unroll
        for (int ks = 0; ks < 7; ++ks) {
            if (ks + G2_DEPTH < 7) { PIN(T[ks][0]); ldtok(ks + G2_DEPTH); }
#pragma unroll
            for (int tt = 0; tt < 4; ++tt)
#pragma unroll
                for (int t = 0; t < 4; ++t) {
                    acc[tt][t] = mma_b(W[ks][t], T[ks][tt], acc[tt][t]);
                }
            SCHED();
        }
        ST(17);
        lds_wait(hw + 7, 4u);
        char* dp = smem + w * 16384 + l * 16;
#pragma unroll
        for (int tt = 1; tt < 4; ++tt)
#pragma unroll
            for (int t = 0; t < 4; ++t) *(f4*)(dp + (((tt + rot) & 3) * 4 + t) * 1024) = acc[tt][t];
        f4 s0, s1;
        if (aw) { *(f4*)(dp + (rot * 4 + 2) * 1024) = acc[0][2]; *(f4*)(dp + (rot * 4 + 3) * 1024) = acc[0][3]; s0 = acc[0][0]; s1 = acc[0][1]; }
        else { *(f4*)(dp + (rot * 4) * 1024) = acc[0][0]; *(f4*)(dp + (rot * 4 + 1) * 1024) = acc[0][1]; s0 = acc[0][2]; s1 = acc[0][3]; }
        BARRIER();
        f4 r0[7], r1[7];
#pragma unroll
        for (int j = 1; j < 8; ++j) {
            const char* rp = smem + (((w + j) & 7) * 16 + rot * 4 + (w >> 2) * 2) * 1024 + l * 16;
            r0[j - 1] = *(const f4*)rp; r1[j - 1] = *(const f4*)(rp + 1024);
        }
        SCHED();
        s0 = ((s0 + r0[0]) + (r0[1] + r0[2])) + ((r0[3] + r0[4]) + (r0[5] + r0[6]));
        s1 = ((s1 + r1[0]) + (r1[1] + r1[2])) + ((r1[3] + r1[4]) + (r1[5] + r1[6]));
        u2 p0 = pk4(s0), p1 = pk4(s1);
        bst4<AUX_OUT>((u4){pk_b2(bf_2(hres[0]) + bf_2(p0[0])), pk_b2(bf_2(hres[1]) + bf_2(p0[1])),
                           pk_b2(bf_2(hres[2]) + bf_2(p1[0])), pk_b2(bf_2(hres[3]) + bf_2(p1[1]))}, ro, ooff, 0);
    }
    ST_END(ws, 0);
}

extern "C" void kernel_launch(void* const* d_in, const int* in_sizes, int n_in, void* d_out, int out_size,
                              void* d_ws, size_t ws_size, hipStream_t stream) {
    (void)hipFuncSetAttribute((const void*)k_fused, hipFuncAttributeMaxDynamicSharedMemorySize, LDS_B);
    k_fused<<<256, 512, LDS_B, stream>>>((const uint16_t*)d_in[0], (const uint16_t*)d_in[1], (const uint16_t*)d_in[2],
                                         (const uint16_t*)d_in[4], (const uint16_t*)d_in[5], (uint16_t*)d_out, (char*)d_ws);
}
```

```cpp
#include <hip/hip_runtime.h>
#include <hip/hip_bf16.h>
#include <stdint.h>

typedef _Float16 h8 __attribute__((ext_vector_type(8)));
typedef _Float16 h4 __attribute__((ext_vector_type(4)));
typedef _Float16 h2 __attribute__((ext_vector_type(2)));
typedef __bf16 b8 __attribute__((ext_vector_type(8)));
typedef __bf16 b2 __attribute__((ext_vector_type(2)));
typedef short s4 __attribute__((ext_vector_type(4)));
typedef float f4 __attribute__((ext_vector_type(4)));
typedef float f2 __attribute__((ext_vector_type(2)));
typedef unsigned u32;
typedef u32 u4 __attribute__((ext_vector_type(4)));
typedef u32 u2 __attribute__((ext_vector_type(2)));
#define LAS __attribute__((address_space(3)))
#define GAS __attribute__((address_space(1)))
#define DI __device__ __forceinline__
#define FENCE() asm volatile("" ::: "memory")
#define PIN(x) asm volatile("" : "+v"(x))
#define SCHED() __builtin_amdgcn_sched_barrier(0)
#define WAITV(n) asm volatile("s_waitcnt vmcnt(%0)" :: "n"(n) : "memory")
#define BARRIER() do { asm volatile("s_waitcnt lgkmcnt(0)" ::: "memory"); __builtin_amdgcn_s_barrier(); FENCE(); } while (0)
constexpr int SC1 = 16, NT = 2;
constexpr u32 SENT = 0xffffffffu;
constexpr int SPIN_MAX = 1 << 20;

DI u32 pk_h(float a, float b) { f2 v = {a, b}; h2 r = __builtin_convertvector(v, h2); return __builtin_bit_cast(u32, r); }
DI u32 pk_b(float a, float b) { f2 v = {a, b}; b2 r = __builtin_convertvector(v, b2); return __builtin_bit_cast(u32, r); }
DI float bf_lo(u32 v) { return __builtin_bit_cast(float, v << 16); }
DI float bf_hi(u32 v) { return __builtin_bit_cast(float, v & 0xffff0000u); }
DI f2 bf_2(u32 v) { return (f2){bf_lo(v), bf_hi(v)}; }
DI u32 pk_b2(f2 v) { return pk_b(v[0], v[1]); }
DI float rbf(float a) { return bf_lo(pk_b(a, a)); }
DI float cosr(float rev) { return __builtin_amdgcn_cosf(rev); }
DI float sinr(float rev) { return __builtin_amdgcn_sinf(rev); }
DI f2 lo2(f4 v) { return __builtin_shufflevector(v, v, 0, 1); }
DI f2 hi2(f4 v) { return __builtin_shufflevector(v, v, 2, 3); }
DI f2 fma2(f2 a, f2 b, f2 c) { return __builtin_elementwise_fma(a, b, c); }
DI u32 umin(u32 a, u32 b) { return a < b ? a : b; }
DI u32 umax(u32 a, u32 b) { return a > b ? a : b; }
DI u32 clean(u32 v) { return umin(v, SENT - 1); }
DI u32 max4(u4 v) { return umax(umax(umax(v[0], v[1]), v[2]), v[3]); }
#define Z4 ((f4){0, 0, 0, 0})
DI f4 mma_h(u4 a, u4 b, f4 c) { return __builtin_amdgcn_mfma_f32_16x16x32_f16(__builtin_bit_cast(h8, a), __builtin_bit_cast(h8, b), c, 0, 0, 0); }
DI f4 mma_b(u4 a, u4 b, f4 c) { return __builtin_amdgcn_mfma_f32_16x16x32_bf16(__builtin_bit_cast(b8, a), __builtin_bit_cast(b8, b), c, 0, 0, 0); }
DI f4 mma16_h(u2 a, u2 b, f4 c) { return __builtin_amdgcn_mfma_f32_16x16x16f16(__builtin_bit_cast(h4, a), __builtin_bit_cast(h4, b), c, 0, 0, 0); }
typedef __amdgpu_buffer_rsrc_t rsrc_t;
DI rsrc_t mkrs(const void* p, u32 bytes) { return __builtin_amdgcn_make_buffer_rsrc((void*)p, 0, bytes, 0x00020000); }
template <int AUX> DI u4 bld4(rsrc_t r, int v, int s) { return __builtin_amdgcn_raw_buffer_load_b128(r, v, s, AUX); }
template <int AUX> DI u2 bld2(rsrc_t r, int v, int s) { return __builtin_amdgcn_raw_buffer_load_b64(r, v, s, AUX); }
template <int AUX> DI void bst4(u4 d, rsrc_t r, int v, int s) {
    __builtin_amdgcn_raw_buffer_store_b128(d, r, v, s, AUX);
    asm volatile("s_nop 1" :: "v"(d));
}
template <int AUX> DI void bst2(u2 d, rsrc_t r, int v, int s) { __builtin_amdgcn_raw_buffer_store_b64(d, r, v, s, AUX); }

template <int CTRL> DI float dpp(float v) {
    return __builtin_bit_cast(float, __builtin_amdgcn_update_dpp(0, __builtin_bit_cast(int, v), CTRL, 0xf, 0xf, true));
}
template <bool ALL> DI float red_lanes(float v) {
    v += dpp<0xB1>(v);
    if (ALL) v += dpp<0x4E>(v);
    v += dpp<0x124>(v); v += dpp<0x128>(v);
    u32 x = __builtin_bit_cast(u32, v), y = x;
    asm("" : "+v"(y));
    auto r = __builtin_amdgcn_permlane16_swap(x, y, false, false);
    v = __builtin_bit_cast(float, (u32)r[0]) + __builtin_bit_cast(float, (u32)r[1]);
    x = __builtin_bit_cast(u32, v); y = x;
    asm("" : "+v"(y));
    auto q = __builtin_amdgcn_permlane32_swap(x, y, false, false);
    return __builtin_bit_cast(float, (u32)q[0]) + __builtin_bit_cast(float, (u32)q[1]);
}
DI float ln_inv(float var) { return __builtin_amdgcn_rcpf(rbf(__builtin_amdgcn_sqrtf(rbf(rbf(var) + 1e-5f)))); }


constexpr u32 WS_U = 0;
constexpr u32 WS_WT = WS_U + (2u << 20);
constexpr u32 WS_V = WS_WT + (2u << 20);
constexpr u32 WS_LN2 = WS_V + (2u << 20);
constexpr u32 WS_WA = WS_LN2 + (2u << 20);
constexpr u32 WS_WB = WS_WA + 8192u;
constexpr u32 WS_FA = WS_WB + 8192u;
constexpr u32 WS_FLAG = WS_FA + 16384u;
constexpr u32 WS_EP = WS_FLAG + 32768u;
constexpr u32 WS_H1 = WS_WA + (128u << 10);
constexpr u32 WS_END = WS_H1 + 7340032u;

constexpr int BG2 = 4, BG4 = 10, BG5 = 15, BG6 = 21;
constexpr int G1_DEPTH = 10, G2_DEPTH = 3;
constexpr int B_R = 5;
constexpr int CODE_PF = 9;
constexpr int LDS_SCR = 114688;
constexpr int SCR_X = 8448;
constexpr int SCR_S = SCR_X + 20480;
constexpr int SCR_F = 49024;
constexpr int XCH = 147456;
constexpr int LDS_B = 163840;

DI void glds16(const void* sbase, u32 voff, u32 lds_dst) {
    asm volatile("s_mov_b32 m0, %2\n\ts_nop 0\n\tglobal_load_lds_dwordx4 %0, %1" :: "v"(voff), "s"(sbase), "s"(lds_dst) : "memory");
}
DI bool any_sent(u32 v) { return __builtin_amdgcn_ballot_w64(v == SENT) != 0; }
DI void poll2(u4& a, u4& c, rsrc_t rw, int vo, int sa, int sc) {
#pragma nounroll
    for (int sp = 0; sp < SPIN_MAX; ++sp) {
        const bool ba = max4(a) == SENT, bc = max4(c) == SENT;
        if (__builtin_amdgcn_ballot_w64(ba || bc) == 0) break;
        __builtin_amdgcn_s_sleep(1);
        FENCE();
        if (ba) a = bld4<SC1>(rw, vo, sa);
        if (bc) c = bld4<SC1>(rw, vo, sc);
    }
}
DI void wait_tok(u4& t0, u4& t1, rsrc_t rw, int vo, int so) {
    if (any_sent(umax(max4(t0), max4(t1)))) {
#pragma nounroll
        for (int sp = 0; sp < SPIN_MAX; ++sp) {
            FENCE();
            t0 = bld4<SC1>(rw, vo, so); t1 = bld4<SC1>(rw, vo, so + 16384);
            if (!any_sent(umax(max4(t0), max4(t1)))) break;
        }
    }
}
DI u4 ldw1(LAS char* wb, int kk, int ft) {
    s4 a0 = __builtin_amdgcn_ds_read_tr16_b64_v4i16((LAS s4*)(wb + kk * 32 * 224 + ft * 32));
    s4 a1 = __builtin_amdgcn_ds_read_tr16_b64_v4i16((LAS s4*)(wb + (kk * 32 + 16) * 224 + ft * 32));
    u2 x0 = __builtin_bit_cast(u2, a0), x1 = __builtin_bit_cast(u2, a1);
    return (u4){x0[0], x0[1], x1[0], x1[1]};
}
DI u2 pk4(f4 a) { return (u2){pk_b(a[0], a[1]), pk_b(a[2], a[3])}; }
DI u2 silu_pk2(u32 p0, u32 p1) {
    u32 o0, o1; float a0, a1, a2, a3, t0, t1, t2, t3;
    asm("v_lshlrev_b32 %2, 16, %10\n\tv_and_b32 %3, 0xffff0000, %10\n\tv_lshlrev_b32 %4, 16, %11\n\tv_and_b32 %5, 0xffff0000, %11\n\t"
        "v_mul_f32 %6, %12, %2\n\tv_mul_f32 %7, %12, %3\n\tv_mul_f32 %8, %12, %4\n\tv_mul_f32 %9, %12, %5\n\t"
        "v_exp_f32 %6, %6\n\tv_exp_f32 %7, %7\n\tv_exp_f32 %8, %8\n\tv_exp_f32 %9, %9\n\t"
        "v_add_f32 %6, 1.0, %6\n\tv_add_f32 %7, 1.0, %7\n\tv_add_f32 %8, 1.0, %8\n\tv_add_f32 %9, 1.0, %9\n\t"
        "v_rcp_f32 %6, %6\n\tv_rcp_f32 %7, %7\n\tv_rcp_f32 %8, %8\n\tv_rcp_f32 %9, %9\n\t"
        "v_mul_f32 %2, %2, %6\n\tv_mul_f32 %3, %3, %7\n\tv_mul_f32 %4, %4, %8\n\tv_mul_f32 %5, %5, %9\n\t"
        "v_cvt_pk_bf16_f32 %0, %2, %3\n\tv_cvt_pk_bf16_f32 %1, %4, %5"
        : "=&v"(o0), "=&v"(o1), "=&v"(a0), "=&v"(a1), "=&v"(a2), "=&v"(a3), "=&v"(t0), "=&v"(t1), "=&v"(t2), "=&v"(t3)
        : "v"(p0), "v"(p1), "s"(-1.4426950408889634f));
    return (u2){o0, o1};
}
DI u4 silu_pk4(u4 p) { u2 a = silu_pk2(p[0], p[1]), b = silu_pk2(p[2], p[3]); return (u4){a[0], a[1], b[0], b[1]}; }
DI void lds_set(LAS u32* p, u32 v) {
    asm volatile("s_waitcnt lgkmcnt(0)" ::: "memory");
    __hip_atomic_store(p, v, __ATOMIC_RELAXED, __HIP_MEMORY_SCOPE_WORKGROUP);
}
DI void lds_wait(LAS u32* p, u32 v) {
#pragma nounroll
    for (int sp = 0; sp < SPIN_MAX; ++sp) {
        if (__builtin_amdgcn_readfirstlane(__hip_atomic_load(p, __ATOMIC_RELAXED, __HIP_MEMORY_SCOPE_WORKGROUP)) >= v) break;
        __builtin_amdgcn_s_sleep(1);
    }
    FENCE();
}
DI void flag_wait(GAS u32* p, u32 tag) {
#pragma nounroll
    for (int sp = 0; sp < SPIN_MAX; ++sp) {
        if (__builtin_amdgcn_ballot_w64(__hip_atomic_load(p, __ATOMIC_RELAXED, __HIP_MEMORY_SCOPE_AGENT) != tag) == 0) break;
        __builtin_amdgcn_s_sleep(2);
    }
    FENCE();
}
#define SENT4 ((u4){SENT, SENT, SENT, SENT})

__global__ __launch_bounds__(512, 2) void k_fused(const uint16_t* x, const uint16_t* wr, const uint16_t* wi, const uint16_t* w1,
                                                  const uint16_t* w2, uint16_t* out, char* ws) {
    extern __shared__ __attribute__((aligned(16))) char smem[];
    const int tid = threadIdx.x, l = tid & 63, g = l >> 4, m = l & 15;
    const int w = __builtin_amdgcn_readfirstlane(tid >> 6);
    const int b = (blockIdx.x & 7) * 32 + (blockIdx.x >> 3);
    const int mt1 = (b >> 6) * 4 + ((b >> 3) & 3), nt1 = ((b >> 5) & 1) * 8 + (b & 7);
    const int grp = b >> 4, th = (b >> 3) & 1, fb = b & 7;
    const rsrc_t rw = mkrs(ws, WS_END), ro = mkrs(out, 2097152);
    char* scr = smem + LDS_SCR;
    LAS u32* hw = (LAS u32*)((LAS char*)smem + LDS_SCR + SCR_F);
    const u32 lds0 = (u32)(uintptr_t)(LAS char*)smem;
    const float mf = (float)m, gf = (float)g, sg = (m & 1) ? -1.0f : 1.0f;
    const bool aw = w < 4;
    unsigned long long pc0;
    asm volatile("s_getpc_b64 %0" : "=s"(pc0));
    const int rq = ((b >> 3) + b) & 3;
    const int row0 = (tid & 255) / 14, cch0 = (tid & 255) - row0 * 14;
    const char* wsrc = (const char*)w1 + nt1 * 224;
    u32 w1o[7];
    auto w1_burst = [&](int i0, int n) {
#pragma unroll
        for (int i = i0; i < i0 + n; ++i) {
            const int q = (i / 7 + rq) & 3, j = i % 7;
            glds16(wsrc + q * (128 * 3584), w1o[j], lds0 + (w - 4) * 1024 + q * 28672 + j * 4096);
        }
    };
    const u32 epoch = __hip_atomic_load((GAS u32*)(ws + WS_EP), __ATOMIC_RELAXED, __HIP_MEMORY_SCOPE_AGENT);
    FENCE();
    const int xoff = (256 * w + b) * 1024 + m * 32 + g * 8;
    u2 xl[2];
    float c16[4], s16[4];
    f2 twc[2], tws[2];
    u4 Gre3, Gim3, SGre, SGim, GA[2];
    auto consts0 = [&]() {
#pragma unroll
        for (int u = 0; u < 4; ++u) {
            float a = (4.0f * gf + u) * (mf * (1.0f / 16));
            c16[u] = cosr(a); s16[u] = sinr(a);
            twc[u >> 1][u & 1] = cosr(a * (1.0f / 32)); tws[u >> 1][u & 1] = sinr(a * (1.0f / 32));
        }
    };
    auto consts3 = [&]() {
#pragma unroll
        for (int u = 0; u < 4; ++u) {
            const float k = 4.0f * gf + u;
            const bool k0 = u == 0 && g == 0;
            float cc = c16[u], ss = s16[u];
            Gre3[u] = pk_b(cc, -ss); Gim3[u] = pk_b(ss, cc);
            float a2 = (2.0f * k + 1.0f) * (mf * (1.0f / 32));
            u32 sp = pk_b(g < 2 ? 2.f * cc : 2.f * sg * cosr(a2), g < 2 ? -2.f * ss : -2.f * sg * sinr(a2));
            SGre[u] = k0 ? pk_b(1.0f, sg) : (g < 2 ? sp : 0u);
            SGim[u] = g < 2 ? 0u : sp;
            float a4 = k * (mf * (1.0f / 32)), c4 = 2.f * cosr(a4), s4_ = -2.f * sinr(a4);
            GA[0][u] = k0 ? pk_b(1.0f, sg) : pk_b(c4, s4_);
            GA[1][u] = k0 ? pk_b(1.0f, sg) : ((u & 1) ? pk_b(-c4, -s4_) : pk_b(c4, s4_));
        }
        PIN(Gre3); PIN(Gim3); PIN(SGre); PIN(SGim); PIN(GA[0]); PIN(GA[1]); PIN(twc[0]); PIN(twc[1]); PIN(tws[0]); PIN(tws[1]);
    };
    const int pw = (w + 4 * th) & 7;
    GAS u32* fl = (GAS u32*)(ws + WS_FLAG);
    int ko[7];
    {
        const int r6 = (fb < 6 ? fb : fb - 5) + 3 * (grp & 1);
#pragma unroll
        for (int ks = 0; ks < 6; ++ks) { int k = ks + r6; k = k >= 6 ? k - 6 : k; ko[ks] = k >= 6 ? k - 6 : k; }
        ko[6] = 6;
    }
    u4 W[7][4];
    const u32 w2vo = (l >> 3) * 1024 + (((l & 7) ^ (((l >> 4) & 1) << 2)) * 16);
    auto w2_glds = [&](int p_, int ks, u32 dst) {
        const int q = ko[ks];
        const char* src = (const char*)w2 + (224 * p_ + (ks == 6 ? 96 : (q < 3 ? 32 * q : 16 + 32 * q))) * 1024 + fb * 128;
        glds16(src, w2vo, dst); glds16(src + 8192, w2vo, dst + 1024);
        glds16(src + (ks == 6 ? 112 : 16) * 1024, w2vo, dst + 2048); glds16(src + (ks == 6 ? 120 : 24) * 1024, w2vo, dst + 3072);
    };
    LAS char* w2rd = (LAS char*)smem + (4 * g + (m >> 2)) * 128 + (((m & 3) ^ (((m >> 3) & 1) << 2)) * 16);
    auto w2_read = [&](u4 (&f)[4], int off) {
#pragma unroll
        for (int t = 0; t < 4; ++t) {
            LAS char* p = (LAS char*)(uintptr_t)((u32)(uintptr_t)w2rd ^ ((t >> 1) * 64)) + off + (t & 1) * 8;
            u2 x0 = __builtin_bit_cast(u2, __builtin_amdgcn_ds_read_tr16_b64_v4i16((LAS s4*)p));
            u2 x1 = __builtin_bit_cast(u2, __builtin_amdgcn_ds_read_tr16_b64_v4i16((LAS s4*)(p + 2048)));
            f[t] = (u4){x0[0], x0[1], x1[0], x1[1]};
        }
    };
    auto bg = [&](int i0, int i1) { w1_burst(i0, i1 - i0); };
    u32 tag;
    if (aw) {
        u32 xd[8];
        const u32* xr = (const u32*)(x + (256 * (2 * w + ((m >> 1) & 1)) + b) * 512) + 8 * g + 2 * (m >> 2) + (m & 1);
#pragma unroll
        for (int j = 0; j < 8; ++j) xd[j] = xr[32 * j];
        if (tid < 8) hw[tid] = 0;
        FENCE();
        {
            u4 FAre, FAim, Fre, Fim, Sre, Sim; u2 Mb; f2 pc[2], ps[2];
            consts0();
            float c0[8], s0[8];
#pragma unroll
            for (int j = 0; j < 8; ++j) {
                float a = (gf + 4.0f * j) * (mf * (1.0f / 32));
                c0[j] = cosr(a); s0[j] = m ? -sinr(a) : ((g & 1) ? -1.0f : 1.0f);
            }
            const float mo = (float)(2 * (m & 7) + 1) * (1.0f / 32);
#pragma unroll
            for (int u = 0; u < 4; ++u) {
                FAre[u] = pk_h(c0[2 * u], c0[2 * u + 1]); FAim[u] = pk_h(s0[2 * u], s0[2 * u + 1]);
                float cc = c16[u], ss = s16[u];
                Fre[u] = pk_h(cc, ss); Fim[u] = pk_h(-ss, cc);
                float a2 = (4.0f * gf + u) * mo;
                u32 re = pk_h(m < 8 ? cc : cosr(a2), 0.f), im = pk_h(m < 8 ? (m ? -ss : ((u & 1) ? -1.0f : 1.0f)) : -sinr(a2), 0.f);
                Sre[u] = m < 8 ? re : re << 16; Sim[u] = m < 8 ? im : im << 16;
                pc[u & 1][u >> 1] = twc[u >> 1][u & 1]; ps[u & 1][u >> 1] = tws[u >> 1][u & 1];
            }
#pragma unroll
            for (int e = 0; e < 2; ++e) {
                float a = (float)(((m >> 1) * (256 * (2 * g + e) + b)) & 2047) * (1.0f / 2048);
                float cc = cosr(a), ss = sinr(a);
                Mb[e] = (m & 1) ? pk_h(-ss, cc) : pk_h(cc, ss);
            }
            PIN(FAre); PIN(FAim); PIN(Fre); PIN(Fim); PIN(Sre); PIN(Sim); PIN(Mb); PIN(pc[0]); PIN(pc[1]); PIN(ps[0]); PIN(ps[1]);
            f2 v[8];
#pragma unroll
            for (int j = 0; j < 8; ++j) v[j] = bf_2(xd[j]);
            f2 s2 = ((v[0] + v[1]) + (v[2] + v[3])) + ((v[4] + v[5]) + (v[6] + v[7]));
            const float mean = red_lanes<false>(s2[0] + s2[1]) * (1.0f / 512);
            f2 q0 = {0.f, 0.f}, q1 = {0.f, 0.f};
#pragma unroll
            for (int j = 0; j < 8; ++j) { v[j] -= mean; if (j & 1) q1 = fma2(v[j], v[j], q1); else q0 = fma2(v[j], v[j], q0); }
            q0 += q1;
            const float inv = ln_inv(red_lanes<false>(q0[0] + q0[1]) * (1.0f / 512));
            const float off = (mean - rbf(mean)) * inv;
#pragma unroll
            for (int j = 0; j < 8; ++j) v[j] = fma2(v[j], (f2){inv, inv}, (f2){off, off});
            u4 B[2];
#pragma unroll
            for (int p = 0; p < 2; ++p) {
                u4 A = {pk_h(v[0][p], v[1][p]), pk_h(v[2][p], v[3][p]), pk_h(v[4][p], v[5][p]), pk_h(v[6][p], v[7][p])};
                f4 Dre = mma_h(A, FAre, Z4), Dim = mma_h(A, FAim, Z4);
#pragma unroll
                for (int r = 0; r < 2; ++r) {
                    f2 re = r ? hi2(Dre) : lo2(Dre), im = r ? hi2(Dim) : lo2(Dim);
                    f2 tr = fma2(im, ps[p], re * pc[p]), ti = fma2(-re, ps[p], im * pc[p]);
                    B[r][p] = pk_h(tr[0], ti[0]); B[r][2 + p] = pk_h(tr[1], ti[1]);
                }
            }
#pragma unroll
            for (int r = 0; r < 2; ++r) {
                f4 Gre = mma_h(Fre, B[r], Z4), Gim = mma_h(Fim, B[r], Z4), Hre = mma_h(Sre, B[r], Z4), Him = mma_h(Sim, B[r], Z4);
                u4 Z;
#pragma unroll
                for (int i = 0; i < 4; ++i) Z[i] = pk_h(m ? Gre[i] : Hre[i], m ? Gim[i] : Him[i]);
                *(u4*)(scr + (2 * w + r) * 1024 + m * 64 + g * 16) = Z;
            }
            BARRIER();
            u4 r0 = *(const u4*)(scr + (2 * g) * 1024 + w * 256 + m * 16);
            u4 r1 = *(const u4*)(scr + (2 * g + 1) * 1024 + w * 256 + m * 16);
#pragma unroll
            for (int t = 0; t < 4; ++t) {
                f4 D = mma16_h(Mb, (u2){r0[t], r1[t]}, Z4);
                *(u2*)(scr + SCR_S + w * 2048 + m * 128 + (((2 * t + (g >> 1)) ^ (m >> 1)) * 16) + (g & 1) * 8) =
                    (u2){clean(pk_h(D[0], D[1])), clean(pk_h(D[2], D[3]))};
            }
        }
        u4 uo0 = *(const u4*)(scr + SCR_S + w * 2048 + (l ^ ((l >> 4) & 7)) * 16);
        u4 uo1 = *(const u4*)(scr + SCR_S + w * 2048 + 1024 + (l ^ (((l >> 4) + 4) & 7)) * 16);
        u4 Fre, Fim, Gre, Gim, N0, N1; f2 tc[2], ts[2];
#pragma unroll
        for (int u = 0; u < 4; ++u) {
            float cc = c16[u], ss = s16[u];
            Fre[u] = pk_h(0.25f * cc, 0.25f * ss); Fim[u] = pk_h(-0.25f * ss, 0.25f * cc);
            Gre[u] = pk_b(cc * (1.0f / 256), -ss * (1.0f / 256)); Gim[u] = pk_b(ss * (1.0f / 256), cc * (1.0f / 256));
            float a3 = (4.0f * gf + u) * (mf * (1.0f / 256));
            tc[u >> 1][u & 1] = cosr(a3); ts[u >> 1][u & 1] = sinr(a3);
        }
        PIN(Fre); PIN(Fim); PIN(Gre); PIN(Gim); PIN(tc[0]); PIN(tc[1]); PIN(ts[0]); PIN(ts[1]);
        BARRIER();
        bst4<SC1>(uo0, rw, l * 16, WS_U + b * 8192 + w * 2048);
        bst4<SC1>(uo1, rw, l * 16 + 1024, WS_U + b * 8192 + w * 2048);
        xl[0] = *(const u2*)((const char*)x + xoff); xl[1] = *(const u2*)((const char*)x + xoff + 512);
        FENCE();
        tag = __builtin_amdgcn_readfirstlane(epoch) + 1u;
        consts3();
#pragma unroll
        for (int u = 0; u < 4; ++u) {
            float a1 = (float)(((4 * (g & 1) + u) * (256 * (m >> 1) + b)) & 2047) * (1.0f / 2048);
            float c1 = cosr(a1), s1 = sinr(a1);
            u32 nb = (m & 1) ? pk_b(s1, c1) : pk_b(c1, -s1);
            N0[u] = g < 2 ? nb : 0u; N1[u] = g < 2 ? 0u : nb;
        }
        PIN(N0); PIN(N1);
        WAITV(0);
        BARRIER();
        u4 d[2], f[2], e[2];
        const int uo = (tid >> 1) * 8192 + (tid & 1) * 16;
        d[0] = bld4<SC1>(rw, uo, WS_U + b * 32); d[1] = bld4<SC1>(rw, uo, WS_U + 128 * 8192 + b * 32);
        const u32 fd0 = __builtin_amdgcn_raw_buffer_load_b32(rw, uo, WS_WT + b * 32, SC1);
        const u32 fd1 = __builtin_amdgcn_raw_buffer_load_b32(rw, uo, WS_WT + 128 * 8192 + b * 32, SC1);
        FENCE();
        poll2(d[0], d[1], rw, uo, WS_U + b * 32, WS_U + 128 * 8192 + b * 32);
        FENCE();
        f[0] = bld4<SC1>(rw, uo, WS_WT + b * 32); f[1] = bld4<SC1>(rw, uo, WS_WT + 128 * 8192 + b * 32);
        char* sp = scr + SCR_S + (tid ^ ((tid >> 4) & 1)) * 16;
        *(u4*)sp = d[0]; *(u4*)(sp + 4096) = d[1];
        BARRIER();
        if (b == 0) {
            f[0] = bld4<SC1>(rw, tid * 16, WS_WA); f[1] = bld4<SC1>(rw, tid * 16 + 4096, WS_WA);
            e[0] = bld4<SC1>(rw, tid * 16, WS_WB); e[1] = bld4<SC1>(rw, tid * 16 + 4096, WS_WB);
            if (tid == 0) __hip_atomic_store((GAS u32*)(ws + WS_EP), tag, __ATOMIC_RELAXED, __HIP_MEMORY_SCOPE_AGENT);
        }
        u2 ld[4], wl[4];
        const int so = (64 * g + m) * 32 + (((w >> 1) ^ (m >> 3)) * 16) + (w & 1) * 8;
#pragma unroll
        for (int q = 0; q < 4; ++q) ld[q] = *(const u2*)(scr + SCR_S + so + 512 * q);
        FENCE();
        f4 Xre[2], Xim[2];
#pragma unroll
        for (int e = 0; e < 2; ++e) {
            u4 A = {ld[0][e], ld[1][e], ld[2][e], ld[3][e]};
            f4 Dre = mma_h(A, Fre, Z4), Dim = mma_h(A, Fim, Z4);
            f2 r0 = fma2(lo2(Dim), ts[0], lo2(Dre) * tc[0]), i0 = fma2(-lo2(Dre), ts[0], lo2(Dim) * tc[0]);
            f2 r1 = fma2(hi2(Dim), ts[1], hi2(Dre) * tc[1]), i1 = fma2(-hi2(Dre), ts[1], hi2(Dim) * tc[1]);
            u4 B = {pk_h(r0[0], i0[0]), pk_h(r0[1], i0[1]), pk_h(r1[0], i1[0]), pk_h(r1[1], i1[1])};
            Xre[e] = mma_h(Fre, B, Z4); Xim[e] = mma_h(Fim, B, Z4);
        }
        *(u4*)(sp + 8192) = f[0]; *(u4*)(sp + 12288) = f[1];
        asm volatile("" :: "v"(fd0), "v"(fd1));
        f2* sx = (f2*)(scr + SCR_X);
        if (b == 0) {
            *(u4*)(sp - SCR_S) = e[0]; *(u4*)(sp - SCR_S + 4096) = e[1];
#pragma unroll
            for (int e = 0; e < 2; ++e)
#pragma unroll
                for (int i = 0; i < 4; ++i) sx[2 * w + e + 8 * (m + 16 * (4 * g + i))] = (f2){Xre[e][i], Xim[e][i]};
        }
        BARRIER();
#pragma unroll
        for (int q = 0; q < 4; ++q) wl[q] = *(const u2*)(scr + SCR_S + so + 8192 + 512 * q);
        FENCE();
        f2 yr[2][2], yi[2][2];
#pragma unroll
        for (int e = 0; e < 2; ++e)
#pragma unroll
            for (int hh = 0; hh < 2; ++hh) {
                f2 wre = {bf_lo(wl[2 * hh][e]), bf_lo(wl[2 * hh + 1][e])}, wim = {bf_hi(wl[2 * hh][e]), bf_hi(wl[2 * hh + 1][e])};
                f2 xr = hh ? hi2(Xre[e]) : lo2(Xre[e]), xi = hh ? hi2(Xim[e]) : lo2(Xim[e]);
                yr[e][hh] = fma2(-xi, wim, xr * wre); yi[e][hh] = fma2(xr, wim, xi * wre);
            }
        if (b == 0) {
#pragma unroll
            for (int i = 0; i < 4; ++i) {
                u2 wb = *(const u2*)(scr + so + 512 * i);
#pragma unroll
                for (int e = 0; e < 2; ++e) {
                    f2 xm = sx[(2048 - (2 * w + e + 8 * (m + 16 * (4 * g + i)))) & 2047];
                    float br = bf_lo(wb[e]), bi = bf_hi(wb[e]);
                    yr[e][i >> 1][i & 1] += xm[0] * br + xm[1] * bi;
                    yi[e][i >> 1][i & 1] += xm[0] * bi - xm[1] * br;
                }
            }
        }
        char* pv = scr + SCR_S;
#pragma unroll
        for (int e = 0; e < 2; ++e) {
            u4 Y = {pk_b(yr[e][0][0], yi[e][0][0]), pk_b(yr[e][0][1], yi[e][0][1]), pk_b(yr[e][1][0], yi[e][1][0]), pk_b(yr[e][1][1], yi[e][1][1])};
            f4 Dre = mma_b(Y, Gre, Z4), Dim = mma_b(Y, Gim, Z4);
            f2 r0 = fma2(-lo2(Dim), ts[0], lo2(Dre) * tc[0]), i0 = fma2(lo2(Dre), ts[0], lo2(Dim) * tc[0]);
            f2 r1 = fma2(-hi2(Dim), ts[1], hi2(Dre) * tc[1]), i1 = fma2(hi2(Dre), ts[1], hi2(Dim) * tc[1]);
            u4 A = {pk_b(r0[0], i0[0]), pk_b(r0[1], i0[1]), pk_b(r1[0], i1[0]), pk_b(r1[1], i1[1])};
            f4 Ere = mma_b(A, Gre, Z4), Eim = mma_b(A, Gim, Z4);
            u4 o;
#pragma unroll
            for (int i = 0; i < 4; ++i) o[i] = clean(pk_b(Ere[i], Eim[i]));
            *(u4*)(pv + (2 * w + e) * 1024 + ((m * 4 + g) ^ ((w >> 1) * 4)) * 16) = o;
        }
        BARRIER();
#pragma unroll
        for (int it = 0; it < 2; ++it) {
            const int ch = tid + 256 * it;
            const u32* q = (const u32*)(pv + (ch & 1) * 4096 + (((ch >> 1) * 4) ^ ((ch & 1) * 64)));
            bst4<SC1>((u4){q[0], q[256], q[512], q[768]}, rw, ch * 16, WS_V + b * 8192);
        }
        WAITV(0);
        BARRIER();
        const int vo = (2 * m + (g >> 1)) * 8192 + (g & 1) * 16, vs = WS_V + w * 64 * 8192 + b * 32;
        u4 v0 = bld4<SC1>(rw, vo, vs), v1 = bld4<SC1>(rw, vo, vs + 32 * 8192);
        poll2(v0, v1, rw, vo, vs, vs + 32 * 8192);
        char* sz = scr + SCR_X + g * 2048 + w * 256 + m * 8;
#pragma unroll
        for (int hh = 0; hh < 2; ++hh) {
            f4 D0 = mma_b(N0, hh ? v1 : v0, Z4), D1 = mma_b(N1, hh ? v1 : v0, Z4);
            *(u2*)(sz + hh * 128) = (u2){pk_b(D0[0], D0[1]), pk_b(D1[0], D1[1])};
            *(u2*)(sz + 1024 + hh * 128) = (u2){pk_b(D0[2], D0[3]), pk_b(D1[2], D1[3])};
        }
    } else {
        auto announce = [&](int which) {
            if (l < 8) __hip_atomic_store((GAS u32*)(ws + WS_FA) + which * 2048 + l * 256 + b, tag, __ATOMIC_RELAXED, __HIP_MEMORY_SCOPE_AGENT);
            FENCE();
        };
        auto all_announced = [&](int which) {
#pragma nounroll
            for (int sp = 0; sp < SPIN_MAX; ++sp) {
                FENCE();
                u4 f = bld4<SC1>(rw, l * 16, WS_FA + which * 8192 + (blockIdx.x & 7) * 1024);
                if (__builtin_amdgcn_ballot_w64(f[0] != tag || f[1] != tag || f[2] != tag || f[3] != tag) == 0) break;
                __builtin_amdgcn_s_sleep(4);
            }
        };
        const rsrc_t rr = mkrs(wr, 1052672), ri = mkrs(wi, 1052672);
        const int j7 = l >> 3, p4 = (l >> 1) & 3, ee = l & 1;
        u2 fr[4], fi[4]; u32 sr_, si_;
        if (w == 4) {
            const int e = ee * 4 * 514 + (32 * j7 + 4 * p4) * 2;
#pragma unroll
            for (int i = 0; i < 4; ++i) { fr[i] = bld2<0>(rr, e + i * 514, 8 * b * 514); fi[i] = bld2<0>(ri, e + i * 514, 8 * b * 514); }
        } else if (w == 5) {
            const int e = (2045 - 8 * b) * 514 + 253 * 2 - ee * 4 * 514 - (32 * j7 + 4 * p4) * 2;
#pragma unroll
            for (int i = 1; i < 4; ++i) { fr[i] = bld2<0>(rr, e + (3 - i) * 514, 0); fi[i] = bld2<0>(ri, e + (3 - i) * 514, 0); }
            const int e0 = e + 3 * 514 - (b == 0 && !ee ? 2048 * 514 : 0);
            fr[0] = bld2<0>(rr, e0, 0); fi[0] = bld2<0>(ri, e0, 0);
            const int e1 = (8 * b + (l & 7)) * 514 + (16 + 32 * j7) * 2;
            sr_ = __builtin_amdgcn_raw_buffer_load_b16(rr, e1, 0, 0); si_ = __builtin_amdgcn_raw_buffer_load_b16(ri, e1, 0, 0);
        } else if (w == 6) {
            const int c = j7 & 3, f = 8 * b + (l & 7);
            const int e = ((c & 1) ? (2048 - f) & 2047 : f) * 514 + (c >> 1) * 512;
            sr_ = __builtin_amdgcn_raw_buffer_load_b16(rr, e, 0, 0); si_ = __builtin_amdgcn_raw_buffer_load_b16(ri, e, 0, 0);
        }
        FENCE();
#pragma unroll
        for (int jj = 0; jj < 7; ++jj) {
            const int cj_ = (4 * jj) % 14, rj = 18 * jj + (4 * jj) / 14;
            w1o[jj] = (row0 + rj) * 3584 + (cch0 + cj_) * 16 + (cch0 + cj_ >= 14 ? 3584 - 224 : 0);
            PIN(w1o[jj]);
        }
        u32 cpf = 0;
        if (w == 6 && l < CODE_PF) cpf = *(const volatile GAS u32*)(pc0 + ((b & 31) * CODE_PF + l) * 128);
        if (w == 7) {
#pragma unroll
            for (int i = 0; i < 8; ++i) bst4<SC1>(SENT4, rw, l * 16 + (i & 3) * 1024, WS_U + b * 8192 + (i >> 2) * 4096);
        }
        BARRIER();
        if (w < 6) {
            const int wo = p4 * 2048 + j7 * 32 + ee * 16, wso = WS_WT + b * 8192 + (w - 4) * 256;
#pragma unroll
            for (int c = 0; c < 4; ++c) {
                u4 o;
#pragma unroll
                for (int i = 0; i < 4; ++i)
                    o[i] = w == 4 ? __builtin_amdgcn_perm(fi[i][c >> 1], fr[i][c >> 1], (c & 1) ? 0x07060302u : 0x05040100u)
                                  : __builtin_amdgcn_perm(fi[i][1 - (c >> 1)], fr[i][1 - (c >> 1)], (c & 1) ? 0x05040100u : 0x07060302u) ^ 0x80000000u;
                bst4<SC1>(o, rw, wo + c * 512, wso);
            }
            if (w == 5) __builtin_amdgcn_raw_buffer_store_b32(sr_ | (si_ << 16), rw, l * 4, wso, SC1);
        } else if (w == 6) {
            float re = bf_lo(sr_), im = bf_lo(si_);
            re += dpp<0x128>(re); im -= dpp<0x128>(im);
            u32 a = __builtin_bit_cast(u32, re), a2 = a, c = __builtin_bit_cast(u32, im), c2 = c;
            asm("" : "+v"(a2), "+v"(c2));
            auto pr = __builtin_amdgcn_permlane16_swap(a, a2, false, false);
            auto pi = __builtin_amdgcn_permlane16_swap(c, c2, false, false);
            const float ar = __builtin_bit_cast(float, (u32)pr[0]), br = __builtin_bit_cast(float, (u32)pr[1]);
            const float ai = __builtin_bit_cast(float, (u32)pi[0]), bi = __builtin_bit_cast(float, (u32)pi[1]);
            if (l < 8) {
                __builtin_amdgcn_raw_buffer_store_b32(pk_b(0.25f * (ar + br), 0.25f * (ai + bi)), rw, l * 4, WS_WA + b * 32, SC1);
                __builtin_amdgcn_raw_buffer_store_b32(pk_b(0.25f * (ar - br), 0.25f * (ai - bi)), rw, l * 4, WS_WB + b * 32, SC1);
            }
        }
        tag = __builtin_amdgcn_readfirstlane(epoch) + 1u;
        WAITV(0);
        asm volatile("" :: "v"(cpf));
        if (w == 7) announce(0);
        BARRIER();
        if (w == 7) all_announced(0);
        else __builtin_amdgcn_s_sleep(2);
        xl[0] = *(const u2*)((const char*)x + xoff); xl[1] = *(const u2*)((const char*)x + xoff + 512);
        FENCE();
        bg(0, BG2);
        BARRIER();
        __builtin_amdgcn_s_sleep(2);
#pragma unroll
        for (int i = 0; i < 2; ++i) bst4<SC1>(SENT4, rw, l * 16 + i * 1024, WS_V + b * 8192 + (w - 4) * 2048);
        FENCE();
        consts0();
        consts3();
        WAITV(0);
        BARRIER();
        if (w == 7) announce(1);
#pragma unroll
        for (int i = 0; i < 2; ++i) bst4<SC1>(SENT4, rw, (l >> 3) * 256 + (l & 7) * 16 + i * 2048, WS_LN2 + (b >> 1) * 16384 + (b & 1) * 128 + (w - 4) * 4096);
        bg(BG2, BG4);
        BARRIER();
        bg(BG4, BG5);
        WAITV(BG5 - BG2);
        BARRIER();
        if (w == 7) all_announced(1);
        else __builtin_amdgcn_s_sleep(3);
        bg(BG5, BG6);
        BARRIER();
    }
    BARRIER();
    {
        u4 A = *(const u4*)(scr + SCR_X + w * 1024 + m * 64 + g * 16);
        f4 Dre = mma_b(A, Gre3, Z4), Dim = mma_b(A, Gim3, Z4), Hre = mma_b(A, SGre, Z4), Him = mma_b(A, SGim, Z4);
        if (g == 0) { Dre[0] = Hre[0]; Dim[0] = Him[0]; }
        f2 r0 = fma2(-lo2(Dim), tws[0], lo2(Dre) * twc[0]), i0 = fma2(lo2(Dre), tws[0], lo2(Dim) * twc[0]);
        f2 r1 = fma2(-hi2(Dim), tws[1], hi2(Dre) * twc[1]), i1 = fma2(hi2(Dre), tws[1], hi2(Dim) * twc[1]);
        u4 A2 = {pk_b(r0[0], i0[0]), pk_b(r0[1], i0[1]), pk_b(r1[0], i1[0]), pk_b(r1[1], i1[1])};
        f2 h[4]; u2 hp[2];
#pragma unroll
        for (int t = 0; t < 2; ++t) {
            f4 Yv = mma_b(A2, GA[t], Z4);
            u32 h0 = pk_b2(bf_2(xl[t][0]) + bf_2(pk_b(Yv[0], Yv[1]))), h1 = pk_b2(bf_2(xl[t][1]) + bf_2(pk_b(Yv[2], Yv[3])));
            hp[t] = (u2){h0, h1};
            h[2 * t] = bf_2(h0); h[2 * t + 1] = bf_2(h1);
        }
        f2 s2 = (h[0] + h[1]) + (h[2] + h[3]);
        const float mean = red_lanes<true>(s2[0] + s2[1]) * (1.0f / 512);
        f2 q2 = {0.f, 0.f};
#pragma unroll
        for (int j = 0; j < 4; ++j) { f2 d = h[j] - mean; q2 = fma2(d, d, q2); }
        const float inv = ln_inv(red_lanes<true>(q2[0] + q2[1]) * (1.0f / 512));
        const float mb = rbf(mean);
#pragma unroll
        for (int t = 0; t < 2; ++t)
            *(u2*)(scr + ((8 * t + (m >> 1)) * 4 + g) * 128 + (w ^ (m >> 1)) * 16 + (m & 1) * 8) =
                (u2){clean(pk_b2(bf_2(pk_b2(h[2 * t] - mb)) * inv)), clean(pk_b2(bf_2(pk_b2(h[2 * t + 1] - mb)) * inv))};
        WAITV(0);
        BARRIER();
        u4 lo = *(const u4*)(scr + (tid ^ ((tid >> 5) & 7)) * 16);
        bst4<SC1>(lo, rw, (tid >> 3) * 256 + (tid & 7) * 16, WS_LN2 + (b >> 1) * 16384 + (b & 1) * 128);
        FENCE();
        if (aw) WAITV(0); else __builtin_amdgcn_s_sleep(8);
        bst2<SC1>(hp[0], ro, xoff, 0); bst2<SC1>(hp[1], ro, xoff + 512, 0);
        FENCE();
    }
    auto stage_out = [&](int r) {
        w2_read(W[r], LDS_SCR + (w & 3) * 8192 + (r & 1) * 4096);
        asm volatile("s_waitcnt lgkmcnt(0)" ::: "memory");
        PIN(W[r][0]); PIN(W[r][1]); PIN(W[r][2]); PIN(W[r][3]);
        if (r + 2 < 7) w2_glds(pw, r + 2, lds0 + LDS_SCR + (w & 3) * 8192 + (r & 1) * 4096);
    };
    auto publish = [&]() {
        u32 prev = 0;
        if (l == 0) prev = __hip_atomic_fetch_add(hw + 4, 1u, __ATOMIC_RELAXED, __HIP_MEMORY_SCOPE_WORKGROUP);
        if (__builtin_amdgcn_readfirstlane(prev) == 7u && l < 2)
            __hip_atomic_store(fl + (l ? 4096 + (b >> 1) * 32 + (b & 1) : (mt1 * 8 + (nt1 >> 1)) * 32 + (nt1 & 1)), tag, __ATOMIC_RELAXED, __HIP_MEMORY_SCOPE_AGENT);
        FENCE();
    };
    {
        const int tg = w & 3;
        char* xch = smem + XCH + tg * 3072;
        const int hoff = WS_H1 + (mt1 * 8 + 2 * tg) * (56 * 1024) + (nt1 >> 1) * 7168;
        if (aw) {
            const int toff = WS_LN2 + (mt1 * 8 + 2 * tg) * 16384;
            LAS char* wb0 = (LAS char*)smem + (4 * g + (m >> 2)) * 224 + (m & 3) * 8;
            LAS char* wb[4];
            int tq[4];
#pragma unroll
            for (int q = 0; q < 4; ++q) { const int qq = (q + rq) & 3; wb[q] = wb0 + qq * 28672; tq[q] = toff + qq * 4096; }
            u4 T[2][16], A[2][7];
            auto ldt = [&](int kk) {
#pragma unroll
                for (int tt = 0; tt < 2; ++tt) T[tt][kk] = bld4<SC1>(rw, l * 16 + (kk & 3) * 1024, tq[kk >> 2] + tt * 16384);
            };
#pragma unroll
            for (int i = 0; i < 7; ++i) A[0][i] = ldw1(wb[0], 0, i);
            if (l == 0) __hip_atomic_fetch_add(hw + 5, 1u, __ATOMIC_RELAXED, __HIP_MEMORY_SCOPE_WORKGROUP);
#pragma unroll
            for (int kk = 0; kk < G1_DEPTH; ++kk) { ldt(kk); SCHED(); }
            f4 acc[2][7];
#pragma unroll
            for (int tt = 0; tt < 2; ++tt)
#pragma unroll
                for (int i = 0; i < 7; ++i) acc[tt][i] = Z4;
#pragma unroll
            for (int kk = 0; kk < 16; ++kk) {
                if ((kk & 1) == 0) {
                    if (__builtin_expect(any_sent(umax(umax(max4(T[0][kk]), max4(T[1][kk])), umax(max4(T[0][kk + 1]), max4(T[1][kk + 1])))), 0)) {
                        wait_tok(T[0][kk], T[1][kk], rw, l * 16, tq[kk >> 2] + (kk & 3) * 1024);
                        wait_tok(T[0][kk + 1], T[1][kk + 1], rw, l * 16, tq[kk >> 2] + ((kk + 1) & 3) * 1024);
                    }
                    if (kk == (BG6 / 7) * 4 - 2) lds_wait(hw + 5, 8u);
                }
                if (kk + G1_DEPTH < 16) ldt(kk + G1_DEPTH);
                if (kk < 15) {
#pragma unroll
                    for (int i = 0; i < 7; ++i) A[(kk + 1) & 1][i] = ldw1(wb[(kk + 1) >> 2], (kk + 1) & 3, i);
                }
#pragma unroll
                for (int i = 0; i < 7; ++i)
#pragma unroll
                    for (int tt = 0; tt < 2; ++tt) acc[tt][i] = mma_b(A[kk & 1][i], T[tt][kk], acc[tt][i]);
#pragma unroll
                for (int i = 0; i < 14; ++i) {
                    __builtin_amdgcn_sched_group_barrier(0x8, 1, 0);
                    if (i == 1 || i == 7) __builtin_amdgcn_sched_group_barrier(0x20, 1, 0);
                    __builtin_amdgcn_sched_group_barrier(0x100, 1, 0);
                    if ((kk & 1) && i >= 3) __builtin_amdgcn_sched_group_barrier(0x2, 1, 0);
                }
                if (kk & 1) {} else SCHED();
            }
#pragma unroll
            for (int q = 0; q < 3; ++q) {
                u2 p0 = pk4(acc[1][2 * q]), p1 = pk4(acc[1][2 * q + 1]);
                *(u4*)(xch + q * 1024 + l * 16) = (u4){p0[0], p0[1], p1[0], p1[1]};
            }
            lds_set(hw + tg, 1u);
#pragma unroll
            for (int q = 0; q < 3; ++q) {
                u2 p0 = pk4(acc[0][2 * q]), p1 = pk4(acc[0][2 * q + 1]);
                bst4<SC1>(silu_pk4((u4){p0[0], p0[1], p1[0], p1[1]}), rw, l * 16, hoff + ((nt1 & 1) * 3 + q) * 1024);
            }
#pragma unroll
            for (int tt = 0; tt < 2; ++tt) { u2 p = pk4(acc[tt][6]); bst2<SC1>(silu_pk2(p[0], p[1]), rw, l * 8, hoff + tt * (56 * 1024) + 6144 + (nt1 & 1) * 512); }
        } else {
            bg(BG6, 28);
            WAITV(0);
            if (l == 0) __hip_atomic_fetch_add(hw + 5, 1u, __ATOMIC_RELAXED, __HIP_MEMORY_SCOPE_WORKGROUP);
            __builtin_amdgcn_s_sleep(8);
            lds_wait(hw + 5, 8u);
            w2_glds(pw, 0, lds0 + LDS_SCR + tg * 8192);
            w2_glds(pw, 1, lds0 + LDS_SCR + tg * 8192 + 4096);
#pragma unroll
            for (int r = 0; r < B_R; ++r) { WAITV(4); stage_out(r); }
            lds_wait(hw + tg, 1u);
#pragma unroll
            for (int q = 0; q < 3; ++q)
                bst4<SC1>(silu_pk4(*(const u4*)(xch + q * 1024 + l * 16)), rw, l * 16, hoff + 56 * 1024 + ((nt1 & 1) * 3 + q) * 1024);
        }
        WAITV(0);
        publish();
        if (aw) {
            lds_wait(hw + 4, 8u);
#pragma unroll
            for (int ks = 0; ks < 7; ++ks) w2_glds(pw, ks, lds0 + tg * 28672 + ks * 4096);
        } else {
            stage_out(B_R);
#pragma unroll
            for (int r = B_R + 1; r < 7; ++r) { if (r + 1 < 7) WAITV(4); else WAITV(0); stage_out(r); }
        }
    }
    {
        const int rot = w & 3;
        if (aw) {
            flag_wait(fl + ((l & 4) ? 4096 + (grp * 8 + th * 4 + rot) * 32 : (grp * 8 + (pw ^ ((l & 2) * 2))) * 32) + (l & 1), tag);
            lds_set(hw + 8 + rot, tag);
        } else {
#pragma nounroll
            for (int sp = 0; sp < SPIN_MAX; ++sp) {
                if (__builtin_amdgcn_readfirstlane(__hip_atomic_load(hw + 8 + rot, __ATOMIC_RELAXED, __HIP_MEMORY_SCOPE_WORKGROUP)) == tag) break;
                __builtin_amdgcn_s_sleep(1);
            }
            FENCE();
        }
        const int toff = WS_H1 + (grp * 8 + th * 4) * (56 * 1024) + pw * 7168;
        int to[4];
#pragma unroll
        for (int tt = 0; tt < 4; ++tt) to[tt] = toff + ((tt + rot) & 3) * (56 * 1024);
        u4 T[7][4];
        auto ldtok = [&](int ks) {
#pragma unroll
            for (int tt = 0; tt < 4; ++tt) {
                if (ks < 6) T[ks][tt] = bld4<SC1>(rw, l * 16, to[tt] + ko[ks] * 1024);
                else {
                    u2 a = bld2<SC1>(rw, l * 8 + 2048, to[tt] + 4096), c = bld2<SC1>(rw, l * 8 + 2560, to[tt] + 4096);
                    T[ks][tt] = (u4){a[0], a[1], c[0], c[1]};
                }
            }
            SCHED();
        };
#pragma unroll
        for (int ks = 0; ks < G2_DEPTH; ++ks) ldtok(ks);
        if (aw) {
#pragma unroll
            for (int ks = 0; ks < 7; ++ks) w2_read(W[ks], rot * 28672 + ks * 4096);
            asm volatile("s_waitcnt lgkmcnt(0)" ::: "memory");
        }
        if (l == 0) __hip_atomic_fetch_add(hw + 7, 1u, __ATOMIC_RELAXED, __HIP_MEMORY_SCOPE_WORKGROUP);
        const int rho = grp * 128 + th * 64 + rot * 16 + m;
        const int ooff = (256 * (rho & 7) + (rho >> 3)) * 1024 + fb * 128 + (w >> 2) * 64 + g * 16;
        const u4 hres = bld4<SC1>(ro, ooff, 0);
        SCHED();
        f4 acc[4][4];
#pragma unroll
        for (int tt = 0; tt < 4; ++tt)
#pragma unroll
            for (int t = 0; t < 4; ++t) acc[tt][t] = Z4;
#pragma unroll
        for (int ks = 0; ks < 7; ++ks) {
            if (ks + G2_DEPTH < 7) { PIN(T[ks][0]); ldtok(ks + G2_DEPTH); }
#pragma unroll
            for (int tt = 0; tt < 4; ++tt)
#pragma unroll
                for (int t = 0; t < 4; ++t) {
                    acc[tt][t] = mma_b(W[ks][t], T[ks][tt], acc[tt][t]);
                }
            SCHED();
        }
        lds_wait(hw + 7, 8u);
        char* dp = smem + w * 16384 + l * 16;
#pragma unroll
        for (int tt = 1; tt < 4; ++tt)
#pragma unroll
            for (int t = 0; t < 4; ++t) *(f4*)(dp + (((tt + rot) & 3) * 4 + t) * 1024) = acc[tt][t];
        f4 s0, s1;
        if (aw) { *(f4*)(dp + (rot * 4 + 2) * 1024) = acc[0][2]; *(f4*)(dp + (rot * 4 + 3) * 1024) = acc[0][3]; s0 = acc[0][0]; s1 = acc[0][1]; }
        else { *(f4*)(dp + (rot * 4) * 1024) = acc[0][0]; *(f4*)(dp + (rot * 4 + 1) * 1024) = acc[0][1]; s0 = acc[0][2]; s1 = acc[0][3]; }
        BARRIER();
        f4 r0[7], r1[7];
#pragma unroll
        for (int j = 1; j < 8; ++j) {
            const char* rp = smem + (((w + j) & 7) * 16 + rot * 4 + (w >> 2) * 2) * 1024 + l * 16;
            r0[j - 1] = *(const f4*)rp; r1[j - 1] = *(const f4*)(rp + 1024);
        }
        SCHED();
        s0 = ((s0 + r0[0]) + (r0[1] + r0[2])) + ((r0[3] + r0[4]) + (r0[5] + r0[6]));
        s1 = ((s1 + r1[0]) + (r1[1] + r1[2])) + ((r1[3] + r1[4]) + (r1[5] + r1[6]));
        u2 p0 = pk4(s0), p1 = pk4(s1);
        bst4<NT>((u4){pk_b2(bf_2(hres[0]) + bf_2(p0[0])), pk_b2(bf_2(hres[1]) + bf_2(p0[1])),
                           pk_b2(bf_2(hres[2]) + bf_2(p1[0])), pk_b2(bf_2(hres[3]) + bf_2(p1[1]))}, ro, ooff, 0);
    }
}

extern "C" void kernel_launch(void* const* d_in, const int* in_sizes, int n_in, void* d_out, int out_size,
                              void* d_ws, size_t ws_size, hipStream_t stream) {
    (void)hipFuncSetAttribute((const void*)k_fused, hipFuncAttributeMaxDynamicSharedMemorySize, LDS_B);
    k_fused<<<256, 512, LDS_B, stream>>>((const uint16_t*)d_in[0], (const uint16_t*)d_in[1], (const uint16_t*)d_in[2],
                                         (const uint16_t*)d_in[4], (const uint16_t*)d_in[5], (uint16_t*)d_out, (char*)d_ws);
}
```

```cpp
#include <hip/hip_runtime.h>
#include <hip/hip_bf16.h>
#include <stdint.h>

typedef _Float16 h8 __attribute__((ext_vector_type(8)));
typedef _Float16 h4 __attribute__((ext_vector_type(4)));
typedef _Float16 h2 __attribute__((ext_vector_type(2)));
typedef __bf16 b8 __attribute__((ext_vector_type(8)));
typedef __bf16 b2 __attribute__((ext_vector_type(2)));
typedef short s4 __attribute__((ext_vector_type(4)));
typedef float f4 __attribute__((ext_vector_type(4)));
typedef float f2 __attribute__((ext_vector_type(2)));
typedef unsigned u32;
typedef u32 u4 __attribute__((ext_vector_type(4)));
typedef u32 u2 __attribute__((ext_vector_type(2)));
#define LAS __attribute__((address_space(3)))
#define GAS __attribute__((address_space(1)))
#define DI __device__ __forceinline__
#define FENCE() asm volatile("" ::: "memory")
#define PIN(x) asm volatile("" : "+v"(x))
#define SCHED() __builtin_amdgcn_sched_barrier(0)
#define WAITV(n) asm volatile("s_waitcnt vmcnt(%0)" :: "n"(n) : "memory")
#define BARRIER() do { asm volatile("s_waitcnt lgkmcnt(0)" ::: "memory"); __builtin_amdgcn_s_barrier(); FENCE(); } while (0)
constexpr int SC1 = 16, NT = 2;
constexpr u32 SENT = 0xffffffffu;
constexpr int SPIN_MAX = 1 << 20;

DI u32 pk_h(float a, float b) { f2 v = {a, b}; h2 r = __builtin_convertvector(v, h2); return __builtin_bit_cast(u32, r); }
DI u32 pk_b(float a, float b) { f2 v = {a, b}; b2 r = __builtin_convertvector(v, b2); return __builtin_bit_cast(u32, r); }
DI float bf_lo(u32 v) { return __builtin_bit_cast(float, v << 16); }
DI float bf_hi(u32 v) { return __builtin_bit_cast(float, v & 0xffff0000u); }
DI f2 bf_2(u32 v) { return (f2){bf_lo(v), bf_hi(v)}; }
DI u32 pk_b2(f2 v) { return pk_b(v[0], v[1]); }
DI float rbf(float a) { return bf_lo(pk_b(a, a)); }
DI float cosr(float rev) { return __builtin_amdgcn_cosf(rev); }
DI float sinr(float rev) { return __builtin_amdgcn_sinf(rev); }
DI f2 lo2(f4 v) { return __builtin_shufflevector(v, v, 0, 1); }
DI f2 hi2(f4 v) { return __builtin_shufflevector(v, v, 2, 3); }
DI f2 fma2(f2 a, f2 b, f2 c) { return __builtin_elementwise_fma(a, b, c); }
DI u32 umin(u32 a, u32 b) { return a < b ? a : b; }
DI u32 umax(u32 a, u32 b) { return a > b ? a : b; }
DI u32 clean(u32 v) { return umin(v, SENT - 1); }
DI u32 max4(u4 v) { return umax(umax(umax(v[0], v[1]), v[2]), v[3]); }
#define Z4 ((f4){0, 0, 0, 0})
DI f4 mma_h(u4 a, u4 b, f4 c) { return __builtin_amdgcn_mfma_f32_16x16x32_f16(__builtin_bit_cast(h8, a), __builtin_bit_cast(h8, b), c, 0, 0, 0); }
DI f4 mma_b(u4 a, u4 b, f4 c) { return __builtin_amdgcn_mfma_f32_16x16x32_bf16(__builtin_bit_cast(b8, a), __builtin_bit_cast(b8, b), c, 0, 0, 0); }
DI f4 mma16_h(u2 a, u2 b, f4 c) { return __builtin_amdgcn_mfma_f32_16x16x16f16(__builtin_bit_cast(h4, a), __builtin_bit_cast(h4, b), c, 0, 0, 0); }
typedef __amdgpu_buffer_rsrc_t rsrc_t;
DI rsrc_t mkrs(const void* p, u32 bytes) { return __builtin_amdgcn_make_buffer_rsrc((void*)p, 0, bytes, 0x00020000); }
template <int AUX> DI u4 bld4(rsrc_t r, int v, int s) { return __builtin_amdgcn_raw_buffer_load_b128(r, v, s, AUX); }
template <int AUX> DI u2 bld2(rsrc_t r, int v, int s) { return __builtin_amdgcn_raw_buffer_load_b64(r, v, s, AUX); }
template <int AUX> DI void bst4(u4 d, rsrc_t r, int v, int s) {
    __builtin_amdgcn_raw_buffer_store_b128(d, r, v, s, AUX);
    asm volatile("s_nop 1" :: "v"(d));
}
template <int AUX> DI void bst2(u2 d, rsrc_t r, int v, int s) { __builtin_amdgcn_raw_buffer_store_b64(d, r, v, s, AUX); }

template <int CTRL> DI float dpp(float v) {
    return __builtin_bit_cast(float, __builtin_amdgcn_update_dpp(0, __builtin_bit_cast(int, v), CTRL, 0xf, 0xf, true));
}
template <bool ALL> DI float red_lanes(float v) {
    v += dpp<0xB1>(v);
    if (ALL) v += dpp<0x4E>(v);
    v += dpp<0x124>(v); v += dpp<0x128>(v);
    u32 x = __builtin_bit_cast(u32, v), y = x;
    asm("" : "+v"(y));
    auto r = __builtin_amdgcn_permlane16_swap(x, y, false, false);
    v = __builtin_bit_cast(float, (u32)r[0]) + __builtin_bit_cast(float, (u32)r[1]);
    x = __builtin_bit_cast(u32, v); y = x;
    asm("" : "+v"(y));
    auto q = __builtin_amdgcn_permlane32_swap(x, y, false, false);
    return __builtin_bit_cast(float, (u32)q[0]) + __builtin_bit_cast(float, (u32)q[1]);
}
DI float ln_inv(float var) { return __builtin_amdgcn_rcpf(rbf(__builtin_amdgcn_sqrtf(rbf(rbf(var) + 1e-5f)))); }

#define ST_DECL
#define ST(i)
#define ST_END(ws, kid)

constexpr u32 WS_U = 0;
constexpr u32 WS_WT = WS_U + (2u << 20);
constexpr u32 WS_V = WS_WT + (2u << 20);
constexpr u32 WS_LN2 = WS_V + (2u << 20);
constexpr u32 WS_WA = WS_LN2 + (2u << 20);
constexpr u32 WS_WB = WS_WA + 8192u;
constexpr u32 WS_FA = WS_WB + 8192u;
constexpr u32 WS_FLAG = WS_FA + 16384u;
constexpr u32 WS_EP = WS_FLAG + 32768u;
constexpr u32 WS_H1 = WS_WA + (128u << 10);
constexpr u32 WS_END = WS_H1 + 7340032u;

constexpr int BG2 = 4, BG4 = 10, BG5 = 15, BG6 = 21;
constexpr int G1_DEPTH = 10, G2_DEPTH = 3;
constexpr int B_R = 5;
constexpr int CODE_PF = 9;
constexpr int LDS_SCR = 114688;
constexpr int SCR_X = 8448;
constexpr int SCR_S = SCR_X + 20480;
constexpr int SCR_F = 49024;
constexpr int XCH = 147456;
constexpr int LDS_B = 163840;

DI void glds16(const void* sbase, u32 voff, u32 lds_dst) {
    asm volatile("s_mov_b32 m0, %2\n\ts_nop 0\n\tglobal_load_lds_dwordx4 %0, %1" :: "v"(voff), "s"(sbase), "s"(lds_dst) : "memory");
}
DI bool any_sent(u32 v) { return __builtin_amdgcn_ballot_w64(v == SENT) != 0; }
DI void poll2(u4& a, u4& c, rsrc_t rw, int vo, int sa, int sc) {
#pragma nounroll
    for (int sp = 0; sp < SPIN_MAX; ++sp) {
        const bool ba = max4(a) == SENT, bc = max4(c) == SENT;
        if (__builtin_amdgcn_ballot_w64(ba || bc) == 0) break;
        __builtin_amdgcn_s_sleep(1);
        FENCE();
        if (ba) a = bld4<SC1>(rw, vo, sa);
        if (bc) c = bld4<SC1>(rw, vo, sc);
    }
}
DI void wait_tok(u4& t0, u4& t1, rsrc_t rw, int vo, int so) {
    if (any_sent(umax(max4(t0), max4(t1)))) {
#pragma nounroll
        for (int sp = 0; sp < SPIN_MAX; ++sp) {
            FENCE();
            t0 = bld4<SC1>(rw, vo, so); t1 = bld4<SC1>(rw, vo, so + 16384);
            if (!any_sent(umax(max4(t0), max4(t1)))) break;
        }
    }
}
DI u4 ldw1(LAS char* wb, int kk, int ft) {
    s4 a0 = __builtin_amdgcn_ds_read_tr16_b64_v4i16((LAS s4*)(wb + kk * 32 * 224 + ft * 32));
    s4 a1 = __builtin_amdgcn_ds_read_tr16_b64_v4i16((LAS s4*)(wb + (kk * 32 + 16) * 224 + ft * 32));
    u2 x0 = __builtin_bit_cast(u2, a0), x1 = __builtin_bit_cast(u2, a1);
    return (u4){x0[0], x0[1], x1[0], x1[1]};
}
DI u2 pk4(f4 a) { return (u2){pk_b(a[0], a[1]), pk_b(a[2], a[3])}; }
DI u2 silu_pk2(u32 p0, u32 p1) {
    u32 o0, o1; float a0, a1, a2, a3, t0, t1, t2, t3;
    asm("v_lshlrev_b32 %2, 16, %10\n\tv_and_b32 %3, 0xffff0000, %10\n\tv_lshlrev_b32 %4, 16, %11\n\tv_and_b32 %5, 0xffff0000, %11\n\t"
        "v_mul_f32 %6, %12, %2\n\tv_mul_f32 %7, %12, %3\n\tv_mul_f32 %8, %12, %4\n\tv_mul_f32 %9, %12, %5\n\t"
        "v_exp_f32 %6, %6\n\tv_exp_f32 %7, %7\n\tv_exp_f32 %8, %8\n\tv_exp_f32 %9, %9\n\t"
        "v_add_f32 %6, 1.0, %6\n\tv_add_f32 %7, 1.0, %7\n\tv_add_f32 %8, 1.0, %8\n\tv_add_f32 %9, 1.0, %9\n\t"
        "v_rcp_f32 %6, %6\n\tv_rcp_f32 %7, %7\n\tv_rcp_f32 %8, %8\n\tv_rcp_f32 %9, %9\n\t"
        "v_mul_f32 %2, %2, %6\n\tv_mul_f32 %3, %3, %7\n\tv_mul_f32 %4, %4, %8\n\tv_mul_f32 %5, %5, %9\n\t"
        "v_cvt_pk_bf16_f32 %0, %2, %3\n\tv_cvt_pk_bf16_f32 %1, %4, %5"
        : "=&v"(o0), "=&v"(o1), "=&v"(a0), "=&v"(a1), "=&v"(a2), "=&v"(a3), "=&v"(t0), "=&v"(t1), "=&v"(t2), "=&v"(t3)
        : "v"(p0), "v"(p1), "s"(-1.4426950408889634f));
    return (u2){o0, o1};
}
DI u4 silu_pk4(u4 p) { u2 a = silu_pk2(p[0], p[1]), b = silu_pk2(p[2], p[3]); return (u4){a[0], a[1], b[0], b[1]}; }
DI void lds_set(LAS u32* p, u32 v) {
    asm volatile("s_waitcnt lgkmcnt(0)" ::: "memory");
    __hip_atomic_store(p, v, __ATOMIC_RELAXED, __HIP_MEMORY_SCOPE_WORKGROUP);
}
DI void lds_wait(LAS u32* p, u32 v) {
#pragma nounroll
    for (int sp = 0; sp < SPIN_MAX; ++sp) {
        if (__builtin_amdgcn_readfirstlane(__hip_atomic_load(p, __ATOMIC_RELAXED, __HIP_MEMORY_SCOPE_WORKGROUP)) >= v) break;
        __builtin_amdgcn_s_sleep(1);
    }
    FENCE();
}
DI void flag_wait(GAS u32* p, u32 tag) {
#pragma nounroll
    for (int sp = 0; sp < SPIN_MAX; ++sp) {
        if (__builtin_amdgcn_ballot_w64(__hip_atomic_load(p, __ATOMIC_RELAXED, __HIP_MEMORY_SCOPE_AGENT) != tag) == 0) break;
        __builtin_amdgcn_s_sleep(2);
    }
    FENCE();
}
#define SENT4 ((u4){SENT, SENT, SENT, SENT})

__global__ __launch_bounds__(512, 2) void k_fused(const uint16_t* x, const uint16_t* wr, const uint16_t* wi, const uint16_t* w1,
                                                  const uint16_t* w2, uint16_t* out, char* ws) {
    extern __shared__ __attribute__((aligned(16))) char smem[];
    const int tid = threadIdx.x, l = tid & 63, g = l >> 4, m = l & 15;
    const int w = __builtin_amdgcn_readfirstlane(tid >> 6);
    const int b = (blockIdx.x & 7) * 32 + (blockIdx.x >> 3);
    const int mt1 = (b >> 6) * 4 + ((b >> 3) & 3), nt1 = ((b >> 5) & 1) * 8 + (b & 7);
    const int grp = b >> 4, th = (b >> 3) & 1, fb = b & 7;
    const rsrc_t rw = mkrs(ws, WS_END), ro = mkrs(out, 2097152);
    char* scr = smem + LDS_SCR;
    LAS u32* hw = (LAS u32*)((LAS char*)smem + LDS_SCR + SCR_F);
    const u32 lds0 = (u32)(uintptr_t)(LAS char*)smem;
    const float mf = (float)m, gf = (float)g, sg = (m & 1) ? -1.0f : 1.0f;
    const bool aw = w < 4;
    ST_DECL;
    unsigned long long pc0;
    asm volatile("s_getpc_b64 %0" : "=s"(pc0));
    const u32 epoch = __hip_atomic_load((GAS u32*)(ws + WS_EP), __ATOMIC_RELAXED, __HIP_MEMORY_SCOPE_AGENT);
    FENCE();
    u32 xd[8], cpf = 0;
    const rsrc_t rr = mkrs(wr, 1052672), ri = mkrs(wi, 1052672);
    const int j7 = l >> 3, p4 = (l >> 1) & 3, ee = l & 1;
    u2 fr[4], fi[4]; u32 sr_, si_;
    if (aw) {
        const u32* xr = (const u32*)(x + (256 * (2 * w + ((m >> 1) & 1)) + b) * 512) + 8 * g + 2 * (m >> 2) + (m & 1);
#pragma unroll
        for (int j = 0; j < 8; ++j) xd[j] = xr[32 * j];
    } else {
        if (w == 4) {
            const int e = ee * 4 * 514 + (32 * j7 + 4 * p4) * 2;
#pragma unroll
            for (int i = 0; i < 4; ++i) { fr[i] = bld2<0>(rr, e + i * 514, 8 * b * 514); fi[i] = bld2<0>(ri, e + i * 514, 8 * b * 514); }
        } else if (w == 5) {
            const int e = (2045 - 8 * b) * 514 + 253 * 2 - ee * 4 * 514 - (32 * j7 + 4 * p4) * 2;
#pragma unroll
            for (int i = 1; i < 4; ++i) { fr[i] = bld2<0>(rr, e + (3 - i) * 514, 0); fi[i] = bld2<0>(ri, e + (3 - i) * 514, 0); }
            const int e0 = e + 3 * 514 - (b == 0 && !ee ? 2048 * 514 : 0);
            fr[0] = bld2<0>(rr, e0, 0); fi[0] = bld2<0>(ri, e0, 0);
            const int e1 = (8 * b + (l & 7)) * 514 + (16 + 32 * j7) * 2;
            sr_ = __builtin_amdgcn_raw_buffer_load_b16(rr, e1, 0, 0); si_ = __builtin_amdgcn_raw_buffer_load_b16(ri, e1, 0, 0);
        } else if (w == 6) {
            const int c = j7 & 3, f = 8 * b + (l & 7);
            const int e = ((c & 1) ? (2048 - f) & 2047 : f) * 514 + (c >> 1) * 512;
            sr_ = __builtin_amdgcn_raw_buffer_load_b16(rr, e, 0, 0); si_ = __builtin_amdgcn_raw_buffer_load_b16(ri, e, 0, 0);
        }
        if (w == 6 && l < CODE_PF) cpf = *(const volatile GAS u32*)(pc0 + ((b & 31) * CODE_PF + l) * 128);
        if (w == 7) {
#pragma unroll
            for (int i = 0; i < 8; ++i) bst4<SC1>(SENT4, rw, l * 16 + (i & 3) * 1024, WS_U + b * 8192 + (i >> 2) * 4096);
        }
    }
    FENCE();
    const int rq = ((b >> 3) + b) & 3;
    const int row0 = (tid & 255) / 14, cch0 = (tid & 255) - row0 * 14;
    const char* wsrc = (const char*)w1 + nt1 * 224;
    u32 w1o[7];
    auto w1_burst = [&](int i0, int n) {
#pragma unroll
        for (int i = i0; i < i0 + n; ++i) {
            const int q = (i / 7 + rq) & 3, j = i % 7;
            glds16(wsrc + q * (128 * 3584), w1o[j], lds0 + (w - 4) * 1024 + q * 28672 + j * 4096);
        }
    };
    const int xoff = (256 * w + b) * 1024 + m * 32 + g * 8;
    u2 xl[2];
    float c16[4], s16[4];
    f2 twc[2], tws[2];
    u4 Gre3, Gim3, SGre, SGim, GA[2];
    auto consts0 = [&]() {
#pragma unroll
        for (int u = 0; u < 4; ++u) {
            float a = (4.0f * gf + u) * (mf * (1.0f / 16));
            c16[u] = cosr(a); s16[u] = sinr(a);
            twc[u >> 1][u & 1] = cosr(a * (1.0f / 32)); tws[u >> 1][u & 1] = sinr(a * (1.0f / 32));
        }
    };
    auto consts3 = [&]() {
#pragma unroll
        for (int u = 0; u < 4; ++u) {
            const float k = 4.0f * gf + u;
            const bool k0 = u == 0 && g == 0;
            float cc = c16[u], ss = s16[u];
            Gre3[u] = pk_b(cc, -ss); Gim3[u] = pk_b(ss, cc);
            float a2 = (2.0f * k + 1.0f) * (mf * (1.0f / 32));
            u32 sp = pk_b(g < 2 ? 2.f * cc : 2.f * sg * cosr(a2), g < 2 ? -2.f * ss : -2.f * sg * sinr(a2));
            SGre[u] = k0 ? pk_b(1.0f, sg) : (g < 2 ? sp : 0u);
            SGim[u] = g < 2 ? 0u : sp;
            float a4 = k * (mf * (1.0f / 32)), c4 = 2.f * cosr(a4), s4_ = -2.f * sinr(a4);
            GA[0][u] = k0 ? pk_b(1.0f, sg) : pk_b(c4, s4_);
            GA[1][u] = k0 ? pk_b(1.0f, sg) : ((u & 1) ? pk_b(-c4, -s4_) : pk_b(c4, s4_));
        }
        PIN(Gre3); PIN(Gim3); PIN(SGre); PIN(SGim); PIN(GA[0]); PIN(GA[1]); PIN(twc[0]); PIN(twc[1]); PIN(tws[0]); PIN(tws[1]);
    };
    const int pw = (w + 4 * th) & 7;
    GAS u32* fl = (GAS u32*)(ws + WS_FLAG);
    int ko[7];
    {
        const int r6 = (fb < 6 ? fb : fb - 5) + 3 * (grp & 1);
#pragma unroll
        for (int ks = 0; ks < 6; ++ks) { int k = ks + r6; k = k >= 6 ? k - 6 : k; ko[ks] = k >= 6 ? k - 6 : k; }
        ko[6] = 6;
    }
    u4 W[7][4];
    const u32 w2vo = (l >> 3) * 1024 + (((l & 7) ^ (((l >> 4) & 1) << 2)) * 16);
    auto w2_glds = [&](int p_, int ks, u32 dst) {
        const int q = ko[ks];
        const char* src = (const char*)w2 + (224 * p_ + (ks == 6 ? 96 : (q < 3 ? 32 * q : 16 + 32 * q))) * 1024 + fb * 128;
        glds16(src, w2vo, dst); glds16(src + 8192, w2vo, dst + 1024);
        glds16(src + (ks == 6 ? 112 : 16) * 1024, w2vo, dst + 2048); glds16(src + (ks == 6 ? 120 : 24) * 1024, w2vo, dst + 3072);
    };
    LAS char* w2rd = (LAS char*)smem + (4 * g + (m >> 2)) * 128 + (((m & 3) ^ (((m >> 3) & 1) << 2)) * 16);
    auto w2_read = [&](u4 (&f)[4], int off) {
#pragma unroll
        for (int t = 0; t < 4; ++t) {
            LAS char* p = (LAS char*)((u32)(uintptr_t)w2rd ^ ((t >> 1) * 64)) + off + (t & 1) * 8;
            u2 x0 = __builtin_bit_cast(u2, __builtin_amdgcn_ds_read_tr16_b64_v4i16((LAS s4*)p));
            u2 x1 = __builtin_bit_cast(u2, __builtin_amdgcn_ds_read_tr16_b64_v4i16((LAS s4*)(p + 2048)));
            f[t] = (u4){x0[0], x0[1], x1[0], x1[1]};
        }
    };
    auto bg = [&](int i0, int i1) { w1_burst(i0, i1 - i0); };
    u32 tag;
    if (aw) {
        if (tid < 8) hw[tid] = 0;
        FENCE();
        ST(2);
        {
            u4 FAre, FAim, Fre, Fim, Sre, Sim; u2 Mb; f2 pc[2], ps[2];
            consts0();
            float c0[8], s0[8];
#pragma unroll
            for (int j = 0; j < 8; ++j) {
                float a = (gf + 4.0f * j) * (mf * (1.0f / 32));
                c0[j] = cosr(a); s0[j] = m ? -sinr(a) : ((g & 1) ? -1.0f : 1.0f);
            }
            const float mo = (float)(2 * (m & 7) + 1) * (1.0f / 32);
#pragma unroll
            for (int u = 0; u < 4; ++u) {
                FAre[u] = pk_h(c0[2 * u], c0[2 * u + 1]); FAim[u] = pk_h(s0[2 * u], s0[2 * u + 1]);
                float cc = c16[u], ss = s16[u];
                Fre[u] = pk_h(cc, ss); Fim[u] = pk_h(-ss, cc);
                float a2 = (4.0f * gf + u) * mo;
                u32 re = pk_h(m < 8 ? cc : cosr(a2), 0.f), im = pk_h(m < 8 ? (m ? -ss : ((u & 1) ? -1.0f : 1.0f)) : -sinr(a2), 0.f);
                Sre[u] = m < 8 ? re : re << 16; Sim[u] = m < 8 ? im : im << 16;
                pc[u & 1][u >> 1] = twc[u >> 1][u & 1]; ps[u & 1][u >> 1] = tws[u >> 1][u & 1];
            }
#pragma unroll
            for (int e = 0; e < 2; ++e) {
                float a = (float)(((m >> 1) * (256 * (2 * g + e) + b)) & 2047) * (1.0f / 2048);
                float cc = cosr(a), ss = sinr(a);
                Mb[e] = (m & 1) ? pk_h(-ss, cc) : pk_h(cc, ss);
            }
            PIN(FAre); PIN(FAim); PIN(Fre); PIN(Fim); PIN(Sre); PIN(Sim); PIN(Mb); PIN(pc[0]); PIN(pc[1]); PIN(ps[0]); PIN(ps[1]);
            ST(3);
            f2 v[8];
#pragma unroll
            for (int j = 0; j < 8; ++j) v[j] = bf_2(xd[j]);
            f2 s2 = ((v[0] + v[1]) + (v[2] + v[3])) + ((v[4] + v[5]) + (v[6] + v[7]));
            const float mean = red_lanes<false>(s2[0] + s2[1]) * (1.0f / 512);
            f2 q0 = {0.f, 0.f}, q1 = {0.f, 0.f};
#pragma unroll
            for (int j = 0; j < 8; ++j) { v[j] -= mean; if (j & 1) q1 = fma2(v[j], v[j], q1); else q0 = fma2(v[j], v[j], q0); }
            q0 += q1;
            const float inv = ln_inv(red_lanes<false>(q0[0] + q0[1]) * (1.0f / 512));
            const float off = (mean - rbf(mean)) * inv;
#pragma unroll
            for (int j = 0; j < 8; ++j) v[j] = fma2(v[j], (f2){inv, inv}, (f2){off, off});
            u4 B[2];
#pragma unroll
            for (int p = 0; p < 2; ++p) {
                u4 A = {pk_h(v[0][p], v[1][p]), pk_h(v[2][p], v[3][p]), pk_h(v[4][p], v[5][p]), pk_h(v[6][p], v[7][p])};
                f4 Dre = mma_h(A, FAre, Z4), Dim = mma_h(A, FAim, Z4);
#pragma unroll
                for (int r = 0; r < 2; ++r) {
                    f2 re = r ? hi2(Dre) : lo2(Dre), im = r ? hi2(Dim) : lo2(Dim);
                    f2 tr = fma2(im, ps[p], re * pc[p]), ti = fma2(-re, ps[p], im * pc[p]);
                    B[r][p] = pk_h(tr[0], ti[0]); B[r][2 + p] = pk_h(tr[1], ti[1]);
                }
            }
#pragma unroll
            for (int r = 0; r < 2; ++r) {
                f4 Gre = mma_h(Fre, B[r], Z4), Gim = mma_h(Fim, B[r], Z4), Hre = mma_h(Sre, B[r], Z4), Him = mma_h(Sim, B[r], Z4);
                u4 Z;
#pragma unroll
                for (int i = 0; i < 4; ++i) Z[i] = pk_h(m ? Gre[i] : Hre[i], m ? Gim[i] : Him[i]);
                *(u4*)(scr + (2 * w + r) * 1024 + m * 64 + g * 16) = Z;
            }
            BARRIER();
            ST(18);
            u4 r0 = *(const u4*)(scr + (2 * g) * 1024 + w * 256 + m * 16);
            u4 r1 = *(const u4*)(scr + (2 * g + 1) * 1024 + w * 256 + m * 16);
#pragma unroll
            for (int t = 0; t < 4; ++t) {
                f4 D = mma16_h(Mb, (u2){r0[t], r1[t]}, Z4);
                *(u2*)(scr + SCR_S + w * 2048 + m * 128 + (((2 * t + (g >> 1)) ^ (m >> 1)) * 16) + (g & 1) * 8) =
                    (u2){clean(pk_h(D[0], D[1])), clean(pk_h(D[2], D[3]))};
            }
        }
        u4 uo0 = *(const u4*)(scr + SCR_S + w * 2048 + (l ^ ((l >> 4) & 7)) * 16);
        u4 uo1 = *(const u4*)(scr + SCR_S + w * 2048 + 1024 + (l ^ (((l >> 4) + 4) & 7)) * 16);
        u4 Fre, Fim, Gre, Gim, N0, N1; f2 tc[2], ts[2];
#pragma unroll
        for (int u = 0; u < 4; ++u) {
            float cc = c16[u], ss = s16[u];
            Fre[u] = pk_h(0.25f * cc, 0.25f * ss); Fim[u] = pk_h(-0.25f * ss, 0.25f * cc);
            Gre[u] = pk_b(cc * (1.0f / 256), -ss * (1.0f / 256)); Gim[u] = pk_b(ss * (1.0f / 256), cc * (1.0f / 256));
            float a3 = (4.0f * gf + u) * (mf * (1.0f / 256));
            tc[u >> 1][u & 1] = cosr(a3); ts[u >> 1][u & 1] = sinr(a3);
        }
        PIN(Fre); PIN(Fim); PIN(Gre); PIN(Gim); PIN(tc[0]); PIN(tc[1]); PIN(ts[0]); PIN(ts[1]);
        ST(19);
        BARRIER();
        ST(4);
        bst4<SC1>(uo0, rw, l * 16, WS_U + b * 8192 + w * 2048);
        bst4<SC1>(uo1, rw, l * 16 + 1024, WS_U + b * 8192 + w * 2048);
        xl[0] = *(const u2*)((const char*)x + xoff); xl[1] = *(const u2*)((const char*)x + xoff + 512);
        FENCE();
        tag = __builtin_amdgcn_readfirstlane(epoch) + 1u;
        consts3();
#pragma unroll
        for (int u = 0; u < 4; ++u) {
            float a1 = (float)(((4 * (g & 1) + u) * (256 * (m >> 1) + b)) & 2047) * (1.0f / 2048);
            float c1 = cosr(a1), s1 = sinr(a1);
            u32 nb = (m & 1) ? pk_b(s1, c1) : pk_b(c1, -s1);
            N0[u] = g < 2 ? nb : 0u; N1[u] = g < 2 ? 0u : nb;
        }
        PIN(N0); PIN(N1);
        WAITV(0);
        BARRIER();
        ST(5);
        u4 d[2], f[2], e[2];
        const int uo = (tid >> 1) * 8192 + (tid & 1) * 16;
        d[0] = bld4<SC1>(rw, uo, WS_U + b * 32); d[1] = bld4<SC1>(rw, uo, WS_U + 128 * 8192 + b * 32);
        const u32 fd0 = __builtin_amdgcn_raw_buffer_load_b32(rw, uo, WS_WT + b * 32, SC1);
        const u32 fd1 = __builtin_amdgcn_raw_buffer_load_b32(rw, uo, WS_WT + 128 * 8192 + b * 32, SC1);
        FENCE();
        poll2(d[0], d[1], rw, uo, WS_U + b * 32, WS_U + 128 * 8192 + b * 32);
        FENCE();
        f[0] = bld4<SC1>(rw, uo, WS_WT + b * 32); f[1] = bld4<SC1>(rw, uo, WS_WT + 128 * 8192 + b * 32);
        char* sp = scr + SCR_S + (tid ^ ((tid >> 4) & 1)) * 16;
        *(u4*)sp = d[0]; *(u4*)(sp + 4096) = d[1];
        BARRIER();
        if (b == 0) {
            f[0] = bld4<SC1>(rw, tid * 16, WS_WA); f[1] = bld4<SC1>(rw, tid * 16 + 4096, WS_WA);
            e[0] = bld4<SC1>(rw, tid * 16, WS_WB); e[1] = bld4<SC1>(rw, tid * 16 + 4096, WS_WB);
            if (tid == 0) __hip_atomic_store((GAS u32*)(ws + WS_EP), tag, __ATOMIC_RELAXED, __HIP_MEMORY_SCOPE_AGENT);
        }
        ST(6);
        u2 ld[4], wl[4];
        const int so = (64 * g + m) * 32 + (((w >> 1) ^ (m >> 3)) * 16) + (w & 1) * 8;
#pragma unroll
        for (int q = 0; q < 4; ++q) ld[q] = *(const u2*)(scr + SCR_S + so + 512 * q);
        FENCE();
        f4 Xre[2], Xim[2];
#pragma unroll
        for (int e = 0; e < 2; ++e) {
            u4 A = {ld[0][e], ld[1][e], ld[2][e], ld[3][e]};
            f4 Dre = mma_h(A, Fre, Z4), Dim = mma_h(A, Fim, Z4);
            f2 r0 = fma2(lo2(Dim), ts[0], lo2(Dre) * tc[0]), i0 = fma2(-lo2(Dre), ts[0], lo2(Dim) * tc[0]);
            f2 r1 = fma2(hi2(Dim), ts[1], hi2(Dre) * tc[1]), i1 = fma2(-hi2(Dre), ts[1], hi2(Dim) * tc[1]);
            u4 B = {pk_h(r0[0], i0[0]), pk_h(r0[1], i0[1]), pk_h(r1[0], i1[0]), pk_h(r1[1], i1[1])};
            Xre[e] = mma_h(Fre, B, Z4); Xim[e] = mma_h(Fim, B, Z4);
        }
        *(u4*)(sp + 8192) = f[0]; *(u4*)(sp + 12288) = f[1];
        asm volatile("" :: "v"(fd0), "v"(fd1));
        f2* sx = (f2*)(scr + SCR_X);
        if (b == 0) {
            *(u4*)(sp - SCR_S) = e[0]; *(u4*)(sp - SCR_S + 4096) = e[1];
#pragma unroll
            for (int e = 0; e < 2; ++e)
#pragma unroll
                for (int i = 0; i < 4; ++i) sx[2 * w + e + 8 * (m + 16 * (4 * g + i))] = (f2){Xre[e][i], Xim[e][i]};
        }
        BARRIER();
        ST(20);
#pragma unroll
        for (int q = 0; q < 4; ++q) wl[q] = *(const u2*)(scr + SCR_S + so + 8192 + 512 * q);
        FENCE();
        f2 yr[2][2], yi[2][2];
#pragma unroll
        for (int e = 0; e < 2; ++e)
#pragma unroll
            for (int hh = 0; hh < 2; ++hh) {
                f2 wre = {bf_lo(wl[2 * hh][e]), bf_lo(wl[2 * hh + 1][e])}, wim = {bf_hi(wl[2 * hh][e]), bf_hi(wl[2 * hh + 1][e])};
                f2 xr = hh ? hi2(Xre[e]) : lo2(Xre[e]), xi = hh ? hi2(Xim[e]) : lo2(Xim[e]);
                yr[e][hh] = fma2(-xi, wim, xr * wre); yi[e][hh] = fma2(xr, wim, xi * wre);
            }
        if (b == 0) {
#pragma unroll
            for (int i = 0; i < 4; ++i) {
                u2 wb = *(const u2*)(scr + so + 512 * i);
#pragma unroll
                for (int e = 0; e < 2; ++e) {
                    f2 xm = sx[(2048 - (2 * w + e + 8 * (m + 16 * (4 * g + i)))) & 2047];
                    float br = bf_lo(wb[e]), bi = bf_hi(wb[e]);
                    yr[e][i >> 1][i & 1] += xm[0] * br + xm[1] * bi;
                    yi[e][i >> 1][i & 1] += xm[0] * bi - xm[1] * br;
                }
            }
        }
        char* pv = scr + SCR_S;
#pragma unroll
        for (int e = 0; e < 2; ++e) {
            u4 Y = {pk_b(yr[e][0][0], yi[e][0][0]), pk_b(yr[e][0][1], yi[e][0][1]), pk_b(yr[e][1][0], yi[e][1][0]), pk_b(yr[e][1][1], yi[e][1][1])};
            f4 Dre = mma_b(Y, Gre, Z4), Dim = mma_b(Y, Gim, Z4);
            f2 r0 = fma2(-lo2(Dim), ts[0], lo2(Dre) * tc[0]), i0 = fma2(lo2(Dre), ts[0], lo2(Dim) * tc[0]);
            f2 r1 = fma2(-hi2(Dim), ts[1], hi2(Dre) * tc[1]), i1 = fma2(hi2(Dre), ts[1], hi2(Dim) * tc[1]);
            u4 A = {pk_b(r0[0], i0[0]), pk_b(r0[1], i0[1]), pk_b(r1[0], i1[0]), pk_b(r1[1], i1[1])};
            f4 Ere = mma_b(A, Gre, Z4), Eim = mma_b(A, Gim, Z4);
            u4 o;
#pragma unroll
            for (int i = 0; i < 4; ++i) o[i] = clean(pk_b(Ere[i], Eim[i]));
            *(u4*)(pv + (2 * w + e) * 1024 + ((m * 4 + g) ^ ((w >> 1) * 4)) * 16) = o;
        }
        BARRIER();
        ST(7);
#pragma unroll
        for (int it = 0; it < 2; ++it) {
            const int ch = tid + 256 * it;
            const u32* q = (const u32*)(pv + (ch & 1) * 4096 + (((ch >> 1) * 4) ^ ((ch & 1) * 64)));
            bst4<SC1>((u4){q[0], q[256], q[512], q[768]}, rw, ch * 16, WS_V + b * 8192);
        }
        WAITV(0);
        BARRIER();
        ST(8);
        const int vo = (2 * m + (g >> 1)) * 8192 + (g & 1) * 16, vs = WS_V + w * 64 * 8192 + b * 32;
        u4 v0 = bld4<SC1>(rw, vo, vs), v1 = bld4<SC1>(rw, vo, vs + 32 * 8192);
        poll2(v0, v1, rw, vo, vs, vs + 32 * 8192);
        ST(9);
        char* sz = scr + SCR_X + g * 2048 + w * 256 + m * 8;
#pragma unroll
        for (int hh = 0; hh < 2; ++hh) {
            f4 D0 = mma_b(N0, hh ? v1 : v0, Z4), D1 = mma_b(N1, hh ? v1 : v0, Z4);
            *(u2*)(sz + hh * 128) = (u2){pk_b(D0[0], D0[1]), pk_b(D1[0], D1[1])};
            *(u2*)(sz + 1024 + hh * 128) = (u2){pk_b(D0[2], D0[3]), pk_b(D1[2], D1[3])};
        }
    } else {
        auto announce = [&](int which) {
            if (l < 8) __hip_atomic_store((GAS u32*)(ws + WS_FA) + which * 2048 + l * 256 + b, tag, __ATOMIC_RELAXED, __HIP_MEMORY_SCOPE_AGENT);
            FENCE();
        };
        auto all_announced = [&](int which) {
#pragma nounroll
            for (int sp = 0; sp < SPIN_MAX; ++sp) {
                FENCE();
                u4 f = bld4<SC1>(rw, l * 16, WS_FA + which * 8192 + (blockIdx.x & 7) * 1024);
                if (__builtin_amdgcn_ballot_w64(f[0] != tag || f[1] != tag || f[2] != tag || f[3] != tag) == 0) break;
                __builtin_amdgcn_s_sleep(4);
            }
        };
        FENCE();
        ST(2);
#pragma unroll
        for (int jj = 0; jj < 7; ++jj) {
            const int cj_ = (4 * jj) % 14, rj = 18 * jj + (4 * jj) / 14;
            w1o[jj] = (row0 + rj) * 3584 + (cch0 + cj_) * 16 + (cch0 + cj_ >= 14 ? 3584 - 224 : 0);
            PIN(w1o[jj]);
        }
        BARRIER();
        if (w < 6) {
            const int wo = p4 * 2048 + j7 * 32 + ee * 16, wso = WS_WT + b * 8192 + (w - 4) * 256;
#pragma unroll
            for (int c = 0; c < 4; ++c) {
                u4 o;
#pragma unroll
                for (int i = 0; i < 4; ++i)
                    o[i] = w == 4 ? __builtin_amdgcn_perm(fi[i][c >> 1], fr[i][c >> 1], (c & 1) ? 0x07060302u : 0x05040100u)
                                  : __builtin_amdgcn_perm(fi[i][1 - (c >> 1)], fr[i][1 - (c >> 1)], (c & 1) ? 0x05040100u : 0x07060302u) ^ 0x80000000u;
                bst4<SC1>(o, rw, wo + c * 512, wso);
            }
            if (w == 5) __builtin_amdgcn_raw_buffer_store_b32(sr_ | (si_ << 16), rw, l * 4, wso, SC1);
        } else if (w == 6) {
            float re = bf_lo(sr_), im = bf_lo(si_);
            re += dpp<0x128>(re); im -= dpp<0x128>(im);
            u32 a = __builtin_bit_cast(u32, re), a2 = a, c = __builtin_bit_cast(u32, im), c2 = c;
            asm("" : "+v"(a2), "+v"(c2));
            auto pr = __builtin_amdgcn_permlane16_swap(a, a2, false, false);
            auto pi = __builtin_amdgcn_permlane16_swap(c, c2, false, false);
            const float ar = __builtin_bit_cast(float, (u32)pr[0]), br = __builtin_bit_cast(float, (u32)pr[1]);
            const float ai = __builtin_bit_cast(float, (u32)pi[0]), bi = __builtin_bit_cast(float, (u32)pi[1]);
            if (l < 8) {
                __builtin_amdgcn_raw_buffer_store_b32(pk_b(0.25f * (ar + br), 0.25f * (ai + bi)), rw, l * 4, WS_WA + b * 32, SC1);
                __builtin_amdgcn_raw_buffer_store_b32(pk_b(0.25f * (ar - br), 0.25f * (ai - bi)), rw, l * 4, WS_WB + b * 32, SC1);
            }
        }
        ST(18);
        tag = __builtin_amdgcn_readfirstlane(epoch) + 1u;
        WAITV(0);
        asm volatile("" :: "v"(cpf));
        if (w == 7) announce(0);
        ST(19);
        BARRIER();
        if (w == 7) all_announced(0);
        else __builtin_amdgcn_s_sleep(2);
        xl[0] = *(const u2*)((const char*)x + xoff); xl[1] = *(const u2*)((const char*)x + xoff + 512);
        FENCE();
        bg(0, BG2);
        ST(4);
        BARRIER();
        __builtin_amdgcn_s_sleep(2);
#pragma unroll
        for (int i = 0; i < 2; ++i) bst4<SC1>(SENT4, rw, l * 16 + i * 1024, WS_V + b * 8192 + (w - 4) * 2048);
        FENCE();
        consts0();
        consts3();
        WAITV(0);
        ST(5);
        BARRIER();
        if (w == 7) announce(1);
#pragma unroll
        for (int i = 0; i < 2; ++i) bst4<SC1>(SENT4, rw, (l >> 3) * 256 + (l & 7) * 16 + i * 2048, WS_LN2 + (b >> 1) * 16384 + (b & 1) * 128 + (w - 4) * 4096);
        bg(BG2, BG4);
        BARRIER();
        bg(BG4, BG5);
        WAITV(BG5 - BG2);
        ST(6);
        BARRIER();
        if (w == 7) all_announced(1);
        else __builtin_amdgcn_s_sleep(3);
        bg(BG5, BG6);
        ST(7);
        BARRIER();
    }
    BARRIER();
    {
        u4 A = *(const u4*)(scr + SCR_X + w * 1024 + m * 64 + g * 16);
        f4 Dre = mma_b(A, Gre3, Z4), Dim = mma_b(A, Gim3, Z4), Hre = mma_b(A, SGre, Z4), Him = mma_b(A, SGim, Z4);
        if (g == 0) { Dre[0] = Hre[0]; Dim[0] = Him[0]; }
        f2 r0 = fma2(-lo2(Dim), tws[0], lo2(Dre) * twc[0]), i0 = fma2(lo2(Dre), tws[0], lo2(Dim) * twc[0]);
        f2 r1 = fma2(-hi2(Dim), tws[1], hi2(Dre) * twc[1]), i1 = fma2(hi2(Dre), tws[1], hi2(Dim) * twc[1]);
        u4 A2 = {pk_b(r0[0], i0[0]), pk_b(r0[1], i0[1]), pk_b(r1[0], i1[0]), pk_b(r1[1], i1[1])};
        f2 h[4]; u2 hp[2];
#pragma unroll
        for (int t = 0; t < 2; ++t) {
            f4 Yv = mma_b(A2, GA[t], Z4);
            u32 h0 = pk_b2(bf_2(xl[t][0]) + bf_2(pk_b(Yv[0], Yv[1]))), h1 = pk_b2(bf_2(xl[t][1]) + bf_2(pk_b(Yv[2], Yv[3])));
            hp[t] = (u2){h0, h1};
            h[2 * t] = bf_2(h0); h[2 * t + 1] = bf_2(h1);
        }
        f2 s2 = (h[0] + h[1]) + (h[2] + h[3]);
        const float mean = red_lanes<true>(s2[0] + s2[1]) * (1.0f / 512);
        f2 q2 = {0.f, 0.f};
#pragma unroll
        for (int j = 0; j < 4; ++j) { f2 d = h[j] - mean; q2 = fma2(d, d, q2); }
        const float inv = ln_inv(red_lanes<true>(q2[0] + q2[1]) * (1.0f / 512));
        const float mb = rbf(mean);
#pragma unroll
        for (int t = 0; t < 2; ++t)
            *(u2*)(scr + ((8 * t + (m >> 1)) * 4 + g) * 128 + (w ^ (m >> 1)) * 16 + (m & 1) * 8) =
                (u2){clean(pk_b2(bf_2(pk_b2(h[2 * t] - mb)) * inv)), clean(pk_b2(bf_2(pk_b2(h[2 * t + 1] - mb)) * inv))};
        WAITV(0);
        BARRIER();
        u4 lo = *(const u4*)(scr + (tid ^ ((tid >> 5) & 7)) * 16);
        bst4<SC1>(lo, rw, (tid >> 3) * 256 + (tid & 7) * 16, WS_LN2 + (b >> 1) * 16384 + (b & 1) * 128);
        FENCE();
        if (aw) WAITV(0); else __builtin_amdgcn_s_sleep(8);
        bst2<SC1>(hp[0], ro, xoff, 0); bst2<SC1>(hp[1], ro, xoff + 512, 0);
        FENCE();
        ST(10);
    }
    auto stage_out = [&](int r) {
        w2_read(W[r], LDS_SCR + (w & 3) * 8192 + (r & 1) * 4096);
        asm volatile("s_waitcnt lgkmcnt(0)" ::: "memory");
        PIN(W[r][0]); PIN(W[r][1]); PIN(W[r][2]); PIN(W[r][3]);
        if (r + 2 < 7) w2_glds(pw, r + 2, lds0 + LDS_SCR + (w & 3) * 8192 + (r & 1) * 4096);
    };
    auto publish = [&]() {
        u32 prev = 0;
        if (l == 0) prev = __hip_atomic_fetch_add(hw + 4, 1u, __ATOMIC_RELAXED, __HIP_MEMORY_SCOPE_WORKGROUP);
        if (__builtin_amdgcn_readfirstlane(prev) == 7u && l < 2)
            __hip_atomic_store(fl + (l ? 4096 + (b >> 1) * 32 + (b & 1) : (mt1 * 8 + (nt1 >> 1)) * 32 + (nt1 & 1)), tag, __ATOMIC_RELAXED, __HIP_MEMORY_SCOPE_AGENT);
        FENCE();
    };
    {
        const int tg = w & 3;
        char* xch = smem + XCH + tg * 3072;
        const int hoff = WS_H1 + (mt1 * 8 + 2 * tg) * (56 * 1024) + (nt1 >> 1) * 7168;
        if (aw) {
            const int toff = WS_LN2 + (mt1 * 8 + 2 * tg) * 16384;
            LAS char* wb0 = (LAS char*)smem + (4 * g + (m >> 2)) * 224 + (m & 3) * 8;
            LAS char* wb[4];
            int tq[4];
#pragma unroll
            for (int q = 0; q < 4; ++q) { const int qq = (q + rq) & 3; wb[q] = wb0 + qq * 28672; tq[q] = toff + qq * 4096; }
            u4 T[2][16], A[2][7];
            auto ldt = [&](int kk) {
#pragma unroll
                for (int tt = 0; tt < 2; ++tt) T[tt][kk] = bld4<SC1>(rw, l * 16 + (kk & 3) * 1024, tq[kk >> 2] + tt * 16384);
            };
#pragma unroll
            for (int i = 0; i < 7; ++i) A[0][i] = ldw1(wb[0], 0, i);
            if (l == 0) __hip_atomic_fetch_add(hw + 5, 1u, __ATOMIC_RELAXED, __HIP_MEMORY_SCOPE_WORKGROUP);
            ST(11);
#pragma unroll
            for (int kk = 0; kk < G1_DEPTH; ++kk) { ldt(kk); SCHED(); }
            f4 acc[2][7];
#pragma unroll
            for (int tt = 0; tt < 2; ++tt)
#pragma unroll
                for (int i = 0; i < 7; ++i) acc[tt][i] = Z4;
#pragma unroll
            for (int kk = 0; kk < 16; ++kk) {
                if ((kk & 1) == 0) {
                    if (__builtin_expect(any_sent(umax(umax(max4(T[0][kk]), max4(T[1][kk])), umax(max4(T[0][kk + 1]), max4(T[1][kk + 1])))), 0)) {
                        wait_tok(T[0][kk], T[1][kk], rw, l * 16, tq[kk >> 2] + (kk & 3) * 1024);
                        wait_tok(T[0][kk + 1], T[1][kk + 1], rw, l * 16, tq[kk >> 2] + ((kk + 1) & 3) * 1024);
                    }
                    if (kk == 0) ST(12);
                    if (kk == (BG6 / 7) * 4 - 2) lds_wait(hw + 5, 8u);
                }
                if (kk + G1_DEPTH < 16) ldt(kk + G1_DEPTH);
                if (kk < 15) {
#pragma unroll
                    for (int i = 0; i < 7; ++i) A[(kk + 1) & 1][i] = ldw1(wb[(kk + 1) >> 2], (kk + 1) & 3, i);
                }
#pragma unroll
                for (int i = 0; i < 7; ++i)
#pragma unroll
                    for (int tt = 0; tt < 2; ++tt) acc[tt][i] = mma_b(A[kk & 1][i], T[tt][kk], acc[tt][i]);
#pragma unroll
                for (int i = 0; i < 14; ++i) {
                    __builtin_amdgcn_sched_group_barrier(0x8, 1, 0);
                    if (i == 1 || i == 7) __builtin_amdgcn_sched_group_barrier(0x20, 1, 0);
                    __builtin_amdgcn_sched_group_barrier(0x100, 1, 0);
                    if ((kk & 1) && i >= 3) __builtin_amdgcn_sched_group_barrier(0x2, 1, 0);
                }
                if (kk & 1) {} else SCHED();
            }
#pragma unroll
            for (int q = 0; q < 3; ++q) {
                u2 p0 = pk4(acc[1][2 * q]), p1 = pk4(acc[1][2 * q + 1]);
                *(u4*)(xch + q * 1024 + l * 16) = (u4){p0[0], p0[1], p1[0], p1[1]};
            }
            lds_set(hw + tg, 1u);
            ST(13);
#pragma unroll
            for (int q = 0; q < 3; ++q) {
                u2 p0 = pk4(acc[0][2 * q]), p1 = pk4(acc[0][2 * q + 1]);
                bst4<SC1>(silu_pk4((u4){p0[0], p0[1], p1[0], p1[1]}), rw, l * 16, hoff + ((nt1 & 1) * 3 + q) * 1024);
            }
#pragma unroll
            for (int tt = 0; tt < 2; ++tt) { u2 p = pk4(acc[tt][6]); bst2<SC1>(silu_pk2(p[0], p[1]), rw, l * 8, hoff + tt * (56 * 1024) + 6144 + (nt1 & 1) * 512); }
        } else {
            bg(BG6, 28);
            WAITV(0);
            if (l == 0) __hip_atomic_fetch_add(hw + 5, 1u, __ATOMIC_RELAXED, __HIP_MEMORY_SCOPE_WORKGROUP);
            __builtin_amdgcn_s_sleep(8);
            lds_wait(hw + 5, 8u);
            w2_glds(pw, 0, lds0 + LDS_SCR + tg * 8192);
            w2_glds(pw, 1, lds0 + LDS_SCR + tg * 8192 + 4096);
#pragma unroll
            for (int r = 0; r < B_R; ++r) { WAITV(4); stage_out(r); }
            ST(12);
            lds_wait(hw + tg, 1u);
            ST(13);
#pragma unroll
            for (int q = 0; q < 3; ++q)
                bst4<SC1>(silu_pk4(*(const u4*)(xch + q * 1024 + l * 16)), rw, l * 16, hoff + 56 * 1024 + ((nt1 & 1) * 3 + q) * 1024);
        }
        ST(14);
        WAITV(0);
        publish();
        ST(21);
        if (aw) {
            lds_wait(hw + 4, 8u);
#pragma unroll
            for (int ks = 0; ks < 7; ++ks) w2_glds(pw, ks, lds0 + tg * 28672 + ks * 4096);
        } else {
            stage_out(B_R);
#pragma unroll
            for (int r = B_R + 1; r < 7; ++r) { if (r + 1 < 7) WAITV(4); else WAITV(0); stage_out(r); }
        }
    }
    {
        ST(15);
        const int rot = w & 3;
        if (aw) {
            flag_wait(fl + ((l & 4) ? 4096 + (grp * 8 + th * 4 + rot) * 32 : (grp * 8 + (pw ^ ((l & 2) * 2))) * 32) + (l & 1), tag);
            lds_set(hw + 8 + rot, tag);
        } else {
#pragma nounroll
            for (int sp = 0; sp < SPIN_MAX; ++sp) {
                if (__builtin_amdgcn_readfirstlane(__hip_atomic_load(hw + 8 + rot, __ATOMIC_RELAXED, __HIP_MEMORY_SCOPE_WORKGROUP)) == tag) break;
                __builtin_amdgcn_s_sleep(1);
            }
            FENCE();
        }
        ST(16);
        const int toff = WS_H1 + (grp * 8 + th * 4) * (56 * 1024) + pw * 7168;
        int to[4];
#pragma unroll
        for (int tt = 0; tt < 4; ++tt) to[tt] = toff + ((tt + rot) & 3) * (56 * 1024);
        u4 T[7][4];
        auto ldtok = [&](int ks) {
#pragma unroll
            for (int tt = 0; tt < 4; ++tt) {
                if (ks < 6) T[ks][tt] = bld4<SC1>(rw, l * 16, to[tt] + ko[ks] * 1024);
                else {
                    u2 a = bld2<SC1>(rw, l * 8 + 2048, to[tt] + 4096), c = bld2<SC1>(rw, l * 8 + 2560, to[tt] + 4096);
                    T[ks][tt] = (u4){a[0], a[1], c[0], c[1]};
                }
            }
            SCHED();
        };
#pragma unroll
        for (int ks = 0; ks < G2_DEPTH; ++ks) ldtok(ks);
        if (aw) {
#pragma unroll
            for (int ks = 0; ks < 7; ++ks) w2_read(W[ks], rot * 28672 + ks * 4096);
            asm volatile("s_waitcnt lgkmcnt(0)" ::: "memory");
        }
        if (l == 0) __hip_atomic_fetch_add(hw + 7, 1u, __ATOMIC_RELAXED, __HIP_MEMORY_SCOPE_WORKGROUP);
        const int rho = grp * 128 + th * 64 + rot * 16 + m;
        const int ooff = (256 * (rho & 7) + (rho >> 3)) * 1024 + fb * 128 + (w >> 2) * 64 + g * 16;
        const u4 hres = bld4<SC1>(ro, ooff, 0);
        SCHED();
        f4 acc[4][4];
#pragma unroll
        for (int tt = 0; tt < 4; ++tt)
#pragma unroll
            for (int t = 0; t < 4; ++t) acc[tt][t] = Z4;
#pragma unroll
        for (int ks = 0; ks < 7; ++ks) {
            if (ks + G2_DEPTH < 7) { PIN(T[ks][0]); ldtok(ks + G2_DEPTH); }
#pragma unroll
            for (int tt = 0; tt < 4; ++tt)
#pragma unroll
                for (int t = 0; t < 4; ++t) {
                    acc[tt][t] = mma_b(W[ks][t], T[ks][tt], acc[tt][t]);
                }
            SCHED();
        }
        ST(17);
        lds_wait(hw + 7, 8u);
        char* dp = smem + w * 16384 + l * 16;
#pragma unroll
        for (int tt = 1; tt < 4; ++tt)
#pragma unroll
            for (int t = 0; t < 4; ++t) *(f4*)(dp + (((tt + rot) & 3) * 4 + t) * 1024) = acc[tt][t];
        f4 s0, s1;
        if (aw) { *(f4*)(dp + (rot * 4 + 2) * 1024) = acc[0][2]; *(f4*)(dp + (rot * 4 + 3) * 1024) = acc[0][3]; s0 = acc[0][0]; s1 = acc[0][1]; }
        else { *(f4*)(dp + (rot * 4) * 1024) = acc[0][0]; *(f4*)(dp + (rot * 4 + 1) * 1024) = acc[0][1]; s0 = acc[0][2]; s1 = acc[0][3]; }
        BARRIER();
        f4 r0[7], r1[7];
#pragma unroll
        for (int j = 1; j < 8; ++j) {
            const char* rp = smem + (((w + j) & 7) * 16 + rot * 4 + (w >> 2) * 2) * 1024 + l * 16;
            r0[j - 1] = *(const f4*)rp; r1[j - 1] = *(const f4*)(rp + 1024);
        }
        SCHED();
        s0 = ((s0 + r0[0]) + (r0[1] + r0[2])) + ((r0[3] + r0[4]) + (r0[5] + r0[6]));
        s1 = ((s1 + r1[0]) + (r1[1] + r1[2])) + ((r1[3] + r1[4]) + (r1[5] + r1[6]));
        u2 p0 = pk4(s0), p1 = pk4(s1);
        bst4<NT>((u4){pk_b2(bf_2(hres[0]) + bf_2(p0[0])), pk_b2(bf_2(hres[1]) + bf_2(p0[1])),
                           pk_b2(bf_2(hres[2]) + bf_2(p1[0])), pk_b2(bf_2(hres[3]) + bf_2(p1[1]))}, ro, ooff, 0);
    }
    ST_END(ws, 0);
}

extern "C" void kernel_launch(void* const* d_in, const int* in_sizes, int n_in, void* d_out, int out_size,
                              void* d_ws, size_t ws_size, hipStream_t stream) {
    (void)hipFuncSetAttribute((const void*)k_fused, hipFuncAttributeMaxDynamicSharedMemorySize, LDS_B);
    k_fused<<<256, 512, LDS_B, stream>>>((const uint16_t*)d_in[0], (const uint16_t*)d_in[1], (const uint16_t*)d_in[2],
                                         (const uint16_t*)d_in[4], (const uint16_t*)d_in[5], (uint16_t*)d_out, (char*)d_ws);
}
```

```cpp
#include <hip/hip_runtime.h>
#include <hip/hip_bf16.h>
#include <stdint.h>

typedef _Float16 h8 __attribute__((ext_vector_type(8)));
typedef _Float16 h4 __attribute__((ext_vector_type(4)));
typedef _Float16 h2 __attribute__((ext_vector_type(2)));
typedef __bf16 b8 __attribute__((ext_vector_type(8)));
typedef __bf16 b2 __attribute__((ext_vector_type(2)));
typedef short s4 __attribute__((ext_vector_type(4)));
typedef float f4 __attribute__((ext_vector_type(4)));
typedef float f2 __attribute__((ext_vector_type(2)));
typedef unsigned u32;
typedef u32 u4 __attribute__((ext_vector_type(4)));
typedef u32 u2 __attribute__((ext_vector_type(2)));
#define LAS __attribute__((address_space(3)))
#define GAS __attribute__((address_space(1)))
#define DI __device__ __forceinline__
#define FENCE() asm volatile("" ::: "memory")
#define PIN(x) asm volatile("" : "+v"(x))
#define SCHED() __builtin_amdgcn_sched_barrier(0)
#define WAITV(n) asm volatile("s_waitcnt vmcnt(%0)" :: "n"(n) : "memory")
#define BARRIER() do { asm volatile("s_waitcnt lgkmcnt(0)" ::: "memory"); __builtin_amdgcn_s_barrier(); FENCE(); } while (0)
constexpr int SC1 = 16, NT = 2;
constexpr u32 SENT = 0xffffffffu;
constexpr int SPIN_MAX = 1 << 20;

DI u32 pk_h(float a, float b) { f2 v = {a, b}; h2 r = __builtin_convertvector(v, h2); return __builtin_bit_cast(u32, r); }
DI u32 pk_b(float a, float b) { f2 v = {a, b}; b2 r = __builtin_convertvector(v, b2); return __builtin_bit_cast(u32, r); }
DI float bf_lo(u32 v) { return __builtin_bit_cast(float, v << 16); }
DI float bf_hi(u32 v) { return __builtin_bit_cast(float, v & 0xffff0000u); }
DI f2 bf_2(u32 v) { return (f2){bf_lo(v), bf_hi(v)}; }
DI u32 pk_b2(f2 v) { return pk_b(v[0], v[1]); }
DI float rbf(float a) { return bf_lo(pk_b(a, a)); }
DI float cosr(float rev) { return __builtin_amdgcn_cosf(rev); }
DI float sinr(float rev) { return __builtin_amdgcn_sinf(rev); }
DI f2 lo2(f4 v) { return __builtin_shufflevector(v, v, 0, 1); }
DI f2 hi2(f4 v) { return __builtin_shufflevector(v, v, 2, 3); }
DI f2 fma2(f2 a, f2 b, f2 c) { return __builtin_elementwise_fma(a, b, c); }
DI u32 umin(u32 a, u32 b) { return a < b ? a : b; }
DI u32 umax(u32 a, u32 b) { return a > b ? a : b; }
DI u32 clean(u32 v) { return umin(v, SENT - 1); }
DI u32 max4(u4 v) { return umax(umax(umax(v[0], v[1]), v[2]), v[3]); }
#define Z4 ((f4){0, 0, 0, 0})
DI f4 mma_h(u4 a, u4 b, f4 c) { return __builtin_amdgcn_mfma_f32_16x16x32_f16(__builtin_bit_cast(h8, a), __builtin_bit_cast(h8, b), c, 0, 0, 0); }
DI f4 mma_b(u4 a, u4 b, f4 c) { return __builtin_amdgcn_mfma_f32_16x16x32_bf16(__builtin_bit_cast(b8, a), __builtin_bit_cast(b8, b), c, 0, 0, 0); }
DI f4 mma16_h(u2 a, u2 b, f4 c) { return __builtin_amdgcn_mfma_f32_16x16x16f16(__builtin_bit_cast(h4, a), __builtin_bit_cast(h4, b), c, 0, 0, 0); }
typedef __amdgpu_buffer_rsrc_t rsrc_t;
DI rsrc_t mkrs(const void* p, u32 bytes) { return __builtin_amdgcn_make_buffer_rsrc((void*)p, 0, bytes, 0x00020000); }
template <int AUX> DI u4 bld4(rsrc_t r, int v, int s) { return __builtin_amdgcn_raw_buffer_load_b128(r, v, s, AUX); }
template <int AUX> DI u2 bld2(rsrc_t r, int v, int s) { return __builtin_amdgcn_raw_buffer_load_b64(r, v, s, AUX); }
template <int AUX> DI void bst4(u4 d, rsrc_t r, int v, int s) {
    __builtin_amdgcn_raw_buffer_store_b128(d, r, v, s, AUX);
    asm volatile("s_nop 1" :: "v"(d));
}
template <int AUX> DI void bst2(u2 d, rsrc_t r, int v, int s) { __builtin_amdgcn_raw_buffer_store_b64(d, r, v, s, AUX); }

template <int CTRL> DI float dpp(float v) {
    return __builtin_bit_cast(float, __builtin_amdgcn_update_dpp(0, __builtin_bit_cast(int, v), CTRL, 0xf, 0xf, true));
}
template <bool ALL> DI float red_lanes(float v) {
    v += dpp<0xB1>(v);
    if (ALL) v += dpp<0x4E>(v);
    v += dpp<0x124>(v); v += dpp<0x128>(v);
    u32 x = __builtin_bit_cast(u32, v), y = x;
    asm("" : "+v"(y));
    auto r = __builtin_amdgcn_permlane16_swap(x, y, false, false);
    v = __builtin_bit_cast(float, (u32)r[0]) + __builtin_bit_cast(float, (u32)r[1]);
    x = __builtin_bit_cast(u32, v); y = x;
    asm("" : "+v"(y));
    auto q = __builtin_amdgcn_permlane32_swap(x, y, false, false);
    return __builtin_bit_cast(float, (u32)q[0]) + __builtin_bit_cast(float, (u32)q[1]);
}
DI float ln_inv(float var) { return __builtin_amdgcn_rcpf(rbf(__builtin_amdgcn_sqrtf(rbf(rbf(var) + 1e-5f)))); }

#define ST_DECL
#define ST(i)
#define ST_END(ws, kid)

constexpr u32 WS_U = 0;
constexpr u32 WS_WT = WS_U + (2u << 20);
constexpr u32 WS_V = WS_WT + (2u << 20);
constexpr u32 WS_LN2 = WS_V + (2u << 20);
constexpr u32 WS_WA = WS_LN2 + (2u << 20);
constexpr u32 WS_WB = WS_WA + 8192u;
constexpr u32 WS_FA = WS_WB + 8192u;
constexpr u32 WS_FLAG = WS_FA + 16384u;
constexpr u32 WS_EP = WS_FLAG + 32768u;
constexpr u32 WS_H1 = WS_WA + (128u << 10);
constexpr u32 WS_END = WS_H1 + 7340032u;

constexpr int BG2 = 4, BG4 = 10, BG5 = 15, BG6 = 21;
constexpr int G1_DEPTH = 10, G2_DEPTH = 3;
constexpr int B_R = 5;
constexpr int CODE_PF = 9;
constexpr int LDS_SCR = 114688;
constexpr int SCR_X = 8448;
constexpr int SCR_S = SCR_X + 20480;
constexpr int SCR_F = 49024;
constexpr int XCH = 147456;
constexpr int LDS_B = 163840;

DI void glds16(const void* sbase, u32 voff, u32 lds_dst) {
    asm volatile("s_mov_b32 m0, %2\n\ts_nop 0\n\tglobal_load_lds_dwordx4 %0, %1" :: "v"(voff), "s"(sbase), "s"(lds_dst) : "memory");
}
DI bool any_sent(u32 v) { return __builtin_amdgcn_ballot_w64(v == SENT) != 0; }
DI void poll2(u4& a, u4& c, rsrc_t rw, int vo, int sa, int sc) {
#pragma nounroll
    for (int sp = 0; sp < SPIN_MAX; ++sp) {
        const bool ba = max4(a) == SENT, bc = max4(c) == SENT;
        if (__builtin_amdgcn_ballot_w64(ba || bc) == 0) break;
        __builtin_amdgcn_s_sleep(1);
        FENCE();
        if (ba) a = bld4<SC1>(rw, vo, sa);
        if (bc) c = bld4<SC1>(rw, vo, sc);
    }
}
DI void wait_tok(u4& t0, u4& t1, rsrc_t rw, int vo, int so) {
    if (any_sent(umax(max4(t0), max4(t1)))) {
#pragma nounroll
        for (int sp = 0; sp < SPIN_MAX; ++sp) {
            FENCE();
            t0 = bld4<SC1>(rw, vo, so); t1 = bld4<SC1>(rw, vo, so + 16384);
            if (!any_sent(umax(max4(t0), max4(t1)))) break;
        }
    }
}
DI u4 ldw1(LAS char* wb, int kk, int ft) {
    s4 a0 = __builtin_amdgcn_ds_read_tr16_b64_v4i16((LAS s4*)(wb + kk * 32 * 224 + ft * 32));
    s4 a1 = __builtin_amdgcn_ds_read_tr16_b64_v4i16((LAS s4*)(wb + (kk * 32 + 16) * 224 + ft * 32));
    u2 x0 = __builtin_bit_cast(u2, a0), x1 = __builtin_bit_cast(u2, a1);
    return (u4){x0[0], x0[1], x1[0], x1[1]};
}
DI u2 pk4(f4 a) { return (u2){pk_b(a[0], a[1]), pk_b(a[2], a[3])}; }
DI u2 silu_pk2(u32 p0, u32 p1) {
    u32 o0, o1; float a0, a1, a2, a3, t0, t1, t2, t3;
    asm("v_lshlrev_b32 %2, 16, %10\n\tv_and_b32 %3, 0xffff0000, %10\n\tv_lshlrev_b32 %4, 16, %11\n\tv_and_b32 %5, 0xffff0000, %11\n\t"
        "v_mul_f32 %6, %12, %2\n\tv_mul_f32 %7, %12, %3\n\tv_mul_f32 %8, %12, %4\n\tv_mul_f32 %9, %12, %5\n\t"
        "v_exp_f32 %6, %6\n\tv_exp_f32 %7, %7\n\tv_exp_f32 %8, %8\n\tv_exp_f32 %9, %9\n\t"
        "v_add_f32 %6, 1.0, %6\n\tv_add_f32 %7, 1.0, %7\n\tv_add_f32 %8, 1.0, %8\n\tv_add_f32 %9, 1.0, %9\n\t"
        "v_rcp_f32 %6, %6\n\tv_rcp_f32 %7, %7\n\tv_rcp_f32 %8, %8\n\tv_rcp_f32 %9, %9\n\t"
        "v_mul_f32 %2, %2, %6\n\tv_mul_f32 %3, %3, %7\n\tv_mul_f32 %4, %4, %8\n\tv_mul_f32 %5, %5, %9\n\t"
        "v_cvt_pk_bf16_f32 %0, %2, %3\n\tv_cvt_pk_bf16_f32 %1, %4, %5"
        : "=&v"(o0), "=&v"(o1), "=&v"(a0), "=&v"(a1), "=&v"(a2), "=&v"(a3), "=&v"(t0), "=&v"(t1), "=&v"(t2), "=&v"(t3)
        : "v"(p0), "v"(p1), "s"(-1.4426950408889634f));
    return (u2){o0, o1};
}
DI u4 silu_pk4(u4 p) { u2 a = silu_pk2(p[0], p[1]), b = silu_pk2(p[2], p[3]); return (u4){a[0], a[1], b[0], b[1]}; }
DI void lds_set(LAS u32* p, u32 v) {
    asm volatile("s_waitcnt lgkmcnt(0)" ::: "memory");
    __hip_atomic_store(p, v, __ATOMIC_RELAXED, __HIP_MEMORY_SCOPE_WORKGROUP);
}
DI void lds_wait(LAS u32* p, u32 v) {
#pragma nounroll
    for (int sp = 0; sp < SPIN_MAX; ++sp) {
        if (__builtin_amdgcn_readfirstlane(__hip_atomic_load(p, __ATOMIC_RELAXED, __HIP_MEMORY_SCOPE_WORKGROUP)) >= v) break;
        __builtin_amdgcn_s_sleep(1);
    }
    FENCE();
}
DI void flag_wait(GAS u32* p, u32 tag) {
#pragma nounroll
    for (int sp = 0; sp < SPIN_MAX; ++sp) {
        if (__builtin_amdgcn_ballot_w64(__hip_atomic_load(p, __ATOMIC_RELAXED, __HIP_MEMORY_SCOPE_AGENT) != tag) == 0) break;
        __builtin_amdgcn_s_sleep(2);
    }
    FENCE();
}
#define SENT4 ((u4){SENT, SENT, SENT, SENT})

__global__ __launch_bounds__(512, 2) void k_fused(const uint16_t* x, const uint16_t* wr, const uint16_t* wi, const uint16_t* w1,
                                                  const uint16_t* w2, uint16_t* out, char* ws) {
    extern __shared__ __attribute__((aligned(16))) char smem[];
    const int tid = threadIdx.x, l = tid & 63, g = l >> 4, m = l & 15;
    const int w = __builtin_amdgcn_readfirstlane(tid >> 6);
    const int b = (blockIdx.x & 7) * 32 + (blockIdx.x >> 3);
    const int mt1 = (b >> 6) * 4 + ((b >> 3) & 3), nt1 = ((b >> 5) & 1) * 8 + (b & 7);
    const int grp = b >> 4, th = (b >> 3) & 1, fb = b & 7;
    const rsrc_t rw = mkrs(ws, WS_END), ro = mkrs(out, 2097152);
    char* scr = smem + LDS_SCR;
    LAS u32* hw = (LAS u32*)((LAS char*)smem + LDS_SCR + SCR_F);
    const u32 lds0 = (u32)(uintptr_t)(LAS char*)smem;
    const float mf = (float)m, gf = (float)g, sg = (m & 1) ? -1.0f : 1.0f;
    const bool aw = w < 4;
    ST_DECL;
    unsigned long long pc0;
    asm volatile("s_getpc_b64 %0" : "=s"(pc0));
    if (tid >= 448 && l < CODE_PF)
        asm volatile("s_mov_b32 m0, %2\n\ts_nop 0\n\tglobal_load_lds_dword %0, %1"
                     :: "v"(((blockIdx.x >> 3) * CODE_PF + l) * 128), "s"(pc0), "s"(lds0 + LDS_SCR + SCR_F + 64) : "memory");
    const u32 epoch = __hip_atomic_load((GAS u32*)(ws + WS_EP), __ATOMIC_RELAXED, __HIP_MEMORY_SCOPE_AGENT);
    FENCE();
    u32 xd[8];
    const rsrc_t rr = mkrs(wr, 1052672), ri = mkrs(wi, 1052672);
    const int j7 = l >> 3, p4 = (l >> 1) & 3, ee = l & 1;
    u2 fr[4], fi[4]; u32 sr_, si_;
    if (aw) {
        const u32* xr = (const u32*)(x + (256 * (2 * w + ((m >> 1) & 1)) + b) * 512) + 8 * g + 2 * (m >> 2) + (m & 1);
#pragma unroll
        for (int j = 0; j < 8; ++j) xd[j] = xr[32 * j];
    } else {
        if (w == 4) {
            const int e = ee * 4 * 514 + (32 * j7 + 4 * p4) * 2;
#pragma unroll
            for (int i = 0; i < 4; ++i) { fr[i] = bld2<0>(rr, e + i * 514, 8 * b * 514); fi[i] = bld2<0>(ri, e + i * 514, 8 * b * 514); }
        } else if (w == 5) {
            const int e = (2045 - 8 * b) * 514 + 253 * 2 - ee * 4 * 514 - (32 * j7 + 4 * p4) * 2;
#pragma unroll
            for (int i = 1; i < 4; ++i) { fr[i] = bld2<0>(rr, e + (3 - i) * 514, 0); fi[i] = bld2<0>(ri, e + (3 - i) * 514, 0); }
            const int e0 = e + 3 * 514 - (b == 0 && !ee ? 2048 * 514 : 0);
            fr[0] = bld2<0>(rr, e0, 0); fi[0] = bld2<0>(ri, e0, 0);
            const int e1 = (8 * b + (l & 7)) * 514 + (16 + 32 * j7) * 2;
            sr_ = __builtin_amdgcn_raw_buffer_load_b16(rr, e1, 0, 0); si_ = __builtin_amdgcn_raw_buffer_load_b16(ri, e1, 0, 0);
        } else if (w == 6) {
            const int c = j7 & 3, f = 8 * b + (l & 7);
            const int e = ((c & 1) ? (2048 - f) & 2047 : f) * 514 + (c >> 1) * 512;
            sr_ = __builtin_amdgcn_raw_buffer_load_b16(rr, e, 0, 0); si_ = __builtin_amdgcn_raw_buffer_load_b16(ri, e, 0, 0);
        }
        if (w == 7) {
#pragma unroll
            for (int i = 0; i < 8; ++i) bst4<SC1>(SENT4, rw, l * 16 + (i & 3) * 1024, WS_U + b * 8192 + (i >> 2) * 4096);
        }
    }
    FENCE();
    const int rq = ((b >> 3) + b) & 3;
    const int row0 = (tid & 255) / 14, cch0 = (tid & 255) - row0 * 14;
    const char* wsrc = (const char*)w1 + nt1 * 224;
    u32 w1o[7];
    auto w1_burst = [&](int i0, int n) {
#pragma unroll
        for (int i = i0; i < i0 + n; ++i) {
            const int q = (i / 7 + rq) & 3, j = i % 7;
            glds16(wsrc + q * (128 * 3584), w1o[j], lds0 + (w - 4) * 1024 + q * 28672 + j * 4096);
        }
    };
    const int xoff = (256 * w + b) * 1024 + m * 32 + g * 8;
    u2 xl[2];
    float c16[4], s16[4];
    f2 twc[2], tws[2];
    u4 Gre3, Gim3, SGre, SGim, GA[2];
    auto consts0 = [&]() {
#pragma unroll
        for (int u = 0; u < 4; ++u) {
            float a = (4.0f * gf + u) * (mf * (1.0f / 16));
            c16[u] = cosr(a); s16[u] = sinr(a);
            twc[u >> 1][u & 1] = cosr(a * (1.0f / 32)); tws[u >> 1][u & 1] = sinr(a * (1.0f / 32));
        }
    };
    auto consts3 = [&]() {
#pragma unroll
        for (int u = 0; u < 4; ++u) {
            const float k = 4.0f * gf + u;
            const bool k0 = u == 0 && g == 0;
            float cc = c16[u], ss = s16[u];
            Gre3[u] = pk_b(cc, -ss); Gim3[u] = pk_b(ss, cc);
            float a2 = (2.0f * k + 1.0f) * (mf * (1.0f / 32));
            u32 sp = pk_b(g < 2 ? 2.f * cc : 2.f * sg * cosr(a2), g < 2 ? -2.f * ss : -2.f * sg * sinr(a2));
            SGre[u] = k0 ? pk_b(1.0f, sg) : (g < 2 ? sp : 0u);
            SGim[u] = g < 2 ? 0u : sp;
            float a4 = k * (mf * (1.0f / 32)), c4 = 2.f * cosr(a4), s4_ = -2.f * sinr(a4);
            GA[0][u] = k0 ? pk_b(1.0f, sg) : pk_b(c4, s4_);
            GA[1][u] = k0 ? pk_b(1.0f, sg) : ((u & 1) ? pk_b(-c4, -s4_) : pk_b(c4, s4_));
        }
        PIN(Gre3); PIN(Gim3); PIN(SGre); PIN(SGim); PIN(GA[0]); PIN(GA[1]); PIN(twc[0]); PIN(twc[1]); PIN(tws[0]); PIN(tws[1]);
    };
    const int pw = (w + 4 * th) & 7;
    GAS u32* fl = (GAS u32*)(ws + WS_FLAG);
    int ko[7];
    {
        const int r6 = (fb < 6 ? fb : fb - 5) + 3 * (grp & 1);
#pragma unroll
        for (int ks = 0; ks < 6; ++ks) { int k = ks + r6; k = k >= 6 ? k - 6 : k; ko[ks] = k >= 6 ? k - 6 : k; }
        ko[6] = 6;
    }
    u4 W[7][4];
    const u32 w2vo = (l >> 3) * 1024 + (((l & 7) ^ (((l >> 4) & 1) << 2)) * 16);
    auto w2_glds = [&](int p_, int ks, u32 dst) {
        const int q = ko[ks];
        const char* src = (const char*)w2 + (224 * p_ + (ks == 6 ? 96 : (q < 3 ? 32 * q : 16 + 32 * q))) * 1024 + fb * 128;
        glds16(src, w2vo, dst); glds16(src + 8192, w2vo, dst + 1024);
        glds16(src + (ks == 6 ? 112 : 16) * 1024, w2vo, dst + 2048); glds16(src + (ks == 6 ? 120 : 24) * 1024, w2vo, dst + 3072);
    };
    LAS char* w2rd = (LAS char*)smem + (4 * g + (m >> 2)) * 128 + (((m & 3) ^ (((m >> 3) & 1) << 2)) * 16);
    auto w2_read = [&](u4 (&f)[4], int off) {
#pragma unroll
        for (int t = 0; t < 4; ++t) {
            LAS char* p = (LAS char*)((u32)(uintptr_t)w2rd ^ ((t >> 1) * 64)) + off + (t & 1) * 8;
            u2 x0 = __builtin_bit_cast(u2, __builtin_amdgcn_ds_read_tr16_b64_v4i16((LAS s4*)p));
            u2 x1 = __builtin_bit_cast(u2, __builtin_amdgcn_ds_read_tr16_b64_v4i16((LAS s4*)(p + 2048)));
            f[t] = (u4){x0[0], x0[1], x1[0], x1[1]};
        }
    };
    auto bg = [&](int i0, int i1) { w1_burst(i0, i1 - i0); };
    u32 tag;
    if (__builtin_expect(aw, 1)) {
        if (tid < 8) hw[tid] = 0;
        FENCE();
        ST(2);
        {
            u4 FAre, FAim, Fre, Fim, Sre, Sim; u2 Mb; f2 pc[2], ps[2];
            consts0();
            float c0[8], s0[8];
#pragma unroll
            for (int j = 0; j < 8; ++j) {
                float a = (gf + 4.0f * j) * (mf * (1.0f / 32));
                c0[j] = cosr(a); s0[j] = m ? -sinr(a) : ((g & 1) ? -1.0f : 1.0f);
            }
            const float mo = (float)(2 * (m & 7) + 1) * (1.0f / 32);
#pragma unroll
            for (int u = 0; u < 4; ++u) {
                FAre[u] = pk_h(c0[2 * u], c0[2 * u + 1]); FAim[u] = pk_h(s0[2 * u], s0[2 * u + 1]);
                float cc = c16[u], ss = s16[u];
                Fre[u] = pk_h(cc, ss); Fim[u] = pk_h(-ss, cc);
                float a2 = (4.0f * gf + u) * mo;
                u32 re = pk_h(m < 8 ? cc : cosr(a2), 0.f), im = pk_h(m < 8 ? (m ? -ss : ((u & 1) ? -1.0f : 1.0f)) : -sinr(a2), 0.f);
                Sre[u] = m < 8 ? re : re << 16; Sim[u] = m < 8 ? im : im << 16;
                pc[u & 1][u >> 1] = twc[u >> 1][u & 1]; ps[u & 1][u >> 1] = tws[u >> 1][u & 1];
            }
#pragma unroll
            for (int e = 0; e < 2; ++e) {
                float a = (float)(((m >> 1) * (256 * (2 * g + e) + b)) & 2047) * (1.0f / 2048);
                float cc = cosr(a), ss = sinr(a);
                Mb[e] = (m & 1) ? pk_h(-ss, cc) : pk_h(cc, ss);
            }
            PIN(FAre); PIN(FAim); PIN(Fre); PIN(Fim); PIN(Sre); PIN(Sim); PIN(Mb); PIN(pc[0]); PIN(pc[1]); PIN(ps[0]); PIN(ps[1]);
            ST(3);
            f2 v[8];
#pragma unroll
            for (int j = 0; j < 8; ++j) v[j] = bf_2(xd[j]);
            f2 s2 = ((v[0] + v[1]) + (v[2] + v[3])) + ((v[4] + v[5]) + (v[6] + v[7]));
            const float mean = red_lanes<false>(s2[0] + s2[1]) * (1.0f / 512);
            f2 q0 = {0.f, 0.f}, q1 = {0.f, 0.f};
#pragma unroll
            for (int j = 0; j < 8; ++j) { v[j] -= mean; if (j & 1) q1 = fma2(v[j], v[j], q1); else q0 = fma2(v[j], v[j], q0); }
            q0 += q1;
            const float inv = ln_inv(red_lanes<false>(q0[0] + q0[1]) * (1.0f / 512));
            const float off = (mean - rbf(mean)) * inv;
#pragma unroll
            for (int j = 0; j < 8; ++j) v[j] = fma2(v[j], (f2){inv, inv}, (f2){off, off});
            u4 B[2];
#pragma unroll
            for (int p = 0; p < 2; ++p) {
                u4 A = {pk_h(v[0][p], v[1][p]), pk_h(v[2][p], v[3][p]), pk_h(v[4][p], v[5][p]), pk_h(v[6][p], v[7][p])};
                f4 Dre = mma_h(A, FAre, Z4), Dim = mma_h(A, FAim, Z4);
#pragma unroll
                for (int r = 0; r < 2; ++r) {
                    f2 re = r ? hi2(Dre) : lo2(Dre), im = r ? hi2(Dim) : lo2(Dim);
                    f2 tr = fma2(im, ps[p], re * pc[p]), ti = fma2(-re, ps[p], im * pc[p]);
                    B[r][p] = pk_h(tr[0], ti[0]); B[r][2 + p] = pk_h(tr[1], ti[1]);
                }
            }
#pragma unroll
            for (int r = 0; r < 2; ++r) {
                f4 Gre = mma_h(Fre, B[r], Z4), Gim = mma_h(Fim, B[r], Z4), Hre = mma_h(Sre, B[r], Z4), Him = mma_h(Sim, B[r], Z4);
                u4 Z;
#pragma unroll
                for (int i = 0; i < 4; ++i) Z[i] = pk_h(m ? Gre[i] : Hre[i], m ? Gim[i] : Him[i]);
                *(u4*)(scr + (2 * w + r) * 1024 + m * 64 + g * 16) = Z;
            }
            BARRIER();
            ST(18);
            u4 r0 = *(const u4*)(scr + (2 * g) * 1024 + w * 256 + m * 16);
            u4 r1 = *(const u4*)(scr + (2 * g + 1) * 1024 + w * 256 + m * 16);
#pragma unroll
            for (int t = 0; t < 4; ++t) {
                f4 D = mma16_h(Mb, (u2){r0[t], r1[t]}, Z4);
                *(u2*)(scr + SCR_S + w * 2048 + m * 128 + (((2 * t + (g >> 1)) ^ (m >> 1)) * 16) + (g & 1) * 8) =
                    (u2){clean(pk_h(D[0], D[1])), clean(pk_h(D[2], D[3]))};
            }
        }
        u4 uo0 = *(const u4*)(scr + SCR_S + w * 2048 + (l ^ ((l >> 4) & 7)) * 16);
        u4 uo1 = *(const u4*)(scr + SCR_S + w * 2048 + 1024 + (l ^ (((l >> 4) + 4) & 7)) * 16);
        u4 Fre, Fim, Gre, Gim, N0, N1; f2 tc[2], ts[2];
#pragma unroll
        for (int u = 0; u < 4; ++u) {
            float cc = c16[u], ss = s16[u];
            Fre[u] = pk_h(0.25f * cc, 0.25f * ss); Fim[u] = pk_h(-0.25f * ss, 0.25f * cc);
            Gre[u] = pk_b(cc * (1.0f / 256), -ss * (1.0f / 256)); Gim[u] = pk_b(ss * (1.0f / 256), cc * (1.0f / 256));
            float a3 = (4.0f * gf + u) * (mf * (1.0f / 256));
            tc[u >> 1][u & 1] = cosr(a3); ts[u >> 1][u & 1] = sinr(a3);
        }
        PIN(Fre); PIN(Fim); PIN(Gre); PIN(Gim); PIN(tc[0]); PIN(tc[1]); PIN(ts[0]); PIN(ts[1]);
        ST(19);
        BARRIER();
        ST(4);
        bst4<SC1>(uo0, rw, l * 16, WS_U + b * 8192 + w * 2048);
        bst4<SC1>(uo1, rw, l * 16 + 1024, WS_U + b * 8192 + w * 2048);
        xl[0] = *(const u2*)((const char*)x + xoff); xl[1] = *(const u2*)((const char*)x + xoff + 512);
        FENCE();
        tag = __builtin_amdgcn_readfirstlane(epoch) + 1u;
        consts3();
#pragma unroll
        for (int u = 0; u < 4; ++u) {
            float a1 = (float)(((4 * (g & 1) + u) * (256 * (m >> 1) + b)) & 2047) * (1.0f / 2048);
            float c1 = cosr(a1), s1 = sinr(a1);
            u32 nb = (m & 1) ? pk_b(s1, c1) : pk_b(c1, -s1);
            N0[u] = g < 2 ? nb : 0u; N1[u] = g < 2 ? 0u : nb;
        }
        PIN(N0); PIN(N1);
        WAITV(0);
        BARRIER();
        ST(5);
        u4 d[2], f[2], e[2];
        const int uo = (tid >> 1) * 8192 + (tid & 1) * 16;
        d[0] = bld4<SC1>(rw, uo, WS_U + b * 32); d[1] = bld4<SC1>(rw, uo, WS_U + 128 * 8192 + b * 32);
        const u32 fd0 = __builtin_amdgcn_raw_buffer_load_b32(rw, uo, WS_WT + b * 32, SC1);
        const u32 fd1 = __builtin_amdgcn_raw_buffer_load_b32(rw, uo, WS_WT + 128 * 8192 + b * 32, SC1);
        FENCE();
        poll2(d[0], d[1], rw, uo, WS_U + b * 32, WS_U + 128 * 8192 + b * 32);
        FENCE();
        f[0] = bld4<SC1>(rw, uo, WS_WT + b * 32); f[1] = bld4<SC1>(rw, uo, WS_WT + 128 * 8192 + b * 32);
        char* sp = scr + SCR_S + (tid ^ ((tid >> 4) & 1)) * 16;
        *(u4*)sp = d[0]; *(u4*)(sp + 4096) = d[1];
        BARRIER();
        if (b == 0) {
            f[0] = bld4<SC1>(rw, tid * 16, WS_WA); f[1] = bld4<SC1>(rw, tid * 16 + 4096, WS_WA);
            e[0] = bld4<SC1>(rw, tid * 16, WS_WB); e[1] = bld4<SC1>(rw, tid * 16 + 4096, WS_WB);
            if (tid == 0) __hip_atomic_store((GAS u32*)(ws + WS_EP), tag, __ATOMIC_RELAXED, __HIP_MEMORY_SCOPE_AGENT);
        }
        ST(6);
        u2 ld[4], wl[4];
        const int so = (64 * g + m) * 32 + (((w >> 1) ^ (m >> 3)) * 16) + (w & 1) * 8;
#pragma unroll
        for (int q = 0; q < 4; ++q) ld[q] = *(const u2*)(scr + SCR_S + so + 512 * q);
        FENCE();
        f4 Xre[2], Xim[2];
#pragma unroll
        for (int e = 0; e < 2; ++e) {
            u4 A = {ld[0][e], ld[1][e], ld[2][e], ld[3][e]};
            f4 Dre = mma_h(A, Fre, Z4), Dim = mma_h(A, Fim, Z4);
            f2 r0 = fma2(lo2(Dim), ts[0], lo2(Dre) * tc[0]), i0 = fma2(-lo2(Dre), ts[0], lo2(Dim) * tc[0]);
            f2 r1 = fma2(hi2(Dim), ts[1], hi2(Dre) * tc[1]), i1 = fma2(-hi2(Dre), ts[1], hi2(Dim) * tc[1]);
            u4 B = {pk_h(r0[0], i0[0]), pk_h(r0[1], i0[1]), pk_h(r1[0], i1[0]), pk_h(r1[1], i1[1])};
            Xre[e] = mma_h(Fre, B, Z4); Xim[e] = mma_h(Fim, B, Z4);
        }
        *(u4*)(sp + 8192) = f[0]; *(u4*)(sp + 12288) = f[1];
        asm volatile("" :: "v"(fd0), "v"(fd1));
        f2* sx = (f2*)(scr + SCR_X);
        if (b == 0) {
            *(u4*)(sp - SCR_S) = e[0]; *(u4*)(sp - SCR_S + 4096) = e[1];
#pragma unroll
            for (int e = 0; e < 2; ++e)
#pragma unroll
                for (int i = 0; i < 4; ++i) sx[2 * w + e + 8 * (m + 16 * (4 * g + i))] = (f2){Xre[e][i], Xim[e][i]};
        }
        BARRIER();
        ST(20);
#pragma unroll
        for (int q = 0; q < 4; ++q) wl[q] = *(const u2*)(scr + SCR_S + so + 8192 + 512 * q);
        FENCE();
        f2 yr[2][2], yi[2][2];
#pragma unroll
        for (int e = 0; e < 2; ++e)
#pragma unroll
            for (int hh = 0; hh < 2; ++hh) {
                f2 wre = {bf_lo(wl[2 * hh][e]), bf_lo(wl[2 * hh + 1][e])}, wim = {bf_hi(wl[2 * hh][e]), bf_hi(wl[2 * hh + 1][e])};
                f2 xr = hh ? hi2(Xre[e]) : lo2(Xre[e]), xi = hh ? hi2(Xim[e]) : lo2(Xim[e]);
                yr[e][hh] = fma2(-xi, wim, xr * wre); yi[e][hh] = fma2(xr, wim, xi * wre);
            }
        if (b == 0) {
#pragma unroll
            for (int i = 0; i < 4; ++i) {
                u2 wb = *(const u2*)(scr + so + 512 * i);
#pragma unroll
                for (int e = 0; e < 2; ++e) {
                    f2 xm = sx[(2048 - (2 * w + e + 8 * (m + 16 * (4 * g + i)))) & 2047];
                    float br = bf_lo(wb[e]), bi = bf_hi(wb[e]);
                    yr[e][i >> 1][i & 1] += xm[0] * br + xm[1] * bi;
                    yi[e][i >> 1][i & 1] += xm[0] * bi - xm[1] * br;
                }
            }
        }
        char* pv = scr + SCR_S;
#pragma unroll
        for (int e = 0; e < 2; ++e) {
            u4 Y = {pk_b(yr[e][0][0], yi[e][0][0]), pk_b(yr[e][0][1], yi[e][0][1]), pk_b(yr[e][1][0], yi[e][1][0]), pk_b(yr[e][1][1], yi[e][1][1])};
            f4 Dre = mma_b(Y, Gre, Z4), Dim = mma_b(Y, Gim, Z4);
            f2 r0 = fma2(-lo2(Dim), ts[0], lo2(Dre) * tc[0]), i0 = fma2(lo2(Dre), ts[0], lo2(Dim) * tc[0]);
            f2 r1 = fma2(-hi2(Dim), ts[1], hi2(Dre) * tc[1]), i1 = fma2(hi2(Dre), ts[1], hi2(Dim) * tc[1]);
            u4 A = {pk_b(r0[0], i0[0]), pk_b(r0[1], i0[1]), pk_b(r1[0], i1[0]), pk_b(r1[1], i1[1])};
            f4 Ere = mma_b(A, Gre, Z4), Eim = mma_b(A, Gim, Z4);
            u4 o;
#pragma unroll
            for (int i = 0; i < 4; ++i) o[i] = clean(pk_b(Ere[i], Eim[i]));
            *(u4*)(pv + (2 * w + e) * 1024 + ((m * 4 + g) ^ ((w >> 1) * 4)) * 16) = o;
        }
        BARRIER();
        ST(7);
#pragma unroll
        for (int it = 0; it < 2; ++it) {
            const int ch = tid + 256 * it;
            const u32* q = (const u32*)(pv + (ch & 1) * 4096 + (((ch >> 1) * 4) ^ ((ch & 1) * 64)));
            bst4<SC1>((u4){q[0], q[256], q[512], q[768]}, rw, ch * 16, WS_V + b * 8192);
        }
        WAITV(0);
        BARRIER();
        ST(8);
        const int vo = (2 * m + (g >> 1)) * 8192 + (g & 1) * 16, vs = WS_V + w * 64 * 8192 + b * 32;
        u4 v0 = bld4<SC1>(rw, vo, vs), v1 = bld4<SC1>(rw, vo, vs + 32 * 8192);
        poll2(v0, v1, rw, vo, vs, vs + 32 * 8192);
        ST(9);
        char* sz = scr + SCR_X + g * 2048 + w * 256 + m * 8;
#pragma unroll
        for (int hh = 0; hh < 2; ++hh) {
            f4 D0 = mma_b(N0, hh ? v1 : v0, Z4), D1 = mma_b(N1, hh ? v1 : v0, Z4);
            *(u2*)(sz + hh * 128) = (u2){pk_b(D0[0], D0[1]), pk_b(D1[0], D1[1])};
            *(u2*)(sz + 1024 + hh * 128) = (u2){pk_b(D0[2], D0[3]), pk_b(D1[2], D1[3])};
        }
    } else {
        auto announce = [&](int which) {
            if (l < 8) __hip_atomic_store((GAS u32*)(ws + WS_FA) + which * 2048 + l * 256 + b, tag, __ATOMIC_RELAXED, __HIP_MEMORY_SCOPE_AGENT);
            FENCE();
        };
        auto all_announced = [&](int which) {
#pragma nounroll
            for (int sp = 0; sp < SPIN_MAX; ++sp) {
                FENCE();
                u4 f = bld4<SC1>(rw, l * 16, WS_FA + which * 8192 + (blockIdx.x & 7) * 1024);
                if (__builtin_amdgcn_ballot_w64(f[0] != tag || f[1] != tag || f[2] != tag || f[3] != tag) == 0) break;
                __builtin_amdgcn_s_sleep(4);
            }
        };
        FENCE();
        ST(2);
#pragma unroll
        for (int jj = 0; jj < 7; ++jj) {
            const int cj_ = (4 * jj) % 14, rj = 18 * jj + (4 * jj) / 14;
            w1o[jj] = (row0 + rj) * 3584 + (cch0 + cj_) * 16 + (cch0 + cj_ >= 14 ? 3584 - 224 : 0);
            PIN(w1o[jj]);
        }
        BARRIER();
        if (w < 6) {
            const int wo = p4 * 2048 + j7 * 32 + ee * 16, wso = WS_WT + b * 8192 + (w - 4) * 256;
#pragma unroll
            for (int c = 0; c < 4; ++c) {
                u4 o;
#pragma unroll
                for (int i = 0; i < 4; ++i)
                    o[i] = w == 4 ? __builtin_amdgcn_perm(fi[i][c >> 1], fr[i][c >> 1], (c & 1) ? 0x07060302u : 0x05040100u)
                                  : __builtin_amdgcn_perm(fi[i][1 - (c >> 1)], fr[i][1 - (c >> 1)], (c & 1) ? 0x05040100u : 0x07060302u) ^ 0x80000000u;
                bst4<SC1>(o, rw, wo + c * 512, wso);
            }
            if (w == 5) __builtin_amdgcn_raw_buffer_store_b32(sr_ | (si_ << 16), rw, l * 4, wso, SC1);
        } else if (w == 6) {
            float re = bf_lo(sr_), im = bf_lo(si_);
            re += dpp<0x128>(re); im -= dpp<0x128>(im);
            u32 a = __builtin_bit_cast(u32, re), a2 = a, c = __builtin_bit_cast(u32, im), c2 = c;
            asm("" : "+v"(a2), "+v"(c2));
            auto pr = __builtin_amdgcn_permlane16_swap(a, a2, false, false);
            auto pi = __builtin_amdgcn_permlane16_swap(c, c2, false, false);
            const float ar = __builtin_bit_cast(float, (u32)pr[0]), br = __builtin_bit_cast(float, (u32)pr[1]);
            const float ai = __builtin_bit_cast(float, (u32)pi[0]), bi = __builtin_bit_cast(float, (u32)pi[1]);
            if (l < 8) {
                __builtin_amdgcn_raw_buffer_store_b32(pk_b(0.25f * (ar + br), 0.25f * (ai + bi)), rw, l * 4, WS_WA + b * 32, SC1);
                __builtin_amdgcn_raw_buffer_store_b32(pk_b(0.25f * (ar - br), 0.25f * (ai - bi)), rw, l * 4, WS_WB + b * 32, SC1);
            }
        }
        ST(18);
        tag = __builtin_amdgcn_readfirstlane(epoch) + 1u;
        WAITV(0);
        if (w == 7) announce(0);
        ST(19);
        BARRIER();
        if (w == 7) all_announced(0);
        else __builtin_amdgcn_s_sleep(2);
        xl[0] = *(const u2*)((const char*)x + xoff); xl[1] = *(const u2*)((const char*)x + xoff + 512);
        FENCE();
        bg(0, BG2);
        ST(4);
        BARRIER();
        __builtin_amdgcn_s_sleep(2);
#pragma unroll
        for (int i = 0; i < 2; ++i) bst4<SC1>(SENT4, rw, l * 16 + i * 1024, WS_V + b * 8192 + (w - 4) * 2048);
        FENCE();
        consts0();
        consts3();
        WAITV(0);
        ST(5);
        BARRIER();
        if (w == 7) announce(1);
#pragma unroll
        for (int i = 0; i < 2; ++i) bst4<SC1>(SENT4, rw, (l >> 3) * 256 + (l & 7) * 16 + i * 2048, WS_LN2 + (b >> 1) * 16384 + (b & 1) * 128 + (w - 4) * 4096);
        bg(BG2, BG4);
        BARRIER();
        bg(BG4, BG5);
        WAITV(BG5 - BG2);
        ST(6);
        BARRIER();
        if (w == 7) all_announced(1);
        else __builtin_amdgcn_s_sleep(3);
        bg(BG5, BG6);
        ST(7);
        BARRIER();
    }
    BARRIER();
    {
        u4 A = *(const u4*)(scr + SCR_X + w * 1024 + m * 64 + g * 16);
        f4 Dre = mma_b(A, Gre3, Z4), Dim = mma_b(A, Gim3, Z4), Hre = mma_b(A, SGre, Z4), Him = mma_b(A, SGim, Z4);
        if (g == 0) { Dre[0] = Hre[0]; Dim[0] = Him[0]; }
        f2 r0 = fma2(-lo2(Dim), tws[0], lo2(Dre) * twc[0]), i0 = fma2(lo2(Dre), tws[0], lo2(Dim) * twc[0]);
        f2 r1 = fma2(-hi2(Dim), tws[1], hi2(Dre) * twc[1]), i1 = fma2(hi2(Dre), tws[1], hi2(Dim) * twc[1]);
        u4 A2 = {pk_b(r0[0], i0[0]), pk_b(r0[1], i0[1]), pk_b(r1[0], i1[0]), pk_b(r1[1], i1[1])};
        f2 h[4]; u2 hp[2];
#pragma unroll
        for (int t = 0; t < 2; ++t) {
            f4 Yv = mma_b(A2, GA[t], Z4);
            u32 h0 = pk_b2(bf_2(xl[t][0]) + bf_2(pk_b(Yv[0], Yv[1]))), h1 = pk_b2(bf_2(xl[t][1]) + bf_2(pk_b(Yv[2], Yv[3])));
            hp[t] = (u2){h0, h1};
            h[2 * t] = bf_2(h0); h[2 * t + 1] = bf_2(h1);
        }
        f2 s2 = (h[0] + h[1]) + (h[2] + h[3]);
        const float mean = red_lanes<true>(s2[0] + s2[1]) * (1.0f / 512);
        f2 q2 = {0.f, 0.f};
#pragma unroll
        for (int j = 0; j < 4; ++j) { f2 d = h[j] - mean; q2 = fma2(d, d, q2); }
        const float inv = ln_inv(red_lanes<true>(q2[0] + q2[1]) * (1.0f / 512));
        const float mb = rbf(mean);
#pragma unroll
        for (int t = 0; t < 2; ++t)
            *(u2*)(scr + ((8 * t + (m >> 1)) * 4 + g) * 128 + (w ^ (m >> 1)) * 16 + (m & 1) * 8) =
                (u2){clean(pk_b2(bf_2(pk_b2(h[2 * t] - mb)) * inv)), clean(pk_b2(bf_2(pk_b2(h[2 * t + 1] - mb)) * inv))};
        WAITV(0);
        BARRIER();
        u4 lo = *(const u4*)(scr + (tid ^ ((tid >> 5) & 7)) * 16);
        bst4<SC1>(lo, rw, (tid >> 3) * 256 + (tid & 7) * 16, WS_LN2 + (b >> 1) * 16384 + (b & 1) * 128);
        FENCE();
        if (aw) WAITV(0); else __builtin_amdgcn_s_sleep(8);
        bst2<SC1>(hp[0], ro, xoff, 0); bst2<SC1>(hp[1], ro, xoff + 512, 0);
        FENCE();
        ST(10);
    }
    auto stage_out = [&](int r) {
        w2_read(W[r], LDS_SCR + (w & 3) * 8192 + (r & 1) * 4096);
        asm volatile("s_waitcnt lgkmcnt(0)" ::: "memory");
        PIN(W[r][0]); PIN(W[r][1]); PIN(W[r][2]); PIN(W[r][3]);
        if (r + 2 < 7) w2_glds(pw, r + 2, lds0 + LDS_SCR + (w & 3) * 8192 + (r & 1) * 4096);
    };
    auto publish = [&]() {
        u32 prev = 0;
        if (l == 0) prev = __hip_atomic_fetch_add(hw + 4, 1u, __ATOMIC_RELAXED, __HIP_MEMORY_SCOPE_WORKGROUP);
        if (__builtin_amdgcn_readfirstlane(prev) == 7u && l < 2)
            __hip_atomic_store(fl + (l ? 4096 + (b >> 1) * 32 + (b & 1) : (mt1 * 8 + (nt1 >> 1)) * 32 + (nt1 & 1)), tag, __ATOMIC_RELAXED, __HIP_MEMORY_SCOPE_AGENT);
        FENCE();
    };
    {
        const int tg = w & 3;
        char* xch = smem + XCH + tg * 3072;
        const int hoff = WS_H1 + (mt1 * 8 + 2 * tg) * (56 * 1024) + (nt1 >> 1) * 7168;
        if (aw) {
            const int toff = WS_LN2 + (mt1 * 8 + 2 * tg) * 16384;
            LAS char* wb0 = (LAS char*)smem + (4 * g + (m >> 2)) * 224 + (m & 3) * 8;
            LAS char* wb[4];
            int tq[4];
#pragma unroll
            for (int q = 0; q < 4; ++q) { const int qq = (q + rq) & 3; wb[q] = wb0 + qq * 28672; tq[q] = toff + qq * 4096; }
            u4 T[2][16], A[2][7];
            auto ldt = [&](int kk) {
#pragma unroll
                for (int tt = 0; tt < 2; ++tt) T[tt][kk] = bld4<SC1>(rw, l * 16 + (kk & 3) * 1024, tq[kk >> 2] + tt * 16384);
            };
#pragma unroll
            for (int i = 0; i < 7; ++i) A[0][i] = ldw1(wb[0], 0, i);
            if (l == 0) __hip_atomic_fetch_add(hw + 5, 1u, __ATOMIC_RELAXED, __HIP_MEMORY_SCOPE_WORKGROUP);
            ST(11);
#pragma unroll
            for (int kk = 0; kk < G1_DEPTH; ++kk) { ldt(kk); SCHED(); }
            f4 acc[2][7];
#pragma unroll
            for (int tt = 0; tt < 2; ++tt)
#pragma unroll
                for (int i = 0; i < 7; ++i) acc[tt][i] = Z4;
#pragma unroll
            for (int kk = 0; kk < 16; ++kk) {
                if ((kk & 1) == 0) {
                    if (__builtin_expect(any_sent(umax(umax(max4(T[0][kk]), max4(T[1][kk])), umax(max4(T[0][kk + 1]), max4(T[1][kk + 1])))), 0)) {
                        wait_tok(T[0][kk], T[1][kk], rw, l * 16, tq[kk >> 2] + (kk & 3) * 1024);
                        wait_tok(T[0][kk + 1], T[1][kk + 1], rw, l * 16, tq[kk >> 2] + ((kk + 1) & 3) * 1024);
                    }
                    if (kk == 0) ST(12);
                    if (kk == (BG6 / 7) * 4 - 2) lds_wait(hw + 5, 8u);
                }
                if (kk + G1_DEPTH < 16) ldt(kk + G1_DEPTH);
                if (kk < 15) {
#pragma unroll
                    for (int i = 0; i < 7; ++i) A[(kk + 1) & 1][i] = ldw1(wb[(kk + 1) >> 2], (kk + 1) & 3, i);
                }
#pragma unroll
                for (int i = 0; i < 7; ++i)
#pragma unroll
                    for (int tt = 0; tt < 2; ++tt) acc[tt][i] = mma_b(A[kk & 1][i], T[tt][kk], acc[tt][i]);
#pragma unroll
                for (int i = 0; i < 14; ++i) {
                    __builtin_amdgcn_sched_group_barrier(0x8, 1, 0);
                    if (i == 1 || i == 7) __builtin_amdgcn_sched_group_barrier(0x20, 1, 0);
                    __builtin_amdgcn_sched_group_barrier(0x100, 1, 0);
                    if ((kk & 1) && i >= 3) __builtin_amdgcn_sched_group_barrier(0x2, 1, 0);
                }
                if (kk & 1) {} else SCHED();
            }
#pragma unroll
            for (int q = 0; q < 3; ++q) {
                u2 p0 = pk4(acc[1][2 * q]), p1 = pk4(acc[1][2 * q + 1]);
                *(u4*)(xch + q * 1024 + l * 16) = (u4){p0[0], p0[1], p1[0], p1[1]};
            }
            lds_set(hw + tg, 1u);
            ST(13);
#pragma unroll
            for (int q = 0; q < 3; ++q) {
                u2 p0 = pk4(acc[0][2 * q]), p1 = pk4(acc[0][2 * q + 1]);
                bst4<SC1>(silu_pk4((u4){p0[0], p0[1], p1[0], p1[1]}), rw, l * 16, hoff + ((nt1 & 1) * 3 + q) * 1024);
            }
#pragma unroll
            for (int tt = 0; tt < 2; ++tt) { u2 p = pk4(acc[tt][6]); bst2<SC1>(silu_pk2(p[0], p[1]), rw, l * 8, hoff + tt * (56 * 1024) + 6144 + (nt1 & 1) * 512); }
        } else {
            bg(BG6, 28);
            WAITV(0);
            if (l == 0) __hip_atomic_fetch_add(hw + 5, 1u, __ATOMIC_RELAXED, __HIP_MEMORY_SCOPE_WORKGROUP);
            __builtin_amdgcn_s_sleep(8);
            lds_wait(hw + 5, 8u);
            w2_glds(pw, 0, lds0 + LDS_SCR + tg * 8192);
            w2_glds(pw, 1, lds0 + LDS_SCR + tg * 8192 + 4096);
#pragma unroll
            for (int r = 0; r < B_R; ++r) { WAITV(4); stage_out(r); }
            ST(12);
            lds_wait(hw + tg, 1u);
            ST(13);
#pragma unroll
            for (int q = 0; q < 3; ++q)
                bst4<SC1>(silu_pk4(*(const u4*)(xch + q * 1024 + l * 16)), rw, l * 16, hoff + 56 * 1024 + ((nt1 & 1) * 3 + q) * 1024);
        }
        ST(14);
        WAITV(0);
        publish();
        ST(21);
        if (aw) {
            lds_wait(hw + 4, 8u);
#pragma unroll
            for (int ks = 0; ks < 7; ++ks) w2_glds(pw, ks, lds0 + tg * 28672 + ks * 4096);
        } else {
            stage_out(B_R);
#pragma unroll
            for (int r = B_R + 1; r < 7; ++r) { if (r + 1 < 7) WAITV(4); else WAITV(0); stage_out(r); }
        }
    }
    {
        ST(15);
        const int rot = w & 3;
        if (aw) {
            flag_wait(fl + ((l & 4) ? 4096 + (grp * 8 + th * 4 + rot) * 32 : (grp * 8 + (pw ^ ((l & 2) * 2))) * 32) + (l & 1), tag);
            lds_set(hw + 8 + rot, tag);
        } else {
#pragma nounroll
            for (int sp = 0; sp < SPIN_MAX; ++sp) {
                if (__builtin_amdgcn_readfirstlane(__hip_atomic_load(hw + 8 + rot, __ATOMIC_RELAXED, __HIP_MEMORY_SCOPE_WORKGROUP)) == tag) break;
                __builtin_amdgcn_s_sleep(1);
            }
            FENCE();
        }
        ST(16);
        const int toff = WS_H1 + (grp * 8 + th * 4) * (56 * 1024) + pw * 7168;
        int to[4];
#pragma unroll
        for (int tt = 0; tt < 4; ++tt) to[tt] = toff + ((tt + rot) & 3) * (56 * 1024);
        u4 T[7][4];
        auto ldtok = [&](int ks) {
#pragma unroll
            for (int tt = 0; tt < 4; ++tt) {
                if (ks < 6) T[ks][tt] = bld4<SC1>(rw, l * 16, to[tt] + ko[ks] * 1024);
                else {
                    u2 a = bld2<SC1>(rw, l * 8 + 2048, to[tt] + 4096), c = bld2<SC1>(rw, l * 8 + 2560, to[tt] + 4096);
                    T[ks][tt] = (u4){a[0], a[1], c[0], c[1]};
                }
            }
            SCHED();
        };
#pragma unroll
        for (int ks = 0; ks < G2_DEPTH; ++ks) ldtok(ks);
        if (aw) {
#pragma unroll
            for (int ks = 0; ks < 7; ++ks) w2_read(W[ks], rot * 28672 + ks * 4096);
            asm volatile("s_waitcnt lgkmcnt(0)" ::: "memory");
        }
        if (l == 0) __hip_atomic_fetch_add(hw + 7, 1u, __ATOMIC_RELAXED, __HIP_MEMORY_SCOPE_WORKGROUP);
        const int rho = grp * 128 + th * 64 + rot * 16 + m;
        const int ooff = (256 * (rho & 7) + (rho >> 3)) * 1024 + fb * 128 + (w >> 2) * 64 + g * 16;
        const u4 hres = bld4<SC1>(ro, ooff, 0);
        SCHED();
        f4 acc[4][4];
#pragma unroll
        for (int tt = 0; tt < 4; ++tt)
#pragma unroll
            for (int t = 0; t < 4; ++t) acc[tt][t] = Z4;
#pragma unroll
        for (int ks = 0; ks < 7; ++ks) {
            if (ks + G2_DEPTH < 7) { PIN(T[ks][0]); ldtok(ks + G2_DEPTH); }
#pragma unroll
            for (int tt = 0; tt < 4; ++tt)
#pragma unroll
                for (int t = 0; t < 4; ++t) {
                    acc[tt][t] = mma_b(W[ks][t], T[ks][tt], acc[tt][t]);
                }
            SCHED();
        }
        ST(17);
        lds_wait(hw + 7, 8u);
        char* dp = smem + w * 16384 + l * 16;
#pragma unroll
        for (int tt = 1; tt < 4; ++tt)
#pragma unroll
            for (int t = 0; t < 4; ++t) *(f4*)(dp + (((tt + rot) & 3) * 4 + t) * 1024) = acc[tt][t];
        f4 s0, s1;
        if (aw) { *(f4*)(dp + (rot * 4 + 2) * 1024) = acc[0][2]; *(f4*)(dp + (rot * 4 + 3) * 1024) = acc[0][3]; s0 = acc[0][0]; s1 = acc[0][1]; }
        else { *(f4*)(dp + (rot * 4) * 1024) = acc[0][0]; *(f4*)(dp + (rot * 4 + 1) * 1024) = acc[0][1]; s0 = acc[0][2]; s1 = acc[0][3]; }
        BARRIER();
        f4 r0[7], r1[7];
#pragma unroll
        for (int j = 1; j < 8; ++j) {
            const char* rp = smem + (((w + j) & 7) * 16 + rot * 4 + (w >> 2) * 2) * 1024 + l * 16;
            r0[j - 1] = *(const f4*)rp; r1[j - 1] = *(const f4*)(rp + 1024);
        }
        SCHED();
        s0 = ((s0 + r0[0]) + (r0[1] + r0[2])) + ((r0[3] + r0[4]) + (r0[5] + r0[6]));
        s1 = ((s1 + r1[0]) + (r1[1] + r1[2])) + ((r1[3] + r1[4]) + (r1[5] + r1[6]));
        u2 p0 = pk4(s0), p1 = pk4(s1);
        bst4<NT>((u4){pk_b2(bf_2(hres[0]) + bf_2(p0[0])), pk_b2(bf_2(hres[1]) + bf_2(p0[1])),
                           pk_b2(bf_2(hres[2]) + bf_2(p1[0])), pk_b2(bf_2(hres[3]) + bf_2(p1[1]))}, ro, ooff, 0);
    }
    ST_END(ws, 0);
}

extern "C" void kernel_launch(void* const* d_in, const int* in_sizes, int n_in, void* d_out, int out_size,
                              void* d_ws, size_t ws_size, hipStream_t stream) {
    (void)hipFuncSetAttribute((const void*)k_fused, hipFuncAttributeMaxDynamicSharedMemorySize, LDS_B);
    k_fused<<<256, 512, LDS_B, stream>>>((const uint16_t*)d_in[0], (const uint16_t*)d_in[1], (const uint16_t*)d_in[2],
                                         (const uint16_t*)d_in[4], (const uint16_t*)d_in[5], (uint16_t*)d_out, (char*)d_ws);
}
```

```cpp
#include <hip/hip_runtime.h>
#include <hip/hip_bf16.h>
#include <stdint.h>

typedef _Float16 h8 __attribute__((ext_vector_type(8)));
typedef _Float16 h4 __attribute__((ext_vector_type(4)));
typedef _Float16 h2 __attribute__((ext_vector_type(2)));
typedef __bf16 b8 __attribute__((ext_vector_type(8)));
typedef __bf16 b2 __attribute__((ext_vector_type(2)));
typedef short s4 __attribute__((ext_vector_type(4)));
typedef float f4 __attribute__((ext_vector_type(4)));
typedef float f2 __attribute__((ext_vector_type(2)));
typedef unsigned u32;
typedef u32 u4 __attribute__((ext_vector_type(4)));
typedef u32 u2 __attribute__((ext_vector_type(2)));
#define LAS __attribute__((address_space(3)))
#define GAS __attribute__((address_space(1)))
#define DI __device__ __forceinline__
#define FENCE() asm volatile("" ::: "memory")
#define PIN(x) asm volatile("" : "+v"(x))
#define SCHED() __builtin_amdgcn_sched_barrier(0)
#define WAITV(n) asm volatile("s_waitcnt vmcnt(%0)" :: "n"(n) : "memory")
#define BARRIER() do { asm volatile("s_waitcnt lgkmcnt(0)" ::: "memory"); __builtin_amdgcn_s_barrier(); FENCE(); } while (0)
constexpr int SC1 = 16, NT = 2;
constexpr u32 SENT = 0xffffffffu;
constexpr int SPIN_MAX = 1 << 20;

DI u32 pk_h(float a, float b) { f2 v = {a, b}; h2 r = __builtin_convertvector(v, h2); return __builtin_bit_cast(u32, r); }
DI u32 pk_b(float a, float b) { f2 v = {a, b}; b2 r = __builtin_convertvector(v, b2); return __builtin_bit_cast(u32, r); }
DI float bf_lo(u32 v) { return __builtin_bit_cast(float, v << 16); }
DI float bf_hi(u32 v) { return __builtin_bit_cast(float, v & 0xffff0000u); }
DI f2 bf_2(u32 v) { return (f2){bf_lo(v), bf_hi(v)}; }
DI u32 pk_b2(f2 v) { return pk_b(v[0], v[1]); }
DI float rbf(float a) { return bf_lo(pk_b(a, a)); }
DI float cosr(float rev) { return __builtin_amdgcn_cosf(rev); }
DI float sinr(float rev) { return __builtin_amdgcn_sinf(rev); }
DI f2 lo2(f4 v) { return __builtin_shufflevector(v, v, 0, 1); }
DI f2 hi2(f4 v) { return __builtin_shufflevector(v, v, 2, 3); }
DI f2 fma2(f2 a, f2 b, f2 c) { return __builtin_elementwise_fma(a, b, c); }
DI u32 umin(u32 a, u32 b) { return a < b ? a : b; }
DI u32 umax(u32 a, u32 b) { return a > b ? a : b; }
DI u32 clean(u32 v) { return umin(v, SENT - 1); }
DI u32 max4(u4 v) { return umax(umax(umax(v[0], v[1]), v[2]), v[3]); }
#define Z4 ((f4){0, 0, 0, 0})
DI f4 mma_h(u4 a, u4 b, f4 c) { return __builtin_amdgcn_mfma_f32_16x16x32_f16(__builtin_bit_cast(h8, a), __builtin_bit_cast(h8, b), c, 0, 0, 0); }
DI f4 mma_b(u4 a, u4 b, f4 c) { return __builtin_amdgcn_mfma_f32_16x16x32_bf16(__builtin_bit_cast(b8, a), __builtin_bit_cast(b8, b), c, 0, 0, 0); }
DI f4 mma16_h(u2 a, u2 b, f4 c) { return __builtin_amdgcn_mfma_f32_16x16x16f16(__builtin_bit_cast(h4, a), __builtin_bit_cast(h4, b), c, 0, 0, 0); }
typedef __amdgpu_buffer_rsrc_t rsrc_t;
DI rsrc_t mkrs(const void* p, u32 bytes) { return __builtin_amdgcn_make_buffer_rsrc((void*)p, 0, bytes, 0x00020000); }
template <int AUX> DI u4 bld4(rsrc_t r, int v, int s) { return __builtin_amdgcn_raw_buffer_load_b128(r, v, s, AUX); }
template <int AUX> DI u2 bld2(rsrc_t r, int v, int s) { return __builtin_amdgcn_raw_buffer_load_b64(r, v, s, AUX); }
template <int AUX> DI void bst4(u4 d, rsrc_t r, int v, int s) {
    __builtin_amdgcn_raw_buffer_store_b128(d, r, v, s, AUX);
    asm volatile("s_nop 1" :: "v"(d));
}
template <int AUX> DI void bst2(u2 d, rsrc_t r, int v, int s) { __builtin_amdgcn_raw_buffer_store_b64(d, r, v, s, AUX); }

template <int CTRL> DI float dpp(float v) {
    return __builtin_bit_cast(float, __builtin_amdgcn_update_dpp(0, __builtin_bit_cast(int, v), CTRL, 0xf, 0xf, true));
}
template <bool ALL> DI float red_lanes(float v) {
    v += dpp<0xB1>(v);
    if (ALL) v += dpp<0x4E>(v);
    v += dpp<0x124>(v); v += dpp<0x128>(v);
    u32 x = __builtin_bit_cast(u32, v), y = x;
    asm("" : "+v"(y));
    auto r = __builtin_amdgcn_permlane16_swap(x, y, false, false);
    v = __builtin_bit_cast(float, (u32)r[0]) + __builtin_bit_cast(float, (u32)r[1]);
    x = __builtin_bit_cast(u32, v); y = x;
    asm("" : "+v"(y));
    auto q = __builtin_amdgcn_permlane32_swap(x, y, false, false);
    return __builtin_bit_cast(float, (u32)q[0]) + __builtin_bit_cast(float, (u32)q[1]);
}
DI float ln_inv(float var) { return __builtin_amdgcn_rcpf(rbf(__builtin_amdgcn_sqrtf(rbf(rbf(var) + 1e-5f)))); }

#define ST_DECL
#define ST(i)
#define ST_END(ws, kid)

constexpr u32 WS_U = 0;
constexpr u32 WS_WT = WS_U + (2u << 20);
constexpr u32 WS_V = WS_WT + (2u << 20);
constexpr u32 WS_LN2 = WS_V + (2u << 20);
constexpr u32 WS_WA = WS_LN2 + (2u << 20);
constexpr u32 WS_WB = WS_WA + 8192u;
constexpr u32 WS_FA = WS_WB + 8192u;
constexpr u32 WS_FLAG = WS_FA + 16384u;
constexpr u32 WS_EP = WS_FLAG + 32768u;
constexpr u32 WS_H1 = WS_WA + (128u << 10);
constexpr u32 WS_END = WS_H1 + 7340032u;

constexpr int BG2 = 4, BG4 = 10, BG5 = 15, BG6 = 21;
constexpr int G1_DEPTH = 10, G2_DEPTH = 3;
constexpr int B_R = 5;
constexpr int CODE_PF = 9;
constexpr int LDS_SCR = 114688;
constexpr int SCR_X = 8448;
constexpr int SCR_S = SCR_X + 20480;
constexpr int SCR_F = 49024;
constexpr int XCH = 147456;
constexpr int LDS_B = 163840;

DI void glds16(const void* sbase, u32 voff, u32 lds_dst) {
    asm volatile("s_mov_b32 m0, %2\n\ts_nop 0\n\tglobal_load_lds_dwordx4 %0, %1" :: "v"(voff), "s"(sbase), "s"(lds_dst) : "memory");
}
DI bool any_sent(u32 v) { return __builtin_amdgcn_ballot_w64(v == SENT) != 0; }
DI void poll2(u4& a, u4& c, rsrc_t rw, int vo, int sa, int sc) {
#pragma nounroll
    for (int sp = 0; sp < SPIN_MAX; ++sp) {
        const bool ba = max4(a) == SENT, bc = max4(c) == SENT;
        if (__builtin_amdgcn_ballot_w64(ba || bc) == 0) break;
        __builtin_amdgcn_s_sleep(1);
        FENCE();
        if (ba) a = bld4<SC1>(rw, vo, sa);
        if (bc) c = bld4<SC1>(rw, vo, sc);
    }
}
DI void wait_tok(u4& t0, u4& t1, rsrc_t rw, int vo, int so) {
    if (any_sent(umax(max4(t0), max4(t1)))) {
#pragma nounroll
        for (int sp = 0; sp < SPIN_MAX; ++sp) {
            FENCE();
            t0 = bld4<SC1>(rw, vo, so); t1 = bld4<SC1>(rw, vo, so + 16384);
            if (!any_sent(umax(max4(t0), max4(t1)))) break;
        }
    }
}
DI u4 ldw1(LAS char* wb, int kk, int ft) {
    s4 a0 = __builtin_amdgcn_ds_read_tr16_b64_v4i16((LAS s4*)(wb + kk * 32 * 224 + ft * 32));
    s4 a1 = __builtin_amdgcn_ds_read_tr16_b64_v4i16((LAS s4*)(wb + (kk * 32 + 16) * 224 + ft * 32));
    u2 x0 = __builtin_bit_cast(u2, a0), x1 = __builtin_bit_cast(u2, a1);
    return (u4){x0[0], x0[1], x1[0], x1[1]};
}
DI u2 pk4(f4 a) { return (u2){pk_b(a[0], a[1]), pk_b(a[2], a[3])}; }
DI u2 silu_pk2(u32 p0, u32 p1) {
    u32 o0, o1; float a0, a1, a2, a3, t0, t1, t2, t3;
    asm("v_lshlrev_b32 %2, 16, %10\n\tv_and_b32 %3, 0xffff0000, %10\n\tv_lshlrev_b32 %4, 16, %11\n\tv_and_b32 %5, 0xffff0000, %11\n\t"
        "v_mul_f32 %6, %12, %2\n\tv_mul_f32 %7, %12, %3\n\tv_mul_f32 %8, %12, %4\n\tv_mul_f32 %9, %12, %5\n\t"
        "v_exp_f32 %6, %6\n\tv_exp_f32 %7, %7\n\tv_exp_f32 %8, %8\n\tv_exp_f32 %9, %9\n\t"
        "v_add_f32 %6, 1.0, %6\n\tv_add_f32 %7, 1.0, %7\n\tv_add_f32 %8, 1.0, %8\n\tv_add_f32 %9, 1.0, %9\n\t"
        "v_rcp_f32 %6, %6\n\tv_rcp_f32 %7, %7\n\tv_rcp_f32 %8, %8\n\tv_rcp_f32 %9, %9\n\t"
        "v_mul_f32 %2, %2, %6\n\tv_mul_f32 %3, %3, %7\n\tv_mul_f32 %4, %4, %8\n\tv_mul_f32 %5, %5, %9\n\t"
        "v_cvt_pk_bf16_f32 %0, %2, %3\n\tv_cvt_pk_bf16_f32 %1, %4, %5"
        : "=&v"(o0), "=&v"(o1), "=&v"(a0), "=&v"(a1), "=&v"(a2), "=&v"(a3), "=&v"(t0), "=&v"(t1), "=&v"(t2), "=&v"(t3)
        : "v"(p0), "v"(p1), "s"(-1.4426950408889634f));
    return (u2){o0, o1};
}
DI u4 silu_pk4(u4 p) { u2 a = silu_pk2(p[0], p[1]), b = silu_pk2(p[2], p[3]); return (u4){a[0], a[1], b[0], b[1]}; }
DI void lds_set(LAS u32* p, u32 v) {
    asm volatile("s_waitcnt lgkmcnt(0)" ::: "memory");
    __hip_atomic_store(p, v, __ATOMIC_RELAXED, __HIP_MEMORY_SCOPE_WORKGROUP);
}
DI void lds_wait(LAS u32* p, u32 v) {
#pragma nounroll
    for (int sp = 0; sp < SPIN_MAX; ++sp) {
        if (__builtin_amdgcn_readfirstlane(__hip_atomic_load(p, __ATOMIC_RELAXED, __HIP_MEMORY_SCOPE_WORKGROUP)) >= v) break;
        __builtin_amdgcn_s_sleep(1);
    }
    FENCE();
}
DI void flag_wait(GAS u32* p, u32 tag) {
#pragma nounroll
    for (int sp = 0; sp < SPIN_MAX; ++sp) {
        if (__builtin_amdgcn_ballot_w64(__hip_atomic_load(p, __ATOMIC_RELAXED, __HIP_MEMORY_SCOPE_AGENT) != tag) == 0) break;
        __builtin_amdgcn_s_sleep(2);
    }
    FENCE();
}
#define SENT4 ((u4){SENT, SENT, SENT, SENT})

__global__ __launch_bounds__(512, 2) void k_fused(const uint16_t* x, const uint16_t* wr, const uint16_t* wi, const uint16_t* w1,
                                                  const uint16_t* w2, uint16_t* out, char* ws) {
    extern __shared__ __attribute__((aligned(16))) char smem[];
    const int tid = threadIdx.x, l = tid & 63, g = l >> 4, m = l & 15;
    const int w = __builtin_amdgcn_readfirstlane(tid >> 6);
    const int b = (blockIdx.x & 7) * 32 + (blockIdx.x >> 3);
    const int mt1 = (b >> 6) * 4 + ((b >> 3) & 3), nt1 = ((b >> 5) & 1) * 8 + (b & 7);
    const int grp = b >> 4, th = (b >> 3) & 1, fb = b & 7;
    const rsrc_t rw = mkrs(ws, WS_END), ro = mkrs(out, 2097152);
    char* scr = smem + LDS_SCR;
    LAS u32* hw = (LAS u32*)((LAS char*)smem + LDS_SCR + SCR_F);
    const u32 lds0 = (u32)(uintptr_t)(LAS char*)smem;
    const float mf = (float)m, gf = (float)g, sg = (m & 1) ? -1.0f : 1.0f;
    const bool aw = w < 4;
    ST_DECL;
    unsigned long long pc0;
    asm volatile("s_getpc_b64 %0" : "=s"(pc0));
    const int rq = ((b >> 3) + b) & 3;
    const int row0 = (tid & 255) / 14, cch0 = (tid & 255) - row0 * 14;
    const char* wsrc = (const char*)w1 + nt1 * 224;
    u32 w1o[7];
    auto w1_burst = [&](int i0, int n) {
#pragma unroll
        for (int i = i0; i < i0 + n; ++i) {
            const int q = (i / 7 + rq) & 3, j = i % 7;
            glds16(wsrc + q * (128 * 3584), w1o[j], lds0 + (w - 4) * 1024 + q * 28672 + j * 4096);
        }
    };
    const u32 epoch = __hip_atomic_load((GAS u32*)(ws + WS_EP), __ATOMIC_RELAXED, __HIP_MEMORY_SCOPE_AGENT);
    FENCE();
    const int xoff = (256 * w + b) * 1024 + m * 32 + g * 8;
    u2 xl[2];
    float c16[4], s16[4];
    f2 twc[2], tws[2];
    u4 Gre3, Gim3, SGre, SGim, GA[2];
    auto consts0 = [&]() {
#pragma unroll
        for (int u = 0; u < 4; ++u) {
            float a = (4.0f * gf + u) * (mf * (1.0f / 16));
            c16[u] = cosr(a); s16[u] = sinr(a);
            twc[u >> 1][u & 1] = cosr(a * (1.0f / 32)); tws[u >> 1][u & 1] = sinr(a * (1.0f / 32));
        }
    };
    auto consts3 = [&]() {
#pragma unroll
        for (int u = 0; u < 4; ++u) {
            const float k = 4.0f * gf + u;
            const bool k0 = u == 0 && g == 0;
            float cc = c16[u], ss = s16[u];
            Gre3[u] = pk_b(cc, -ss); Gim3[u] = pk_b(ss, cc);
            float a2 = (2.0f * k + 1.0f) * (mf * (1.0f / 32));
            u32 sp = pk_b(g < 2 ? 2.f * cc : 2.f * sg * cosr(a2), g < 2 ? -2.f * ss : -2.f * sg * sinr(a2));
            SGre[u] = k0 ? pk_b(1.0f, sg) : (g < 2 ? sp : 0u);
            SGim[u] = g < 2 ? 0u : sp;
            float a4 = k * (mf * (1.0f / 32)), c4 = 2.f * cosr(a4), s4_ = -2.f * sinr(a4);
            GA[0][u] = k0 ? pk_b(1.0f, sg) : pk_b(c4, s4_);
            GA[1][u] = k0 ? pk_b(1.0f, sg) : ((u & 1) ? pk_b(-c4, -s4_) : pk_b(c4, s4_));
        }
        PIN(Gre3); PIN(Gim3); PIN(SGre); PIN(SGim); PIN(GA[0]); PIN(GA[1]); PIN(twc[0]); PIN(twc[1]); PIN(tws[0]); PIN(tws[1]);
    };
    const int pw = (w + 4 * th) & 7;
    GAS u32* fl = (GAS u32*)(ws + WS_FLAG);
    int ko[7];
    {
        const int r6 = (fb < 6 ? fb : fb - 5) + 3 * (grp & 1);
#pragma unroll
        for (int ks = 0; ks < 6; ++ks) { int k = ks + r6; k = k >= 6 ? k - 6 : k; ko[ks] = k >= 6 ? k - 6 : k; }
        ko[6] = 6;
    }
    u4 W[7][4];
    const u32 w2vo = (l >> 3) * 1024 + (((l & 7) ^ (((l >> 4) & 1) << 2)) * 16);
    auto w2_glds = [&](int p_, int ks, u32 dst) {
        const int q = ko[ks];
        const char* src = (const char*)w2 + (224 * p_ + (ks == 6 ? 96 : (q < 3 ? 32 * q : 16 + 32 * q))) * 1024 + fb * 128;
        glds16(src, w2vo, dst); glds16(src + 8192, w2vo, dst + 1024);
        glds16(src + (ks == 6 ? 112 : 16) * 1024, w2vo, dst + 2048); glds16(src + (ks == 6 ? 120 : 24) * 1024, w2vo, dst + 3072);
    };
    LAS char* w2rd = (LAS char*)smem + (4 * g + (m >> 2)) * 128 + (((m & 3) ^ (((m >> 3) & 1) << 2)) * 16);
    auto w2_read = [&](u4 (&f)[4], int off) {
#pragma unroll
        for (int t = 0; t < 4; ++t) {
            LAS char* p = (LAS char*)((u32)(uintptr_t)w2rd ^ ((t >> 1) * 64)) + off + (t & 1) * 8;
            u2 x0 = __builtin_bit_cast(u2, __builtin_amdgcn_ds_read_tr16_b64_v4i16((LAS s4*)p));
            u2 x1 = __builtin_bit_cast(u2, __builtin_amdgcn_ds_read_tr16_b64_v4i16((LAS s4*)(p + 2048)));
            f[t] = (u4){x0[0], x0[1], x1[0], x1[1]};
        }
    };
    auto bg = [&](int i0, int i1) { w1_burst(i0, i1 - i0); };
    u32 tag;
    if (aw) {
        u32 xd[8];
        const u32* xr = (const u32*)(x + (256 * (2 * w + ((m >> 1) & 1)) + b) * 512) + 8 * g + 2 * (m >> 2) + (m & 1);
#pragma unroll
        for (int j = 0; j < 8; ++j) xd[j] = xr[32 * j];
        if (tid < 8) hw[tid] = 0;
        FENCE();
        ST(2);
        {
            u4 FAre, FAim, Fre, Fim, Sre, Sim; u2 Mb; f2 pc[2], ps[2];
            consts0();
            float c0[8], s0[8];
#pragma unroll
            for (int j = 0; j < 8; ++j) {
                float a = (gf + 4.0f * j) * (mf * (1.0f / 32));
                c0[j] = cosr(a); s0[j] = m ? -sinr(a) : ((g & 1) ? -1.0f : 1.0f);
            }
            const float mo = (float)(2 * (m & 7) + 1) * (1.0f / 32);
#pragma unroll
            for (int u = 0; u < 4; ++u) {
                FAre[u] = pk_h(c0[2 * u], c0[2 * u + 1]); FAim[u] = pk_h(s0[2 * u], s0[2 * u + 1]);
                float cc = c16[u], ss = s16[u];
                Fre[u] = pk_h(cc, ss); Fim[u] = pk_h(-ss, cc);
                float a2 = (4.0f * gf + u) * mo;
                u32 re = pk_h(m < 8 ? cc : cosr(a2), 0.f), im = pk_h(m < 8 ? (m ? -ss : ((u & 1) ? -1.0f : 1.0f)) : -sinr(a2), 0.f);
                Sre[u] = m < 8 ? re : re << 16; Sim[u] = m < 8 ? im : im << 16;
                pc[u & 1][u >> 1] = twc[u >> 1][u & 1]; ps[u & 1][u >> 1] = tws[u >> 1][u & 1];
            }
#pragma unroll
            for (int e = 0; e < 2; ++e) {
                float a = (float)(((m >> 1) * (256 * (2 * g + e) + b)) & 2047) * (1.0f / 2048);
                float cc = cosr(a), ss = sinr(a);
                Mb[e] = (m & 1) ? pk_h(-ss, cc) : pk_h(cc, ss);
            }
            PIN(FAre); PIN(FAim); PIN(Fre); PIN(Fim); PIN(Sre); PIN(Sim); PIN(Mb); PIN(pc[0]); PIN(pc[1]); PIN(ps[0]); PIN(ps[1]);
            ST(3);
            f2 v[8];
#pragma unroll
            for (int j = 0; j < 8; ++j) v[j] = bf_2(xd[j]);
            f2 s2 = ((v[0] + v[1]) + (v[2] + v[3])) + ((v[4] + v[5]) + (v[6] + v[7]));
            const float mean = red_lanes<false>(s2[0] + s2[1]) * (1.0f / 512);
            f2 q0 = {0.f, 0.f}, q1 = {0.f, 0.f};
#pragma unroll
            for (int j = 0; j < 8; ++j) { v[j] -= mean; if (j & 1) q1 = fma2(v[j], v[j], q1); else q0 = fma2(v[j], v[j], q0); }
            q0 += q1;
            const float inv = ln_inv(red_lanes<false>(q0[0] + q0[1]) * (1.0f / 512));
            const float off = (mean - rbf(mean)) * inv;
#pragma unroll
            for (int j = 0; j < 8; ++j) v[j] = fma2(v[j], (f2){inv, inv}, (f2){off, off});
            u4 B[2];
#pragma unroll
            for (int p = 0; p < 2; ++p) {
                u4 A = {pk_h(v[0][p], v[1][p]), pk_h(v[2][p], v[3][p]), pk_h(v[4][p], v[5][p]), pk_h(v[6][p], v[7][p])};
                f4 Dre = mma_h(A, FAre, Z4), Dim = mma_h(A, FAim, Z4);
#pragma unroll
                for (int r = 0; r < 2; ++r) {
                    f2 re = r ? hi2(Dre) : lo2(Dre), im = r ? hi2(Dim) : lo2(Dim);
                    f2 tr = fma2(im, ps[p], re * pc[p]), ti = fma2(-re, ps[p], im * pc[p]);
                    B[r][p] = pk_h(tr[0], ti[0]); B[r][2 + p] = pk_h(tr[1], ti[1]);
                }
            }
#pragma unroll
            for (int r = 0; r < 2; ++r) {
                f4 Gre = mma_h(Fre, B[r], Z4), Gim = mma_h(Fim, B[r], Z4), Hre = mma_h(Sre, B[r], Z4), Him = mma_h(Sim, B[r], Z4);
                u4 Z;
#pragma unroll
                for (int i = 0; i < 4; ++i) Z[i] = pk_h(m ? Gre[i] : Hre[i], m ? Gim[i] : Him[i]);
                *(u4*)(scr + (2 * w + r) * 1024 + m * 64 + g * 16) = Z;
            }
            BARRIER();
            ST(18);
            u4 r0 = *(const u4*)(scr + (2 * g) * 1024 + w * 256 + m * 16);
            u4 r1 = *(const u4*)(scr + (2 * g + 1) * 1024 + w * 256 + m * 16);
#pragma unroll
            for (int t = 0; t < 4; ++t) {
                f4 D = mma16_h(Mb, (u2){r0[t], r1[t]}, Z4);
                *(u2*)(scr + SCR_S + w * 2048 + m * 128 + (((2 * t + (g >> 1)) ^ (m >> 1)) * 16) + (g & 1) * 8) =
                    (u2){clean(pk_h(D[0], D[1])), clean(pk_h(D[2], D[3]))};
            }
        }
        u4 uo0 = *(const u4*)(scr + SCR_S + w * 2048 + (l ^ ((l >> 4) & 7)) * 16);
        u4 uo1 = *(const u4*)(scr + SCR_S + w * 2048 + 1024 + (l ^ (((l >> 4) + 4) & 7)) * 16);
        u4 Fre, Fim, Gre, Gim, N0, N1; f2 tc[2], ts[2];
#pragma unroll
        for (int u = 0; u < 4; ++u) {
            float cc = c16[u], ss = s16[u];
            Fre[u] = pk_h(0.25f * cc, 0.25f * ss); Fim[u] = pk_h(-0.25f * ss, 0.25f * cc);
            Gre[u] = pk_b(cc * (1.0f / 256), -ss * (1.0f / 256)); Gim[u] = pk_b(ss * (1.0f / 256), cc * (1.0f / 256));
            float a3 = (4.0f * gf + u) * (mf * (1.0f / 256));
            tc[u >> 1][u & 1] = cosr(a3); ts[u >> 1][u & 1] = sinr(a3);
        }
        PIN(Fre); PIN(Fim); PIN(Gre); PIN(Gim); PIN(tc[0]); PIN(tc[1]); PIN(ts[0]); PIN(ts[1]);
        ST(19);
        BARRIER();
        ST(4);
        bst4<SC1>(uo0, rw, l * 16, WS_U + b * 8192 + w * 2048);
        bst4<SC1>(uo1, rw, l * 16 + 1024, WS_U + b * 8192 + w * 2048);
        xl[0] = *(const u2*)((const char*)x + xoff); xl[1] = *(const u2*)((const char*)x + xoff + 512);
        FENCE();
        tag = __builtin_amdgcn_readfirstlane(epoch) + 1u;
        consts3();
#pragma unroll
        for (int u = 0; u < 4; ++u) {
            float a1 = (float)(((4 * (g & 1) + u) * (256 * (m >> 1) + b)) & 2047) * (1.0f / 2048);
            float c1 = cosr(a1), s1 = sinr(a1);
            u32 nb = (m & 1) ? pk_b(s1, c1) : pk_b(c1, -s1);
            N0[u] = g < 2 ? nb : 0u; N1[u] = g < 2 ? 0u : nb;
        }
        PIN(N0); PIN(N1);
        WAITV(0);
        BARRIER();
        ST(5);
        u4 d[2], f[2], e[2];
        const int uo = (tid >> 1) * 8192 + (tid & 1) * 16;
        d[0] = bld4<SC1>(rw, uo, WS_U + b * 32); d[1] = bld4<SC1>(rw, uo, WS_U + 128 * 8192 + b * 32);
        const u32 fd0 = __builtin_amdgcn_raw_buffer_load_b32(rw, uo, WS_WT + b * 32, SC1);
        const u32 fd1 = __builtin_amdgcn_raw_buffer_load_b32(rw, uo, WS_WT + 128 * 8192 + b * 32, SC1);
        FENCE();
        poll2(d[0], d[1], rw, uo, WS_U + b * 32, WS_U + 128 * 8192 + b * 32);
        FENCE();
        f[0] = bld4<SC1>(rw, uo, WS_WT + b * 32); f[1] = bld4<SC1>(rw, uo, WS_WT + 128 * 8192 + b * 32);
        char* sp = scr + SCR_S + (tid ^ ((tid >> 4) & 1)) * 16;
        *(u4*)sp = d[0]; *(u4*)(sp + 4096) = d[1];
        BARRIER();
        if (b == 0) {
            f[0] = bld4<SC1>(rw, tid * 16, WS_WA); f[1] = bld4<SC1>(rw, tid * 16 + 4096, WS_WA);
            e[0] = bld4<SC1>(rw, tid * 16, WS_WB); e[1] = bld4<SC1>(rw, tid * 16 + 4096, WS_WB);
            if (tid == 0) __hip_atomic_store((GAS u32*)(ws + WS_EP), tag, __ATOMIC_RELAXED, __HIP_MEMORY_SCOPE_AGENT);
        }
        ST(6);
        u2 ld[4], wl[4];
        const int so = (64 * g + m) * 32 + (((w >> 1) ^ (m >> 3)) * 16) + (w & 1) * 8;
#pragma unroll
        for (int q = 0; q < 4; ++q) ld[q] = *(const u2*)(scr + SCR_S + so + 512 * q);
        FENCE();
        f4 Xre[2], Xim[2];
#pragma unroll
        for (int e = 0; e < 2; ++e) {
            u4 A = {ld[0][e], ld[1][e], ld[2][e], ld[3][e]};
            f4 Dre = mma_h(A, Fre, Z4), Dim = mma_h(A, Fim, Z4);
            f2 r0 = fma2(lo2(Dim), ts[0], lo2(Dre) * tc[0]), i0 = fma2(-lo2(Dre), ts[0], lo2(Dim) * tc[0]);
            f2 r1 = fma2(hi2(Dim), ts[1], hi2(Dre) * tc[1]), i1 = fma2(-hi2(Dre), ts[1], hi2(Dim) * tc[1]);
            u4 B = {pk_h(r0[0], i0[0]), pk_h(r0[1], i0[1]), pk_h(r1[0], i1[0]), pk_h(r1[1], i1[1])};
            Xre[e] = mma_h(Fre, B, Z4); Xim[e] = mma_h(Fim, B, Z4);
        }
        *(u4*)(sp + 8192) = f[0]; *(u4*)(sp + 12288) = f[1];
        asm volatile("" :: "v"(fd0), "v"(fd1));
        f2* sx = (f2*)(scr + SCR_X);
        if (b == 0) {
            *(u4*)(sp - SCR_S) = e[0]; *(u4*)(sp - SCR_S + 4096) = e[1];
#pragma unroll
            for (int e = 0; e < 2; ++e)
#pragma unroll
                for (int i = 0; i < 4; ++i) sx[2 * w + e + 8 * (m + 16 * (4 * g + i))] = (f2){Xre[e][i], Xim[e][i]};
        }
        BARRIER();
        ST(20);
#pragma unroll
        for (int q = 0; q < 4; ++q) wl[q] = *(const u2*)(scr + SCR_S + so + 8192 + 512 * q);
        FENCE();
        f2 yr[2][2], yi[2][2];
#pragma unroll
        for (int e = 0; e < 2; ++e)
#pragma unroll
            for (int hh = 0; hh < 2; ++hh) {
                f2 wre = {bf_lo(wl[2 * hh][e]), bf_lo(wl[2 * hh + 1][e])}, wim = {bf_hi(wl[2 * hh][e]), bf_hi(wl[2 * hh + 1][e])};
                f2 xr = hh ? hi2(Xre[e]) : lo2(Xre[e]), xi = hh ? hi2(Xim[e]) : lo2(Xim[e]);
                yr[e][hh] = fma2(-xi, wim, xr * wre); yi[e][hh] = fma2(xr, wim, xi * wre);
            }
        if (b == 0) {
#pragma unroll
            for (int i = 0; i < 4; ++i) {
                u2 wb = *(const u2*)(scr + so + 512 * i);
#pragma unroll
                for (int e = 0; e < 2; ++e) {
                    f2 xm = sx[(2048 - (2 * w + e + 8 * (m + 16 * (4 * g + i)))) & 2047];
                    float br = bf_lo(wb[e]), bi = bf_hi(wb[e]);
                    yr[e][i >> 1][i & 1] += xm[0] * br + xm[1] * bi;
                    yi[e][i >> 1][i & 1] += xm[0] * bi - xm[1] * br;
                }
            }
        }
        char* pv = scr + SCR_S;
#pragma unroll
        for (int e = 0; e < 2; ++e) {
            u4 Y = {pk_b(yr[e][0][0], yi[e][0][0]), pk_b(yr[e][0][1], yi[e][0][1]), pk_b(yr[e][1][0], yi[e][1][0]), pk_b(yr[e][1][1], yi[e][1][1])};
            f4 Dre = mma_b(Y, Gre, Z4), Dim = mma_b(Y, Gim, Z4);
            f2 r0 = fma2(-lo2(Dim), ts[0], lo2(Dre) * tc[0]), i0 = fma2(lo2(Dre), ts[0], lo2(Dim) * tc[0]);
            f2 r1 = fma2(-hi2(Dim), ts[1], hi2(Dre) * tc[1]), i1 = fma2(hi2(Dre), ts[1], hi2(Dim) * tc[1]);
            u4 A = {pk_b(r0[0], i0[0]), pk_b(r0[1], i0[1]), pk_b(r1[0], i1[0]), pk_b(r1[1], i1[1])};
            f4 Ere = mma_b(A, Gre, Z4), Eim = mma_b(A, Gim, Z4);
            u4 o;
#pragma unroll
            for (int i = 0; i < 4; ++i) o[i] = clean(pk_b(Ere[i], Eim[i]));
            *(u4*)(pv + (2 * w + e) * 1024 + ((m * 4 + g) ^ ((w >> 1) * 4)) * 16) = o;
        }
        BARRIER();
        ST(7);
#pragma unroll
        for (int it = 0; it < 2; ++it) {
            const int ch = tid + 256 * it;
            const u32* q = (const u32*)(pv + (ch & 1) * 4096 + (((ch >> 1) * 4) ^ ((ch & 1) * 64)));
            bst4<SC1>((u4){q[0], q[256], q[512], q[768]}, rw, ch * 16, WS_V + b * 8192);
        }
        WAITV(0);
        BARRIER();
        ST(8);
        const int vo = (2 * m + (g >> 1)) * 8192 + (g & 1) * 16, vs = WS_V + w * 64 * 8192 + b * 32;
        u4 v0 = bld4<SC1>(rw, vo, vs), v1 = bld4<SC1>(rw, vo, vs + 32 * 8192);
        poll2(v0, v1, rw, vo, vs, vs + 32 * 8192);
        ST(9);
        char* sz = scr + SCR_X + g * 2048 + w * 256 + m * 8;
#pragma unroll
        for (int hh = 0; hh < 2; ++hh) {
            f4 D0 = mma_b(N0, hh ? v1 : v0, Z4), D1 = mma_b(N1, hh ? v1 : v0, Z4);
            *(u2*)(sz + hh * 128) = (u2){pk_b(D0[0], D0[1]), pk_b(D1[0], D1[1])};
            *(u2*)(sz + 1024 + hh * 128) = (u2){pk_b(D0[2], D0[3]), pk_b(D1[2], D1[3])};
        }
    } else {
        auto announce = [&](int which) {
            if (l < 8) __hip_atomic_store((GAS u32*)(ws + WS_FA) + which * 2048 + l * 256 + b, tag, __ATOMIC_RELAXED, __HIP_MEMORY_SCOPE_AGENT);
            FENCE();
        };
        auto all_announced = [&](int which) {
#pragma nounroll
            for (int sp = 0; sp < SPIN_MAX; ++sp) {
                FENCE();
                u4 f = bld4<SC1>(rw, l * 16, WS_FA + which * 8192 + (blockIdx.x & 7) * 1024);
                if (__builtin_amdgcn_ballot_w64(f[0] != tag || f[1] != tag || f[2] != tag || f[3] != tag) == 0) break;
                __builtin_amdgcn_s_sleep(4);
            }
        };
        const rsrc_t rr = mkrs(wr, 1052672), ri = mkrs(wi, 1052672);
        const int j7 = l >> 3, p4 = (l >> 1) & 3, ee = l & 1;
        u2 fr[4], fi[4]; u32 sr_, si_;
        if (w == 4) {
            const int e = ee * 4 * 514 + (32 * j7 + 4 * p4) * 2;
#pragma unroll
            for (int i = 0; i < 4; ++i) { fr[i] = bld2<0>(rr, e + i * 514, 8 * b * 514); fi[i] = bld2<0>(ri, e + i * 514, 8 * b * 514); }
        } else if (w == 5) {
            const int e = (2045 - 8 * b) * 514 + 253 * 2 - ee * 4 * 514 - (32 * j7 + 4 * p4) * 2;
#pragma unroll
            for (int i = 1; i < 4; ++i) { fr[i] = bld2<0>(rr, e + (3 - i) * 514, 0); fi[i] = bld2<0>(ri, e + (3 - i) * 514, 0); }
            const int e0 = e + 3 * 514 - (b == 0 && !ee ? 2048 * 514 : 0);
            fr[0] = bld2<0>(rr, e0, 0); fi[0] = bld2<0>(ri, e0, 0);
            const int e1 = (8 * b + (l & 7)) * 514 + (16 + 32 * j7) * 2;
            sr_ = __builtin_amdgcn_raw_buffer_load_b16(rr, e1, 0, 0); si_ = __builtin_amdgcn_raw_buffer_load_b16(ri, e1, 0, 0);
        } else if (w == 6) {
            const int c = j7 & 3, f = 8 * b + (l & 7);
            const int e = ((c & 1) ? (2048 - f) & 2047 : f) * 514 + (c >> 1) * 512;
            sr_ = __builtin_amdgcn_raw_buffer_load_b16(rr, e, 0, 0); si_ = __builtin_amdgcn_raw_buffer_load_b16(ri, e, 0, 0);
        }
        FENCE();
        ST(2);
#pragma unroll
        for (int jj = 0; jj < 7; ++jj) {
            const int cj_ = (4 * jj) % 14, rj = 18 * jj + (4 * jj) / 14;
            w1o[jj] = (row0 + rj) * 3584 + (cch0 + cj_) * 16 + (cch0 + cj_ >= 14 ? 3584 - 224 : 0);
            PIN(w1o[jj]);
        }
        u32 cpf = 0;
        if (w == 6 && l < CODE_PF) cpf = *(const volatile GAS u32*)(pc0 + ((b & 31) * CODE_PF + l) * 128);
        if (w == 7) {
#pragma unroll
            for (int i = 0; i < 8; ++i) bst4<SC1>(SENT4, rw, l * 16 + (i & 3) * 1024, WS_U + b * 8192 + (i >> 2) * 4096);
        }
        BARRIER();
        if (w < 6) {
            const int wo = p4 * 2048 + j7 * 32 + ee * 16, wso = WS_WT + b * 8192 + (w - 4) * 256;
#pragma unroll
            for (int c = 0; c < 4; ++c) {
                u4 o;
#pragma unroll
                for (int i = 0; i < 4; ++i)
                    o[i] = w == 4 ? __builtin_amdgcn_perm(fi[i][c >> 1], fr[i][c >> 1], (c & 1) ? 0x07060302u : 0x05040100u)
                                  : __builtin_amdgcn_perm(fi[i][1 - (c >> 1)], fr[i][1 - (c >> 1)], (c & 1) ? 0x05040100u : 0x07060302u) ^ 0x80000000u;
                bst4<SC1>(o, rw, wo + c * 512, wso);
            }
            if (w == 5) __builtin_amdgcn_raw_buffer_store_b32(sr_ | (si_ << 16), rw, l * 4, wso, SC1);
        } else if (w == 6) {
            float re = bf_lo(sr_), im = bf_lo(si_);
            re += dpp<0x128>(re); im -= dpp<0x128>(im);
            u32 a = __builtin_bit_cast(u32, re), a2 = a, c = __builtin_bit_cast(u32, im), c2 = c;
            asm("" : "+v"(a2), "+v"(c2));
            auto pr = __builtin_amdgcn_permlane16_swap(a, a2, false, false);
            auto pi = __builtin_amdgcn_permlane16_swap(c, c2, false, false);
            const float ar = __builtin_bit_cast(float, (u32)pr[0]), br = __builtin_bit_cast(float, (u32)pr[1]);
            const float ai = __builtin_bit_cast(float, (u32)pi[0]), bi = __builtin_bit_cast(float, (u32)pi[1]);
            if (l < 8) {
                __builtin_amdgcn_raw_buffer_store_b32(pk_b(0.25f * (ar + br), 0.25f * (ai + bi)), rw, l * 4, WS_WA + b * 32, SC1);
                __builtin_amdgcn_raw_buffer_store_b32(pk_b(0.25f * (ar - br), 0.25f * (ai - bi)), rw, l * 4, WS_WB + b * 32, SC1);
            }
        }
        ST(18);
        tag = __builtin_amdgcn_readfirstlane(epoch) + 1u;
        WAITV(0);
        asm volatile("" :: "v"(cpf));
        if (w == 7) announce(0);
        ST(19);
        BARRIER();
        if (w == 7) all_announced(0);
        else __builtin_amdgcn_s_sleep(2);
        xl[0] = *(const u2*)((const char*)x + xoff); xl[1] = *(const u2*)((const char*)x + xoff + 512);
        FENCE();
        bg(0, BG2);
        ST(4);
        BARRIER();
        __builtin_amdgcn_s_sleep(2);
#pragma unroll
        for (int i = 0; i < 2; ++i) bst4<SC1>(SENT4, rw, l * 16 + i * 1024, WS_V + b * 8192 + (w - 4) * 2048);
        FENCE();
        consts0();
        consts3();
        WAITV(0);
        ST(5);
        BARRIER();
        if (w == 7) announce(1);
#pragma unroll
        for (int i = 0; i < 2; ++i) bst4<SC1>(SENT4, rw, (l >> 3) * 256 + (l & 7) * 16 + i * 2048, WS_LN2 + (b >> 1) * 16384 + (b & 1) * 128 + (w - 4) * 4096);
        bg(BG2, BG4);
        BARRIER();
        bg(BG4, BG5);
        WAITV(BG5 - BG2);
        ST(6);
        BARRIER();
        if (w == 7) all_announced(1);
        else __builtin_amdgcn_s_sleep(3);
        bg(BG5, BG6);
        ST(7);
        BARRIER();
    }
    BARRIER();
    {
        u4 A = *(const u4*)(scr + SCR_X + w * 1024 + m * 64 + g * 16);
        f4 Dre = mma_b(A, Gre3, Z4), Dim = mma_b(A, Gim3, Z4), Hre = mma_b(A, SGre, Z4), Him = mma_b(A, SGim, Z4);
        if (g == 0) { Dre[0] = Hre[0]; Dim[0] = Him[0]; }
        f2 r0 = fma2(-lo2(Dim), tws[0], lo2(Dre) * twc[0]), i0 = fma2(lo2(Dre), tws[0], lo2(Dim) * twc[0]);
        f2 r1 = fma2(-hi2(Dim), tws[1], hi2(Dre) * twc[1]), i1 = fma2(hi2(Dre), tws[1], hi2(Dim) * twc[1]);
        u4 A2 = {pk_b(r0[0], i0[0]), pk_b(r0[1], i0[1]), pk_b(r1[0], i1[0]), pk_b(r1[1], i1[1])};
        f2 h[4]; u2 hp[2];
#pragma unroll
        for (int t = 0; t < 2; ++t) {
            f4 Yv = mma_b(A2, GA[t], Z4);
            u32 h0 = pk_b2(bf_2(xl[t][0]) + bf_2(pk_b(Yv[0], Yv[1]))), h1 = pk_b2(bf_2(xl[t][1]) + bf_2(pk_b(Yv[2], Yv[3])));
            hp[t] = (u2){h0, h1};
            h[2 * t] = bf_2(h0); h[2 * t + 1] = bf_2(h1);
        }
        f2 s2 = (h[0] + h[1]) + (h[2] + h[3]);
        const float mean = red_lanes<true>(s2[0] + s2[1]) * (1.0f / 512);
        f2 q2 = {0.f, 0.f};
#pragma unroll
        for (int j = 0; j < 4; ++j) { f2 d = h[j] - mean; q2 = fma2(d, d, q2); }
        const float inv = ln_inv(red_lanes<true>(q2[0] + q2[1]) * (1.0f / 512));
        const float mb = rbf(mean);
#pragma unroll
        for (int t = 0; t < 2; ++t)
            *(u2*)(scr + ((8 * t + (m >> 1)) * 4 + g) * 128 + (w ^ (m >> 1)) * 16 + (m & 1) * 8) =
                (u2){clean(pk_b2(bf_2(pk_b2(h[2 * t] - mb)) * inv)), clean(pk_b2(bf_2(pk_b2(h[2 * t + 1] - mb)) * inv))};
        WAITV(0);
        BARRIER();
        u4 lo = *(const u4*)(scr + (tid ^ ((tid >> 5) & 7)) * 16);
        bst4<SC1>(lo, rw, (tid >> 3) * 256 + (tid & 7) * 16, WS_LN2 + (b >> 1) * 16384 + (b & 1) * 128);
        FENCE();
        if (aw) WAITV(0); else __builtin_amdgcn_s_sleep(8);
        bst2<SC1>(hp[0], ro, xoff, 0); bst2<SC1>(hp[1], ro, xoff + 512, 0);
        FENCE();
        ST(10);
    }
    auto stage_out = [&](int r) {
        w2_read(W[r], LDS_SCR + (w & 3) * 8192 + (r & 1) * 4096);
        asm volatile("s_waitcnt lgkmcnt(0)" ::: "memory");
        PIN(W[r][0]); PIN(W[r][1]); PIN(W[r][2]); PIN(W[r][3]);
        if (r + 2 < 7) w2_glds(pw, r + 2, lds0 + LDS_SCR + (w & 3) * 8192 + (r & 1) * 4096);
    };
    auto publish = [&]() {
        u32 prev = 0;
        if (l == 0) prev = __hip_atomic_fetch_add(hw + 4, 1u, __ATOMIC_RELAXED, __HIP_MEMORY_SCOPE_WORKGROUP);
        if (__builtin_amdgcn_readfirstlane(prev) == 7u && l < 2)
            __hip_atomic_store(fl + (l ? 4096 + (b >> 1) * 32 + (b & 1) : (mt1 * 8 + (nt1 >> 1)) * 32 + (nt1 & 1)), tag, __ATOMIC_RELAXED, __HIP_MEMORY_SCOPE_AGENT);
        FENCE();
    };
    {
        const int tg = w & 3;
        char* xch = smem + XCH + tg * 3072;
        const int hoff = WS_H1 + (mt1 * 8 + 2 * tg) * (56 * 1024) + (nt1 >> 1) * 7168;
        if (aw) {
            const int toff = WS_LN2 + (mt1 * 8 + 2 * tg) * 16384;
            LAS char* wb0 = (LAS char*)smem + (4 * g + (m >> 2)) * 224 + (m & 3) * 8;
            LAS char* wb[4];
            int tq[4];
#pragma unroll
            for (int q = 0; q < 4; ++q) { const int qq = (q + rq) & 3; wb[q] = wb0 + qq * 28672; tq[q] = toff + qq * 4096; }
            u4 T[2][16], A[2][7];
            auto ldt = [&](int kk) {
#pragma unroll
                for (int tt = 0; tt < 2; ++tt) T[tt][kk] = bld4<SC1>(rw, l * 16 + (kk & 3) * 1024, tq[kk >> 2] + tt * 16384);
            };
#pragma unroll
            for (int i = 0; i < 7; ++i) A[0][i] = ldw1(wb[0], 0, i);
            if (l == 0) __hip_atomic_fetch_add(hw + 5, 1u, __ATOMIC_RELAXED, __HIP_MEMORY_SCOPE_WORKGROUP);
            ST(11);
#pragma unroll
            for (int kk = 0; kk < G1_DEPTH; ++kk) { ldt(kk); SCHED(); }
            f4 acc[2][7];
#pragma unroll
            for (int tt = 0; tt < 2; ++tt)
#pragma unroll
                for (int i = 0; i < 7; ++i) acc[tt][i] = Z4;
#pragma unroll
            for (int kk = 0; kk < 16; ++kk) {
                if ((kk & 1) == 0) {
                    if (__builtin_expect(any_sent(umax(umax(max4(T[0][kk]), max4(T[1][kk])), umax(max4(T[0][kk + 1]), max4(T[1][kk + 1])))), 0)) {
                        wait_tok(T[0][kk], T[1][kk], rw, l * 16, tq[kk >> 2] + (kk & 3) * 1024);
                        wait_tok(T[0][kk + 1], T[1][kk + 1], rw, l * 16, tq[kk >> 2] + ((kk + 1) & 3) * 1024);
                    }
                    if (kk == 0) ST(12);
                    if (kk == (BG6 / 7) * 4 - 2) lds_wait(hw + 5, 8u);
                }
                if (kk + G1_DEPTH < 16) ldt(kk + G1_DEPTH);
                if (kk < 15) {
#pragma unroll
                    for (int i = 0; i < 7; ++i) A[(kk + 1) & 1][i] = ldw1(wb[(kk + 1) >> 2], (kk + 1) & 3, i);
                }
#pragma unroll
                for (int i = 0; i < 7; ++i)
#pragma unroll
                    for (int tt = 0; tt < 2; ++tt) acc[tt][i] = mma_b(A[kk & 1][i], T[tt][kk], acc[tt][i]);
#pragma unroll
                for (int i = 0; i < 14; ++i) {
                    __builtin_amdgcn_sched_group_barrier(0x8, 1, 0);
                    if (i == 1 || i == 7) __builtin_amdgcn_sched_group_barrier(0x20, 1, 0);
                    __builtin_amdgcn_sched_group_barrier(0x100, 1, 0);
                    if ((kk & 1) && i >= 3) __builtin_amdgcn_sched_group_barrier(0x2, 1, 0);
                }
                if (kk & 1) {} else SCHED();
            }
#pragma unroll
            for (int q = 0; q < 3; ++q) {
                u2 p0 = pk4(acc[1][2 * q]), p1 = pk4(acc[1][2 * q + 1]);
                *(u4*)(xch + q * 1024 + l * 16) = (u4){p0[0], p0[1], p1[0], p1[1]};
            }
            lds_set(hw + tg, 1u);
            ST(13);
#pragma unroll
            for (int q = 0; q < 3; ++q) {
                u2 p0 = pk4(acc[0][2 * q]), p1 = pk4(acc[0][2 * q + 1]);
                bst4<SC1>(silu_pk4((u4){p0[0], p0[1], p1[0], p1[1]}), rw, l * 16, hoff + ((nt1 & 1) * 3 + q) * 1024);
            }
#pragma unroll
            for (int tt = 0; tt < 2; ++tt) { u2 p = pk4(acc[tt][6]); bst2<SC1>(silu_pk2(p[0], p[1]), rw, l * 8, hoff + tt * (56 * 1024) + 6144 + (nt1 & 1) * 512); }
        } else {
            bg(BG6, 28);
            WAITV(0);
            if (l == 0) __hip_atomic_fetch_add(hw + 5, 1u, __ATOMIC_RELAXED, __HIP_MEMORY_SCOPE_WORKGROUP);
            __builtin_amdgcn_s_sleep(8);
            lds_wait(hw + 5, 8u);
            w2_glds(pw, 0, lds0 + LDS_SCR + tg * 8192);
            w2_glds(pw, 1, lds0 + LDS_SCR + tg * 8192 + 4096);
#pragma unroll
            for (int r = 0; r < B_R; ++r) { WAITV(4); stage_out(r); }
            ST(12);
            lds_wait(hw + tg, 1u);
            ST(13);
#pragma unroll
            for (int q = 0; q < 3; ++q)
                bst4<SC1>(silu_pk4(*(const u4*)(xch + q * 1024 + l * 16)), rw, l * 16, hoff + 56 * 1024 + ((nt1 & 1) * 3 + q) * 1024);
        }
        ST(14);
        WAITV(0);
        publish();
        ST(21);
        if (aw) {
            lds_wait(hw + 4, 8u);
#pragma unroll
            for (int ks = 0; ks < 7; ++ks) w2_glds(pw, ks, lds0 + tg * 28672 + ks * 4096);
        } else {
            stage_out(B_R);
#pragma unroll
            for (int r = B_R + 1; r < 7; ++r) { if (r + 1 < 7) WAITV(4); else WAITV(0); stage_out(r); }
        }
    }
    {
        ST(15);
        const int rot = w & 3;
        if (aw) {
            flag_wait(fl + ((l & 4) ? 4096 + (grp * 8 + th * 4 + rot) * 32 : (grp * 8 + (pw ^ ((l & 2) * 2))) * 32) + (l & 1), tag);
            lds_set(hw + 8 + rot, tag);
        } else {
#pragma nounroll
            for (int sp = 0; sp < SPIN_MAX; ++sp) {
                if (__builtin_amdgcn_readfirstlane(__hip_atomic_load(hw + 8 + rot, __ATOMIC_RELAXED, __HIP_MEMORY_SCOPE_WORKGROUP)) == tag) break;
                __builtin_amdgcn_s_sleep(1);
            }
            FENCE();
        }
        ST(16);
        const int toff = WS_H1 + (grp * 8 + th * 4) * (56 * 1024) + pw * 7168;
        int to[4];
#pragma unroll
        for (int tt = 0; tt < 4; ++tt) to[tt] = toff + ((tt + rot) & 3) * (56 * 1024);
        u4 T[7][4];
        auto ldtok = [&](int ks) {
#pragma unroll
            for (int tt = 0; tt < 4; ++tt) {
                if (ks < 6) T[ks][tt] = bld4<SC1>(rw, l * 16, to[tt] + ko[ks] * 1024);
                else {
                    u2 a = bld2<SC1>(rw, l * 8 + 2048, to[tt] + 4096), c = bld2<SC1>(rw, l * 8 + 2560, to[tt] + 4096);
                    T[ks][tt] = (u4){a[0], a[1], c[0], c[1]};
                }
            }
            SCHED();
        };
#pragma unroll
        for (int ks = 0; ks < G2_DEPTH; ++ks) ldtok(ks);
        if (aw) {
#pragma unroll
            for (int ks = 0; ks < 7; ++ks) w2_read(W[ks], rot * 28672 + ks * 4096);
            asm volatile("s_waitcnt lgkmcnt(0)" ::: "memory");
        }
        if (l == 0) __hip_atomic_fetch_add(hw + 7, 1u, __ATOMIC_RELAXED, __HIP_MEMORY_SCOPE_WORKGROUP);
        const int rho = grp * 128 + th * 64 + rot * 16 + m;
        const int ooff = (256 * (rho & 7) + (rho >> 3)) * 1024 + fb * 128 + (w >> 2) * 64 + g * 16;
        const u4 hres = bld4<SC1>(ro, ooff, 0);
        SCHED();
        f4 acc[4][4];
#pragma unroll
        for (int tt = 0; tt < 4; ++tt)
#pragma unroll
            for (int t = 0; t < 4; ++t) acc[tt][t] = Z4;
#pragma unroll
        for (int ks = 0; ks < 7; ++ks) {
            if (ks + G2_DEPTH < 7) { PIN(T[ks][0]); ldtok(ks + G2_DEPTH); }
#pragma unroll
            for (int tt = 0; tt < 4; ++tt)
#pragma unroll
                for (int t = 0; t < 4; ++t) {
                    acc[tt][t] = mma_b(W[ks][t], T[ks][tt], acc[tt][t]);
                }
            SCHED();
        }
        ST(17);
        lds_wait(hw + 7, 8u);
        char* dp = smem + w * 16384 + l * 16;
#pragma unroll
        for (int tt = 1; tt < 4; ++tt)
#pragma unroll
            for (int t = 0; t < 4; ++t) *(f4*)(dp + (((tt + rot) & 3) * 4 + t) * 1024) = acc[tt][t];
        f4 s0, s1;
        if (aw) { *(f4*)(dp + (rot * 4 + 2) * 1024) = acc[0][2]; *(f4*)(dp + (rot * 4 + 3) * 1024) = acc[0][3]; s0 = acc[0][0]; s1 = acc[0][1]; }
        else { *(f4*)(dp + (rot * 4) * 1024) = acc[0][0]; *(f4*)(dp + (rot * 4 + 1) * 1024) = acc[0][1]; s0 = acc[0][2]; s1 = acc[0][3]; }
        BARRIER();
        f4 r0[7], r1[7];
#pragma unroll
        for (int j = 1; j < 8; ++j) {
            const char* rp = smem + (((w + j) & 7) * 16 + rot * 4 + (w >> 2) * 2) * 1024 + l * 16;
            r0[j - 1] = *(const f4*)rp; r1[j - 1] = *(const f4*)(rp + 1024);
        }
        SCHED();
        s0 = ((s0 + r0[0]) + (r0[1] + r0[2])) + ((r0[3] + r0[4]) + (r0[5] + r0[6]));
        s1 = ((s1 + r1[0]) + (r1[1] + r1[2])) + ((r1[3] + r1[4]) + (r1[5] + r1[6]));
        u2 p0 = pk4(s0), p1 = pk4(s1);
        bst4<NT>((u4){pk_b2(bf_2(hres[0]) + bf_2(p0[0])), pk_b2(bf_2(hres[1]) + bf_2(p0[1])),
                           pk_b2(bf_2(hres[2]) + bf_2(p1[0])), pk_b2(bf_2(hres[3]) + bf_2(p1[1]))}, ro, ooff, 0);
    }
    ST_END(ws, 0);
}

extern "C" void kernel_launch(void* const* d_in, const int* in_sizes, int n_in, void* d_out, int out_size,
                              void* d_ws, size_t ws_size, hipStream_t stream) {
    (void)hipFuncSetAttribute((const void*)k_fused, hipFuncAttributeMaxDynamicSharedMemorySize, LDS_B);
    k_fused<<<256, 512, LDS_B, stream>>>((const uint16_t*)d_in[0], (const uint16_t*)d_in[1], (const uint16_t*)d_in[2],
                                         (const uint16_t*)d_in[4], (const uint16_t*)d_in[5], (uint16_t*)d_out, (char*)d_ws);
}
```

```cpp
#include <hip/hip_runtime.h>
#include <hip/hip_bf16.h>
#include <stdint.h>

typedef _Float16 h8 __attribute__((ext_vector_type(8)));
typedef _Float16 h4 __attribute__((ext_vector_type(4)));
typedef _Float16 h2 __attribute__((ext_vector_type(2)));
typedef __bf16 b8 __attribute__((ext_vector_type(8)));
typedef __bf16 b2 __attribute__((ext_vector_type(2)));
typedef short s4 __attribute__((ext_vector_type(4)));
typedef float f4 __attribute__((ext_vector_type(4)));
typedef float f2 __attribute__((ext_vector_type(2)));
typedef unsigned u32;
typedef u32 u4 __attribute__((ext_vector_type(4)));
typedef u32 u2 __attribute__((ext_vector_type(2)));
#define LAS __attribute__((address_space(3)))
#define GAS __attribute__((address_space(1)))
#define DI __device__ __forceinline__
#define FENCE() asm volatile("" ::: "memory")
#define PIN(x) asm volatile("" : "+v"(x))
#define SCHED() __builtin_amdgcn_sched_barrier(0)
#define WAITV(n) asm volatile("s_waitcnt vmcnt(%0)" :: "n"(n) : "memory")
#define BARRIER() do { asm volatile("s_waitcnt lgkmcnt(0)" ::: "memory"); __builtin_amdgcn_s_barrier(); FENCE(); } while (0)
constexpr int SC1 = 16, NT = 2;
constexpr u32 SENT = 0xffffffffu;
constexpr int SPIN_MAX = 1 << 20;

DI u32 pk_h(float a, float b) { f2 v = {a, b}; h2 r = __builtin_convertvector(v, h2); return __builtin_bit_cast(u32, r); }
DI u32 pk_b(float a, float b) { f2 v = {a, b}; b2 r = __builtin_convertvector(v, b2); return __builtin_bit_cast(u32, r); }
DI float bf_lo(u32 v) { return __builtin_bit_cast(float, v << 16); }
DI float bf_hi(u32 v) { return __builtin_bit_cast(float, v & 0xffff0000u); }
DI f2 bf_2(u32 v) { return (f2){bf_lo(v), bf_hi(v)}; }
DI u32 pk_b2(f2 v) { return pk_b(v[0], v[1]); }
DI float rbf(float a) { return bf_lo(pk_b(a, a)); }
DI float cosr(float rev) { return __builtin_amdgcn_cosf(rev); }
DI float sinr(float rev) { return __builtin_amdgcn_sinf(rev); }
DI f2 lo2(f4 v) { return __builtin_shufflevector(v, v, 0, 1); }
DI f2 hi2(f4 v) { return __builtin_shufflevector(v, v, 2, 3); }
DI f2 fma2(f2 a, f2 b, f2 c) { return __builtin_elementwise_fma(a, b, c); }
DI u32 umin(u32 a, u32 b) { return a < b ? a : b; }
DI u32 umax(u32 a, u32 b) { return a > b ? a : b; }
DI u32 clean(u32 v) { return umin(v, SENT - 1); }
DI u32 max4(u4 v) { return umax(umax(umax(v[0], v[1]), v[2]), v[3]); }
#define Z4 ((f4){0, 0, 0, 0})
DI f4 mma_h(u4 a, u4 b, f4 c) { return __builtin_amdgcn_mfma_f32_16x16x32_f16(__builtin_bit_cast(h8, a), __builtin_bit_cast(h8, b), c, 0, 0, 0); }
DI f4 mma_b(u4 a, u4 b, f4 c) { return __builtin_amdgcn_mfma_f32_16x16x32_bf16(__builtin_bit_cast(b8, a), __builtin_bit_cast(b8, b), c, 0, 0, 0); }
DI f4 mma16_h(u2 a, u2 b, f4 c) { return __builtin_amdgcn_mfma_f32_16x16x16f16(__builtin_bit_cast(h4, a), __builtin_bit_cast(h4, b), c, 0, 0, 0); }
typedef __amdgpu_buffer_rsrc_t rsrc_t;
DI rsrc_t mkrs(const void* p, u32 bytes) { return __builtin_amdgcn_make_buffer_rsrc((void*)p, 0, bytes, 0x00020000); }
template <int AUX> DI u4 bld4(rsrc_t r, int v, int s) { return __builtin_amdgcn_raw_buffer_load_b128(r, v, s, AUX); }
template <int AUX> DI u2 bld2(rsrc_t r, int v, int s) { return __builtin_amdgcn_raw_buffer_load_b64(r, v, s, AUX); }
template <int AUX> DI void bst4(u4 d, rsrc_t r, int v, int s) {
    __builtin_amdgcn_raw_buffer_store_b128(d, r, v, s, AUX);
    asm volatile("s_nop 1" :: "v"(d));
}
template <int AUX> DI void bst2(u2 d, rsrc_t r, int v, int s) { __builtin_amdgcn_raw_buffer_store_b64(d, r, v, s, AUX); }

template <int CTRL> DI float dpp(float v) {
    return __builtin_bit_cast(float, __builtin_amdgcn_update_dpp(0, __builtin_bit_cast(int, v), CTRL, 0xf, 0xf, true));
}
template <bool ALL> DI float red_lanes(float v) {
    v += dpp<0xB1>(v);
    if (ALL) v += dpp<0x4E>(v);
    v += dpp<0x124>(v); v += dpp<0x128>(v);
    u32 x = __builtin_bit_cast(u32, v), y = x;
    asm("" : "+v"(y));
    auto r = __builtin_amdgcn_permlane16_swap(x, y, false, false);
    v = __builtin_bit_cast(float, (u32)r[0]) + __builtin_bit_cast(float, (u32)r[1]);
    x = __builtin_bit_cast(u32, v); y = x;
    asm("" : "+v"(y));
    auto q = __builtin_amdgcn_permlane32_swap(x, y, false, false);
    return __builtin_bit_cast(float, (u32)q[0]) + __builtin_bit_cast(float, (u32)q[1]);
}
DI float ln_inv(float var) { return __builtin_amdgcn_rcpf(rbf(__builtin_amdgcn_sqrtf(rbf(rbf(var) + 1e-5f)))); }

#define ST_DECL
#define ST(i)
#define ST_END(ws, kid)

constexpr u32 WS_U = 0;
constexpr u32 WS_WT = WS_U + (2u << 20);
constexpr u32 WS_V = WS_WT + (2u << 20);
constexpr u32 WS_LN2 = WS_V + (2u << 20);
constexpr u32 WS_WA = WS_LN2 + (2u << 20);
constexpr u32 WS_WB = WS_WA + 8192u;
constexpr u32 WS_FA = WS_WB + 8192u;
constexpr u32 WS_FLAG = WS_FA + 24576u;
constexpr u32 WS_EP = WS_FLAG + 32768u;
constexpr u32 WS_H1 = WS_WA + (128u << 10);
constexpr u32 WS_END = WS_H1 + 7340032u;

#define W_EARLY 0
constexpr int BG2 = 4, BG4 = 10, BG5 = 15, BG6 = 21;
constexpr int G1_DEPTH = 10, G2_DEPTH = 3;
constexpr int B_R = 5;
constexpr int CODE_PF = 9;
constexpr int LDS_SCR = 114688;
constexpr int SCR_X = 8448;
constexpr int SCR_S = SCR_X + 20480;
constexpr int SCR_F = 49024;
constexpr int XCH = 147456;
constexpr int LDS_B = 163840;

DI void glds16(const void* sbase, u32 voff, u32 lds_dst) {
    asm volatile("s_mov_b32 m0, %2\n\ts_nop 0\n\tglobal_load_lds_dwordx4 %0, %1" :: "v"(voff), "s"(sbase), "s"(lds_dst) : "memory");
}
DI bool any_sent(u32 v) { return __builtin_amdgcn_ballot_w64(v == SENT) != 0; }
DI void poll2(u4& a, u4& c, rsrc_t rw, int vo, int sa, int sc) {
#pragma nounroll
    for (int sp = 0; sp < SPIN_MAX; ++sp) {
        const bool ba = max4(a) == SENT, bc = max4(c) == SENT;
        if (__builtin_amdgcn_ballot_w64(ba || bc) == 0) break;
        __builtin_amdgcn_s_sleep(1);
        FENCE();
        if (ba) a = bld4<SC1>(rw, vo, sa);
        if (bc) c = bld4<SC1>(rw, vo, sc);
    }
}
DI void wait_tok(u4& t0, u4& t1, rsrc_t rw, int vo, int so) {
    if (any_sent(umax(max4(t0), max4(t1)))) {
#pragma nounroll
        for (int sp = 0; sp < SPIN_MAX; ++sp) {
            FENCE();
            t0 = bld4<SC1>(rw, vo, so); t1 = bld4<SC1>(rw, vo, so + 16384);
            if (!any_sent(umax(max4(t0), max4(t1)))) break;
        }
    }
}
DI u4 ldw1(LAS char* wb, int kk, int ft) {
    s4 a0 = __builtin_amdgcn_ds_read_tr16_b64_v4i16((LAS s4*)(wb + kk * 32 * 224 + ft * 32));
    s4 a1 = __builtin_amdgcn_ds_read_tr16_b64_v4i16((LAS s4*)(wb + (kk * 32 + 16) * 224 + ft * 32));
    u2 x0 = __builtin_bit_cast(u2, a0), x1 = __builtin_bit_cast(u2, a1);
    return (u4){x0[0], x0[1], x1[0], x1[1]};
}
DI u2 pk4(f4 a) { return (u2){pk_b(a[0], a[1]), pk_b(a[2], a[3])}; }
DI u2 silu_pk2(u32 p0, u32 p1) {
    u32 o0, o1; float a0, a1, a2, a3, t0, t1, t2, t3;
    asm("v_lshlrev_b32 %2, 16, %10\n\tv_and_b32 %3, 0xffff0000, %10\n\tv_lshlrev_b32 %4, 16, %11\n\tv_and_b32 %5, 0xffff0000, %11\n\t"
        "v_mul_f32 %6, %12, %2\n\tv_mul_f32 %7, %12, %3\n\tv_mul_f32 %8, %12, %4\n\tv_mul_f32 %9, %12, %5\n\t"
        "v_exp_f32 %6, %6\n\tv_exp_f32 %7, %7\n\tv_exp_f32 %8, %8\n\tv_exp_f32 %9, %9\n\t"
        "v_add_f32 %6, 1.0, %6\n\tv_add_f32 %7, 1.0, %7\n\tv_add_f32 %8, 1.0, %8\n\tv_add_f32 %9, 1.0, %9\n\t"
        "v_rcp_f32 %6, %6\n\tv_rcp_f32 %7, %7\n\tv_rcp_f32 %8, %8\n\tv_rcp_f32 %9, %9\n\t"
        "v_mul_f32 %2, %2, %6\n\tv_mul_f32 %3, %3, %7\n\tv_mul_f32 %4, %4, %8\n\tv_mul_f32 %5, %5, %9\n\t"
        "v_cvt_pk_bf16_f32 %0, %2, %3\n\tv_cvt_pk_bf16_f32 %1, %4, %5"
        : "=&v"(o0), "=&v"(o1), "=&v"(a0), "=&v"(a1), "=&v"(a2), "=&v"(a3), "=&v"(t0), "=&v"(t1), "=&v"(t2), "=&v"(t3)
        : "v"(p0), "v"(p1), "s"(-1.4426950408889634f));
    return (u2){o0, o1};
}
DI u4 silu_pk4(u4 p) { u2 a = silu_pk2(p[0], p[1]), b = silu_pk2(p[2], p[3]); return (u4){a[0], a[1], b[0], b[1]}; }
DI void lds_set(LAS u32* p, u32 v) {
    asm volatile("s_waitcnt lgkmcnt(0)" ::: "memory");
    __hip_atomic_store(p, v, __ATOMIC_RELAXED, __HIP_MEMORY_SCOPE_WORKGROUP);
}
DI void lds_wait(LAS u32* p, u32 v) {
#pragma nounroll
    for (int sp = 0; sp < SPIN_MAX; ++sp) {
        if (__builtin_amdgcn_readfirstlane(__hip_atomic_load(p, __ATOMIC_RELAXED, __HIP_MEMORY_SCOPE_WORKGROUP)) >= v) break;
        __builtin_amdgcn_s_sleep(1);
    }
    FENCE();
}
DI void flag_wait(GAS u32* p, u32 tag) {
#pragma nounroll
    for (int sp = 0; sp < SPIN_MAX; ++sp) {
        if (__builtin_amdgcn_ballot_w64(__hip_atomic_load(p, __ATOMIC_RELAXED, __HIP_MEMORY_SCOPE_AGENT) != tag) == 0) break;
        __builtin_amdgcn_s_sleep(2);
    }
    FENCE();
}
#define SENT4 ((u4){SENT, SENT, SENT, SENT})

__global__ __launch_bounds__(512, 2) void k_fused(const uint16_t* x, const uint16_t* wr, const uint16_t* wi, const uint16_t* w1,
                                                  const uint16_t* w2, uint16_t* out, char* ws) {
    extern __shared__ __attribute__((aligned(16))) char smem[];
    const int tid = threadIdx.x, l = tid & 63, g = l >> 4, m = l & 15;
    const int w = __builtin_amdgcn_readfirstlane(tid >> 6);
    const int b = (blockIdx.x & 7) * 32 + (blockIdx.x >> 3);
    const int mt1 = (b >> 6) * 4 + ((b >> 3) & 3), nt1 = ((b >> 5) & 1) * 8 + (b & 7);
    const int grp = b >> 4, th = (b >> 3) & 1, fb = b & 7;
    const rsrc_t rw = mkrs(ws, WS_END), ro = mkrs(out, 2097152);
    char* scr = smem + LDS_SCR;
    LAS u32* hw = (LAS u32*)((LAS char*)smem + LDS_SCR + SCR_F);
    const u32 lds0 = (u32)(uintptr_t)(LAS char*)smem;
    const float mf = (float)m, gf = (float)g, sg = (m & 1) ? -1.0f : 1.0f;
    const bool aw = w < 4;
    ST_DECL;
    unsigned long long pc0;
    asm volatile("s_getpc_b64 %0" : "=s"(pc0));
    if (tid >= 448 && l < CODE_PF)
        asm volatile("s_mov_b32 m0, %2\n\ts_nop 0\n\tglobal_load_lds_dword %0, %1"
                     :: "v"(((blockIdx.x >> 3) * CODE_PF + l) * 128), "s"(pc0), "s"(lds0 + LDS_SCR + SCR_F + 64) : "memory");
    const int rq = ((b >> 3) + b) & 3;
    const int row0 = (tid & 255) / 14, cch0 = (tid & 255) - row0 * 14;
    const char* wsrc = (const char*)w1 + nt1 * 224;
    u32 w1o[7];
    auto w1_burst = [&](int i0, int n) {
#pragma unroll
        for (int i = i0; i < i0 + n; ++i) {
            const int q = (i / 7 + rq) & 3, j = i % 7;
            glds16(wsrc + q * (128 * 3584), w1o[j], lds0 + (w - 4) * 1024 + q * 28672 + j * 4096);
        }
    };
    const u32 epoch = __hip_atomic_load((GAS u32*)(ws + WS_EP), __ATOMIC_RELAXED, __HIP_MEMORY_SCOPE_AGENT);
    FENCE();
    const int xoff = (256 * w + b) * 1024 + m * 32 + g * 8;
    u2 xl[2];
    float c16[4], s16[4];
    f2 twc[2], tws[2];
    u4 Gre3, Gim3, SGre, SGim, GA[2];
    auto consts0 = [&]() {
#pragma unroll
        for (int u = 0; u < 4; ++u) {
            float a = (4.0f * gf + u) * (mf * (1.0f / 16));
            c16[u] = cosr(a); s16[u] = sinr(a);
            twc[u >> 1][u & 1] = cosr(a * (1.0f / 32)); tws[u >> 1][u & 1] = sinr(a * (1.0f / 32));
        }
    };
    auto consts3 = [&]() {
#pragma unroll
        for (int u = 0; u < 4; ++u) {
            const float k = 4.0f * gf + u;
            const bool k0 = u == 0 && g == 0;
            float cc = c16[u], ss = s16[u];
            Gre3[u] = pk_b(cc, -ss); Gim3[u] = pk_b(ss, cc);
            float a2 = (2.0f * k + 1.0f) * (mf * (1.0f / 32));
            u32 sp = pk_b(g < 2 ? 2.f * cc : 2.f * sg * cosr(a2), g < 2 ? -2.f * ss : -2.f * sg * sinr(a2));
            SGre[u] = k0 ? pk_b(1.0f, sg) : (g < 2 ? sp : 0u);
            SGim[u] = g < 2 ? 0u : sp;
            float a4 = k * (mf * (1.0f / 32)), c4 = 2.f * cosr(a4), s4_ = -2.f * sinr(a4);
            GA[0][u] = k0 ? pk_b(1.0f, sg) : pk_b(c4, s4_);
            GA[1][u] = k0 ? pk_b(1.0f, sg) : ((u & 1) ? pk_b(-c4, -s4_) : pk_b(c4, s4_));
        }
        PIN(Gre3); PIN(Gim3); PIN(SGre); PIN(SGim); PIN(GA[0]); PIN(GA[1]); PIN(twc[0]); PIN(twc[1]); PIN(tws[0]); PIN(tws[1]);
    };
    const int pw = (w + 4 * th) & 7;
    GAS u32* fl = (GAS u32*)(ws + WS_FLAG);
    int ko[7];
    {
        const int r6 = (fb < 6 ? fb : fb - 5) + 3 * (grp & 1);
#pragma unroll
        for (int ks = 0; ks < 6; ++ks) { int k = ks + r6; k = k >= 6 ? k - 6 : k; ko[ks] = k >= 6 ? k - 6 : k; }
        ko[6] = 6;
    }
    u4 W[7][4];
    const u32 w2vo = (l >> 3) * 1024 + (((l & 7) ^ (((l >> 4) & 1) << 2)) * 16);
    auto w2_glds = [&](int p_, int ks, u32 dst) {
        const int q = ko[ks];
        const char* src = (const char*)w2 + (224 * p_ + (ks == 6 ? 96 : (q < 3 ? 32 * q : 16 + 32 * q))) * 1024 + fb * 128;
        glds16(src, w2vo, dst); glds16(src + 8192, w2vo, dst + 1024);
        glds16(src + (ks == 6 ? 112 : 16) * 1024, w2vo, dst + 2048); glds16(src + (ks == 6 ? 120 : 24) * 1024, w2vo, dst + 3072);
    };
    LAS char* w2rd = (LAS char*)smem + (4 * g + (m >> 2)) * 128 + (((m & 3) ^ (((m >> 3) & 1) << 2)) * 16);
    auto w2_read = [&](u4 (&f)[4], int off) {
#pragma unroll
        for (int t = 0; t < 4; ++t) {
            LAS char* p = (LAS char*)((u32)(uintptr_t)w2rd ^ ((t >> 1) * 64)) + off + (t & 1) * 8;
            u2 x0 = __builtin_bit_cast(u2, __builtin_amdgcn_ds_read_tr16_b64_v4i16((LAS s4*)p));
            u2 x1 = __builtin_bit_cast(u2, __builtin_amdgcn_ds_read_tr16_b64_v4i16((LAS s4*)(p + 2048)));
            f[t] = (u4){x0[0], x0[1], x1[0], x1[1]};
        }
    };
    auto bg = [&](int i0, int i1) { w1_burst(i0, i1 - i0); };
    u32 tag;
    if (__builtin_expect(aw, 1)) {
        u32 xd[8];
        const u32* xr = (const u32*)(x + (256 * (2 * w + ((m >> 1) & 1)) + b) * 512) + 8 * g + 2 * (m >> 2) + (m & 1);
#pragma unroll
        for (int j = 0; j < 8; ++j) xd[j] = xr[32 * j];
        if (tid < 16) hw[tid] = 0;
        FENCE();
        ST(2);
        {
            u4 FAre, FAim, Fre, Fim, Sre, Sim; u2 Mb; f2 pc[2], ps[2];
            consts0();
            float c0[8], s0[8];
#pragma unroll
            for (int j = 0; j < 8; ++j) {
                float a = (gf + 4.0f * j) * (mf * (1.0f / 32));
                c0[j] = cosr(a); s0[j] = m ? -sinr(a) : ((g & 1) ? -1.0f : 1.0f);
            }
            const float mo = (float)(2 * (m & 7) + 1) * (1.0f / 32);
#pragma unroll
            for (int u = 0; u < 4; ++u) {
                FAre[u] = pk_h(c0[2 * u], c0[2 * u + 1]); FAim[u] = pk_h(s0[2 * u], s0[2 * u + 1]);
                float cc = c16[u], ss = s16[u];
                Fre[u] = pk_h(cc, ss); Fim[u] = pk_h(-ss, cc);
                float a2 = (4.0f * gf + u) * mo;
                u32 re = pk_h(m < 8 ? cc : cosr(a2), 0.f), im = pk_h(m < 8 ? (m ? -ss : ((u & 1) ? -1.0f : 1.0f)) : -sinr(a2), 0.f);
                Sre[u] = m < 8 ? re : re << 16; Sim[u] = m < 8 ? im : im << 16;
                pc[u & 1][u >> 1] = twc[u >> 1][u & 1]; ps[u & 1][u >> 1] = tws[u >> 1][u & 1];
            }
#pragma unroll
            for (int e = 0; e < 2; ++e) {
                float a = (float)(((m >> 1) * (256 * (2 * g + e) + b)) & 2047) * (1.0f / 2048);
                float cc = cosr(a), ss = sinr(a);
                Mb[e] = (m & 1) ? pk_h(-ss, cc) : pk_h(cc, ss);
            }
            PIN(FAre); PIN(FAim); PIN(Fre); PIN(Fim); PIN(Sre); PIN(Sim); PIN(Mb); PIN(pc[0]); PIN(pc[1]); PIN(ps[0]); PIN(ps[1]);
            ST(3);
            f2 v[8];
#pragma unroll
            for (int j = 0; j < 8; ++j) v[j] = bf_2(xd[j]);
            f2 s2 = ((v[0] + v[1]) + (v[2] + v[3])) + ((v[4] + v[5]) + (v[6] + v[7]));
            const float mean = red_lanes<false>(s2[0] + s2[1]) * (1.0f / 512);
            f2 q0 = {0.f, 0.f}, q1 = {0.f, 0.f};
#pragma unroll
            for (int j = 0; j < 8; ++j) { v[j] -= mean; if (j & 1) q1 = fma2(v[j], v[j], q1); else q0 = fma2(v[j], v[j], q0); }
            q0 += q1;
            const float inv = ln_inv(red_lanes<false>(q0[0] + q0[1]) * (1.0f / 512));
            const float off = (mean - rbf(mean)) * inv;
#pragma unroll
            for (int j = 0; j < 8; ++j) v[j] = fma2(v[j], (f2){inv, inv}, (f2){off, off});
            u4 B[2];
#pragma unroll
            for (int p = 0; p < 2; ++p) {
                u4 A = {pk_h(v[0][p], v[1][p]), pk_h(v[2][p], v[3][p]), pk_h(v[4][p], v[5][p]), pk_h(v[6][p], v[7][p])};
                f4 Dre = mma_h(A, FAre, Z4), Dim = mma_h(A, FAim, Z4);
#pragma unroll
                for (int r = 0; r < 2; ++r) {
                    f2 re = r ? hi2(Dre) : lo2(Dre), im = r ? hi2(Dim) : lo2(Dim);
                    f2 tr = fma2(im, ps[p], re * pc[p]), ti = fma2(-re, ps[p], im * pc[p]);
                    B[r][p] = pk_h(tr[0], ti[0]); B[r][2 + p] = pk_h(tr[1], ti[1]);
                }
            }
#pragma unroll
            for (int r = 0; r < 2; ++r) {
                f4 Gre = mma_h(Fre, B[r], Z4), Gim = mma_h(Fim, B[r], Z4), Hre = mma_h(Sre, B[r], Z4), Him = mma_h(Sim, B[r], Z4);
                u4 Z;
#pragma unroll
                for (int i = 0; i < 4; ++i) Z[i] = pk_h(m ? Gre[i] : Hre[i], m ? Gim[i] : Him[i]);
                *(u4*)(scr + (2 * w + r) * 1024 + m * 64 + g * 16) = Z;
            }
            BARRIER();
            ST(18);
            u4 r0 = *(const u4*)(scr + (2 * g) * 1024 + w * 256 + m * 16);
            u4 r1 = *(const u4*)(scr + (2 * g + 1) * 1024 + w * 256 + m * 16);
#pragma unroll
            for (int t = 0; t < 4; ++t) {
                f4 D = mma16_h(Mb, (u2){r0[t], r1[t]}, Z4);
                *(u2*)(scr + SCR_S + w * 2048 + m * 128 + (((2 * t + (g >> 1)) ^ (m >> 1)) * 16) + (g & 1) * 8) =
                    (u2){clean(pk_h(D[0], D[1])), clean(pk_h(D[2], D[3]))};
            }
        }
        BARRIER();
        ST(4);
        u4 Fre, Fim, Gre, Gim, N0, N1; f2 tc[2], ts[2];
#pragma unroll
        for (int u = 0; u < 4; ++u) {
            float cc = c16[u], ss = s16[u];
            Fre[u] = pk_h(0.25f * cc, 0.25f * ss); Fim[u] = pk_h(-0.25f * ss, 0.25f * cc);
            Gre[u] = pk_b(cc * (1.0f / 256), -ss * (1.0f / 256)); Gim[u] = pk_b(ss * (1.0f / 256), cc * (1.0f / 256));
            float a3 = (4.0f * gf + u) * (mf * (1.0f / 256));
            tc[u >> 1][u & 1] = cosr(a3); ts[u >> 1][u & 1] = sinr(a3);
        }
        PIN(Fre); PIN(Fim); PIN(Gre); PIN(Gim); PIN(tc[0]); PIN(tc[1]); PIN(ts[0]); PIN(ts[1]);
        xl[0] = *(const u2*)((const char*)x + xoff); xl[1] = *(const u2*)((const char*)x + xoff + 512);
        FENCE();
        tag = __builtin_amdgcn_readfirstlane(epoch) + 1u;
        consts3();
#pragma unroll
        for (int u = 0; u < 4; ++u) {
            float a1 = (float)(((4 * (g & 1) + u) * (256 * (m >> 1) + b)) & 2047) * (1.0f / 2048);
            float c1 = cosr(a1), s1 = sinr(a1);
            u32 nb = (m & 1) ? pk_b(s1, c1) : pk_b(c1, -s1);
            N0[u] = g < 2 ? nb : 0u; N1[u] = g < 2 ? 0u : nb;
        }
        PIN(N0); PIN(N1);
        ST(19);
        BARRIER();
        ST(5);
        u4 d[2], f[2], e[2];
        const int uo = (tid >> 1) * 8192 + (tid & 1) * 16;
        d[0] = bld4<SC1>(rw, uo, WS_U + b * 32); d[1] = bld4<SC1>(rw, uo, WS_U + 128 * 8192 + b * 32);
        const u32 fd0 = __builtin_amdgcn_raw_buffer_load_b32(rw, uo, WS_WT + b * 32, SC1);
        const u32 fd1 = __builtin_amdgcn_raw_buffer_load_b32(rw, uo, WS_WT + 128 * 8192 + b * 32, SC1);
        FENCE();
        poll2(d[0], d[1], rw, uo, WS_U + b * 32, WS_U + 128 * 8192 + b * 32);
        char* sp = scr + SCR_S + (tid ^ ((tid >> 4) & 1)) * 16;
        *(u4*)sp = d[0]; *(u4*)(sp + 4096) = d[1];
        BARRIER();
        f[0] = bld4<SC1>(rw, uo, WS_WT + b * 32); f[1] = bld4<SC1>(rw, uo, WS_WT + 128 * 8192 + b * 32);
        if (b == 0) {
            f[0] = bld4<SC1>(rw, tid * 16, WS_WA); f[1] = bld4<SC1>(rw, tid * 16 + 4096, WS_WA);
            e[0] = bld4<SC1>(rw, tid * 16, WS_WB); e[1] = bld4<SC1>(rw, tid * 16 + 4096, WS_WB);
            if (tid == 0) __hip_atomic_store((GAS u32*)(ws + WS_EP), tag, __ATOMIC_RELAXED, __HIP_MEMORY_SCOPE_AGENT);
        }
        ST(6);
        u2 ld[4], wl[4];
        const int so = (64 * g + m) * 32 + (((w >> 1) ^ (m >> 3)) * 16) + (w & 1) * 8;
#pragma unroll
        for (int q = 0; q < 4; ++q) ld[q] = *(const u2*)(scr + SCR_S + so + 512 * q);
        FENCE();
        f4 Xre[2], Xim[2];
#pragma unroll
        for (int e = 0; e < 2; ++e) {
            u4 A = {ld[0][e], ld[1][e], ld[2][e], ld[3][e]};
            f4 Dre = mma_h(A, Fre, Z4), Dim = mma_h(A, Fim, Z4);
            f2 r0 = fma2(lo2(Dim), ts[0], lo2(Dre) * tc[0]), i0 = fma2(-lo2(Dre), ts[0], lo2(Dim) * tc[0]);
            f2 r1 = fma2(hi2(Dim), ts[1], hi2(Dre) * tc[1]), i1 = fma2(-hi2(Dre), ts[1], hi2(Dim) * tc[1]);
            u4 B = {pk_h(r0[0], i0[0]), pk_h(r0[1], i0[1]), pk_h(r1[0], i1[0]), pk_h(r1[1], i1[1])};
            Xre[e] = mma_h(Fre, B, Z4); Xim[e] = mma_h(Fim, B, Z4);
        }
        *(u4*)(sp + 8192) = f[0]; *(u4*)(sp + 12288) = f[1];
        asm volatile("" :: "v"(fd0), "v"(fd1));
        f2* sx = (f2*)(scr + SCR_X);
        if (b == 0) {
            *(u4*)(sp - SCR_S) = e[0]; *(u4*)(sp - SCR_S + 4096) = e[1];
#pragma unroll
            for (int e = 0; e < 2; ++e)
#pragma unroll
                for (int i = 0; i < 4; ++i) sx[2 * w + e + 8 * (m + 16 * (4 * g + i))] = (f2){Xre[e][i], Xim[e][i]};
        }
        BARRIER();
        ST(20);
#pragma unroll
        for (int q = 0; q < 4; ++q) wl[q] = *(const u2*)(scr + SCR_S + so + 8192 + 512 * q);
        FENCE();
        f2 yr[2][2], yi[2][2];
#pragma unroll
        for (int e = 0; e < 2; ++e)
#pragma unroll
            for (int hh = 0; hh < 2; ++hh) {
                f2 wre = {bf_lo(wl[2 * hh][e]), bf_lo(wl[2 * hh + 1][e])}, wim = {bf_hi(wl[2 * hh][e]), bf_hi(wl[2 * hh + 1][e])};
                f2 xr = hh ? hi2(Xre[e]) : lo2(Xre[e]), xi = hh ? hi2(Xim[e]) : lo2(Xim[e]);
                yr[e][hh] = fma2(-xi, wim, xr * wre); yi[e][hh] = fma2(xr, wim, xi * wre);
            }
        if (b == 0) {
#pragma unroll
            for (int i = 0; i < 4; ++i) {
                u2 wb = *(const u2*)(scr + so + 512 * i);
#pragma unroll
                for (int e = 0; e < 2; ++e) {
                    f2 xm = sx[(2048 - (2 * w + e + 8 * (m + 16 * (4 * g + i)))) & 2047];
                    float br = bf_lo(wb[e]), bi = bf_hi(wb[e]);
                    yr[e][i >> 1][i & 1] += xm[0] * br + xm[1] * bi;
                    yi[e][i >> 1][i & 1] += xm[0] * bi - xm[1] * br;
                }
            }
        }
        char* pv = scr + SCR_S;
#pragma unroll
        for (int e = 0; e < 2; ++e) {
            u4 Y = {pk_b(yr[e][0][0], yi[e][0][0]), pk_b(yr[e][0][1], yi[e][0][1]), pk_b(yr[e][1][0], yi[e][1][0]), pk_b(yr[e][1][1], yi[e][1][1])};
            f4 Dre = mma_b(Y, Gre, Z4), Dim = mma_b(Y, Gim, Z4);
            f2 r0 = fma2(-lo2(Dim), ts[0], lo2(Dre) * tc[0]), i0 = fma2(lo2(Dre), ts[0], lo2(Dim) * tc[0]);
            f2 r1 = fma2(-hi2(Dim), ts[1], hi2(Dre) * tc[1]), i1 = fma2(hi2(Dre), ts[1], hi2(Dim) * tc[1]);
            u4 A = {pk_b(r0[0], i0[0]), pk_b(r0[1], i0[1]), pk_b(r1[0], i1[0]), pk_b(r1[1], i1[1])};
            f4 Ere = mma_b(A, Gre, Z4), Eim = mma_b(A, Gim, Z4);
            u4 o;
#pragma unroll
            for (int i = 0; i < 4; ++i) o[i] = clean(pk_b(Ere[i], Eim[i]));
            *(u4*)(pv + (2 * w + e) * 1024 + ((m * 4 + g) ^ ((w >> 1) * 4)) * 16) = o;
        }
        BARRIER();
        ST(7);
#pragma unroll
        for (int it = 0; it < 2; ++it) {
            const int ch = tid + 256 * it;
            const u32* q = (const u32*)(pv + (ch & 1) * 4096 + (((ch >> 1) * 4) ^ ((ch & 1) * 64)));
            bst4<SC1>((u4){q[0], q[256], q[512], q[768]}, rw, ch * 16, WS_V + b * 8192);
        }
        WAITV(0);
        BARRIER();
        ST(8);
        const int vo = (2 * m + (g >> 1)) * 8192 + (g & 1) * 16, vs = WS_V + w * 64 * 8192 + b * 32;
        u4 v0 = bld4<SC1>(rw, vo, vs), v1 = bld4<SC1>(rw, vo, vs + 32 * 8192);
        poll2(v0, v1, rw, vo, vs, vs + 32 * 8192);
        ST(9);
        char* sz = scr + SCR_X + g * 2048 + w * 256 + m * 8;
#pragma unroll
        for (int hh = 0; hh < 2; ++hh) {
            f4 D0 = mma_b(N0, hh ? v1 : v0, Z4), D1 = mma_b(N1, hh ? v1 : v0, Z4);
            *(u2*)(sz + hh * 128) = (u2){pk_b(D0[0], D0[1]), pk_b(D1[0], D1[1])};
            *(u2*)(sz + 1024 + hh * 128) = (u2){pk_b(D0[2], D0[3]), pk_b(D1[2], D1[3])};
        }
    } else {
        auto announce = [&](int which) {
            if (l < 8) __hip_atomic_store((GAS u32*)(ws + WS_FA) + which * 2048 + l * 256 + b, tag, __ATOMIC_RELAXED, __HIP_MEMORY_SCOPE_AGENT);
            FENCE();
        };
        auto all_announced = [&](int which) {
#pragma nounroll
            for (int sp = 0; sp < SPIN_MAX; ++sp) {
                FENCE();
                u4 f = bld4<SC1>(rw, l * 16, WS_FA + which * 8192 + (b >> 5) * 1024);
                if (__builtin_amdgcn_ballot_w64(f[0] != tag || f[1] != tag || f[2] != tag || f[3] != tag) == 0) break;
                __builtin_amdgcn_s_sleep(4);
            }
        };
        const rsrc_t rr = mkrs(wr, 1052672), ri = mkrs(wi, 1052672);
        const int j7 = l >> 3, p4 = (l >> 1) & 3, ee = l & 1;
        u2 fr[4], fi[4]; u32 sr_, si_;
        if (w == 4) {
            const int e = ee * 4 * 514 + (32 * j7 + 4 * p4) * 2;
#pragma unroll
            for (int i = 0; i < 4; ++i) { fr[i] = bld2<0>(rr, e + i * 514, 8 * b * 514); fi[i] = bld2<0>(ri, e + i * 514, 8 * b * 514); }
        } else if (w == 5) {
            const int e = (2045 - 8 * b) * 514 + 253 * 2 - ee * 4 * 514 - (32 * j7 + 4 * p4) * 2;
#pragma unroll
            for (int i = 1; i < 4; ++i) { fr[i] = bld2<0>(rr, e + (3 - i) * 514, 0); fi[i] = bld2<0>(ri, e + (3 - i) * 514, 0); }
            const int e0 = e + 3 * 514 - (b == 0 && !ee ? 2048 * 514 : 0);
            fr[0] = bld2<0>(rr, e0, 0); fi[0] = bld2<0>(ri, e0, 0);
            const int e1 = (8 * b + (l & 7)) * 514 + (16 + 32 * j7) * 2;
            sr_ = __builtin_amdgcn_raw_buffer_load_b16(rr, e1, 0, 0); si_ = __builtin_amdgcn_raw_buffer_load_b16(ri, e1, 0, 0);
        } else if (w == 6) {
            const int c = j7 & 3, f = 8 * b + (l & 7);
            const int e = ((c & 1) ? (2048 - f) & 2047 : f) * 514 + (c >> 1) * 512;
            sr_ = __builtin_amdgcn_raw_buffer_load_b16(rr, e, 0, 0); si_ = __builtin_amdgcn_raw_buffer_load_b16(ri, e, 0, 0);
        }
        FENCE();
        ST(2);
#pragma unroll
        for (int jj = 0; jj < 7; ++jj) {
            const int cj_ = (4 * jj) % 14, rj = 18 * jj + (4 * jj) / 14;
            w1o[jj] = (row0 + rj) * 3584 + (cch0 + cj_) * 16 + (cch0 + cj_ >= 14 ? 3584 - 224 : 0);
            PIN(w1o[jj]);
        }
        bst4<SC1>(SENT4, rw, l * 16, WS_U + b * 8192 + (w - 4) * 2048); bst4<SC1>(SENT4, rw, l * 16 + 1024, WS_U + b * 8192 + (w - 4) * 2048);
        tag = __builtin_amdgcn_readfirstlane(epoch) + 1u;
        auto count_in = [&](int word, u32 last, int which) {
            u32 prev = 0;
            if (l == 0) prev = __hip_atomic_fetch_add(hw + word, 1u, __ATOMIC_RELAXED, __HIP_MEMORY_SCOPE_WORKGROUP);
            if (__builtin_amdgcn_readfirstlane(prev) == last) announce(which);
        };
        WAITV(0);
        if (l < 2) __hip_atomic_store((GAS u32*)(ws + WS_FA) + (2 * (w - 4) + l) * 256 + b, tag, __ATOMIC_RELAXED, __HIP_MEMORY_SCOPE_AGENT);
        FENCE();
        if (!W_EARLY) BARRIER();
        if (w < 6) {
            const int wo = p4 * 2048 + j7 * 32 + ee * 16, wso = WS_WT + b * 8192 + (w - 4) * 256;
#pragma unroll
            for (int c = 0; c < 4; ++c) {
                u4 o;
#pragma unroll
                for (int i = 0; i < 4; ++i)
                    o[i] = w == 4 ? __builtin_amdgcn_perm(fi[i][c >> 1], fr[i][c >> 1], (c & 1) ? 0x07060302u : 0x05040100u)
                                  : __builtin_amdgcn_perm(fi[i][1 - (c >> 1)], fr[i][1 - (c >> 1)], (c & 1) ? 0x05040100u : 0x07060302u) ^ 0x80000000u;
                bst4<SC1>(o, rw, wo + c * 512, wso);
            }
            if (w == 5) __builtin_amdgcn_raw_buffer_store_b32(sr_ | (si_ << 16), rw, l * 4, wso, SC1);
        } else if (w == 6) {
            float re = bf_lo(sr_), im = bf_lo(si_);
            re += dpp<0x128>(re); im -= dpp<0x128>(im);
            u32 a = __builtin_bit_cast(u32, re), a2 = a, c = __builtin_bit_cast(u32, im), c2 = c;
            asm("" : "+v"(a2), "+v"(c2));
            auto pr = __builtin_amdgcn_permlane16_swap(a, a2, false, false);
            auto pi = __builtin_amdgcn_permlane16_swap(c, c2, false, false);
            const float ar = __builtin_bit_cast(float, (u32)pr[0]), br = __builtin_bit_cast(float, (u32)pr[1]);
            const float ai = __builtin_bit_cast(float, (u32)pi[0]), bi = __builtin_bit_cast(float, (u32)pi[1]);
            if (l < 8) {
                __builtin_amdgcn_raw_buffer_store_b32(pk_b(0.25f * (ar + br), 0.25f * (ai + bi)), rw, l * 4, WS_WA + b * 32, SC1);
                __builtin_amdgcn_raw_buffer_store_b32(pk_b(0.25f * (ar - br), 0.25f * (ai - bi)), rw, l * 4, WS_WB + b * 32, SC1);
            }
        }
        ST(18);
        if (W_EARLY) BARRIER();
        BARRIER();
        {
            const char* up = scr + SCR_S + (w - 4) * 2048;
            bst4<SC1>(*(const u4*)(up + (l ^ ((l >> 4) & 7)) * 16), rw, l * 16, WS_U + b * 8192 + (w - 4) * 2048);
            bst4<SC1>(*(const u4*)(up + 1024 + (l ^ (((l >> 4) + 4) & 7)) * 16), rw, l * 16 + 1024, WS_U + b * 8192 + (w - 4) * 2048);
        }
        if (w < 7) { WAITV(2); count_in(13, 2u, 2); }
        else all_announced(0);
        WAITV(0);
        ST(19);
        xl[0] = *(const u2*)((const char*)x + xoff); xl[1] = *(const u2*)((const char*)x + xoff + 512);
        FENCE();
        bg(0, BG2);
        ST(4);
        BARRIER();
        __builtin_amdgcn_s_sleep(2);
#pragma unroll
        for (int i = 0; i < 2; ++i) bst4<SC1>(SENT4, rw, l * 16 + i * 1024, WS_V + b * 8192 + (w - 4) * 2048);
        FENCE();
        consts0();
        consts3();
        WAITV(0);
        if (w == 7) all_announced(2);
        ST(5);
        BARRIER();
        if (w == 7) announce(1);
#pragma unroll
        for (int i = 0; i < 2; ++i) bst4<SC1>(SENT4, rw, (l >> 3) * 256 + (l & 7) * 16 + i * 2048, WS_LN2 + (b >> 1) * 16384 + (b & 1) * 128 + (w - 4) * 4096);
        bg(BG2, BG4);
        BARRIER();
        bg(BG4, BG5);
        WAITV(BG5 - BG2);
        ST(6);
        BARRIER();
        if (w == 7) all_announced(1);
        else __builtin_amdgcn_s_sleep(3);
        bg(BG5, BG6);
        ST(7);
        BARRIER();
    }
    BARRIER();
    {
        u4 A = *(const u4*)(scr + SCR_X + w * 1024 + m * 64 + g * 16);
        f4 Dre = mma_b(A, Gre3, Z4), Dim = mma_b(A, Gim3, Z4), Hre = mma_b(A, SGre, Z4), Him = mma_b(A, SGim, Z4);
        if (g == 0) { Dre[0] = Hre[0]; Dim[0] = Him[0]; }
        f2 r0 = fma2(-lo2(Dim), tws[0], lo2(Dre) * twc[0]), i0 = fma2(lo2(Dre), tws[0], lo2(Dim) * twc[0]);
        f2 r1 = fma2(-hi2(Dim), tws[1], hi2(Dre) * twc[1]), i1 = fma2(hi2(Dre), tws[1], hi2(Dim) * twc[1]);
        u4 A2 = {pk_b(r0[0], i0[0]), pk_b(r0[1], i0[1]), pk_b(r1[0], i1[0]), pk_b(r1[1], i1[1])};
        f2 h[4]; u2 hp[2];
#pragma unroll
        for (int t = 0; t < 2; ++t) {
            f4 Yv = mma_b(A2, GA[t], Z4);
            u32 h0 = pk_b2(bf_2(xl[t][0]) + bf_2(pk_b(Yv[0], Yv[1]))), h1 = pk_b2(bf_2(xl[t][1]) + bf_2(pk_b(Yv[2], Yv[3])));
            hp[t] = (u2){h0, h1};
            h[2 * t] = bf_2(h0); h[2 * t + 1] = bf_2(h1);
        }
        f2 s2 = (h[0] + h[1]) + (h[2] + h[3]);
        const float mean = red_lanes<true>(s2[0] + s2[1]) * (1.0f / 512);
        f2 q2 = {0.f, 0.f};
#pragma unroll
        for (int j = 0; j < 4; ++j) { f2 d = h[j] - mean; q2 = fma2(d, d, q2); }
        const float inv = ln_inv(red_lanes<true>(q2[0] + q2[1]) * (1.0f / 512));
        const float mb = rbf(mean);
#pragma unroll
        for (int t = 0; t < 2; ++t)
            *(u2*)(scr + ((8 * t + (m >> 1)) * 4 + g) * 128 + (w ^ (m >> 1)) * 16 + (m & 1) * 8) =
                (u2){clean(pk_b2(bf_2(pk_b2(h[2 * t] - mb)) * inv)), clean(pk_b2(bf_2(pk_b2(h[2 * t + 1] - mb)) * inv))};
        WAITV(0);
        BARRIER();
        u4 lo = *(const u4*)(scr + (tid ^ ((tid >> 5) & 7)) * 16);
        bst4<SC1>(lo, rw, (tid >> 3) * 256 + (tid & 7) * 16, WS_LN2 + (b >> 1) * 16384 + (b & 1) * 128);
        FENCE();
        if (aw) WAITV(0); else __builtin_amdgcn_s_sleep(8);
        bst2<SC1>(hp[0], ro, xoff, 0); bst2<SC1>(hp[1], ro, xoff + 512, 0);
        FENCE();
        ST(10);
    }
    auto stage_out = [&](int r) {
        w2_read(W[r], LDS_SCR + (w & 3) * 8192 + (r & 1) * 4096);
        asm volatile("s_waitcnt lgkmcnt(0)" ::: "memory");
        PIN(W[r][0]); PIN(W[r][1]); PIN(W[r][2]); PIN(W[r][3]);
        if (r + 2 < 7) w2_glds(pw, r + 2, lds0 + LDS_SCR + (w & 3) * 8192 + (r & 1) * 4096);
    };
    auto publish = [&]() {
        u32 prev = 0;
        if (l == 0) prev = __hip_atomic_fetch_add(hw + 4, 1u, __ATOMIC_RELAXED, __HIP_MEMORY_SCOPE_WORKGROUP);
        if (__builtin_amdgcn_readfirstlane(prev) == 7u && l < 2)
            __hip_atomic_store(fl + (l ? 4096 + (b >> 1) * 32 + (b & 1) : (mt1 * 8 + (nt1 >> 1)) * 32 + (nt1 & 1)), tag, __ATOMIC_RELAXED, __HIP_MEMORY_SCOPE_AGENT);
        FENCE();
    };
    {
        const int tg = w & 3;
        char* xch = smem + XCH + tg * 3072;
        const int hoff = WS_H1 + (mt1 * 8 + 2 * tg) * (56 * 1024) + (nt1 >> 1) * 7168;
        if (aw) {
            const int toff = WS_LN2 + (mt1 * 8 + 2 * tg) * 16384;
            LAS char* wb0 = (LAS char*)smem + (4 * g + (m >> 2)) * 224 + (m & 3) * 8;
            LAS char* wb[4];
            int tq[4];
#pragma unroll
            for (int q = 0; q < 4; ++q) { const int qq = (q + rq) & 3; wb[q] = wb0 + qq * 28672; tq[q] = toff + qq * 4096; }
            u4 T[2][16], A[2][7];
            auto ldt = [&](int kk) {
#pragma unroll
                for (int tt = 0; tt < 2; ++tt) T[tt][kk] = bld4<SC1>(rw, l * 16 + (kk & 3) * 1024, tq[kk >> 2] + tt * 16384);
            };
#pragma unroll
            for (int i = 0; i < 7; ++i) A[0][i] = ldw1(wb[0], 0, i);
            if (l == 0) __hip_atomic_fetch_add(hw + 5, 1u, __ATOMIC_RELAXED, __HIP_MEMORY_SCOPE_WORKGROUP);
            ST(11);
#pragma unroll
            for (int kk = 0; kk < G1_DEPTH; ++kk) { ldt(kk); SCHED(); }
            f4 acc[2][7];
#pragma unroll
            for (int tt = 0; tt < 2; ++tt)
#pragma unroll
                for (int i = 0; i < 7; ++i) acc[tt][i] = Z4;
#pragma unroll
            for (int kk = 0; kk < 16; ++kk) {
                if ((kk & 1) == 0) {
                    if (__builtin_expect(any_sent(umax(umax(max4(T[0][kk]), max4(T[1][kk])), umax(max4(T[0][kk + 1]), max4(T[1][kk + 1])))), 0)) {
                        wait_tok(T[0][kk], T[1][kk], rw, l * 16, tq[kk >> 2] + (kk & 3) * 1024);
                        wait_tok(T[0][kk + 1], T[1][kk + 1], rw, l * 16, tq[kk >> 2] + ((kk + 1) & 3) * 1024);
                    }
                    if (kk == 0) ST(12);
                    if (kk == (BG6 / 7) * 4 - 2) lds_wait(hw + 5, 8u);
                }
                if (kk + G1_DEPTH < 16) ldt(kk + G1_DEPTH);
                if (kk < 15) {
#pragma unroll
                    for (int i = 0; i < 7; ++i) A[(kk + 1) & 1][i] = ldw1(wb[(kk + 1) >> 2], (kk + 1) & 3, i);
                }
#pragma unroll
                for (int i = 0; i < 7; ++i)
#pragma unroll
                    for (int tt = 0; tt < 2; ++tt) acc[tt][i] = mma_b(A[kk & 1][i], T[tt][kk], acc[tt][i]);
#pragma unroll
                for (int i = 0; i < 14; ++i) {
                    __builtin_amdgcn_sched_group_barrier(0x8, 1, 0);
                    if (i == 1 || i == 7) __builtin_amdgcn_sched_group_barrier(0x20, 1, 0);
                    __builtin_amdgcn_sched_group_barrier(0x100, 1, 0);
                    if ((kk & 1) && i >= 3) __builtin_amdgcn_sched_group_barrier(0x2, 1, 0);
                }
                if (kk & 1) {} else SCHED();
            }
#pragma unroll
            for (int q = 0; q < 3; ++q) {
                u2 p0 = pk4(acc[1][2 * q]), p1 = pk4(acc[1][2 * q + 1]);
                *(u4*)(xch + q * 1024 + l * 16) = (u4){p0[0], p0[1], p1[0], p1[1]};
            }
            lds_set(hw + tg, 1u);
            ST(13);
#pragma unroll
            for (int q = 0; q < 3; ++q) {
                u2 p0 = pk4(acc[0][2 * q]), p1 = pk4(acc[0][2 * q + 1]);
                bst4<SC1>(silu_pk4((u4){p0[0], p0[1], p1[0], p1[1]}), rw, l * 16, hoff + ((nt1 & 1) * 3 + q) * 1024);
            }
#pragma unroll
            for (int tt = 0; tt < 2; ++tt) { u2 p = pk4(acc[tt][6]); bst2<SC1>(silu_pk2(p[0], p[1]), rw, l * 8, hoff + tt * (56 * 1024) + 6144 + (nt1 & 1) * 512); }
        } else {
            bg(BG6, 28);
            WAITV(0);
            if (l == 0) __hip_atomic_fetch_add(hw + 5, 1u, __ATOMIC_RELAXED, __HIP_MEMORY_SCOPE_WORKGROUP);
            __builtin_amdgcn_s_sleep(8);
            lds_wait(hw + 5, 8u);
            w2_glds(pw, 0, lds0 + LDS_SCR + tg * 8192);
            w2_glds(pw, 1, lds0 + LDS_SCR + tg * 8192 + 4096);
#pragma unroll
            for (int r = 0; r < B_R; ++r) { WAITV(4); stage_out(r); }
            ST(12);
            lds_wait(hw + tg, 1u);
            ST(13);
#pragma unroll
            for (int q = 0; q < 3; ++q)
                bst4<SC1>(silu_pk4(*(const u4*)(xch + q * 1024 + l * 16)), rw, l * 16, hoff + 56 * 1024 + ((nt1 & 1) * 3 + q) * 1024);
        }
        ST(14);
        WAITV(0);
        publish();
        ST(21);
        if (aw) {
            lds_wait(hw + 4, 8u);
#pragma unroll
            for (int ks = 0; ks < 7; ++ks) w2_glds(pw, ks, lds0 + tg * 28672 + ks * 4096);
        } else {
            stage_out(B_R);
#pragma unroll
            for (int r = B_R + 1; r < 7; ++r) { if (r + 1 < 7) WAITV(4); else WAITV(0); stage_out(r); }
        }
    }
    {
        ST(15);
        const int rot = w & 3;
        if (aw) {
            flag_wait(fl + ((l & 4) ? 4096 + (grp * 8 + th * 4 + rot) * 32 : (grp * 8 + (pw ^ ((l & 2) * 2))) * 32) + (l & 1), tag);
            lds_set(hw + 8 + rot, tag);
        } else {
#pragma nounroll
            for (int sp = 0; sp < SPIN_MAX; ++sp) {
                if (__builtin_amdgcn_readfirstlane(__hip_atomic_load(hw + 8 + rot, __ATOMIC_RELAXED, __HIP_MEMORY_SCOPE_WORKGROUP)) == tag) break;
                __builtin_amdgcn_s_sleep(1);
            }
            FENCE();
        }
        ST(16);
        const int toff = WS_H1 + (grp * 8 + th * 4) * (56 * 1024) + pw * 7168;
        int to[4];
#pragma unroll
        for (int tt = 0; tt < 4; ++tt) to[tt] = toff + ((tt + rot) & 3) * (56 * 1024);
        u4 T[7][4];
        auto ldtok = [&](int ks) {
#pragma unroll
            for (int tt = 0; tt < 4; ++tt) {
                if (ks < 6) T[ks][tt] = bld4<SC1>(rw, l * 16, to[tt] + ko[ks] * 1024);
                else {
                    u2 a = bld2<SC1>(rw, l * 8 + 2048, to[tt] + 4096), c = bld2<SC1>(rw, l * 8 + 2560, to[tt] + 4096);
                    T[ks][tt] = (u4){a[0], a[1], c[0], c[1]};
                }
            }
            SCHED();
        };
#pragma unroll
        for (int ks = 0; ks < G2_DEPTH; ++ks) ldtok(ks);
        if (aw) {
#pragma unroll
            for (int ks = 0; ks < 7; ++ks) w2_read(W[ks], rot * 28672 + ks * 4096);
            asm volatile("s_waitcnt lgkmcnt(0)" ::: "memory");
        }
        if (l == 0) __hip_atomic_fetch_add(hw + 7, 1u, __ATOMIC_RELAXED, __HIP_MEMORY_SCOPE_WORKGROUP);
        const int rho = grp * 128 + th * 64 + rot * 16 + m;
        const int ooff = (256 * (rho & 7) + (rho >> 3)) * 1024 + fb * 128 + (w >> 2) * 64 + g * 16;
        const u4 hres = bld4<SC1>(ro, ooff, 0);
        SCHED();
        f4 acc[4][4];
#pragma unroll
        for (int tt = 0; tt < 4; ++tt)
#pragma unroll
            for (int t = 0; t < 4; ++t) acc[tt][t] = Z4;
#pragma unroll
        for (int ks = 0; ks < 7; ++ks) {
            if (ks + G2_DEPTH < 7) { PIN(T[ks][0]); ldtok(ks + G2_DEPTH); }
#pragma unroll
            for (int tt = 0; tt < 4; ++tt)
#pragma unroll
                for (int t = 0; t < 4; ++t) {
                    acc[tt][t] = mma_b(W[ks][t], T[ks][tt], acc[tt][t]);
                }
            SCHED();
        }
        ST(17);
        lds_wait(hw + 7, 8u);
        char* dp = smem + w * 16384 + l * 16;
#pragma unroll
        for (int tt = 1; tt < 4; ++tt)
#pragma unroll
            for (int t = 0; t < 4; ++t) *(f4*)(dp + (((tt + rot) & 3) * 4 + t) * 1024) = acc[tt][t];
        f4 s0, s1;
        if (aw) { *(f4*)(dp + (rot * 4 + 2) * 1024) = acc[0][2]; *(f4*)(dp + (rot * 4 + 3) * 1024) = acc[0][3]; s0 = acc[0][0]; s1 = acc[0][1]; }
        else { *(f4*)(dp + (rot * 4) * 1024) = acc[0][0]; *(f4*)(dp + (rot * 4 + 1) * 1024) = acc[0][1]; s0 = acc[0][2]; s1 = acc[0][3]; }
        BARRIER();
        f4 r0[7], r1[7];
#pragma unroll
        for (int j = 1; j < 8; ++j) {
            const char* rp = smem + (((w + j) & 7) * 16 + rot * 4 + (w >> 2) * 2) * 1024 + l * 16;
            r0[j - 1] = *(const f4*)rp; r1[j - 1] = *(const f4*)(rp + 1024);
        }
        SCHED();
        s0 = ((s0 + r0[0]) + (r0[1] + r0[2])) + ((r0[3] + r0[4]) + (r0[5] + r0[6]));
        s1 = ((s1 + r1[0]) + (r1[1] + r1[2])) + ((r1[3] + r1[4]) + (r1[5] + r1[6]));
        u2 p0 = pk4(s0), p1 = pk4(s1);
        bst4<NT>((u4){pk_b2(bf_2(hres[0]) + bf_2(p0[0])), pk_b2(bf_2(hres[1]) + bf_2(p0[1])),
                           pk_b2(bf_2(hres[2]) + bf_2(p1[0])), pk_b2(bf_2(hres[3]) + bf_2(p1[1]))}, ro, ooff, 0);
    }
    ST_END(ws, 0);
}

extern "C" void kernel_launch(void* const* d_in, const int* in_sizes, int n_in, void* d_out, int out_size,
                              void* d_ws, size_t ws_size, hipStream_t stream) {
    (void)hipFuncSetAttribute((const void*)k_fused, hipFuncAttributeMaxDynamicSharedMemorySize, LDS_B);
    k_fused<<<256, 512, LDS_B, stream>>>((const uint16_t*)d_in[0], (const uint16_t*)d_in[1], (const uint16_t*)d_in[2],
                                         (const uint16_t*)d_in[4], (const uint16_t*)d_in[5], (uint16_t*)d_out, (char*)d_ws);
}
```

```cpp
#include <hip/hip_runtime.h>
#include <hip/hip_bf16.h>
#include <stdint.h>

typedef _Float16 h8 __attribute__((ext_vector_type(8)));
typedef _Float16 h4 __attribute__((ext_vector_type(4)));
typedef _Float16 h2 __attribute__((ext_vector_type(2)));
typedef __bf16 b8 __attribute__((ext_vector_type(8)));
typedef __bf16 b2 __attribute__((ext_vector_type(2)));
typedef short s4 __attribute__((ext_vector_type(4)));
typedef float f4 __attribute__((ext_vector_type(4)));
typedef float f2 __attribute__((ext_vector_type(2)));
typedef unsigned u32;
typedef u32 u4 __attribute__((ext_vector_type(4)));
typedef u32 u2 __attribute__((ext_vector_type(2)));
#define LAS __attribute__((address_space(3)))
#define GAS __attribute__((address_space(1)))
#define DI __device__ __forceinline__
#define FENCE() asm volatile("" ::: "memory")
#define PIN(x) asm volatile("" : "+v"(x))
#define SCHED() __builtin_amdgcn_sched_barrier(0)
#define WAITV(n) asm volatile("s_waitcnt vmcnt(%0)" :: "n"(n) : "memory")
#define BARRIER() do { asm volatile("s_waitcnt lgkmcnt(0)" ::: "memory"); __builtin_amdgcn_s_barrier(); FENCE(); } while (0)
constexpr int SC1 = 16, NT = 2;
constexpr u32 SENT = 0xffffffffu;
constexpr int SPIN_MAX = 1 << 20;

DI u32 pk_h(float a, float b) { f2 v = {a, b}; h2 r = __builtin_convertvector(v, h2); return __builtin_bit_cast(u32, r); }
DI u32 pk_b(float a, float b) { f2 v = {a, b}; b2 r = __builtin_convertvector(v, b2); return __builtin_bit_cast(u32, r); }
DI float bf_lo(u32 v) { return __builtin_bit_cast(float, v << 16); }
DI float bf_hi(u32 v) { return __builtin_bit_cast(float, v & 0xffff0000u); }
DI f2 bf_2(u32 v) { return (f2){bf_lo(v), bf_hi(v)}; }
DI u32 pk_b2(f2 v) { return pk_b(v[0], v[1]); }
DI float rbf(float a) { return bf_lo(pk_b(a, a)); }
DI float cosr(float rev) { return __builtin_amdgcn_cosf(rev); }
DI float sinr(float rev) { return __builtin_amdgcn_sinf(rev); }
DI f2 lo2(f4 v) { return __builtin_shufflevector(v, v, 0, 1); }
DI f2 hi2(f4 v) { return __builtin_shufflevector(v, v, 2, 3); }
DI f2 fma2(f2 a, f2 b, f2 c) { return __builtin_elementwise_fma(a, b, c); }
DI u32 umin(u32 a, u32 b) { return a < b ? a : b; }
DI u32 umax(u32 a, u32 b) { return a > b ? a : b; }
DI u32 clean(u32 v) { return umin(v, SENT - 1); }
DI u32 max4(u4 v) { return umax(umax(umax(v[0], v[1]), v[2]), v[3]); }
#define Z4 ((f4){0, 0, 0, 0})
DI f4 mma_h(u4 a, u4 b, f4 c) { return __builtin_amdgcn_mfma_f32_16x16x32_f16(__builtin_bit_cast(h8, a), __builtin_bit_cast(h8, b), c, 0, 0, 0); }
DI f4 mma_b(u4 a, u4 b, f4 c) { return __builtin_amdgcn_mfma_f32_16x16x32_bf16(__builtin_bit_cast(b8, a), __builtin_bit_cast(b8, b), c, 0, 0, 0); }
DI f4 mma16_h(u2 a, u2 b, f4 c) { return __builtin_amdgcn_mfma_f32_16x16x16f16(__builtin_bit_cast(h4, a), __builtin_bit_cast(h4, b), c, 0, 0, 0); }
typedef __amdgpu_buffer_rsrc_t rsrc_t;
DI rsrc_t mkrs(const void* p, u32 bytes) { return __builtin_amdgcn_make_buffer_rsrc((void*)p, 0, bytes, 0x00020000); }
template <int AUX> DI u4 bld4(rsrc_t r, int v, int s) { return __builtin_amdgcn_raw_buffer_load_b128(r, v, s, AUX); }
template <int AUX> DI u2 bld2(rsrc_t r, int v, int s) { return __builtin_amdgcn_raw_buffer_load_b64(r, v, s, AUX); }
template <int AUX> DI void bst4(u4 d, rsrc_t r, int v, int s) {
    __builtin_amdgcn_raw_buffer_store_b128(d, r, v, s, AUX);
    asm volatile("s_nop 1" :: "v"(d));
}
template <int AUX> DI void bst2(u2 d, rsrc_t r, int v, int s) { __builtin_amdgcn_raw_buffer_store_b64(d, r, v, s, AUX); }

template <int CTRL> DI float dpp(float v) {
    return __builtin_bit_cast(float, __builtin_amdgcn_update_dpp(0, __builtin_bit_cast(int, v), CTRL, 0xf, 0xf, true));
}
template <bool ALL> DI float red_lanes(float v) {
    v += dpp<0xB1>(v);
    if (ALL) v += dpp<0x4E>(v);
    v += dpp<0x124>(v); v += dpp<0x128>(v);
    u32 x = __builtin_bit_cast(u32, v), y = x;
    asm("" : "+v"(y));
    auto r = __builtin_amdgcn_permlane16_swap(x, y, false, false);
    v = __builtin_bit_cast(float, (u32)r[0]) + __builtin_bit_cast(float, (u32)r[1]);
    x = __builtin_bit_cast(u32, v); y = x;
    asm("" : "+v"(y));
    auto q = __builtin_amdgcn_permlane32_swap(x, y, false, false);
    return __builtin_bit_cast(float, (u32)q[0]) + __builtin_bit_cast(float, (u32)q[1]);
}
DI float ln_inv(float var) { return __builtin_amdgcn_rcpf(rbf(__builtin_amdgcn_sqrtf(rbf(rbf(var) + 1e-5f)))); }

#define ST_DECL
#define ST(i)
#define ST_END(ws, kid)

constexpr u32 WS_U = 0;
constexpr u32 WS_WT = WS_U + (2u << 20);
constexpr u32 WS_V = WS_WT + (2u << 20);
constexpr u32 WS_LN2 = WS_V + (2u << 20);
constexpr u32 WS_WA = WS_LN2 + (2u << 20);
constexpr u32 WS_WB = WS_WA + 8192u;
constexpr u32 WS_FA = WS_WB + 8192u;
constexpr u32 WS_FLAG = WS_FA + 24576u;
constexpr u32 WS_EP = WS_FLAG + 32768u;
constexpr u32 WS_H1 = WS_WA + (128u << 10);
constexpr u32 WS_END = WS_H1 + 7340032u;

#define W_EARLY 0
#define PRO_SLEEP 1
constexpr int BG2 = 4, BG4 = 10, BG5 = 15, BG6 = 21;
constexpr int G1_DEPTH = 10, G2_DEPTH = 3;
constexpr int B_R = 5;
constexpr int CODE_LINES = 290, CODE_PF = 10;
constexpr int LDS_SCR = 114688;
constexpr int SCR_X = 8448;
constexpr int SCR_S = SCR_X + 20480;
constexpr int SCR_F = 49024;
constexpr int XCH = 147456;
constexpr int LDS_B = 163840;

DI void glds16(const void* sbase, u32 voff, u32 lds_dst) {
    asm volatile("s_mov_b32 m0, %2\n\ts_nop 0\n\tglobal_load_lds_dwordx4 %0, %1" :: "v"(voff), "s"(sbase), "s"(lds_dst) : "memory");
}
DI bool any_sent(u32 v) { return __builtin_amdgcn_ballot_w64(v == SENT) != 0; }
DI void poll2(u4& a, u4& c, rsrc_t rw, int vo, int sa, int sc) {
#pragma nounroll
    for (int sp = 0; sp < SPIN_MAX; ++sp) {
        const bool ba = max4(a) == SENT, bc = max4(c) == SENT;
        if (__builtin_amdgcn_ballot_w64(ba || bc) == 0) break;
        __builtin_amdgcn_s_sleep(1);
        FENCE();
        if (ba) a = bld4<SC1>(rw, vo, sa);
        if (bc) c = bld4<SC1>(rw, vo, sc);
    }
}
DI void wait_tok(u4& t0, u4& t1, rsrc_t rw, int vo, int so) {
    if (any_sent(umax(max4(t0), max4(t1)))) {
#pragma nounroll
        for (int sp = 0; sp < SPIN_MAX; ++sp) {
            FENCE();
            t0 = bld4<SC1>(rw, vo, so); t1 = bld4<SC1>(rw, vo, so + 16384);
            if (!any_sent(umax(max4(t0), max4(t1)))) break;
        }
    }
}
DI u4 ldw1(LAS char* wb, int kk, int ft) {
    s4 a0 = __builtin_amdgcn_ds_read_tr16_b64_v4i16((LAS s4*)(wb + kk * 32 * 224 + ft * 32));
    s4 a1 = __builtin_amdgcn_ds_read_tr16_b64_v4i16((LAS s4*)(wb + (kk * 32 + 16) * 224 + ft * 32));
    u2 x0 = __builtin_bit_cast(u2, a0), x1 = __builtin_bit_cast(u2, a1);
    return (u4){x0[0], x0[1], x1[0], x1[1]};
}
DI u2 pk4(f4 a) { return (u2){pk_b(a[0], a[1]), pk_b(a[2], a[3])}; }
DI u2 silu_pk2(u32 p0, u32 p1) {
    u32 o0, o1; float a0, a1, a2, a3, t0, t1, t2, t3;
    asm("v_lshlrev_b32 %2, 16, %10\n\tv_and_b32 %3, 0xffff0000, %10\n\tv_lshlrev_b32 %4, 16, %11\n\tv_and_b32 %5, 0xffff0000, %11\n\t"
        "v_mul_f32 %6, %12, %2\n\tv_mul_f32 %7, %12, %3\n\tv_mul_f32 %8, %12, %4\n\tv_mul_f32 %9, %12, %5\n\t"
        "v_exp_f32 %6, %6\n\tv_exp_f32 %7, %7\n\tv_exp_f32 %8, %8\n\tv_exp_f32 %9, %9\n\t"
        "v_add_f32 %6, 1.0, %6\n\tv_add_f32 %7, 1.0, %7\n\tv_add_f32 %8, 1.0, %8\n\tv_add_f32 %9, 1.0, %9\n\t"
        "v_rcp_f32 %6, %6\n\tv_rcp_f32 %7, %7\n\tv_rcp_f32 %8, %8\n\tv_rcp_f32 %9, %9\n\t"
        "v_mul_f32 %2, %2, %6\n\tv_mul_f32 %3, %3, %7\n\tv_mul_f32 %4, %4, %8\n\tv_mul_f32 %5, %5, %9\n\t"
        "v_cvt_pk_bf16_f32 %0, %2, %3\n\tv_cvt_pk_bf16_f32 %1, %4, %5"
        : "=&v"(o0), "=&v"(o1), "=&v"(a0), "=&v"(a1), "=&v"(a2), "=&v"(a3), "=&v"(t0), "=&v"(t1), "=&v"(t2), "=&v"(t3)
        : "v"(p0), "v"(p1), "s"(-1.4426950408889634f));
    return (u2){o0, o1};
}
DI u4 silu_pk4(u4 p) { u2 a = silu_pk2(p[0], p[1]), b = silu_pk2(p[2], p[3]); return (u4){a[0], a[1], b[0], b[1]}; }
DI void lds_set(LAS u32* p, u32 v) {
    asm volatile("s_waitcnt lgkmcnt(0)" ::: "memory");
    __hip_atomic_store(p, v, __ATOMIC_RELAXED, __HIP_MEMORY_SCOPE_WORKGROUP);
}
DI void lds_wait(LAS u32* p, u32 v) {
#pragma nounroll
    for (int sp = 0; sp < SPIN_MAX; ++sp) {
        if (__builtin_amdgcn_readfirstlane(__hip_atomic_load(p, __ATOMIC_RELAXED, __HIP_MEMORY_SCOPE_WORKGROUP)) >= v) break;
        __builtin_amdgcn_s_sleep(1);
    }
    FENCE();
}
DI void flag_wait(GAS u32* p, u32 tag) {
#pragma nounroll
    for (int sp = 0; sp < SPIN_MAX; ++sp) {
        if (__builtin_amdgcn_ballot_w64(__hip_atomic_load(p, __ATOMIC_RELAXED, __HIP_MEMORY_SCOPE_AGENT) != tag) == 0) break;
        __builtin_amdgcn_s_sleep(2);
    }
    FENCE();
}
#define SENT4 ((u4){SENT, SENT, SENT, SENT})

__global__ __launch_bounds__(512, 2) void k_fused(const uint16_t* x, const uint16_t* wr, const uint16_t* wi, const uint16_t* w1,
                                                  const uint16_t* w2, uint16_t* out, char* ws) {
    extern __shared__ __attribute__((aligned(16))) char smem[];
    const int tid = threadIdx.x, l = tid & 63, g = l >> 4, m = l & 15;
    const int w = __builtin_amdgcn_readfirstlane(tid >> 6);
    const int b = (blockIdx.x & 7) * 32 + (blockIdx.x >> 3);
    const int mt1 = (b >> 6) * 4 + ((b >> 3) & 3), nt1 = ((b >> 5) & 1) * 8 + (b & 7);
    const int grp = b >> 4, th = (b >> 3) & 1, fb = b & 7;
    const rsrc_t rw = mkrs(ws, WS_END), ro = mkrs(out, 2097152);
    char* scr = smem + LDS_SCR;
    LAS u32* hw = (LAS u32*)((LAS char*)smem + LDS_SCR + SCR_F);
    const u32 lds0 = (u32)(uintptr_t)(LAS char*)smem;
    const float mf = (float)m, gf = (float)g, sg = (m & 1) ? -1.0f : 1.0f;
    const bool aw = w < 4;
    ST_DECL;
    unsigned long long pc0;
    asm volatile("s_getpc_b64 %0" : "=s"(pc0));
    if (tid >= 448 && l < CODE_PF)
        asm volatile("s_mov_b32 m0, %2\n\ts_nop 0\n\tglobal_load_lds_dword %0, %1"
                     :: "v"(umin((blockIdx.x >> 3) * CODE_PF + l, CODE_LINES - 1) * 128), "s"(pc0), "s"(lds0 + LDS_SCR + SCR_F + 64) : "memory");
    const u32 epoch = __hip_atomic_load((GAS u32*)(ws + WS_EP), __ATOMIC_RELAXED, __HIP_MEMORY_SCOPE_AGENT);
    FENCE();
    u32 xd[8];
    const rsrc_t rr = mkrs(wr, 1052672), ri = mkrs(wi, 1052672);
    const int j7 = l >> 3, p4 = (l >> 1) & 3, ee = l & 1;
    u2 fr[4], fi[4]; u32 sr_, si_;
    if (aw) {
        const u32* xr = (const u32*)(x + (256 * (2 * w + ((m >> 1) & 1)) + b) * 512) + 8 * g + 2 * (m >> 2) + (m & 1);
#pragma unroll
        for (int j = 0; j < 8; ++j) xd[j] = xr[32 * j];
    } else {
        __builtin_amdgcn_s_sleep(PRO_SLEEP);
        if (w == 4) {
            const int e = ee * 4 * 514 + (32 * j7 + 4 * p4) * 2;
#pragma unroll
            for (int i = 0; i < 4; ++i) { fr[i] = bld2<0>(rr, e + i * 514, 8 * b * 514); fi[i] = bld2<0>(ri, e + i * 514, 8 * b * 514); }
        } else if (w == 5) {
            const int e = (2045 - 8 * b) * 514 + 253 * 2 - ee * 4 * 514 - (32 * j7 + 4 * p4) * 2;
#pragma unroll
            for (int i = 1; i < 4; ++i) { fr[i] = bld2<0>(rr, e + (3 - i) * 514, 0); fi[i] = bld2<0>(ri, e + (3 - i) * 514, 0); }
            const int e0 = e + 3 * 514 - (b == 0 && !ee ? 2048 * 514 : 0);
            fr[0] = bld2<0>(rr, e0, 0); fi[0] = bld2<0>(ri, e0, 0);
            const int e1 = (8 * b + (l & 7)) * 514 + (16 + 32 * j7) * 2;
            sr_ = __builtin_amdgcn_raw_buffer_load_b16(rr, e1, 0, 0); si_ = __builtin_amdgcn_raw_buffer_load_b16(ri, e1, 0, 0);
        } else if (w == 6) {
            const int c = j7 & 3, f = 8 * b + (l & 7);
            const int e = ((c & 1) ? (2048 - f) & 2047 : f) * 514 + (c >> 1) * 512;
            sr_ = __builtin_amdgcn_raw_buffer_load_b16(rr, e, 0, 0); si_ = __builtin_amdgcn_raw_buffer_load_b16(ri, e, 0, 0);
        }
        FENCE();
        bst4<SC1>(SENT4, rw, l * 16, WS_U + b * 8192 + (w - 4) * 2048); bst4<SC1>(SENT4, rw, l * 16 + 1024, WS_U + b * 8192 + (w - 4) * 2048);
    }
    FENCE();
    const int rq = ((b >> 3) + b) & 3;
    const int row0 = (tid & 255) / 14, cch0 = (tid & 255) - row0 * 14;
    const char* wsrc = (const char*)w1 + nt1 * 224;
    u32 w1o[7];
    auto w1_burst = [&](int i0, int n) {
#pragma unroll
        for (int i = i0; i < i0 + n; ++i) {
            const int q = (i / 7 + rq) & 3, j = i % 7;
            glds16(wsrc + q * (128 * 3584), w1o[j], lds0 + (w - 4) * 1024 + q * 28672 + j * 4096);
        }
    };
    const int xoff = (256 * w + b) * 1024 + m * 32 + g * 8;
    u2 xl[2];
    float c16[4], s16[4];
    f2 twc[2], tws[2];
    u4 Gre3, Gim3, SGre, SGim, GA[2];
    auto consts0 = [&]() {
#pragma unroll
        for (int u = 0; u < 4; ++u) {
            float a = (4.0f * gf + u) * (mf * (1.0f / 16));
            c16[u] = cosr(a); s16[u] = sinr(a);
            twc[u >> 1][u & 1] = cosr(a * (1.0f / 32)); tws[u >> 1][u & 1] = sinr(a * (1.0f / 32));
        }
    };
    auto consts3 = [&]() {
#pragma unroll
        for (int u = 0; u < 4; ++u) {
            const float k = 4.0f * gf + u;
            const bool k0 = u == 0 && g == 0;
            float cc = c16[u], ss = s16[u];
            Gre3[u] = pk_b(cc, -ss); Gim3[u] = pk_b(ss, cc);
            float a2 = (2.0f * k + 1.0f) * (mf * (1.0f / 32));
            u32 sp = pk_b(g < 2 ? 2.f * cc : 2.f * sg * cosr(a2), g < 2 ? -2.f * ss : -2.f * sg * sinr(a2));
            SGre[u] = k0 ? pk_b(1.0f, sg) : (g < 2 ? sp : 0u);
            SGim[u] = g < 2 ? 0u : sp;
            float a4 = k * (mf * (1.0f / 32)), c4 = 2.f * cosr(a4), s4_ = -2.f * sinr(a4);
            GA[0][u] = k0 ? pk_b(1.0f, sg) : pk_b(c4, s4_);
            GA[1][u] = k0 ? pk_b(1.0f, sg) : ((u & 1) ? pk_b(-c4, -s4_) : pk_b(c4, s4_));
        }
        PIN(Gre3); PIN(Gim3); PIN(SGre); PIN(SGim); PIN(GA[0]); PIN(GA[1]); PIN(twc[0]); PIN(twc[1]); PIN(tws[0]); PIN(tws[1]);
    };
    const int pw = (w + 4 * th) & 7;
    GAS u32* fl = (GAS u32*)(ws + WS_FLAG);
    int ko[7];
    {
        const int r6 = (fb < 6 ? fb : fb - 5) + 3 * (grp & 1);
#pragma unroll
        for (int ks = 0; ks < 6; ++ks) { int k = ks + r6; k = k >= 6 ? k - 6 : k; ko[ks] = k >= 6 ? k - 6 : k; }
        ko[6] = 6;
    }
    u4 W[7][4];
    const u32 w2vo = (l >> 3) * 1024 + (((l & 7) ^ (((l >> 4) & 1) << 2)) * 16);
    auto w2_glds = [&](int p_, int ks, u32 dst) {
        const int q = ko[ks];
        const char* src = (const char*)w2 + (224 * p_ + (ks == 6 ? 96 : (q < 3 ? 32 * q : 16 + 32 * q))) * 1024 + fb * 128;
        glds16(src, w2vo, dst); glds16(src + 8192, w2vo, dst + 1024);
        glds16(src + (ks == 6 ? 112 : 16) * 1024, w2vo, dst + 2048); glds16(src + (ks == 6 ? 120 : 24) * 1024, w2vo, dst + 3072);
    };
    LAS char* w2rd = (LAS char*)smem + (4 * g + (m >> 2)) * 128 + (((m & 3) ^ (((m >> 3) & 1) << 2)) * 16);
    auto w2_read = [&](u4 (&f)[4], int off) {
#pragma unroll
        for (int t = 0; t < 4; ++t) {
            LAS char* p = (LAS char*)((u32)(uintptr_t)w2rd ^ ((t >> 1) * 64)) + off + (t & 1) * 8;
            u2 x0 = __builtin_bit_cast(u2, __builtin_amdgcn_ds_read_tr16_b64_v4i16((LAS s4*)p));
            u2 x1 = __builtin_bit_cast(u2, __builtin_amdgcn_ds_read_tr16_b64_v4i16((LAS s4*)(p + 2048)));
            f[t] = (u4){x0[0], x0[1], x1[0], x1[1]};
        }
    };
    auto bg = [&](int i0, int i1) { w1_burst(i0, i1 - i0); };
    u32 tag;
    if (__builtin_expect(aw, 1)) {
        if (tid < 16) hw[tid] = 0;
        FENCE();
        ST(2);
        {
            u4 FAre, FAim, Fre, Fim, Sre, Sim; u2 Mb; f2 pc[2], ps[2];
            consts0();
            float c0[8], s0[8];
#pragma unroll
            for (int j = 0; j < 8; ++j) {
                float a = (gf + 4.0f * j) * (mf * (1.0f / 32));
                c0[j] = cosr(a); s0[j] = m ? -sinr(a) : ((g & 1) ? -1.0f : 1.0f);
            }
            const float mo = (float)(2 * (m & 7) + 1) * (1.0f / 32);
#pragma unroll
            for (int u = 0; u < 4; ++u) {
                FAre[u] = pk_h(c0[2 * u], c0[2 * u + 1]); FAim[u] = pk_h(s0[2 * u], s0[2 * u + 1]);
                float cc = c16[u], ss = s16[u];
                Fre[u] = pk_h(cc, ss); Fim[u] = pk_h(-ss, cc);
                float a2 = (4.0f * gf + u) * mo;
                u32 re = pk_h(m < 8 ? cc : cosr(a2), 0.f), im = pk_h(m < 8 ? (m ? -ss : ((u & 1) ? -1.0f : 1.0f)) : -sinr(a2), 0.f);
                Sre[u] = m < 8 ? re : re << 16; Sim[u] = m < 8 ? im : im << 16;
                pc[u & 1][u >> 1] = twc[u >> 1][u & 1]; ps[u & 1][u >> 1] = tws[u >> 1][u & 1];
            }
#pragma unroll
            for (int e = 0; e < 2; ++e) {
                float a = (float)(((m >> 1) * (256 * (2 * g + e) + b)) & 2047) * (1.0f / 2048);
                float cc = cosr(a), ss = sinr(a);
                Mb[e] = (m & 1) ? pk_h(-ss, cc) : pk_h(cc, ss);
            }
            PIN(FAre); PIN(FAim); PIN(Fre); PIN(Fim); PIN(Sre); PIN(Sim); PIN(Mb); PIN(pc[0]); PIN(pc[1]); PIN(ps[0]); PIN(ps[1]);
            ST(3);
            f2 v[8];
#pragma unroll
            for (int j = 0; j < 8; ++j) v[j] = bf_2(xd[j]);
            f2 s2 = ((v[0] + v[1]) + (v[2] + v[3])) + ((v[4] + v[5]) + (v[6] + v[7]));
            const float mean = red_lanes<false>(s2[0] + s2[1]) * (1.0f / 512);
            f2 q0 = {0.f, 0.f}, q1 = {0.f, 0.f};
#pragma unroll
            for (int j = 0; j < 8; ++j) { v[j] -= mean; if (j & 1) q1 = fma2(v[j], v[j], q1); else q0 = fma2(v[j], v[j], q0); }
            q0 += q1;
            const float inv = ln_inv(red_lanes<false>(q0[0] + q0[1]) * (1.0f / 512));
            const float off = (mean - rbf(mean)) * inv;
#pragma unroll
            for (int j = 0; j < 8; ++j) v[j] = fma2(v[j], (f2){inv, inv}, (f2){off, off});
            u4 B[2];
#pragma unroll
            for (int p = 0; p < 2; ++p) {
                u4 A = {pk_h(v[0][p], v[1][p]), pk_h(v[2][p], v[3][p]), pk_h(v[4][p], v[5][p]), pk_h(v[6][p], v[7][p])};
                f4 Dre = mma_h(A, FAre, Z4), Dim = mma_h(A, FAim, Z4);
#pragma unroll
                for (int r = 0; r < 2; ++r) {
                    f2 re = r ? hi2(Dre) : lo2(Dre), im = r ? hi2(Dim) : lo2(Dim);
                    f2 tr = fma2(im, ps[p], re * pc[p]), ti = fma2(-re, ps[p], im * pc[p]);
                    B[r][p] = pk_h(tr[0], ti[0]); B[r][2 + p] = pk_h(tr[1], ti[1]);
                }
            }
#pragma unroll
            for (int r = 0; r < 2; ++r) {
                f4 Gre = mma_h(Fre, B[r], Z4), Gim = mma_h(Fim, B[r], Z4), Hre = mma_h(Sre, B[r], Z4), Him = mma_h(Sim, B[r], Z4);
                u4 Z;
#pragma unroll
                for (int i = 0; i < 4; ++i) Z[i] = pk_h(m ? Gre[i] : Hre[i], m ? Gim[i] : Him[i]);
                *(u4*)(scr + (2 * w + r) * 1024 + m * 64 + g * 16) = Z;
            }
            BARRIER();
            ST(18);
            u4 r0 = *(const u4*)(scr + (2 * g) * 1024 + w * 256 + m * 16);
            u4 r1 = *(const u4*)(scr + (2 * g + 1) * 1024 + w * 256 + m * 16);
#pragma unroll
            for (int t = 0; t < 4; ++t) {
                f4 D = mma16_h(Mb, (u2){r0[t], r1[t]}, Z4);
                *(u2*)(scr + SCR_S + w * 2048 + m * 128 + (((2 * t + (g >> 1)) ^ (m >> 1)) * 16) + (g & 1) * 8) =
                    (u2){clean(pk_h(D[0], D[1])), clean(pk_h(D[2], D[3]))};
            }
        }
        BARRIER();
        ST(4);
        u4 Fre, Fim, Gre, Gim, N0, N1; f2 tc[2], ts[2];
#pragma unroll
        for (int u = 0; u < 4; ++u) {
            float cc = c16[u], ss = s16[u];
            Fre[u] = pk_h(0.25f * cc, 0.25f * ss); Fim[u] = pk_h(-0.25f * ss, 0.25f * cc);
            Gre[u] = pk_b(cc * (1.0f / 256), -ss * (1.0f / 256)); Gim[u] = pk_b(ss * (1.0f / 256), cc * (1.0f / 256));
            float a3 = (4.0f * gf + u) * (mf * (1.0f / 256));
            tc[u >> 1][u & 1] = cosr(a3); ts[u >> 1][u & 1] = sinr(a3);
        }
        PIN(Fre); PIN(Fim); PIN(Gre); PIN(Gim); PIN(tc[0]); PIN(tc[1]); PIN(ts[0]); PIN(ts[1]);
        xl[0] = *(const u2*)((const char*)x + xoff); xl[1] = *(const u2*)((const char*)x + xoff + 512);
        FENCE();
        tag = __builtin_amdgcn_readfirstlane(epoch) + 1u;
        consts3();
#pragma unroll
        for (int u = 0; u < 4; ++u) {
            float a1 = (float)(((4 * (g & 1) + u) * (256 * (m >> 1) + b)) & 2047) * (1.0f / 2048);
            float c1 = cosr(a1), s1 = sinr(a1);
            u32 nb = (m & 1) ? pk_b(s1, c1) : pk_b(c1, -s1);
            N0[u] = g < 2 ? nb : 0u; N1[u] = g < 2 ? 0u : nb;
        }
        PIN(N0); PIN(N1);
        ST(19);
        BARRIER();
        ST(5);
        u4 d[2], f[2], e[2];
        const int uo = (tid >> 1) * 8192 + (tid & 1) * 16;
        d[0] = bld4<SC1>(rw, uo, WS_U + b * 32); d[1] = bld4<SC1>(rw, uo, WS_U + 128 * 8192 + b * 32);
        const u32 fd0 = __builtin_amdgcn_raw_buffer_load_b32(rw, uo, WS_WT + b * 32, SC1);
        const u32 fd1 = __builtin_amdgcn_raw_buffer_load_b32(rw, uo, WS_WT + 128 * 8192 + b * 32, SC1);
        FENCE();
        poll2(d[0], d[1], rw, uo, WS_U + b * 32, WS_U + 128 * 8192 + b * 32);
        char* sp = scr + SCR_S + (tid ^ ((tid >> 4) & 1)) * 16;
        *(u4*)sp = d[0]; *(u4*)(sp + 4096) = d[1];
        BARRIER();
        f[0] = bld4<SC1>(rw, uo, WS_WT + b * 32); f[1] = bld4<SC1>(rw, uo, WS_WT + 128 * 8192 + b * 32);
        if (b == 0) {
            f[0] = bld4<SC1>(rw, tid * 16, WS_WA); f[1] = bld4<SC1>(rw, tid * 16 + 4096, WS_WA);
            e[0] = bld4<SC1>(rw, tid * 16, WS_WB); e[1] = bld4<SC1>(rw, tid * 16 + 4096, WS_WB);
            if (tid == 0) __hip_atomic_store((GAS u32*)(ws + WS_EP), tag, __ATOMIC_RELAXED, __HIP_MEMORY_SCOPE_AGENT);
        }
        ST(6);
        u2 ld[4], wl[4];
        const int so = (64 * g + m) * 32 + (((w >> 1) ^ (m >> 3)) * 16) + (w & 1) * 8;
#pragma unroll
        for (int q = 0; q < 4; ++q) ld[q] = *(const u2*)(scr + SCR_S + so + 512 * q);
        FENCE();
        f4 Xre[2], Xim[2];
#pragma unroll
        for (int e = 0; e < 2; ++e) {
            u4 A = {ld[0][e], ld[1][e], ld[2][e], ld[3][e]};
            f4 Dre = mma_h(A, Fre, Z4), Dim = mma_h(A, Fim, Z4);
            f2 r0 = fma2(lo2(Dim), ts[0], lo2(Dre) * tc[0]), i0 = fma2(-lo2(Dre), ts[0], lo2(Dim) * tc[0]);
            f2 r1 = fma2(hi2(Dim), ts[1], hi2(Dre) * tc[1]), i1 = fma2(-hi2(Dre), ts[1], hi2(Dim) * tc[1]);
            u4 B = {pk_h(r0[0], i0[0]), pk_h(r0[1], i0[1]), pk_h(r1[0], i1[0]), pk_h(r1[1], i1[1])};
            Xre[e] = mma_h(Fre, B, Z4); Xim[e] = mma_h(Fim, B, Z4);
        }
        *(u4*)(sp + 8192) = f[0]; *(u4*)(sp + 12288) = f[1];
        asm volatile("" :: "v"(fd0), "v"(fd1));
        f2* sx = (f2*)(scr + SCR_X);
        if (b == 0) {
            *(u4*)(sp - SCR_S) = e[0]; *(u4*)(sp - SCR_S + 4096) = e[1];
#pragma unroll
            for (int e = 0; e < 2; ++e)
#pragma unroll
                for (int i = 0; i < 4; ++i) sx[2 * w + e + 8 * (m + 16 * (4 * g + i))] = (f2){Xre[e][i], Xim[e][i]};
        }
        BARRIER();
        ST(20);
#pragma unroll
        for (int q = 0; q < 4; ++q) wl[q] = *(const u2*)(scr + SCR_S + so + 8192 + 512 * q);
        FENCE();
        f2 yr[2][2], yi[2][2];
#pragma unroll
        for (int e = 0; e < 2; ++e)
#pragma unroll
            for (int hh = 0; hh < 2; ++hh) {
                f2 wre = {bf_lo(wl[2 * hh][e]), bf_lo(wl[2 * hh + 1][e])}, wim = {bf_hi(wl[2 * hh][e]), bf_hi(wl[2 * hh + 1][e])};
                f2 xr = hh ? hi2(Xre[e]) : lo2(Xre[e]), xi = hh ? hi2(Xim[e]) : lo2(Xim[e]);
                yr[e][hh] = fma2(-xi, wim, xr * wre); yi[e][hh] = fma2(xr, wim, xi * wre);
            }
        if (b == 0) {
#pragma unroll
            for (int i = 0; i < 4; ++i) {
                u2 wb = *(const u2*)(scr + so + 512 * i);
#pragma unroll
                for (int e = 0; e < 2; ++e) {
                    f2 xm = sx[(2048 - (2 * w + e + 8 * (m + 16 * (4 * g + i)))) & 2047];
                    float br = bf_lo(wb[e]), bi = bf_hi(wb[e]);
                    yr[e][i >> 1][i & 1] += xm[0] * br + xm[1] * bi;
                    yi[e][i >> 1][i & 1] += xm[0] * bi - xm[1] * br;
                }
            }
        }
        char* pv = scr + SCR_S;
#pragma unroll
        for (int e = 0; e < 2; ++e) {
            u4 Y = {pk_b(yr[e][0][0], yi[e][0][0]), pk_b(yr[e][0][1], yi[e][0][1]), pk_b(yr[e][1][0], yi[e][1][0]), pk_b(yr[e][1][1], yi[e][1][1])};
            f4 Dre = mma_b(Y, Gre, Z4), Dim = mma_b(Y, Gim, Z4);
            f2 r0 = fma2(-lo2(Dim), ts[0], lo2(Dre) * tc[0]), i0 = fma2(lo2(Dre), ts[0], lo2(Dim) * tc[0]);
            f2 r1 = fma2(-hi2(Dim), ts[1], hi2(Dre) * tc[1]), i1 = fma2(hi2(Dre), ts[1], hi2(Dim) * tc[1]);
            u4 A = {pk_b(r0[0], i0[0]), pk_b(r0[1], i0[1]), pk_b(r1[0], i1[0]), pk_b(r1[1], i1[1])};
            f4 Ere = mma_b(A, Gre, Z4), Eim = mma_b(A, Gim, Z4);
            u4 o;
#pragma unroll
            for (int i = 0; i < 4; ++i) o[i] = clean(pk_b(Ere[i], Eim[i]));
            *(u4*)(pv + (2 * w + e) * 1024 + ((m * 4 + g) ^ ((w >> 1) * 4)) * 16) = o;
        }
        BARRIER();
        ST(7);
#pragma unroll
        for (int it = 0; it < 2; ++it) {
            const int ch = tid + 256 * it;
            const u32* q = (const u32*)(pv + (ch & 1) * 4096 + (((ch >> 1) * 4) ^ ((ch & 1) * 64)));
            bst4<SC1>((u4){q[0], q[256], q[512], q[768]}, rw, ch * 16, WS_V + b * 8192);
        }
        WAITV(0);
        BARRIER();
        ST(8);
        const int vo = (2 * m + (g >> 1)) * 8192 + (g & 1) * 16, vs = WS_V + w * 64 * 8192 + b * 32;
        u4 v0 = bld4<SC1>(rw, vo, vs), v1 = bld4<SC1>(rw, vo, vs + 32 * 8192);
        poll2(v0, v1, rw, vo, vs, vs + 32 * 8192);
        ST(9);
        char* sz = scr + SCR_X + g * 2048 + w * 256 + m * 8;
#pragma unroll
        for (int hh = 0; hh < 2; ++hh) {
            f4 D0 = mma_b(N0, hh ? v1 : v0, Z4), D1 = mma_b(N1, hh ? v1 : v0, Z4);
            *(u2*)(sz + hh * 128) = (u2){pk_b(D0[0], D0[1]), pk_b(D1[0], D1[1])};
            *(u2*)(sz + 1024 + hh * 128) = (u2){pk_b(D0[2], D0[3]), pk_b(D1[2], D1[3])};
        }
    } else {
        auto announce = [&](int which) {
            if (l < 8) __hip_atomic_store((GAS u32*)(ws + WS_FA) + which * 2048 + l * 256 + b, tag, __ATOMIC_RELAXED, __HIP_MEMORY_SCOPE_AGENT);
            FENCE();
        };
        auto all_announced = [&](int which) {
#pragma nounroll
            for (int sp = 0; sp < SPIN_MAX; ++sp) {
                FENCE();
                u4 f = bld4<SC1>(rw, l * 16, WS_FA + which * 8192 + (b >> 5) * 1024);
                if (__builtin_amdgcn_ballot_w64(f[0] != tag || f[1] != tag || f[2] != tag || f[3] != tag) == 0) break;
                __builtin_amdgcn_s_sleep(4);
            }
        };
        FENCE();
        ST(2);
#pragma unroll
        for (int jj = 0; jj < 7; ++jj) {
            const int cj_ = (4 * jj) % 14, rj = 18 * jj + (4 * jj) / 14;
            w1o[jj] = (row0 + rj) * 3584 + (cch0 + cj_) * 16 + (cch0 + cj_ >= 14 ? 3584 - 224 : 0);
            PIN(w1o[jj]);
        }
        tag = __builtin_amdgcn_readfirstlane(epoch) + 1u;
        auto count_in = [&](int word, u32 last, int which) {
            u32 prev = 0;
            if (l == 0) prev = __hip_atomic_fetch_add(hw + word, 1u, __ATOMIC_RELAXED, __HIP_MEMORY_SCOPE_WORKGROUP);
            if (__builtin_amdgcn_readfirstlane(prev) == last) announce(which);
        };
        WAITV(0);
        if (l < 2) __hip_atomic_store((GAS u32*)(ws + WS_FA) + (2 * (w - 4) + l) * 256 + b, tag, __ATOMIC_RELAXED, __HIP_MEMORY_SCOPE_AGENT);
        FENCE();
        if (!W_EARLY) BARRIER();
        if (w < 6) {
            const int wo = p4 * 2048 + j7 * 32 + ee * 16, wso = WS_WT + b * 8192 + (w - 4) * 256;
#pragma unroll
            for (int c = 0; c < 4; ++c) {
                u4 o;
#pragma unroll
                for (int i = 0; i < 4; ++i)
                    o[i] = w == 4 ? __builtin_amdgcn_perm(fi[i][c >> 1], fr[i][c >> 1], (c & 1) ? 0x07060302u : 0x05040100u)
                                  : __builtin_amdgcn_perm(fi[i][1 - (c >> 1)], fr[i][1 - (c >> 1)], (c & 1) ? 0x05040100u : 0x07060302u) ^ 0x80000000u;
                bst4<SC1>(o, rw, wo + c * 512, wso);
            }
            if (w == 5) __builtin_amdgcn_raw_buffer_store_b32(sr_ | (si_ << 16), rw, l * 4, wso, SC1);
        } else if (w == 6) {
            float re = bf_lo(sr_), im = bf_lo(si_);
            re += dpp<0x128>(re); im -= dpp<0x128>(im);
            u32 a = __builtin_bit_cast(u32, re), a2 = a, c = __builtin_bit_cast(u32, im), c2 = c;
            asm("" : "+v"(a2), "+v"(c2));
            auto pr = __builtin_amdgcn_permlane16_swap(a, a2, false, false);
            auto pi = __builtin_amdgcn_permlane16_swap(c, c2, false, false);
            const float ar = __builtin_bit_cast(float, (u32)pr[0]), br = __builtin_bit_cast(float, (u32)pr[1]);
            const float ai = __builtin_bit_cast(float, (u32)pi[0]), bi = __builtin_bit_cast(float, (u32)pi[1]);
            if (l < 8) {
                __builtin_amdgcn_raw_buffer_store_b32(pk_b(0.25f * (ar + br), 0.25f * (ai + bi)), rw, l * 4, WS_WA + b * 32, SC1);
                __builtin_amdgcn_raw_buffer_store_b32(pk_b(0.25f * (ar - br), 0.25f * (ai - bi)), rw, l * 4, WS_WB + b * 32, SC1);
            }
        }
        ST(18);
        if (W_EARLY) BARRIER();
        BARRIER();
        {
            const char* up = scr + SCR_S + (w - 4) * 2048;
            bst4<SC1>(*(const u4*)(up + (l ^ ((l >> 4) & 7)) * 16), rw, l * 16, WS_U + b * 8192 + (w - 4) * 2048);
            bst4<SC1>(*(const u4*)(up + 1024 + (l ^ (((l >> 4) + 4) & 7)) * 16), rw, l * 16 + 1024, WS_U + b * 8192 + (w - 4) * 2048);
        }
        if (w < 7) { WAITV(2); count_in(13, 2u, 2); }
        else all_announced(0);
        WAITV(0);
        ST(19);
        xl[0] = *(const u2*)((const char*)x + xoff); xl[1] = *(const u2*)((const char*)x + xoff + 512);
        FENCE();
        bg(0, BG2);
        ST(4);
        BARRIER();
        __builtin_amdgcn_s_sleep(2);
#pragma unroll
        for (int i = 0; i < 2; ++i) bst4<SC1>(SENT4, rw, l * 16 + i * 1024, WS_V + b * 8192 + (w - 4) * 2048);
        FENCE();
        consts0();
        consts3();
        WAITV(0);
        if (w == 7) all_announced(2);
        ST(5);
        BARRIER();
        if (w == 7) announce(1);
#pragma unroll
        for (int i = 0; i < 2; ++i) bst4<SC1>(SENT4, rw, (l >> 3) * 256 + (l & 7) * 16 + i * 2048, WS_LN2 + (b >> 1) * 16384 + (b & 1) * 128 + (w - 4) * 4096);
        bg(BG2, BG4);
        BARRIER();
        bg(BG4, BG5);
        WAITV(BG5 - BG2);
        ST(6);
        BARRIER();
        if (w == 7) all_announced(1);
        else __builtin_amdgcn_s_sleep(3);
        bg(BG5, BG6);
        ST(7);
        BARRIER();
    }
    BARRIER();
    {
        u4 A = *(const u4*)(scr + SCR_X + w * 1024 + m * 64 + g * 16);
        f4 Dre = mma_b(A, Gre3, Z4), Dim = mma_b(A, Gim3, Z4), Hre = mma_b(A, SGre, Z4), Him = mma_b(A, SGim, Z4);
        if (g == 0) { Dre[0] = Hre[0]; Dim[0] = Him[0]; }
        f2 r0 = fma2(-lo2(Dim), tws[0], lo2(Dre) * twc[0]), i0 = fma2(lo2(Dre), tws[0], lo2(Dim) * twc[0]);
        f2 r1 = fma2(-hi2(Dim), tws[1], hi2(Dre) * twc[1]), i1 = fma2(hi2(Dre), tws[1], hi2(Dim) * twc[1]);
        u4 A2 = {pk_b(r0[0], i0[0]), pk_b(r0[1], i0[1]), pk_b(r1[0], i1[0]), pk_b(r1[1], i1[1])};
        f2 h[4]; u2 hp[2];
#pragma unroll
        for (int t = 0; t < 2; ++t) {
            f4 Yv = mma_b(A2, GA[t], Z4);
            u32 h0 = pk_b2(bf_2(xl[t][0]) + bf_2(pk_b(Yv[0], Yv[1]))), h1 = pk_b2(bf_2(xl[t][1]) + bf_2(pk_b(Yv[2], Yv[3])));
            hp[t] = (u2){h0, h1};
            h[2 * t] = bf_2(h0); h[2 * t + 1] = bf_2(h1);
        }
        f2 s2 = (h[0] + h[1]) + (h[2] + h[3]);
        const float mean = red_lanes<true>(s2[0] + s2[1]) * (1.0f / 512);
        f2 q2 = {0.f, 0.f};
#pragma unroll
        for (int j = 0; j < 4; ++j) { f2 d = h[j] - mean; q2 = fma2(d, d, q2); }
        const float inv = ln_inv(red_lanes<true>(q2[0] + q2[1]) * (1.0f / 512));
        const float mb = rbf(mean);
#pragma unroll
        for (int t = 0; t < 2; ++t)
            *(u2*)(scr + ((8 * t + (m >> 1)) * 4 + g) * 128 + (w ^ (m >> 1)) * 16 + (m & 1) * 8) =
                (u2){clean(pk_b2(bf_2(pk_b2(h[2 * t] - mb)) * inv)), clean(pk_b2(bf_2(pk_b2(h[2 * t + 1] - mb)) * inv))};
        WAITV(0);
        BARRIER();
        u4 lo = *(const u4*)(scr + (tid ^ ((tid >> 5) & 7)) * 16);
        bst4<SC1>(lo, rw, (tid >> 3) * 256 + (tid & 7) * 16, WS_LN2 + (b >> 1) * 16384 + (b & 1) * 128);
        FENCE();
        if (aw) WAITV(0); else __builtin_amdgcn_s_sleep(8);
        bst2<SC1>(hp[0], ro, xoff, 0); bst2<SC1>(hp[1], ro, xoff + 512, 0);
        FENCE();
        ST(10);
    }
    auto stage_out = [&](int r) {
        w2_read(W[r], LDS_SCR + (w & 3) * 8192 + (r & 1) * 4096);
        asm volatile("s_waitcnt lgkmcnt(0)" ::: "memory");
        PIN(W[r][0]); PIN(W[r][1]); PIN(W[r][2]); PIN(W[r][3]);
        if (r + 2 < 7) w2_glds(pw, r + 2, lds0 + LDS_SCR + (w & 3) * 8192 + (r & 1) * 4096);
    };
    auto publish = [&]() {
        u32 prev = 0;
        if (l == 0) prev = __hip_atomic_fetch_add(hw + 4, 1u, __ATOMIC_RELAXED, __HIP_MEMORY_SCOPE_WORKGROUP);
        if (__builtin_amdgcn_readfirstlane(prev) == 7u && l < 2)
            __hip_atomic_store(fl + (l ? 4096 + (b >> 1) * 32 + (b & 1) : (mt1 * 8 + (nt1 >> 1)) * 32 + (nt1 & 1)), tag, __ATOMIC_RELAXED, __HIP_MEMORY_SCOPE_AGENT);
        FENCE();
    };
    {
        const int tg = w & 3;
        char* xch = smem + XCH + tg * 3072;
        const int hoff = WS_H1 + (mt1 * 8 + 2 * tg) * (56 * 1024) + (nt1 >> 1) * 7168;
        if (__builtin_expect(aw, 1)) {
            const int toff = WS_LN2 + (mt1 * 8 + 2 * tg) * 16384;
            LAS char* wb0 = (LAS char*)smem + (4 * g + (m >> 2)) * 224 + (m & 3) * 8;
            LAS char* wb[4];
            int tq[4];
#pragma unroll
            for (int q = 0; q < 4; ++q) { const int qq = (q + rq) & 3; wb[q] = wb0 + qq * 28672; tq[q] = toff + qq * 4096; }
            u4 T[2][16], A[2][7];
            auto ldt = [&](int kk) {
#pragma unroll
                for (int tt = 0; tt < 2; ++tt) T[tt][kk] = bld4<SC1>(rw, l * 16 + (kk & 3) * 1024, tq[kk >> 2] + tt * 16384);
            };
#pragma unroll
            for (int i = 0; i < 7; ++i) A[0][i] = ldw1(wb[0], 0, i);
            if (l == 0) __hip_atomic_fetch_add(hw + 5, 1u, __ATOMIC_RELAXED, __HIP_MEMORY_SCOPE_WORKGROUP);
            ST(11);
#pragma unroll
            for (int kk = 0; kk < G1_DEPTH; ++kk) { ldt(kk); SCHED(); }
            f4 acc[2][7];
#pragma unroll
            for (int tt = 0; tt < 2; ++tt)
#pragma unroll
                for (int i = 0; i < 7; ++i) acc[tt][i] = Z4;
#pragma unroll
            for (int kk = 0; kk < 16; ++kk) {
                if ((kk & 1) == 0) {
                    if (__builtin_expect(any_sent(umax(umax(max4(T[0][kk]), max4(T[1][kk])), umax(max4(T[0][kk + 1]), max4(T[1][kk + 1])))), 0)) {
                        wait_tok(T[0][kk], T[1][kk], rw, l * 16, tq[kk >> 2] + (kk & 3) * 1024);
                        wait_tok(T[0][kk + 1], T[1][kk + 1], rw, l * 16, tq[kk >> 2] + ((kk + 1) & 3) * 1024);
                    }
                    if (kk == 0) ST(12);
                    if (kk == (BG6 / 7) * 4 - 2) lds_wait(hw + 5, 8u);
                }
                if (kk + G1_DEPTH < 16) ldt(kk + G1_DEPTH);
                if (kk < 15) {
#pragma unroll
                    for (int i = 0; i < 7; ++i) A[(kk + 1) & 1][i] = ldw1(wb[(kk + 1) >> 2], (kk + 1) & 3, i);
                }
#pragma unroll
                for (int i = 0; i < 7; ++i)
#pragma unroll
                    for (int tt = 0; tt < 2; ++tt) acc[tt][i] = mma_b(A[kk & 1][i], T[tt][kk], acc[tt][i]);
#pragma unroll
                for (int i = 0; i < 14; ++i) {
                    __builtin_amdgcn_sched_group_barrier(0x8, 1, 0);
                    if (i == 1 || i == 7) __builtin_amdgcn_sched_group_barrier(0x20, 1, 0);
                    __builtin_amdgcn_sched_group_barrier(0x100, 1, 0);
                    if ((kk & 1) && i >= 3) __builtin_amdgcn_sched_group_barrier(0x2, 1, 0);
                }
                if (kk & 1) {} else SCHED();
            }
#pragma unroll
            for (int q = 0; q < 3; ++q) {
                u2 p0 = pk4(acc[1][2 * q]), p1 = pk4(acc[1][2 * q + 1]);
                *(u4*)(xch + q * 1024 + l * 16) = (u4){p0[0], p0[1], p1[0], p1[1]};
            }
            lds_set(hw + tg, 1u);
            ST(13);
#pragma unroll
            for (int q = 0; q < 3; ++q) {
                u2 p0 = pk4(acc[0][2 * q]), p1 = pk4(acc[0][2 * q + 1]);
                bst4<SC1>(silu_pk4((u4){p0[0], p0[1], p1[0], p1[1]}), rw, l * 16, hoff + ((nt1 & 1) * 3 + q) * 1024);
            }
#pragma unroll
            for (int tt = 0; tt < 2; ++tt) { u2 p = pk4(acc[tt][6]); bst2<SC1>(silu_pk2(p[0], p[1]), rw, l * 8, hoff + tt * (56 * 1024) + 6144 + (nt1 & 1) * 512); }
        } else {
            bg(BG6, 28);
            WAITV(0);
            if (l == 0) __hip_atomic_fetch_add(hw + 5, 1u, __ATOMIC_RELAXED, __HIP_MEMORY_SCOPE_WORKGROUP);
            __builtin_amdgcn_s_sleep(8);
            lds_wait(hw + 5, 8u);
            w2_glds(pw, 0, lds0 + LDS_SCR + tg * 8192);
            w2_glds(pw, 1, lds0 + LDS_SCR + tg * 8192 + 4096);
#pragma unroll
            for (int r = 0; r < B_R; ++r) { WAITV(4); stage_out(r); }
            ST(12);
            lds_wait(hw + tg, 1u);
            ST(13);
#pragma unroll
            for (int q = 0; q < 3; ++q)
                bst4<SC1>(silu_pk4(*(const u4*)(xch + q * 1024 + l * 16)), rw, l * 16, hoff + 56 * 1024 + ((nt1 & 1) * 3 + q) * 1024);
        }
        ST(14);
        WAITV(0);
        publish();
        ST(21);
        if (__builtin_expect(aw, 1)) {
            lds_wait(hw + 4, 8u);
#pragma unroll
            for (int ks = 0; ks < 7; ++ks) w2_glds(pw, ks, lds0 + tg * 28672 + ks * 4096);
        } else {
            stage_out(B_R);
#pragma unroll
            for (int r = B_R + 1; r < 7; ++r) { if (r + 1 < 7) WAITV(4); else WAITV(0); stage_out(r); }
        }
    }
    {
        ST(15);
        const int rot = w & 3;
        if (aw) {
            flag_wait(fl + ((l & 4) ? 4096 + (grp * 8 + th * 4 + rot) * 32 : (grp * 8 + (pw ^ ((l & 2) * 2))) * 32) + (l & 1), tag);
            lds_set(hw + 8 + rot, tag);
        } else {
#pragma nounroll
            for (int sp = 0; sp < SPIN_MAX; ++sp) {
                if (__builtin_amdgcn_readfirstlane(__hip_atomic_load(hw + 8 + rot, __ATOMIC_RELAXED, __HIP_MEMORY_SCOPE_WORKGROUP)) == tag) break;
                __builtin_amdgcn_s_sleep(1);
            }
            FENCE();
        }
        ST(16);
        const int toff = WS_H1 + (grp * 8 + th * 4) * (56 * 1024) + pw * 7168;
        int to[4];
#pragma unroll
        for (int tt = 0; tt < 4; ++tt) to[tt] = toff + ((tt + rot) & 3) * (56 * 1024);
        u4 T[7][4];
        auto ldtok = [&](int ks) {
#pragma unroll
            for (int tt = 0; tt < 4; ++tt) {
                if (ks < 6) T[ks][tt] = bld4<SC1>(rw, l * 16, to[tt] + ko[ks] * 1024);
                else {
                    u2 a = bld2<SC1>(rw, l * 8 + 2048, to[tt] + 4096), c = bld2<SC1>(rw, l * 8 + 2560, to[tt] + 4096);
                    T[ks][tt] = (u4){a[0], a[1], c[0], c[1]};
                }
            }
            SCHED();
        };
#pragma unroll
        for (int ks = 0; ks < G2_DEPTH; ++ks) ldtok(ks);
        if (aw) {
#pragma unroll
            for (int ks = 0; ks < 7; ++ks) w2_read(W[ks], rot * 28672 + ks * 4096);
            asm volatile("s_waitcnt lgkmcnt(0)" ::: "memory");
        }
        if (l == 0) __hip_atomic_fetch_add(hw + 7, 1u, __ATOMIC_RELAXED, __HIP_MEMORY_SCOPE_WORKGROUP);
        const int rho = grp * 128 + th * 64 + rot * 16 + m;
        const int ooff = (256 * (rho & 7) + (rho >> 3)) * 1024 + fb * 128 + (w >> 2) * 64 + g * 16;
        const u4 hres = bld4<SC1>(ro, ooff, 0);
        SCHED();
        f4 acc[4][4];
#pragma unroll
        for (int tt = 0; tt < 4; ++tt)
#pragma unroll
            for (int t = 0; t < 4; ++t) acc[tt][t] = Z4;
#pragma unroll
        for (int ks = 0; ks < 7; ++ks) {
            if (ks + G2_DEPTH < 7) { PIN(T[ks][0]); ldtok(ks + G2_DEPTH); }
#pragma unroll
            for (int tt = 0; tt < 4; ++tt)
#pragma unroll
                for (int t = 0; t < 4; ++t) {
                    acc[tt][t] = mma_b(W[ks][t], T[ks][tt], acc[tt][t]);
                }
            SCHED();
        }
        ST(17);
        lds_wait(hw + 7, 8u);
        char* dp = smem + w * 16384 + l * 16;
#pragma unroll
        for (int tt = 1; tt < 4; ++tt)
#pragma unroll
            for (int t = 0; t < 4; ++t) *(f4*)(dp + (((tt + rot) & 3) * 4 + t) * 1024) = acc[tt][t];
        f4 s0, s1;
        if (aw) { *(f4*)(dp + (rot * 4 + 2) * 1024) = acc[0][2]; *(f4*)(dp + (rot * 4 + 3) * 1024) = acc[0][3]; s0 = acc[0][0]; s1 = acc[0][1]; }
        else { *(f4*)(dp + (rot * 4) * 1024) = acc[0][0]; *(f4*)(dp + (rot * 4 + 1) * 1024) = acc[0][1]; s0 = acc[0][2]; s1 = acc[0][3]; }
        BARRIER();
        f4 r0[7], r1[7];
#pragma unroll
        for (int j = 1; j < 8; ++j) {
            const char* rp = smem + (((w + j) & 7) * 16 + rot * 4 + (w >> 2) * 2) * 1024 + l * 16;
            r0[j - 1] = *(const f4*)rp; r1[j - 1] = *(const f4*)(rp + 1024);
        }
        SCHED();
        s0 = ((s0 + r0[0]) + (r0[1] + r0[2])) + ((r0[3] + r0[4]) + (r0[5] + r0[6]));
        s1 = ((s1 + r1[0]) + (r1[1] + r1[2])) + ((r1[3] + r1[4]) + (r1[5] + r1[6]));
        u2 p0 = pk4(s0), p1 = pk4(s1);
        bst4<NT>((u4){pk_b2(bf_2(hres[0]) + bf_2(p0[0])), pk_b2(bf_2(hres[1]) + bf_2(p0[1])),
                           pk_b2(bf_2(hres[2]) + bf_2(p1[0])), pk_b2(bf_2(hres[3]) + bf_2(p1[1]))}, ro, ooff, 0);
    }
    ST_END(ws, 0);
}

extern "C" void kernel_launch(void* const* d_in, const int* in_sizes, int n_in, void* d_out, int out_size,
                              void* d_ws, size_t ws_size, hipStream_t stream) {
    (void)hipFuncSetAttribute((const void*)k_fused, hipFuncAttributeMaxDynamicSharedMemorySize, LDS_B);
    k_fused<<<256, 512, LDS_B, stream>>>((const uint16_t*)d_in[0], (const uint16_t*)d_in[1], (const uint16_t*)d_in[2],
                                         (const uint16_t*)d_in[4], (const uint16_t*)d_in[5], (uint16_t*)d_out, (char*)d_ws);
}
```

```cpp
#define DIAG_PT 3
#include <hip/hip_runtime.h>
#include <hip/hip_bf16.h>
#include <stdint.h>

typedef _Float16 h8 __attribute__((ext_vector_type(8)));
typedef _Float16 h4 __attribute__((ext_vector_type(4)));
typedef _Float16 h2 __attribute__((ext_vector_type(2)));
typedef __bf16 b8 __attribute__((ext_vector_type(8)));
typedef __bf16 b2 __attribute__((ext_vector_type(2)));
typedef short s4 __attribute__((ext_vector_type(4)));
typedef float f4 __attribute__((ext_vector_type(4)));
typedef float f2 __attribute__((ext_vector_type(2)));
typedef unsigned u32;
typedef u32 u4 __attribute__((ext_vector_type(4)));
typedef u32 u2 __attribute__((ext_vector_type(2)));
#define LAS __attribute__((address_space(3)))
#define GAS __attribute__((address_space(1)))
#define DI __device__ __forceinline__
#define FENCE() asm volatile("" ::: "memory")
#define PIN(x) asm volatile("" : "+v"(x))
#define SCHED() __builtin_amdgcn_sched_barrier(0)
#define WAITV(n) asm volatile("s_waitcnt vmcnt(%0)" :: "n"(n) : "memory")
#define BARRIER() do { asm volatile("s_waitcnt lgkmcnt(0)" ::: "memory"); __builtin_amdgcn_s_barrier(); FENCE(); } while (0)
constexpr int SC1 = 16, NT = 2;
constexpr u32 SENT = 0xffffffffu;
constexpr int SPIN_MAX = 1 << 20;

DI u32 pk_h(float a, float b) { f2 v = {a, b}; h2 r = __builtin_convertvector(v, h2); return __builtin_bit_cast(u32, r); }
DI u32 pk_b(float a, float b) { f2 v = {a, b}; b2 r = __builtin_convertvector(v, b2); return __builtin_bit_cast(u32, r); }
DI float bf_lo(u32 v) { return __builtin_bit_cast(float, v << 16); }
DI float bf_hi(u32 v) { return __builtin_bit_cast(float, v & 0xffff0000u); }
DI f2 bf_2(u32 v) { return (f2){bf_lo(v), bf_hi(v)}; }
DI u32 pk_b2(f2 v) { return pk_b(v[0], v[1]); }
DI float rbf(float a) { return bf_lo(pk_b(a, a)); }
DI float cosr(float rev) { return __builtin_amdgcn_cosf(rev); }
DI float sinr(float rev) { return __builtin_amdgcn_sinf(rev); }
DI f2 lo2(f4 v) { return __builtin_shufflevector(v, v, 0, 1); }
DI f2 hi2(f4 v) { return __builtin_shufflevector(v, v, 2, 3); }
DI f2 fma2(f2 a, f2 b, f2 c) { return __builtin_elementwise_fma(a, b, c); }
DI u32 umin(u32 a, u32 b) { return a < b ? a : b; }
DI u32 umax(u32 a, u32 b) { return a > b ? a : b; }
DI u32 clean(u32 v) { return umin(v, SENT - 1); }
DI u32 max4(u4 v) { return umax(umax(umax(v[0], v[1]), v[2]), v[3]); }
#define Z4 ((f4){0, 0, 0, 0})
DI f4 mma_h(u4 a, u4 b, f4 c) { return __builtin_amdgcn_mfma_f32_16x16x32_f16(__builtin_bit_cast(h8, a), __builtin_bit_cast(h8, b), c, 0, 0, 0); }
DI f4 mma_b(u4 a, u4 b, f4 c) { return __builtin_amdgcn_mfma_f32_16x16x32_bf16(__builtin_bit_cast(b8, a), __builtin_bit_cast(b8, b), c, 0, 0, 0); }
DI f4 mma16_h(u2 a, u2 b, f4 c) { return __builtin_amdgcn_mfma_f32_16x16x16f16(__builtin_bit_cast(h4, a), __builtin_bit_cast(h4, b), c, 0, 0, 0); }
typedef __amdgpu_buffer_rsrc_t rsrc_t;
DI rsrc_t mkrs(const void* p, u32 bytes) { return __builtin_amdgcn_make_buffer_rsrc((void*)p, 0, bytes, 0x00020000); }
template <int AUX> DI u4 bld4(rsrc_t r, int v, int s) { return __builtin_amdgcn_raw_buffer_load_b128(r, v, s, AUX); }
template <int AUX> DI u2 bld2(rsrc_t r, int v, int s) { return __builtin_amdgcn_raw_buffer_load_b64(r, v, s, AUX); }
template <int AUX> DI void bst4(u4 d, rsrc_t r, int v, int s) {
    __builtin_amdgcn_raw_buffer_store_b128(d, r, v, s, AUX);
    asm volatile("s_nop 1" :: "v"(d));
}
template <int AUX> DI void bst2(u2 d, rsrc_t r, int v, int s) { __builtin_amdgcn_raw_buffer_store_b64(d, r, v, s, AUX); }

template <int CTRL> DI float dpp(float v) {
    return __builtin_bit_cast(float, __builtin_amdgcn_update_dpp(0, __builtin_bit_cast(int, v), CTRL, 0xf, 0xf, true));
}
template <bool ALL> DI float red_lanes(float v) {
    v += dpp<0xB1>(v);
    if (ALL) v += dpp<0x4E>(v);
    v += dpp<0x124>(v); v += dpp<0x128>(v);
    u32 x = __builtin_bit_cast(u32, v), y = x;
    asm("" : "+v"(y));
    auto r = __builtin_amdgcn_permlane16_swap(x, y, false, false);
    v = __builtin_bit_cast(float, (u32)r[0]) + __builtin_bit_cast(float, (u32)r[1]);
    x = __builtin_bit_cast(u32, v); y = x;
    asm("" : "+v"(y));
    auto q = __builtin_amdgcn_permlane32_swap(x, y, false, false);
    return __builtin_bit_cast(float, (u32)q[0]) + __builtin_bit_cast(float, (u32)q[1]);
}
DI float ln_inv(float var) { return __builtin_amdgcn_rcpf(rbf(__builtin_amdgcn_sqrtf(rbf(rbf(var) + 1e-5f)))); }

#define ST_DECL unsigned long long dg0_ = __builtin_amdgcn_s_memrealtime(), dg1_ = dg0_
#define ST(i) do { if (i == DIAG_PT) dg1_ = __builtin_amdgcn_s_memrealtime(); } while (0)
#define ST_END(ws, kid) do { WAITV(0); ST(22); unsigned long long e_ = __builtin_amdgcn_s_memrealtime() + (dg1_ - dg0_); if (aw) while (__builtin_amdgcn_s_memrealtime() < e_) __builtin_amdgcn_s_sleep(1); } while (0)

constexpr u32 WS_U = 0;
constexpr u32 WS_WT = WS_U + (2u << 20);
constexpr u32 WS_V = WS_WT + (2u << 20);
constexpr u32 WS_LN2 = WS_V + (2u << 20);
constexpr u32 WS_WA = WS_LN2 + (2u << 20);
constexpr u32 WS_WB = WS_WA + 8192u;
constexpr u32 WS_FA = WS_WB + 8192u;
constexpr u32 WS_FLAG = WS_FA + 24576u;
constexpr u32 WS_EP = WS_FLAG + 32768u;
constexpr u32 WS_H1 = WS_WA + (128u << 10);
constexpr u32 WS_END = WS_H1 + 7340032u;

#define W_EARLY 0
#define PRO_SLEEP 1
constexpr int BG2 = 4, BG4 = 10, BG5 = 15, BG6 = 21;
constexpr int G1_DEPTH = 10, G2_DEPTH = 3;
constexpr int B_R = 5;
constexpr int CODE_LINES = 290, CODE_PF = 10;
constexpr int LDS_SCR = 114688;
constexpr int SCR_X = 8448;
constexpr int SCR_S = SCR_X + 20480;
constexpr int SCR_F = 49024;
constexpr int XCH = 147456;
constexpr int LDS_B = 163840;

DI void glds16(const void* sbase, u32 voff, u32 lds_dst) {
    asm volatile("s_mov_b32 m0, %2\n\ts_nop 0\n\tglobal_load_lds_dwordx4 %0, %1" :: "v"(voff), "s"(sbase), "s"(lds_dst) : "memory");
}
DI bool any_sent(u32 v) { return __builtin_amdgcn_ballot_w64(v == SENT) != 0; }
DI void poll2(u4& a, u4& c, rsrc_t rw, int vo, int sa, int sc) {
#pragma nounroll
    for (int sp = 0; sp < SPIN_MAX; ++sp) {
        const bool ba = max4(a) == SENT, bc = max4(c) == SENT;
        if (__builtin_amdgcn_ballot_w64(ba || bc) == 0) break;
        __builtin_amdgcn_s_sleep(1);
        FENCE();
        if (ba) a = bld4<SC1>(rw, vo, sa);
        if (bc) c = bld4<SC1>(rw, vo, sc);
    }
}
DI void wait_tok(u4& t0, u4& t1, rsrc_t rw, int vo, int so) {
    if (any_sent(umax(max4(t0), max4(t1)))) {
#pragma nounroll
        for (int sp = 0; sp < SPIN_MAX; ++sp) {
            FENCE();
            t0 = bld4<SC1>(rw, vo, so); t1 = bld4<SC1>(rw, vo, so + 16384);
            if (!any_sent(umax(max4(t0), max4(t1)))) break;
        }
    }
}
DI u4 ldw1(LAS char* wb, int kk, int ft) {
    s4 a0 = __builtin_amdgcn_ds_read_tr16_b64_v4i16((LAS s4*)(wb + kk * 32 * 224 + ft * 32));
    s4 a1 = __builtin_amdgcn_ds_read_tr16_b64_v4i16((LAS s4*)(wb + (kk * 32 + 16) * 224 + ft * 32));
    u2 x0 = __builtin_bit_cast(u2, a0), x1 = __builtin_bit_cast(u2, a1);
    return (u4){x0[0], x0[1], x1[0], x1[1]};
}
DI u2 pk4(f4 a) { return (u2){pk_b(a[0], a[1]), pk_b(a[2], a[3])}; }
DI u2 silu_pk2(u32 p0, u32 p1) {
    u32 o0, o1; float a0, a1, a2, a3, t0, t1, t2, t3;
    asm("v_lshlrev_b32 %2, 16, %10\n\tv_and_b32 %3, 0xffff0000, %10\n\tv_lshlrev_b32 %4, 16, %11\n\tv_and_b32 %5, 0xffff0000, %11\n\t"
        "v_mul_f32 %6, %12, %2\n\tv_mul_f32 %7, %12, %3\n\tv_mul_f32 %8, %12, %4\n\tv_mul_f32 %9, %12, %5\n\t"
        "v_exp_f32 %6, %6\n\tv_exp_f32 %7, %7\n\tv_exp_f32 %8, %8\n\tv_exp_f32 %9, %9\n\t"
        "v_add_f32 %6, 1.0, %6\n\tv_add_f32 %7, 1.0, %7\n\tv_add_f32 %8, 1.0, %8\n\tv_add_f32 %9, 1.0, %9\n\t"
        "v_rcp_f32 %6, %6\n\tv_rcp_f32 %7, %7\n\tv_rcp_f32 %8, %8\n\tv_rcp_f32 %9, %9\n\t"
        "v_mul_f32 %2, %2, %6\n\tv_mul_f32 %3, %3, %7\n\tv_mul_f32 %4, %4, %8\n\tv_mul_f32 %5, %5, %9\n\t"
        "v_cvt_pk_bf16_f32 %0, %2, %3\n\tv_cvt_pk_bf16_f32 %1, %4, %5"
        : "=&v"(o0), "=&v"(o1), "=&v"(a0), "=&v"(a1), "=&v"(a2), "=&v"(a3), "=&v"(t0), "=&v"(t1), "=&v"(t2), "=&v"(t3)
        : "v"(p0), "v"(p1), "s"(-1.4426950408889634f));
    return (u2){o0, o1};
}
DI u4 silu_pk4(u4 p) { u2 a = silu_pk2(p[0], p[1]), b = silu_pk2(p[2], p[3]); return (u4){a[0], a[1], b[0], b[1]}; }
DI void lds_set(LAS u32* p, u32 v) {
    asm volatile("s_waitcnt lgkmcnt(0)" ::: "memory");
    __hip_atomic_store(p, v, __ATOMIC_RELAXED, __HIP_MEMORY_SCOPE_WORKGROUP);
}
DI void lds_wait(LAS u32* p, u32 v) {
#pragma nounroll
    for (int sp = 0; sp < SPIN_MAX; ++sp) {
        if (__builtin_amdgcn_readfirstlane(__hip_atomic_load(p, __ATOMIC_RELAXED, __HIP_MEMORY_SCOPE_WORKGROUP)) >= v) break;
        __builtin_amdgcn_s_sleep(1);
    }
    FENCE();
}
DI void flag_wait(GAS u32* p, u32 tag) {
#pragma nounroll
    for (int sp = 0; sp < SPIN_MAX; ++sp) {
        if (__builtin_amdgcn_ballot_w64(__hip_atomic_load(p, __ATOMIC_RELAXED, __HIP_MEMORY_SCOPE_AGENT) != tag) == 0) break;
        __builtin_amdgcn_s_sleep(2);
    }
    FENCE();
}
#define SENT4 ((u4){SENT, SENT, SENT, SENT})

__global__ __launch_bounds__(512, 2) void k_fused(const uint16_t* x, const uint16_t* wr, const uint16_t* wi, const uint16_t* w1,
                                                  const uint16_t* w2, uint16_t* out, char* ws) {
    extern __shared__ __attribute__((aligned(16))) char smem[];
    const int tid = threadIdx.x, l = tid & 63, g = l >> 4, m = l & 15;
    const int w = __builtin_amdgcn_readfirstlane(tid >> 6);
    const int b = (blockIdx.x & 7) * 32 + (blockIdx.x >> 3);
    const int mt1 = (b >> 6) * 4 + ((b >> 3) & 3), nt1 = ((b >> 5) & 1) * 8 + (b & 7);
    const int grp = b >> 4, th = (b >> 3) & 1, fb = b & 7;
    const rsrc_t rw = mkrs(ws, WS_END), ro = mkrs(out, 2097152);
    char* scr = smem + LDS_SCR;
    LAS u32* hw = (LAS u32*)((LAS char*)smem + LDS_SCR + SCR_F);
    const u32 lds0 = (u32)(uintptr_t)(LAS char*)smem;
    const float mf = (float)m, gf = (float)g, sg = (m & 1) ? -1.0f : 1.0f;
    const bool aw = w < 4;
    ST_DECL;
    unsigned long long pc0;
    asm volatile("s_getpc_b64 %0" : "=s"(pc0));
    if (tid >= 448 && l < CODE_PF)
        asm volatile("s_mov_b32 m0, %2\n\ts_nop 0\n\tglobal_load_lds_dword %0, %1"
                     :: "v"(umin((blockIdx.x >> 3) * CODE_PF + l, CODE_LINES - 1) * 128), "s"(pc0), "s"(lds0 + LDS_SCR + SCR_F + 64) : "memory");
    const u32 epoch = __hip_atomic_load((GAS u32*)(ws + WS_EP), __ATOMIC_RELAXED, __HIP_MEMORY_SCOPE_AGENT);
    FENCE();
    u32 xd[8];
    const rsrc_t rr = mkrs(wr, 1052672), ri = mkrs(wi, 1052672);
    const int j7 = l >> 3, p4 = (l >> 1) & 3, ee = l & 1;
    u2 fr[4], fi[4]; u32 sr_, si_;
    if (aw) {
        const u32* xr = (const u32*)(x + (256 * (2 * w + ((m >> 1) & 1)) + b) * 512) + 8 * g + 2 * (m >> 2) + (m & 1);
#pragma unroll
        for (int j = 0; j < 8; ++j) xd[j] = xr[32 * j];
    } else {
        __builtin_amdgcn_s_sleep(PRO_SLEEP);
        if (w == 4) {
            const int e = ee * 4 * 514 + (32 * j7 + 4 * p4) * 2;
#pragma unroll
            for (int i = 0; i < 4; ++i) { fr[i] = bld2<0>(rr, e + i * 514, 8 * b * 514); fi[i] = bld2<0>(ri, e + i * 514, 8 * b * 514); }
        } else if (w == 5) {
            const int e = (2045 - 8 * b) * 514 + 253 * 2 - ee * 4 * 514 - (32 * j7 + 4 * p4) * 2;
#pragma unroll
            for (int i = 1; i < 4; ++i) { fr[i] = bld2<0>(rr, e + (3 - i) * 514, 0); fi[i] = bld2<0>(ri, e + (3 - i) * 514, 0); }
            const int e0 = e + 3 * 514 - (b == 0 && !ee ? 2048 * 514 : 0);
            fr[0] = bld2<0>(rr, e0, 0); fi[0] = bld2<0>(ri, e0, 0);
            const int e1 = (8 * b + (l & 7)) * 514 + (16 + 32 * j7) * 2;
            sr_ = __builtin_amdgcn_raw_buffer_load_b16(rr, e1, 0, 0); si_ = __builtin_amdgcn_raw_buffer_load_b16(ri, e1, 0, 0);
        } else if (w == 6) {
            const int c = j7 & 3, f = 8 * b + (l & 7);
            const int e = ((c & 1) ? (2048 - f) & 2047 : f) * 514 + (c >> 1) * 512;
            sr_ = __builtin_amdgcn_raw_buffer_load_b16(rr, e, 0, 0); si_ = __builtin_amdgcn_raw_buffer_load_b16(ri, e, 0, 0);
        }
        FENCE();
        bst4<SC1>(SENT4, rw, l * 16, WS_U + b * 8192 + (w - 4) * 2048); bst4<SC1>(SENT4, rw, l * 16 + 1024, WS_U + b * 8192 + (w - 4) * 2048);
    }
    FENCE();
    const int rq = ((b >> 3) + b) & 3;
    const int row0 = (tid & 255) / 14, cch0 = (tid & 255) - row0 * 14;
    const char* wsrc = (const char*)w1 + nt1 * 224;
    u32 w1o[7];
    auto w1_burst = [&](int i0, int n) {
#pragma unroll
        for (int i = i0; i < i0 + n; ++i) {
            const int q = (i / 7 + rq) & 3, j = i % 7;
            glds16(wsrc + q * (128 * 3584), w1o[j], lds0 + (w - 4) * 1024 + q * 28672 + j * 4096);
        }
    };
    const int xoff = (256 * w + b) * 1024 + m * 32 + g * 8;
    u2 xl[2];
    float c16[4], s16[4];
    f2 twc[2], tws[2];
    u4 Gre3, Gim3, SGre, SGim, GA[2];
    auto consts0 = [&]() {
#pragma unroll
        for (int u = 0; u < 4; ++u) {
            float a = (4.0f * gf + u) * (mf * (1.0f / 16));
            c16[u] = cosr(a); s16[u] = sinr(a);
            twc[u >> 1][u & 1] = cosr(a * (1.0f / 32)); tws[u >> 1][u & 1] = sinr(a * (1.0f / 32));
        }
    };
    auto consts3 = [&]() {
#pragma unroll
        for (int u = 0; u < 4; ++u) {
            const float k = 4.0f * gf + u;
            const bool k0 = u == 0 && g == 0;
            float cc = c16[u], ss = s16[u];
            Gre3[u] = pk_b(cc, -ss); Gim3[u] = pk_b(ss, cc);
            float a2 = (2.0f * k + 1.0f) * (mf * (1.0f / 32));
            u32 sp = pk_b(g < 2 ? 2.f * cc : 2.f * sg * cosr(a2), g < 2 ? -2.f * ss : -2.f * sg * sinr(a2));
            SGre[u] = k0 ? pk_b(1.0f, sg) : (g < 2 ? sp : 0u);
            SGim[u] = g < 2 ? 0u : sp;
            float a4 = k * (mf * (1.0f / 32)), c4 = 2.f * cosr(a4), s4_ = -2.f * sinr(a4);
            GA[0][u] = k0 ? pk_b(1.0f, sg) : pk_b(c4, s4_);
            GA[1][u] = k0 ? pk_b(1.0f, sg) : ((u & 1) ? pk_b(-c4, -s4_) : pk_b(c4, s4_));
        }
        PIN(Gre3); PIN(Gim3); PIN(SGre); PIN(SGim); PIN(GA[0]); PIN(GA[1]); PIN(twc[0]); PIN(twc[1]); PIN(tws[0]); PIN(tws[1]);
    };
    const int pw = (w + 4 * th) & 7;
    GAS u32* fl = (GAS u32*)(ws + WS_FLAG);
    int ko[7];
    {
        const int r6 = (fb < 6 ? fb : fb - 5) + 3 * (grp & 1);
#pragma unroll
        for (int ks = 0; ks < 6; ++ks) { int k = ks + r6; k = k >= 6 ? k - 6 : k; ko[ks] = k >= 6 ? k - 6 : k; }
        ko[6] = 6;
    }
    u4 W[7][4];
    const u32 w2vo = (l >> 3) * 1024 + (((l & 7) ^ (((l >> 4) & 1) << 2)) * 16);
    auto w2_glds = [&](int p_, int ks, u32 dst) {
        const int q = ko[ks];
        const char* src = (const char*)w2 + (224 * p_ + (ks == 6 ? 96 : (q < 3 ? 32 * q : 16 + 32 * q))) * 1024 + fb * 128;
        glds16(src, w2vo, dst); glds16(src + 8192, w2vo, dst + 1024);
        glds16(src + (ks == 6 ? 112 : 16) * 1024, w2vo, dst + 2048); glds16(src + (ks == 6 ? 120 : 24) * 1024, w2vo, dst + 3072);
    };
    LAS char* w2rd = (LAS char*)smem + (4 * g + (m >> 2)) * 128 + (((m & 3) ^ (((m >> 3) & 1) << 2)) * 16);
    auto w2_read = [&](u4 (&f)[4], int off) {
#pragma unroll
        for (int t = 0; t < 4; ++t) {
            LAS char* p = (LAS char*)((u32)(uintptr_t)w2rd ^ ((t >> 1) * 64)) + off + (t & 1) * 8;
            u2 x0 = __builtin_bit_cast(u2, __builtin_amdgcn_ds_read_tr16_b64_v4i16((LAS s4*)p));
            u2 x1 = __builtin_bit_cast(u2, __builtin_amdgcn_ds_read_tr16_b64_v4i16((LAS s4*)(p + 2048)));
            f[t] = (u4){x0[0], x0[1], x1[0], x1[1]};
        }
    };
    auto bg = [&](int i0, int i1) { w1_burst(i0, i1 - i0); };
    u32 tag;
    if (__builtin_expect(aw, 1)) {
        if (tid < 16) hw[tid] = 0;
        FENCE();
        ST(2);
        {
            u4 FAre, FAim, Fre, Fim, Sre, Sim; u2 Mb; f2 pc[2], ps[2];
            consts0();
            float c0[8], s0[8];
#pragma unroll
            for (int j = 0; j < 8; ++j) {
                float a = (gf + 4.0f * j) * (mf * (1.0f / 32));
                c0[j] = cosr(a); s0[j] = m ? -sinr(a) : ((g & 1) ? -1.0f : 1.0f);
            }
            const float mo = (float)(2 * (m & 7) + 1) * (1.0f / 32);
#pragma unroll
            for (int u = 0; u < 4; ++u) {
                FAre[u] = pk_h(c0[2 * u], c0[2 * u + 1]); FAim[u] = pk_h(s0[2 * u], s0[2 * u + 1]);
                float cc = c16[u], ss = s16[u];
                Fre[u] = pk_h(cc, ss); Fim[u] = pk_h(-ss, cc);
                float a2 = (4.0f * gf + u) * mo;
                u32 re = pk_h(m < 8 ? cc : cosr(a2), 0.f), im = pk_h(m < 8 ? (m ? -ss : ((u & 1) ? -1.0f : 1.0f)) : -sinr(a2), 0.f);
                Sre[u] = m < 8 ? re : re << 16; Sim[u] = m < 8 ? im : im << 16;
                pc[u & 1][u >> 1] = twc[u >> 1][u & 1]; ps[u & 1][u >> 1] = tws[u >> 1][u & 1];
            }
#pragma unroll
            for (int e = 0; e < 2; ++e) {
                float a = (float)(((m >> 1) * (256 * (2 * g + e) + b)) & 2047) * (1.0f / 2048);
                float cc = cosr(a), ss = sinr(a);
                Mb[e] = (m & 1) ? pk_h(-ss, cc) : pk_h(cc, ss);
            }
            PIN(FAre); PIN(FAim); PIN(Fre); PIN(Fim); PIN(Sre); PIN(Sim); PIN(Mb); PIN(pc[0]); PIN(pc[1]); PIN(ps[0]); PIN(ps[1]);
            ST(3);
            f2 v[8];
#pragma unroll
            for (int j = 0; j < 8; ++j) v[j] = bf_2(xd[j]);
            f2 s2 = ((v[0] + v[1]) + (v[2] + v[3])) + ((v[4] + v[5]) + (v[6] + v[7]));
            const float mean = red_lanes<false>(s2[0] + s2[1]) * (1.0f / 512);
            f2 q0 = {0.f, 0.f}, q1 = {0.f, 0.f};
#pragma unroll
            for (int j = 0; j < 8; ++j) { v[j] -= mean; if (j & 1) q1 = fma2(v[j], v[j], q1); else q0 = fma2(v[j], v[j], q0); }
            q0 += q1;
            const float inv = ln_inv(red_lanes<false>(q0[0] + q0[1]) * (1.0f / 512));
            const float off = (mean - rbf(mean)) * inv;
#pragma unroll
            for (int j = 0; j < 8; ++j) v[j] = fma2(v[j], (f2){inv, inv}, (f2){off, off});
            u4 B[2];
#pragma unroll
            for (int p = 0; p < 2; ++p) {
                u4 A = {pk_h(v[0][p], v[1][p]), pk_h(v[2][p], v[3][p]), pk_h(v[4][p], v[5][p]), pk_h(v[6][p], v[7][p])};
                f4 Dre = mma_h(A, FAre, Z4), Dim = mma_h(A, FAim, Z4);
#pragma unroll
                for (int r = 0; r < 2; ++r) {
                    f2 re = r ? hi2(Dre) : lo2(Dre), im = r ? hi2(Dim) : lo2(Dim);
                    f2 tr = fma2(im, ps[p], re * pc[p]), ti = fma2(-re, ps[p], im * pc[p]);
                    B[r][p] = pk_h(tr[0], ti[0]); B[r][2 + p] = pk_h(tr[1], ti[1]);
                }
            }
#pragma unroll
            for (int r = 0; r < 2; ++r) {
                f4 Gre = mma_h(Fre, B[r], Z4), Gim = mma_h(Fim, B[r], Z4), Hre = mma_h(Sre, B[r], Z4), Him = mma_h(Sim, B[r], Z4);
                u4 Z;
#pragma unroll
                for (int i = 0; i < 4; ++i) Z[i] = pk_h(m ? Gre[i] : Hre[i], m ? Gim[i] : Him[i]);
                *(u4*)(scr + (2 * w + r) * 1024 + m * 64 + g * 16) = Z;
            }
            BARRIER();
            ST(18);
            u4 r0 = *(const u4*)(scr + (2 * g) * 1024 + w * 256 + m * 16);
            u4 r1 = *(const u4*)(scr + (2 * g + 1) * 1024 + w * 256 + m * 16);
#pragma unroll
            for (int t = 0; t < 4; ++t) {
                f4 D = mma16_h(Mb, (u2){r0[t], r1[t]}, Z4);
                *(u2*)(scr + SCR_S + w * 2048 + m * 128 + (((2 * t + (g >> 1)) ^ (m >> 1)) * 16) + (g & 1) * 8) =
                    (u2){clean(pk_h(D[0], D[1])), clean(pk_h(D[2], D[3]))};
            }
        }
        BARRIER();
        ST(4);
        u4 Fre, Fim, Gre, Gim, N0, N1; f2 tc[2], ts[2];
#pragma unroll
        for (int u = 0; u < 4; ++u) {
            float cc = c16[u], ss = s16[u];
            Fre[u] = pk_h(0.25f * cc, 0.25f * ss); Fim[u] = pk_h(-0.25f * ss, 0.25f * cc);
            Gre[u] = pk_b(cc * (1.0f / 256), -ss * (1.0f / 256)); Gim[u] = pk_b(ss * (1.0f / 256), cc * (1.0f / 256));
            float a3 = (4.0f * gf + u) * (mf * (1.0f / 256));
            tc[u >> 1][u & 1] = cosr(a3); ts[u >> 1][u & 1] = sinr(a3);
        }
        PIN(Fre); PIN(Fim); PIN(Gre); PIN(Gim); PIN(tc[0]); PIN(tc[1]); PIN(ts[0]); PIN(ts[1]);
        xl[0] = *(const u2*)((const char*)x + xoff); xl[1] = *(const u2*)((const char*)x + xoff + 512);
        FENCE();
        tag = __builtin_amdgcn_readfirstlane(epoch) + 1u;
        consts3();
#pragma unroll
        for (int u = 0; u < 4; ++u) {
            float a1 = (float)(((4 * (g & 1) + u) * (256 * (m >> 1) + b)) & 2047) * (1.0f / 2048);
            float c1 = cosr(a1), s1 = sinr(a1);
            u32 nb = (m & 1) ? pk_b(s1, c1) : pk_b(c1, -s1);
            N0[u] = g < 2 ? nb : 0u; N1[u] = g < 2 ? 0u : nb;
        }
        PIN(N0); PIN(N1);
        ST(19);
        BARRIER();
        ST(5);
        u4 d[2], f[2], e[2];
        const int uo = (tid >> 1) * 8192 + (tid & 1) * 16;
        d[0] = bld4<SC1>(rw, uo, WS_U + b * 32); d[1] = bld4<SC1>(rw, uo, WS_U + 128 * 8192 + b * 32);
        const u32 fd0 = __builtin_amdgcn_raw_buffer_load_b32(rw, uo, WS_WT + b * 32, SC1);
        const u32 fd1 = __builtin_amdgcn_raw_buffer_load_b32(rw, uo, WS_WT + 128 * 8192 + b * 32, SC1);
        FENCE();
        poll2(d[0], d[1], rw, uo, WS_U + b * 32, WS_U + 128 * 8192 + b * 32);
        char* sp = scr + SCR_S + (tid ^ ((tid >> 4) & 1)) * 16;
        *(u4*)sp = d[0]; *(u4*)(sp + 4096) = d[1];
        BARRIER();
        f[0] = bld4<SC1>(rw, uo, WS_WT + b * 32); f[1] = bld4<SC1>(rw, uo, WS_WT + 128 * 8192 + b * 32);
        if (b == 0) {
            f[0] = bld4<SC1>(rw, tid * 16, WS_WA); f[1] = bld4<SC1>(rw, tid * 16 + 4096, WS_WA);
            e[0] = bld4<SC1>(rw, tid * 16, WS_WB); e[1] = bld4<SC1>(rw, tid * 16 + 4096, WS_WB);
            if (tid == 0) __hip_atomic_store((GAS u32*)(ws + WS_EP), tag, __ATOMIC_RELAXED, __HIP_MEMORY_SCOPE_AGENT);
        }
        ST(6);
        u2 ld[4], wl[4];
        const int so = (64 * g + m) * 32 + (((w >> 1) ^ (m >> 3)) * 16) + (w & 1) * 8;
#pragma unroll
        for (int q = 0; q < 4; ++q) ld[q] = *(const u2*)(scr + SCR_S + so + 512 * q);
        FENCE();
        f4 Xre[2], Xim[2];
#pragma unroll
        for (int e = 0; e < 2; ++e) {
            u4 A = {ld[0][e], ld[1][e], ld[2][e], ld[3][e]};
            f4 Dre = mma_h(A, Fre, Z4), Dim = mma_h(A, Fim, Z4);
            f2 r0 = fma2(lo2(Dim), ts[0], lo2(Dre) * tc[0]), i0 = fma2(-lo2(Dre), ts[0], lo2(Dim) * tc[0]);
            f2 r1 = fma2(hi2(Dim), ts[1], hi2(Dre) * tc[1]), i1 = fma2(-hi2(Dre), ts[1], hi2(Dim) * tc[1]);
            u4 B = {pk_h(r0[0], i0[0]), pk_h(r0[1], i0[1]), pk_h(r1[0], i1[0]), pk_h(r1[1], i1[1])};
            Xre[e] = mma_h(Fre, B, Z4); Xim[e] = mma_h(Fim, B, Z4);
        }
        *(u4*)(sp + 8192) = f[0]; *(u4*)(sp + 12288) = f[1];
        asm volatile("" :: "v"(fd0), "v"(fd1));
        f2* sx = (f2*)(scr + SCR_X);
        if (b == 0) {
            *(u4*)(sp - SCR_S) = e[0]; *(u4*)(sp - SCR_S + 4096) = e[1];
#pragma unroll
            for (int e = 0; e < 2; ++e)
#pragma unroll
                for (int i = 0; i < 4; ++i) sx[2 * w + e + 8 * (m + 16 * (4 * g + i))] = (f2){Xre[e][i], Xim[e][i]};
        }
        BARRIER();
        ST(20);
#pragma unroll
        for (int q = 0; q < 4; ++q) wl[q] = *(const u2*)(scr + SCR_S + so + 8192 + 512 * q);
        FENCE();
        f2 yr[2][2], yi[2][2];
#pragma unroll
        for (int e = 0; e < 2; ++e)
#pragma unroll
            for (int hh = 0; hh < 2; ++hh) {
                f2 wre = {bf_lo(wl[2 * hh][e]), bf_lo(wl[2 * hh + 1][e])}, wim = {bf_hi(wl[2 * hh][e]), bf_hi(wl[2 * hh + 1][e])};
                f2 xr = hh ? hi2(Xre[e]) : lo2(Xre[e]), xi = hh ? hi2(Xim[e]) : lo2(Xim[e]);
                yr[e][hh] = fma2(-xi, wim, xr * wre); yi[e][hh] = fma2(xr, wim, xi * wre);
            }
        if (b == 0) {
#pragma unroll
            for (int i = 0; i < 4; ++i) {
                u2 wb = *(const u2*)(scr + so + 512 * i);
#pragma unroll
                for (int e = 0; e < 2; ++e) {
                    f2 xm = sx[(2048 - (2 * w + e + 8 * (m + 16 * (4 * g + i)))) & 2047];
                    float br = bf_lo(wb[e]), bi = bf_hi(wb[e]);
                    yr[e][i >> 1][i & 1] += xm[0] * br + xm[1] * bi;
                    yi[e][i >> 1][i & 1] += xm[0] * bi - xm[1] * br;
                }
            }
        }
        char* pv = scr + SCR_S;
#pragma unroll
        for (int e = 0; e < 2; ++e) {
            u4 Y = {pk_b(yr[e][0][0], yi[e][0][0]), pk_b(yr[e][0][1], yi[e][0][1]), pk_b(yr[e][1][0], yi[e][1][0]), pk_b(yr[e][1][1], yi[e][1][1])};
            f4 Dre = mma_b(Y, Gre, Z4), Dim = mma_b(Y, Gim, Z4);
            f2 r0 = fma2(-lo2(Dim), ts[0], lo2(Dre) * tc[0]), i0 = fma2(lo2(Dre), ts[0], lo2(Dim) * tc[0]);
            f2 r1 = fma2(-hi2(Dim), ts[1], hi2(Dre) * tc[1]), i1 = fma2(hi2(Dre), ts[1], hi2(Dim) * tc[1]);
            u4 A = {pk_b(r0[0], i0[0]), pk_b(r0[1], i0[1]), pk_b(r1[0], i1[0]), pk_b(r1[1], i1[1])};
            f4 Ere = mma_b(A, Gre, Z4), Eim = mma_b(A, Gim, Z4);
            u4 o;
#pragma unroll
            for (int i = 0; i < 4; ++i) o[i] = clean(pk_b(Ere[i], Eim[i]));
            *(u4*)(pv + (2 * w + e) * 1024 + ((m * 4 + g) ^ ((w >> 1) * 4)) * 16) = o;
        }
        BARRIER();
        ST(7);
#pragma unroll
        for (int it = 0; it < 2; ++it) {
            const int ch = tid + 256 * it;
            const u32* q = (const u32*)(pv + (ch & 1) * 4096 + (((ch >> 1) * 4) ^ ((ch & 1) * 64)));
            bst4<SC1>((u4){q[0], q[256], q[512], q[768]}, rw, ch * 16, WS_V + b * 8192);
        }
        WAITV(0);
        BARRIER();
        ST(8);
        const int vo = (2 * m + (g >> 1)) * 8192 + (g & 1) * 16, vs = WS_V + w * 64 * 8192 + b * 32;
        u4 v0 = bld4<SC1>(rw, vo, vs), v1 = bld4<SC1>(rw, vo, vs + 32 * 8192);
        poll2(v0, v1, rw, vo, vs, vs + 32 * 8192);
        ST(9);
        char* sz = scr + SCR_X + g * 2048 + w * 256 + m * 8;
#pragma unroll
        for (int hh = 0; hh < 2; ++hh) {
            f4 D0 = mma_b(N0, hh ? v1 : v0, Z4), D1 = mma_b(N1, hh ? v1 : v0, Z4);
            *(u2*)(sz + hh * 128) = (u2){pk_b(D0[0], D0[1]), pk_b(D1[0], D1[1])};
            *(u2*)(sz + 1024 + hh * 128) = (u2){pk_b(D0[2], D0[3]), pk_b(D1[2], D1[3])};
        }
    } else {
        auto announce = [&](int which) {
            if (l < 8) __hip_atomic_store((GAS u32*)(ws + WS_FA) + which * 2048 + l * 256 + b, tag, __ATOMIC_RELAXED, __HIP_MEMORY_SCOPE_AGENT);
            FENCE();
        };
        auto all_announced = [&](int which) {
#pragma nounroll
            for (int sp = 0; sp < SPIN_MAX; ++sp) {
                FENCE();
                u4 f = bld4<SC1>(rw, l * 16, WS_FA + which * 8192 + (b >> 5) * 1024);
                if (__builtin_amdgcn_ballot_w64(f[0] != tag || f[1] != tag || f[2] != tag || f[3] != tag) == 0) break;
                __builtin_amdgcn_s_sleep(4);
            }
        };
        FENCE();
        ST(2);
#pragma unroll
        for (int jj = 0; jj < 7; ++jj) {
            const int cj_ = (4 * jj) % 14, rj = 18 * jj + (4 * jj) / 14;
            w1o[jj] = (row0 + rj) * 3584 + (cch0 + cj_) * 16 + (cch0 + cj_ >= 14 ? 3584 - 224 : 0);
            PIN(w1o[jj]);
        }
        tag = __builtin_amdgcn_readfirstlane(epoch) + 1u;
        auto count_in = [&](int word, u32 last, int which) {
            u32 prev = 0;
            if (l == 0) prev = __hip_atomic_fetch_add(hw + word, 1u, __ATOMIC_RELAXED, __HIP_MEMORY_SCOPE_WORKGROUP);
            if (__builtin_amdgcn_readfirstlane(prev) == last) announce(which);
        };
        WAITV(0);
        if (l < 2) __hip_atomic_store((GAS u32*)(ws + WS_FA) + (2 * (w - 4) + l) * 256 + b, tag, __ATOMIC_RELAXED, __HIP_MEMORY_SCOPE_AGENT);
        FENCE();
        if (!W_EARLY) BARRIER();
        if (w < 6) {
            const int wo = p4 * 2048 + j7 * 32 + ee * 16, wso = WS_WT + b * 8192 + (w - 4) * 256;
#pragma unroll
            for (int c = 0; c < 4; ++c) {
                u4 o;
#pragma unroll
                for (int i = 0; i < 4; ++i)
                    o[i] = w == 4 ? __builtin_amdgcn_perm(fi[i][c >> 1], fr[i][c >> 1], (c & 1) ? 0x07060302u : 0x05040100u)
                                  : __builtin_amdgcn_perm(fi[i][1 - (c >> 1)], fr[i][1 - (c >> 1)], (c & 1) ? 0x05040100u : 0x07060302u) ^ 0x80000000u;
                bst4<SC1>(o, rw, wo + c * 512, wso);
            }
            if (w == 5) __builtin_amdgcn_raw_buffer_store_b32(sr_ | (si_ << 16), rw, l * 4, wso, SC1);
        } else if (w == 6) {
            float re = bf_lo(sr_), im = bf_lo(si_);
            re += dpp<0x128>(re); im -= dpp<0x128>(im);
            u32 a = __builtin_bit_cast(u32, re), a2 = a, c = __builtin_bit_cast(u32, im), c2 = c;
            asm("" : "+v"(a2), "+v"(c2));
            auto pr = __builtin_amdgcn_permlane16_swap(a, a2, false, false);
            auto pi = __builtin_amdgcn_permlane16_swap(c, c2, false, false);
            const float ar = __builtin_bit_cast(float, (u32)pr[0]), br = __builtin_bit_cast(float, (u32)pr[1]);
            const float ai = __builtin_bit_cast(float, (u32)pi[0]), bi = __builtin_bit_cast(float, (u32)pi[1]);
            if (l < 8) {
                __builtin_amdgcn_raw_buffer_store_b32(pk_b(0.25f * (ar + br), 0.25f * (ai + bi)), rw, l * 4, WS_WA + b * 32, SC1);
                __builtin_amdgcn_raw_buffer_store_b32(pk_b(0.25f * (ar - br), 0.25f * (ai - bi)), rw, l * 4, WS_WB + b * 32, SC1);
            }
        }
        ST(18);
        if (W_EARLY) BARRIER();
        BARRIER();
        {
            const char* up = scr + SCR_S + (w - 4) * 2048;
            bst4<SC1>(*(const u4*)(up + (l ^ ((l >> 4) & 7)) * 16), rw, l * 16, WS_U + b * 8192 + (w - 4) * 2048);
            bst4<SC1>(*(const u4*)(up + 1024 + (l ^ (((l >> 4) + 4) & 7)) * 16), rw, l * 16 + 1024, WS_U + b * 8192 + (w - 4) * 2048);
        }
        if (w < 7) { WAITV(2); count_in(13, 2u, 2); }
        else all_announced(0);
        WAITV(0);
        ST(19);
        xl[0] = *(const u2*)((const char*)x + xoff); xl[1] = *(const u2*)((const char*)x + xoff + 512);
        FENCE();
        bg(0, BG2);
        ST(4);
        BARRIER();
        __builtin_amdgcn_s_sleep(2);
#pragma unroll
        for (int i = 0; i < 2; ++i) bst4<SC1>(SENT4, rw, l * 16 + i * 1024, WS_V + b * 8192 + (w - 4) * 2048);
        FENCE();
        consts0();
        consts3();
        WAITV(0);
        if (w == 7) all_announced(2);
        ST(5);
        BARRIER();
        if (w == 7) announce(1);
#pragma unroll
        for (int i = 0; i < 2; ++i) bst4<SC1>(SENT4, rw, (l >> 3) * 256 + (l & 7) * 16 + i * 2048, WS_LN2 + (b >> 1) * 16384 + (b & 1) * 128 + (w - 4) * 4096);
        bg(BG2, BG4);
        BARRIER();
        bg(BG4, BG5);
        WAITV(BG5 - BG2);
        ST(6);
        BARRIER();
        if (w == 7) all_announced(1);
        else __builtin_amdgcn_s_sleep(3);
        bg(BG5, BG6);
        ST(7);
        BARRIER();
    }
    BARRIER();
    {
        u4 A = *(const u4*)(scr + SCR_X + w * 1024 + m * 64 + g * 16);
        f4 Dre = mma_b(A, Gre3, Z4), Dim = mma_b(A, Gim3, Z4), Hre = mma_b(A, SGre, Z4), Him = mma_b(A, SGim, Z4);
        if (g == 0) { Dre[0] = Hre[0]; Dim[0] = Him[0]; }
        f2 r0 = fma2(-lo2(Dim), tws[0], lo2(Dre) * twc[0]), i0 = fma2(lo2(Dre), tws[0], lo2(Dim) * twc[0]);
        f2 r1 = fma2(-hi2(Dim), tws[1], hi2(Dre) * twc[1]), i1 = fma2(hi2(Dre), tws[1], hi2(Dim) * twc[1]);
        u4 A2 = {pk_b(r0[0], i0[0]), pk_b(r0[1], i0[1]), pk_b(r1[0], i1[0]), pk_b(r1[1], i1[1])};
        f2 h[4]; u2 hp[2];
#pragma unroll
        for (int t = 0; t < 2; ++t) {
            f4 Yv = mma_b(A2, GA[t], Z4);
            u32 h0 = pk_b2(bf_2(xl[t][0]) + bf_2(pk_b(Yv[0], Yv[1]))), h1 = pk_b2(bf_2(xl[t][1]) + bf_2(pk_b(Yv[2], Yv[3])));
            hp[t] = (u2){h0, h1};
            h[2 * t] = bf_2(h0); h[2 * t + 1] = bf_2(h1);
        }
        f2 s2 = (h[0] + h[1]) + (h[2] + h[3]);
        const float mean = red_lanes<true>(s2[0] + s2[1]) * (1.0f / 512);
        f2 q2 = {0.f, 0.f};
#pragma unroll
        for (int j = 0; j < 4; ++j) { f2 d = h[j] - mean; q2 = fma2(d, d, q2); }
        const float inv = ln_inv(red_lanes<true>(q2[0] + q2[1]) * (1.0f / 512));
        const float mb = rbf(mean);
#pragma unroll
        for (int t = 0; t < 2; ++t)
            *(u2*)(scr + ((8 * t + (m >> 1)) * 4 + g) * 128 + (w ^ (m >> 1)) * 16 + (m & 1) * 8) =
                (u2){clean(pk_b2(bf_2(pk_b2(h[2 * t] - mb)) * inv)), clean(pk_b2(bf_2(pk_b2(h[2 * t + 1] - mb)) * inv))};
        WAITV(0);
        BARRIER();
        u4 lo = *(const u4*)(scr + (tid ^ ((tid >> 5) & 7)) * 16);
        bst4<SC1>(lo, rw, (tid >> 3) * 256 + (tid & 7) * 16, WS_LN2 + (b >> 1) * 16384 + (b & 1) * 128);
        FENCE();
        if (aw) WAITV(0); else __builtin_amdgcn_s_sleep(8);
        bst2<SC1>(hp[0], ro, xoff, 0); bst2<SC1>(hp[1], ro, xoff + 512, 0);
        FENCE();
        ST(10);
    }
    auto stage_out = [&](int r) {
        w2_read(W[r], LDS_SCR + (w & 3) * 8192 + (r & 1) * 4096);
        asm volatile("s_waitcnt lgkmcnt(0)" ::: "memory");
        PIN(W[r][0]); PIN(W[r][1]); PIN(W[r][2]); PIN(W[r][3]);
        if (r + 2 < 7) w2_glds(pw, r + 2, lds0 + LDS_SCR + (w & 3) * 8192 + (r & 1) * 4096);
    };
    auto publish = [&]() {
        u32 prev = 0;
        if (l == 0) prev = __hip_atomic_fetch_add(hw + 4, 1u, __ATOMIC_RELAXED, __HIP_MEMORY_SCOPE_WORKGROUP);
        if (__builtin_amdgcn_readfirstlane(prev) == 7u && l < 2)
            __hip_atomic_store(fl + (l ? 4096 + (b >> 1) * 32 + (b & 1) : (mt1 * 8 + (nt1 >> 1)) * 32 + (nt1 & 1)), tag, __ATOMIC_RELAXED, __HIP_MEMORY_SCOPE_AGENT);
        FENCE();
    };
    {
        const int tg = w & 3;
        char* xch = smem + XCH + tg * 3072;
        const int hoff = WS_H1 + (mt1 * 8 + 2 * tg) * (56 * 1024) + (nt1 >> 1) * 7168;
        if (__builtin_expect(aw, 1)) {
            const int toff = WS_LN2 + (mt1 * 8 + 2 * tg) * 16384;
            LAS char* wb0 = (LAS char*)smem + (4 * g + (m >> 2)) * 224 + (m & 3) * 8;
            LAS char* wb[4];
            int tq[4];
#pragma unroll
            for (int q = 0; q < 4; ++q) { const int qq = (q + rq) & 3; wb[q] = wb0 + qq * 28672; tq[q] = toff + qq * 4096; }
            u4 T[2][16], A[2][7];
            auto ldt = [&](int kk) {
#pragma unroll
                for (int tt = 0; tt < 2; ++tt) T[tt][kk] = bld4<SC1>(rw, l * 16 + (kk & 3) * 1024, tq[kk >> 2] + tt * 16384);
            };
#pragma unroll
            for (int i = 0; i < 7; ++i) A[0][i] = ldw1(wb[0], 0, i);
            if (l == 0) __hip_atomic_fetch_add(hw + 5, 1u, __ATOMIC_RELAXED, __HIP_MEMORY_SCOPE_WORKGROUP);
            ST(11);
#pragma unroll
            for (int kk = 0; kk < G1_DEPTH; ++kk) { ldt(kk); SCHED(); }
            f4 acc[2][7];
#pragma unroll
            for (int tt = 0; tt < 2; ++tt)
#pragma unroll
                for (int i = 0; i < 7; ++i) acc[tt][i] = Z4;
#pragma unroll
            for (int kk = 0; kk < 16; ++kk) {
                if ((kk & 1) == 0) {
                    if (__builtin_expect(any_sent(umax(umax(max4(T[0][kk]), max4(T[1][kk])), umax(max4(T[0][kk + 1]), max4(T[1][kk + 1])))), 0)) {
                        wait_tok(T[0][kk], T[1][kk], rw, l * 16, tq[kk >> 2] + (kk & 3) * 1024);
                        wait_tok(T[0][kk + 1], T[1][kk + 1], rw, l * 16, tq[kk >> 2] + ((kk + 1) & 3) * 1024);
                    }
                    if (kk == 0) ST(12);
                    if (kk == (BG6 / 7) * 4 - 2) lds_wait(hw + 5, 8u);
                }
                if (kk + G1_DEPTH < 16) ldt(kk + G1_DEPTH);
                if (kk < 15) {
#pragma unroll
                    for (int i = 0; i < 7; ++i) A[(kk + 1) & 1][i] = ldw1(wb[(kk + 1) >> 2], (kk + 1) & 3, i);
                }
#pragma unroll
                for (int i = 0; i < 7; ++i)
#pragma unroll
                    for (int tt = 0; tt < 2; ++tt) acc[tt][i] = mma_b(A[kk & 1][i], T[tt][kk], acc[tt][i]);
#pragma unroll
                for (int i = 0; i < 14; ++i) {
                    __builtin_amdgcn_sched_group_barrier(0x8, 1, 0);
                    if (i == 1 || i == 7) __builtin_amdgcn_sched_group_barrier(0x20, 1, 0);
                    __builtin_amdgcn_sched_group_barrier(0x100, 1, 0);
                    if ((kk & 1) && i >= 3) __builtin_amdgcn_sched_group_barrier(0x2, 1, 0);
                }
                if (kk & 1) {} else SCHED();
            }
#pragma unroll
            for (int q = 0; q < 3; ++q) {
                u2 p0 = pk4(acc[1][2 * q]), p1 = pk4(acc[1][2 * q + 1]);
                *(u4*)(xch + q * 1024 + l * 16) = (u4){p0[0], p0[1], p1[0], p1[1]};
            }
            lds_set(hw + tg, 1u);
            ST(13);
#pragma unroll
            for (int q = 0; q < 3; ++q) {
                u2 p0 = pk4(acc[0][2 * q]), p1 = pk4(acc[0][2 * q + 1]);
                bst4<SC1>(silu_pk4((u4){p0[0], p0[1], p1[0], p1[1]}), rw, l * 16, hoff + ((nt1 & 1) * 3 + q) * 1024);
            }
#pragma unroll
            for (int tt = 0; tt < 2; ++tt) { u2 p = pk4(acc[tt][6]); bst2<SC1>(silu_pk2(p[0], p[1]), rw, l * 8, hoff + tt * (56 * 1024) + 6144 + (nt1 & 1) * 512); }
        } else {
            bg(BG6, 28);
            WAITV(0);
            if (l == 0) __hip_atomic_fetch_add(hw + 5, 1u, __ATOMIC_RELAXED, __HIP_MEMORY_SCOPE_WORKGROUP);
            __builtin_amdgcn_s_sleep(8);
            lds_wait(hw + 5, 8u);
            w2_glds(pw, 0, lds0 + LDS_SCR + tg * 8192);
            w2_glds(pw, 1, lds0 + LDS_SCR + tg * 8192 + 4096);
#pragma unroll
            for (int r = 0; r < B_R; ++r) { WAITV(4); stage_out(r); }
            ST(12);
            lds_wait(hw + tg, 1u);
            ST(13);
#pragma unroll
            for (int q = 0; q < 3; ++q)
                bst4<SC1>(silu_pk4(*(const u4*)(xch + q * 1024 + l * 16)), rw, l * 16, hoff + 56 * 1024 + ((nt1 & 1) * 3 + q) * 1024);
        }
        ST(14);
        WAITV(0);
        publish();
        ST(21);
        if (__builtin_expect(aw, 1)) {
            lds_wait(hw + 4, 8u);
#pragma unroll
            for (int ks = 0; ks < 7; ++ks) w2_glds(pw, ks, lds0 + tg * 28672 + ks * 4096);
        } else {
            stage_out(B_R);
#pragma unroll
            for (int r = B_R + 1; r < 7; ++r) { if (r + 1 < 7) WAITV(4); else WAITV(0); stage_out(r); }
        }
    }
    {
        ST(15);
        const int rot = w & 3;
        if (aw) {
            flag_wait(fl + ((l & 4) ? 4096 + (grp * 8 + th * 4 + rot) * 32 : (grp * 8 + (pw ^ ((l & 2) * 2))) * 32) + (l & 1), tag);
            lds_set(hw + 8 + rot, tag);
        } else {
#pragma nounroll
            for (int sp = 0; sp < SPIN_MAX; ++sp) {
                if (__builtin_amdgcn_readfirstlane(__hip_atomic_load(hw + 8 + rot, __ATOMIC_RELAXED, __HIP_MEMORY_SCOPE_WORKGROUP)) == tag) break;
                __builtin_amdgcn_s_sleep(1);
            }
            FENCE();
        }
        ST(16);
        const int toff = WS_H1 + (grp * 8 + th * 4) * (56 * 1024) + pw * 7168;
        int to[4];
#pragma unroll
        for (int tt = 0; tt < 4; ++tt) to[tt] = toff + ((tt + rot) & 3) * (56 * 1024);
        u4 T[7][4];
        auto ldtok = [&](int ks) {
#pragma unroll
            for (int tt = 0; tt < 4; ++tt) {
                if (ks < 6) T[ks][tt] = bld4<SC1>(rw, l * 16, to[tt] + ko[ks] * 1024);
                else {
                    u2 a = bld2<SC1>(rw, l * 8 + 2048, to[tt] + 4096), c = bld2<SC1>(rw, l * 8 + 2560, to[tt] + 4096);
                    T[ks][tt] = (u4){a[0], a[1], c[0], c[1]};
                }
            }
            SCHED();
        };
#pragma unroll
        for (int ks = 0; ks < G2_DEPTH; ++ks) ldtok(ks);
        if (aw) {
#pragma unroll
            for (int ks = 0; ks < 7; ++ks) w2_read(W[ks], rot * 28672 + ks * 4096);
            asm volatile("s_waitcnt lgkmcnt(0)" ::: "memory");
        }
        if (l == 0) __hip_atomic_fetch_add(hw + 7, 1u, __ATOMIC_RELAXED, __HIP_MEMORY_SCOPE_WORKGROUP);
        const int rho = grp * 128 + th * 64 + rot * 16 + m;
        const int ooff = (256 * (rho & 7) + (rho >> 3)) * 1024 + fb * 128 + (w >> 2) * 64 + g * 16;
        const u4 hres = bld4<SC1>(ro, ooff, 0);
        SCHED();
        f4 acc[4][4];
#pragma unroll
        for (int tt = 0; tt < 4; ++tt)
#pragma unroll
            for (int t = 0; t < 4; ++t) acc[tt][t] = Z4;
#pragma unroll
        for (int ks = 0; ks < 7; ++ks) {
            if (ks + G2_DEPTH < 7) { PIN(T[ks][0]); ldtok(ks + G2_DEPTH); }
#pragma unroll
            for (int tt = 0; tt < 4; ++tt)
#pragma unroll
                for (int t = 0; t < 4; ++t) {
                    acc[tt][t] = mma_b(W[ks][t], T[ks][tt], acc[tt][t]);
                }
            SCHED();
        }
        ST(17);
        lds_wait(hw + 7, 8u);
        char* dp = smem + w * 16384 + l * 16;
#pragma unroll
        for (int tt = 1; tt < 4; ++tt)
#pragma unroll
            for (int t = 0; t < 4; ++t) *(f4*)(dp + (((tt + rot) & 3) * 4 + t) * 1024) = acc[tt][t];
        f4 s0, s1;
        if (aw) { *(f4*)(dp + (rot * 4 + 2) * 1024) = acc[0][2]; *(f4*)(dp + (rot * 4 + 3) * 1024) = acc[0][3]; s0 = acc[0][0]; s1 = acc[0][1]; }
        else { *(f4*)(dp + (rot * 4) * 1024) = acc[0][0]; *(f4*)(dp + (rot * 4 + 1) * 1024) = acc[0][1]; s0 = acc[0][2]; s1 = acc[0][3]; }
        BARRIER();
        f4 r0[7], r1[7];
#pragma unroll
        for (int j = 1; j < 8; ++j) {
            const char* rp = smem + (((w + j) & 7) * 16 + rot * 4 + (w >> 2) * 2) * 1024 + l * 16;
            r0[j - 1] = *(const f4*)rp; r1[j - 1] = *(const f4*)(rp + 1024);
        }
        SCHED();
        s0 = ((s0 + r0[0]) + (r0[1] + r0[2])) + ((r0[3] + r0[4]) + (r0[5] + r0[6]));
        s1 = ((s1 + r1[0]) + (r1[1] + r1[2])) + ((r1[3] + r1[4]) + (r1[5] + r1[6]));
        u2 p0 = pk4(s0), p1 = pk4(s1);
        bst4<NT>((u4){pk_b2(bf_2(hres[0]) + bf_2(p0[0])), pk_b2(bf_2(hres[1]) + bf_2(p0[1])),
                           pk_b2(bf_2(hres[2]) + bf_2(p1[0])), pk_b2(bf_2(hres[3]) + bf_2(p1[1]))}, ro, ooff, 0);
    }
    ST_END(ws, 0);
}

extern "C" void kernel_launch(void* const* d_in, const int* in_sizes, int n_in, void* d_out, int out_size,
                              void* d_ws, size_t ws_size, hipStream_t stream) {
    (void)hipFuncSetAttribute((const void*)k_fused, hipFuncAttributeMaxDynamicSharedMemorySize, LDS_B);
    k_fused<<<256, 512, LDS_B, stream>>>((const uint16_t*)d_in[0], (const uint16_t*)d_in[1], (const uint16_t*)d_in[2],
                                         (const uint16_t*)d_in[4], (const uint16_t*)d_in[5], (uint16_t*)d_out, (char*)d_ws);
}
```

```cpp
#include <hip/hip_runtime.h>
#include <hip/hip_bf16.h>
#include <stdint.h>

typedef _Float16 h8 __attribute__((ext_vector_type(8)));
typedef _Float16 h4 __attribute__((ext_vector_type(4)));
typedef _Float16 h2 __attribute__((ext_vector_type(2)));
typedef __bf16 b8 __attribute__((ext_vector_type(8)));
typedef __bf16 b2 __attribute__((ext_vector_type(2)));
typedef short s4 __attribute__((ext_vector_type(4)));
typedef float f4 __attribute__((ext_vector_type(4)));
typedef float f2 __attribute__((ext_vector_type(2)));
typedef unsigned u32;
typedef u32 u4 __attribute__((ext_vector_type(4)));
typedef u32 u2 __attribute__((ext_vector_type(2)));
#define LAS __attribute__((address_space(3)))
#define GAS __attribute__((address_space(1)))
#define DI __device__ __forceinline__
#define FENCE() asm volatile("" ::: "memory")
#define PIN(x) asm volatile("" : "+v"(x))
#define SCHED() __builtin_amdgcn_sched_barrier(0)
#define WAITV(n) asm volatile("s_waitcnt vmcnt(%0)" :: "n"(n) : "memory")
#define BARRIER() do { asm volatile("s_waitcnt lgkmcnt(0)" ::: "memory"); __builtin_amdgcn_s_barrier(); FENCE(); } while (0)
constexpr int SC1 = 16, NT = 2;
constexpr u32 SENT = 0xffffffffu;
constexpr int SPIN_MAX = 1 << 20;

DI u32 pk_h(float a, float b) { f2 v = {a, b}; h2 r = __builtin_convertvector(v, h2); return __builtin_bit_cast(u32, r); }
DI u32 pk_b(float a, float b) { f2 v = {a, b}; b2 r = __builtin_convertvector(v, b2); return __builtin_bit_cast(u32, r); }
DI float bf_lo(u32 v) { return __builtin_bit_cast(float, v << 16); }
DI float bf_hi(u32 v) { return __builtin_bit_cast(float, v & 0xffff0000u); }
DI f2 bf_2(u32 v) { return (f2){bf_lo(v), bf_hi(v)}; }
DI u32 pk_b2(f2 v) { return pk_b(v[0], v[1]); }
DI float rbf(float a) { return bf_lo(pk_b(a, a)); }
DI float cosr(float rev) { return __builtin_amdgcn_cosf(rev); }
DI float sinr(float rev) { return __builtin_amdgcn_sinf(rev); }
DI f2 lo2(f4 v) { return __builtin_shufflevector(v, v, 0, 1); }
DI f2 hi2(f4 v) { return __builtin_shufflevector(v, v, 2, 3); }
DI f2 fma2(f2 a, f2 b, f2 c) { return __builtin_elementwise_fma(a, b, c); }
DI u32 umin(u32 a, u32 b) { return a < b ? a : b; }
DI u32 umax(u32 a, u32 b) { return a > b ? a : b; }
DI u32 clean(u32 v) { return umin(v, SENT - 1); }
DI u32 max4(u4 v) { return umax(umax(umax(v[0], v[1]), v[2]), v[3]); }
#define Z4 ((f4){0, 0, 0, 0})
DI f4 mma_h(u4 a, u4 b, f4 c) { return __builtin_amdgcn_mfma_f32_16x16x32_f16(__builtin_bit_cast(h8, a), __builtin_bit_cast(h8, b), c, 0, 0, 0); }
DI f4 mma_b(u4 a, u4 b, f4 c) { return __builtin_amdgcn_mfma_f32_16x16x32_bf16(__builtin_bit_cast(b8, a), __builtin_bit_cast(b8, b), c, 0, 0, 0); }
DI f4 mma16_h(u2 a, u2 b, f4 c) { return __builtin_amdgcn_mfma_f32_16x16x16f16(__builtin_bit_cast(h4, a), __builtin_bit_cast(h4, b), c, 0, 0, 0); }
typedef __amdgpu_buffer_rsrc_t rsrc_t;
DI rsrc_t mkrs(const void* p, u32 bytes) { return __builtin_amdgcn_make_buffer_rsrc((void*)p, 0, bytes, 0x00020000); }
template <int AUX> DI u4 bld4(rsrc_t r, int v, int s) { return __builtin_amdgcn_raw_buffer_load_b128(r, v, s, AUX); }
template <int AUX> DI u2 bld2(rsrc_t r, int v, int s) { return __builtin_amdgcn_raw_buffer_load_b64(r, v, s, AUX); }
template <int AUX> DI void bst4(u4 d, rsrc_t r, int v, int s) {
    __builtin_amdgcn_raw_buffer_store_b128(d, r, v, s, AUX);
    asm volatile("s_nop 1" :: "v"(d));
}
template <int AUX> DI void bst2(u2 d, rsrc_t r, int v, int s) { __builtin_amdgcn_raw_buffer_store_b64(d, r, v, s, AUX); }

template <int CTRL> DI float dpp(float v) {
    return __builtin_bit_cast(float, __builtin_amdgcn_update_dpp(0, __builtin_bit_cast(int, v), CTRL, 0xf, 0xf, true));
}
template <bool ALL> DI float red_lanes(float v) {
    v += dpp<0xB1>(v);
    if (ALL) v += dpp<0x4E>(v);
    v += dpp<0x124>(v); v += dpp<0x128>(v);
    u32 x = __builtin_bit_cast(u32, v), y = x;
    asm("" : "+v"(y));
    auto r = __builtin_amdgcn_permlane16_swap(x, y, false, false);
    v = __builtin_bit_cast(float, (u32)r[0]) + __builtin_bit_cast(float, (u32)r[1]);
    x = __builtin_bit_cast(u32, v); y = x;
    asm("" : "+v"(y));
    auto q = __builtin_amdgcn_permlane32_swap(x, y, false, false);
    return __builtin_bit_cast(float, (u32)q[0]) + __builtin_bit_cast(float, (u32)q[1]);
}
DI float ln_inv(float var) { return __builtin_amdgcn_rcpf(rbf(__builtin_amdgcn_sqrtf(rbf(rbf(var) + 1e-5f)))); }

#define ST_DECL
#define ST(i)
#define ST_END(ws, kid)

constexpr u32 WS_U = 0;
constexpr u32 WS_WT = WS_U + (2u << 20);
constexpr u32 WS_V = WS_WT + (2u << 20);
constexpr u32 WS_LN2 = WS_V + (2u << 20);
constexpr u32 WS_WA = WS_LN2 + (2u << 20);
constexpr u32 WS_WB = WS_WA + 8192u;
constexpr u32 WS_FA = WS_WB + 8192u;
constexpr u32 WS_FLAG = WS_FA + 24576u;
constexpr u32 WS_EP = WS_FLAG + 32768u;
constexpr u32 WS_H1 = WS_WA + (128u << 10);
constexpr u32 WS_END = WS_H1 + 7340032u;

#define W_EARLY 0
#define PRO_SLEEP 1
constexpr int BG4 = 7, BG5 = 14, BG6 = 21;
constexpr int G1_DEPTH = 10, G2_DEPTH = 3;
constexpr int B_R = 5;
constexpr int CODE_LINES = 290, CODE_PF = 10;
constexpr int LDS_SCR = 114688;
constexpr int SCR_X = 8448;
constexpr int SCR_S = SCR_X + 20480;
constexpr int SCR_F = 49024;
constexpr int XCH = 147456;
constexpr int LDS_B = 163840;

DI void glds16(const void* sbase, u32 voff, u32 lds_dst) {
    asm volatile("s_mov_b32 m0, %2\n\ts_nop 0\n\tglobal_load_lds_dwordx4 %0, %1" :: "v"(voff), "s"(sbase), "s"(lds_dst) : "memory");
}
DI bool any_sent(u32 v) { return __builtin_amdgcn_ballot_w64(v == SENT) != 0; }
DI void poll2(u4& a, u4& c, rsrc_t rw, int vo, int sa, int sc) {
#pragma nounroll
    for (int sp = 0; sp < SPIN_MAX; ++sp) {
        const bool ba = max4(a) == SENT, bc = max4(c) == SENT;
        if (__builtin_amdgcn_ballot_w64(ba || bc) == 0) break;
        __builtin_amdgcn_s_sleep(1);
        FENCE();
        if (ba) a = bld4<SC1>(rw, vo, sa);
        if (bc) c = bld4<SC1>(rw, vo, sc);
    }
}
DI void wait_tok(u4& t0, u4& t1, rsrc_t rw, int vo, int so) {
    if (any_sent(umax(max4(t0), max4(t1)))) {
#pragma nounroll
        for (int sp = 0; sp < SPIN_MAX; ++sp) {
            FENCE();
            t0 = bld4<SC1>(rw, vo, so); t1 = bld4<SC1>(rw, vo, so + 16384);
            if (!any_sent(umax(max4(t0), max4(t1)))) break;
        }
    }
}
DI u4 ldw1(LAS char* wb, int kk, int ft) {
    s4 a0 = __builtin_amdgcn_ds_read_tr16_b64_v4i16((LAS s4*)(wb + kk * 32 * 224 + ft * 32));
    s4 a1 = __builtin_amdgcn_ds_read_tr16_b64_v4i16((LAS s4*)(wb + (kk * 32 + 16) * 224 + ft * 32));
    u2 x0 = __builtin_bit_cast(u2, a0), x1 = __builtin_bit_cast(u2, a1);
    return (u4){x0[0], x0[1], x1[0], x1[1]};
}
DI u2 pk4(f4 a) { return (u2){pk_b(a[0], a[1]), pk_b(a[2], a[3])}; }
DI u2 silu_pk2(u32 p0, u32 p1) {
    u32 o0, o1; float a0, a1, a2, a3, t0, t1, t2, t3;
    asm("v_lshlrev_b32 %2, 16, %10\n\tv_and_b32 %3, 0xffff0000, %10\n\tv_lshlrev_b32 %4, 16, %11\n\tv_and_b32 %5, 0xffff0000, %11\n\t"
        "v_mul_f32 %6, %12, %2\n\tv_mul_f32 %7, %12, %3\n\tv_mul_f32 %8, %12, %4\n\tv_mul_f32 %9, %12, %5\n\t"
        "v_exp_f32 %6, %6\n\tv_exp_f32 %7, %7\n\tv_exp_f32 %8, %8\n\tv_exp_f32 %9, %9\n\t"
        "v_add_f32 %6, 1.0, %6\n\tv_add_f32 %7, 1.0, %7\n\tv_add_f32 %8, 1.0, %8\n\tv_add_f32 %9, 1.0, %9\n\t"
        "v_rcp_f32 %6, %6\n\tv_rcp_f32 %7, %7\n\tv_rcp_f32 %8, %8\n\tv_rcp_f32 %9, %9\n\t"
        "v_mul_f32 %2, %2, %6\n\tv_mul_f32 %3, %3, %7\n\tv_mul_f32 %4, %4, %8\n\tv_mul_f32 %5, %5, %9\n\t"
        "v_cvt_pk_bf16_f32 %0, %2, %3\n\tv_cvt_pk_bf16_f32 %1, %4, %5"
        : "=&v"(o0), "=&v"(o1), "=&v"(a0), "=&v"(a1), "=&v"(a2), "=&v"(a3), "=&v"(t0), "=&v"(t1), "=&v"(t2), "=&v"(t3)
        : "v"(p0), "v"(p1), "s"(-1.4426950408889634f));
    return (u2){o0, o1};
}
DI u4 silu_pk4(u4 p) { u2 a = silu_pk2(p[0], p[1]), b = silu_pk2(p[2], p[3]); return (u4){a[0], a[1], b[0], b[1]}; }
DI void lds_set(LAS u32* p, u32 v) {
    asm volatile("s_waitcnt lgkmcnt(0)" ::: "memory");
    __hip_atomic_store(p, v, __ATOMIC_RELAXED, __HIP_MEMORY_SCOPE_WORKGROUP);
}
DI void lds_wait(LAS u32* p, u32 v) {
#pragma nounroll
    for (int sp = 0; sp < SPIN_MAX; ++sp) {
        if (__builtin_amdgcn_readfirstlane(__hip_atomic_load(p, __ATOMIC_RELAXED, __HIP_MEMORY_SCOPE_WORKGROUP)) >= v) break;
        __builtin_amdgcn_s_sleep(1);
    }
    FENCE();
}
DI void flag_wait(GAS u32* p, u32 tag) {
#pragma nounroll
    for (int sp = 0; sp < SPIN_MAX; ++sp) {
        if (__builtin_amdgcn_ballot_w64(__hip_atomic_load(p, __ATOMIC_RELAXED, __HIP_MEMORY_SCOPE_AGENT) != tag) == 0) break;
        __builtin_amdgcn_s_sleep(2);
    }
    FENCE();
}
#define SENT4 ((u4){SENT, SENT, SENT, SENT})

__global__ __launch_bounds__(512, 2) void k_fused(const uint16_t* x, const uint16_t* wr, const uint16_t* wi, const uint16_t* w1,
                                                  const uint16_t* w2, uint16_t* out, char* ws) {
    extern __shared__ __attribute__((aligned(16))) char smem[];
    const int tid = threadIdx.x, l = tid & 63, g = l >> 4, m = l & 15;
    const int w = __builtin_amdgcn_readfirstlane(tid >> 6);
    const int b = (blockIdx.x & 7) * 32 + (blockIdx.x >> 3);
    const int mt1 = (b >> 6) * 4 + ((b >> 3) & 3), nt1 = ((b >> 5) & 1) * 8 + (b & 7);
    const int grp = b >> 4, th = (b >> 3) & 1, fb = b & 7;
    const rsrc_t rw = mkrs(ws, WS_END), ro = mkrs(out, 2097152);
    char* scr = smem + LDS_SCR;
    LAS u32* hw = (LAS u32*)((LAS char*)smem + LDS_SCR + SCR_F);
    const u32 lds0 = (u32)(uintptr_t)(LAS char*)smem;
    const float mf = (float)m, gf = (float)g, sg = (m & 1) ? -1.0f : 1.0f;
    const bool aw = w < 4;
    ST_DECL;
    unsigned long long pc0;
    asm volatile("s_getpc_b64 %0" : "=s"(pc0));
    if (tid >= 448 && l < CODE_PF)
        asm volatile("s_mov_b32 m0, %2\n\ts_nop 0\n\tglobal_load_lds_dword %0, %1"
                     :: "v"(umin((blockIdx.x >> 3) * CODE_PF + l, CODE_LINES - 1) * 128), "s"(pc0), "s"(lds0 + LDS_SCR + SCR_F + 64) : "memory");
    const u32 epoch = __hip_atomic_load((GAS u32*)(ws + WS_EP), __ATOMIC_RELAXED, __HIP_MEMORY_SCOPE_AGENT);
    FENCE();
    u32 xd[8];
    const rsrc_t rr = mkrs(wr, 1052672), ri = mkrs(wi, 1052672);
    const int j7 = l >> 3, p4 = (l >> 1) & 3, ee = l & 1;
    u2 fr[4], fi[4]; u32 sr_, si_;
    if (aw) {
        const u32* xr = (const u32*)(x + (256 * (2 * w + ((m >> 1) & 1)) + b) * 512) + 8 * g + 2 * (m >> 2) + (m & 1);
#pragma unroll
        for (int j = 0; j < 8; ++j) xd[j] = xr[32 * j];
    } else {
        __builtin_amdgcn_s_sleep(PRO_SLEEP);
        if (w == 4) {
            const int e = ee * 4 * 514 + (32 * j7 + 4 * p4) * 2;
#pragma unroll
            for (int i = 0; i < 4; ++i) { fr[i] = bld2<0>(rr, e + i * 514, 8 * b * 514); fi[i] = bld2<0>(ri, e + i * 514, 8 * b * 514); }
        } else if (w == 5) {
            const int e = (2045 - 8 * b) * 514 + 253 * 2 - ee * 4 * 514 - (32 * j7 + 4 * p4) * 2;
#pragma unroll
            for (int i = 1; i < 4; ++i) { fr[i] = bld2<0>(rr, e + (3 - i) * 514, 0); fi[i] = bld2<0>(ri, e + (3 - i) * 514, 0); }
            const int e0 = e + 3 * 514 - (b == 0 && !ee ? 2048 * 514 : 0);
            fr[0] = bld2<0>(rr, e0, 0); fi[0] = bld2<0>(ri, e0, 0);
            const int e1 = (8 * b + (l & 7)) * 514 + (16 + 32 * j7) * 2;
            sr_ = __builtin_amdgcn_raw_buffer_load_b16(rr, e1, 0, 0); si_ = __builtin_amdgcn_raw_buffer_load_b16(ri, e1, 0, 0);
        } else if (w == 6) {
            const int c = j7 & 3, f = 8 * b + (l & 7);
            const int e = ((c & 1) ? (2048 - f) & 2047 : f) * 514 + (c >> 1) * 512;
            sr_ = __builtin_amdgcn_raw_buffer_load_b16(rr, e, 0, 0); si_ = __builtin_amdgcn_raw_buffer_load_b16(ri, e, 0, 0);
        }
        FENCE();
        bst4<SC1>(SENT4, rw, l * 16, WS_U + b * 8192 + (w - 4) * 2048); bst4<SC1>(SENT4, rw, l * 16 + 1024, WS_U + b * 8192 + (w - 4) * 2048);
    }
    FENCE();
    const int rq = ((b >> 3) + b) & 3;
    const int row0 = (tid & 255) / 14, cch0 = (tid & 255) - row0 * 14;
    const char* wsrc = (const char*)w1 + nt1 * 224;
    u32 w1o[7];
    auto w1_burst = [&](int i0, int n) {
#pragma unroll
        for (int i = i0; i < i0 + n; ++i) {
            const int q = (i / 7 + rq) & 3, j = i % 7;
            glds16(wsrc + q * (128 * 3584), w1o[j], lds0 + (w - 4) * 1024 + q * 28672 + j * 4096);
        }
    };
    const int xoff = (256 * w + b) * 1024 + m * 32 + g * 8;
    u2 xl[2];
    float c16[4], s16[4];
    f2 twc[2], tws[2];
    u4 Gre3, Gim3, SGre, SGim, GA[2];
    auto consts0 = [&]() {
#pragma unroll
        for (int u = 0; u < 4; ++u) {
            float a = (4.0f * gf + u) * (mf * (1.0f / 16));
            c16[u] = cosr(a); s16[u] = sinr(a);
            twc[u >> 1][u & 1] = cosr(a * (1.0f / 32)); tws[u >> 1][u & 1] = sinr(a * (1.0f / 32));
        }
    };
    auto consts3 = [&]() {
#pragma unroll
        for (int u = 0; u < 4; ++u) {
            const float k = 4.0f * gf + u;
            const bool k0 = u == 0 && g == 0;
            float cc = c16[u], ss = s16[u];
            Gre3[u] = pk_b(cc, -ss); Gim3[u] = pk_b(ss, cc);
            float a2 = (2.0f * k + 1.0f) * (mf * (1.0f / 32));
            u32 sp = pk_b(g < 2 ? 2.f * cc : 2.f * sg * cosr(a2), g < 2 ? -2.f * ss : -2.f * sg * sinr(a2));
            SGre[u] = k0 ? pk_b(1.0f, sg) : (g < 2 ? sp : 0u);
            SGim[u] = g < 2 ? 0u : sp;
            float a4 = k * (mf * (1.0f / 32)), c4 = 2.f * cosr(a4), s4_ = -2.f * sinr(a4);
            GA[0][u] = k0 ? pk_b(1.0f, sg) : pk_b(c4, s4_);
            GA[1][u] = k0 ? pk_b(1.0f, sg) : ((u & 1) ? pk_b(-c4, -s4_) : pk_b(c4, s4_));
        }
        PIN(Gre3); PIN(Gim3); PIN(SGre); PIN(SGim); PIN(GA[0]); PIN(GA[1]); PIN(twc[0]); PIN(twc[1]); PIN(tws[0]); PIN(tws[1]);
    };
    const int pw = (w + 4 * th) & 7;
    GAS u32* fl = (GAS u32*)(ws + WS_FLAG);
    int ko[7];
    {
        const int r6 = (fb < 6 ? fb : fb - 5) + 3 * (grp & 1);
#pragma unroll
        for (int ks = 0; ks < 6; ++ks) { int k = ks + r6; k = k >= 6 ? k - 6 : k; ko[ks] = k >= 6 ? k - 6 : k; }
        ko[6] = 6;
    }
    u4 W[7][4];
    const u32 w2vo = (l >> 3) * 1024 + (((l & 7) ^ (((l >> 4) & 1) << 2)) * 16);
    auto w2_glds = [&](int p_, int ks, u32 dst) {
        const int q = ko[ks];
        const char* src = (const char*)w2 + (224 * p_ + (ks == 6 ? 96 : (q < 3 ? 32 * q : 16 + 32 * q))) * 1024 + fb * 128;
        glds16(src, w2vo, dst); glds16(src + 8192, w2vo, dst + 1024);
        glds16(src + (ks == 6 ? 112 : 16) * 1024, w2vo, dst + 2048); glds16(src + (ks == 6 ? 120 : 24) * 1024, w2vo, dst + 3072);
    };
    LAS char* w2rd = (LAS char*)smem + (4 * g + (m >> 2)) * 128 + (((m & 3) ^ (((m >> 3) & 1) << 2)) * 16);
    auto w2_read = [&](u4 (&f)[4], int off) {
#pragma unroll
        for (int t = 0; t < 4; ++t) {
            LAS char* p = (LAS char*)((u32)(uintptr_t)w2rd ^ ((t >> 1) * 64)) + off + (t & 1) * 8;
            u2 x0 = __builtin_bit_cast(u2, __builtin_amdgcn_ds_read_tr16_b64_v4i16((LAS s4*)p));
            u2 x1 = __builtin_bit_cast(u2, __builtin_amdgcn_ds_read_tr16_b64_v4i16((LAS s4*)(p + 2048)));
            f[t] = (u4){x0[0], x0[1], x1[0], x1[1]};
        }
    };
    auto bg = [&](int i0, int i1) { w1_burst(i0, i1 - i0); };
    u32 tag;
    if (__builtin_expect(aw, 1)) {
        if (tid < 16) hw[tid] = 0;
        FENCE();
        ST(2);
        {
            u4 FAre, FAim, Fre, Fim, Sre, Sim; u2 Mb; f2 pc[2], ps[2];
            consts0();
            float c0[8], s0[8];
#pragma unroll
            for (int j = 0; j < 8; ++j) {
                float a = (gf + 4.0f * j) * (mf * (1.0f / 32));
                c0[j] = cosr(a); s0[j] = m ? -sinr(a) : ((g & 1) ? -1.0f : 1.0f);
            }
            const float mo = (float)(2 * (m & 7) + 1) * (1.0f / 32);
#pragma unroll
            for (int u = 0; u < 4; ++u) {
                FAre[u] = pk_h(c0[2 * u], c0[2 * u + 1]); FAim[u] = pk_h(s0[2 * u], s0[2 * u + 1]);
                float cc = c16[u], ss = s16[u];
                Fre[u] = pk_h(cc, ss); Fim[u] = pk_h(-ss, cc);
                float a2 = (4.0f * gf + u) * mo;
                u32 re = pk_h(m < 8 ? cc : cosr(a2), 0.f), im = pk_h(m < 8 ? (m ? -ss : ((u & 1) ? -1.0f : 1.0f)) : -sinr(a2), 0.f);
                Sre[u] = m < 8 ? re : re << 16; Sim[u] = m < 8 ? im : im << 16;
                pc[u & 1][u >> 1] = twc[u >> 1][u & 1]; ps[u & 1][u >> 1] = tws[u >> 1][u & 1];
            }
#pragma unroll
            for (int e = 0; e < 2; ++e) {
                float a = (float)(((m >> 1) * (256 * (2 * g + e) + b)) & 2047) * (1.0f / 2048);
                float cc = cosr(a), ss = sinr(a);
                Mb[e] = (m & 1) ? pk_h(-ss, cc) : pk_h(cc, ss);
            }
            PIN(FAre); PIN(FAim); PIN(Fre); PIN(Fim); PIN(Sre); PIN(Sim); PIN(Mb); PIN(pc[0]); PIN(pc[1]); PIN(ps[0]); PIN(ps[1]);
            ST(3);
            f2 v[8];
#pragma unroll
            for (int j = 0; j < 8; ++j) v[j] = bf_2(xd[j]);
            f2 s2 = ((v[0] + v[1]) + (v[2] + v[3])) + ((v[4] + v[5]) + (v[6] + v[7]));
            const float mean = red_lanes<false>(s2[0] + s2[1]) * (1.0f / 512);
            f2 q0 = {0.f, 0.f}, q1 = {0.f, 0.f};
#pragma unroll
            for (int j = 0; j < 8; ++j) { v[j] -= mean; if (j & 1) q1 = fma2(v[j], v[j], q1); else q0 = fma2(v[j], v[j], q0); }
            q0 += q1;
            const float inv = ln_inv(red_lanes<false>(q0[0] + q0[1]) * (1.0f / 512));
            const float off = (mean - rbf(mean)) * inv;
#pragma unroll
            for (int j = 0; j < 8; ++j) v[j] = fma2(v[j], (f2){inv, inv}, (f2){off, off});
            u4 B[2];
#pragma unroll
            for (int p = 0; p < 2; ++p) {
                u4 A = {pk_h(v[0][p], v[1][p]), pk_h(v[2][p], v[3][p]), pk_h(v[4][p], v[5][p]), pk_h(v[6][p], v[7][p])};
                f4 Dre = mma_h(A, FAre, Z4), Dim = mma_h(A, FAim, Z4);
#pragma unroll
                for (int r = 0; r < 2; ++r) {
                    f2 re = r ? hi2(Dre) : lo2(Dre), im = r ? hi2(Dim) : lo2(Dim);
                    f2 tr = fma2(im, ps[p], re * pc[p]), ti = fma2(-re, ps[p], im * pc[p]);
                    B[r][p] = pk_h(tr[0], ti[0]); B[r][2 + p] = pk_h(tr[1], ti[1]);
                }
            }
#pragma unroll
            for (int r = 0; r < 2; ++r) {
                f4 Gre = mma_h(Fre, B[r], Z4), Gim = mma_h(Fim, B[r], Z4), Hre = mma_h(Sre, B[r], Z4), Him = mma_h(Sim, B[r], Z4);
                u4 Z;
#pragma unroll
                for (int i = 0; i < 4; ++i) Z[i] = pk_h(m ? Gre[i] : Hre[i], m ? Gim[i] : Him[i]);
                *(u4*)(scr + (2 * w + r) * 1024 + m * 64 + g * 16) = Z;
            }
            BARRIER();
            ST(18);
            u4 r0 = *(const u4*)(scr + (2 * g) * 1024 + w * 256 + m * 16);
            u4 r1 = *(const u4*)(scr + (2 * g + 1) * 1024 + w * 256 + m * 16);
#pragma unroll
            for (int t = 0; t < 4; ++t) {
                f4 D = mma16_h(Mb, (u2){r0[t], r1[t]}, Z4);
                *(u2*)(scr + SCR_S + w * 2048 + m * 128 + (((2 * t + (g >> 1)) ^ (m >> 1)) * 16) + (g & 1) * 8) =
                    (u2){clean(pk_h(D[0], D[1])), clean(pk_h(D[2], D[3]))};
            }
        }
        BARRIER();
        ST(4);
        u4 Fre, Fim, Gre, Gim, N0, N1; f2 tc[2], ts[2];
#pragma unroll
        for (int u = 0; u < 4; ++u) {
            float cc = c16[u], ss = s16[u];
            Fre[u] = pk_h(0.25f * cc, 0.25f * ss); Fim[u] = pk_h(-0.25f * ss, 0.25f * cc);
            Gre[u] = pk_b(cc * (1.0f / 256), -ss * (1.0f / 256)); Gim[u] = pk_b(ss * (1.0f / 256), cc * (1.0f / 256));
            float a3 = (4.0f * gf + u) * (mf * (1.0f / 256));
            tc[u >> 1][u & 1] = cosr(a3); ts[u >> 1][u & 1] = sinr(a3);
        }
        PIN(Fre); PIN(Fim); PIN(Gre); PIN(Gim); PIN(tc[0]); PIN(tc[1]); PIN(ts[0]); PIN(ts[1]);
        xl[0] = *(const u2*)((const char*)x + xoff); xl[1] = *(const u2*)((const char*)x + xoff + 512);
        FENCE();
        tag = __builtin_amdgcn_readfirstlane(epoch) + 1u;
        consts3();
#pragma unroll
        for (int u = 0; u < 4; ++u) {
            float a1 = (float)(((4 * (g & 1) + u) * (256 * (m >> 1) + b)) & 2047) * (1.0f / 2048);
            float c1 = cosr(a1), s1 = sinr(a1);
            u32 nb = (m & 1) ? pk_b(s1, c1) : pk_b(c1, -s1);
            N0[u] = g < 2 ? nb : 0u; N1[u] = g < 2 ? 0u : nb;
        }
        PIN(N0); PIN(N1);
        ST(19);
        BARRIER();
        ST(5);
        u4 d[2], f[2], e[2];
        const int uo = (tid >> 1) * 8192 + (tid & 1) * 16;
        d[0] = bld4<SC1>(rw, uo, WS_U + b * 32); d[1] = bld4<SC1>(rw, uo, WS_U + 128 * 8192 + b * 32);
        const u32 fd0 = __builtin_amdgcn_raw_buffer_load_b32(rw, uo, WS_WT + b * 32, SC1);
        const u32 fd1 = __builtin_amdgcn_raw_buffer_load_b32(rw, uo, WS_WT + 128 * 8192 + b * 32, SC1);
        FENCE();
        poll2(d[0], d[1], rw, uo, WS_U + b * 32, WS_U + 128 * 8192 + b * 32);
        char* sp = scr + SCR_S + (tid ^ ((tid >> 4) & 1)) * 16;
        *(u4*)sp = d[0]; *(u4*)(sp + 4096) = d[1];
        BARRIER();
        f[0] = bld4<SC1>(rw, uo, WS_WT + b * 32); f[1] = bld4<SC1>(rw, uo, WS_WT + 128 * 8192 + b * 32);
        if (b == 0) {
            f[0] = bld4<SC1>(rw, tid * 16, WS_WA); f[1] = bld4<SC1>(rw, tid * 16 + 4096, WS_WA);
            e[0] = bld4<SC1>(rw, tid * 16, WS_WB); e[1] = bld4<SC1>(rw, tid * 16 + 4096, WS_WB);
            if (tid == 0) __hip_atomic_store((GAS u32*)(ws + WS_EP), tag, __ATOMIC_RELAXED, __HIP_MEMORY_SCOPE_AGENT);
        }
        ST(6);
        u2 ld[4], wl[4];
        const int so = (64 * g + m) * 32 + (((w >> 1) ^ (m >> 3)) * 16) + (w & 1) * 8;
#pragma unroll
        for (int q = 0; q < 4; ++q) ld[q] = *(const u2*)(scr + SCR_S + so + 512 * q);
        FENCE();
        f4 Xre[2], Xim[2];
#pragma unroll
        for (int e = 0; e < 2; ++e) {
            u4 A = {ld[0][e], ld[1][e], ld[2][e], ld[3][e]};
            f4 Dre = mma_h(A, Fre, Z4), Dim = mma_h(A, Fim, Z4);
            f2 r0 = fma2(lo2(Dim), ts[0], lo2(Dre) * tc[0]), i0 = fma2(-lo2(Dre), ts[0], lo2(Dim) * tc[0]);
            f2 r1 = fma2(hi2(Dim), ts[1], hi2(Dre) * tc[1]), i1 = fma2(-hi2(Dre), ts[1], hi2(Dim) * tc[1]);
            u4 B = {pk_h(r0[0], i0[0]), pk_h(r0[1], i0[1]), pk_h(r1[0], i1[0]), pk_h(r1[1], i1[1])};
            Xre[e] = mma_h(Fre, B, Z4); Xim[e] = mma_h(Fim, B, Z4);
        }
        *(u4*)(sp + 8192) = f[0]; *(u4*)(sp + 12288) = f[1];
        asm volatile("" :: "v"(fd0), "v"(fd1));
        f2* sx = (f2*)(scr + SCR_X);
        if (b == 0) {
            *(u4*)(sp - SCR_S) = e[0]; *(u4*)(sp - SCR_S + 4096) = e[1];
#pragma unroll
            for (int e = 0; e < 2; ++e)
#pragma unroll
                for (int i = 0; i < 4; ++i) sx[2 * w + e + 8 * (m + 16 * (4 * g + i))] = (f2){Xre[e][i], Xim[e][i]};
        }
        BARRIER();
        ST(20);
#pragma unroll
        for (int q = 0; q < 4; ++q) wl[q] = *(const u2*)(scr + SCR_S + so + 8192 + 512 * q);
        FENCE();
        f2 yr[2][2], yi[2][2];
#pragma unroll
        for (int e = 0; e < 2; ++e)
#pragma unroll
            for (int hh = 0; hh < 2; ++hh) {
                f2 wre = {bf_lo(wl[2 * hh][e]), bf_lo(wl[2 * hh + 1][e])}, wim = {bf_hi(wl[2 * hh][e]), bf_hi(wl[2 * hh + 1][e])};
                f2 xr = hh ? hi2(Xre[e]) : lo2(Xre[e]), xi = hh ? hi2(Xim[e]) : lo2(Xim[e]);
                yr[e][hh] = fma2(-xi, wim, xr * wre); yi[e][hh] = fma2(xr, wim, xi * wre);
            }
        if (b == 0) {
#pragma unroll
            for (int i = 0; i < 4; ++i) {
                u2 wb = *(const u2*)(scr + so + 512 * i);
#pragma unroll
                for (int e = 0; e < 2; ++e) {
                    f2 xm = sx[(2048 - (2 * w + e + 8 * (m + 16 * (4 * g + i)))) & 2047];
                    float br = bf_lo(wb[e]), bi = bf_hi(wb[e]);
                    yr[e][i >> 1][i & 1] += xm[0] * br + xm[1] * bi;
                    yi[e][i >> 1][i & 1] += xm[0] * bi - xm[1] * br;
                }
            }
        }
        char* pv = scr + SCR_S;
#pragma unroll
        for (int e = 0; e < 2; ++e) {
            u4 Y = {pk_b(yr[e][0][0], yi[e][0][0]), pk_b(yr[e][0][1], yi[e][0][1]), pk_b(yr[e][1][0], yi[e][1][0]), pk_b(yr[e][1][1], yi[e][1][1])};
            f4 Dre = mma_b(Y, Gre, Z4), Dim = mma_b(Y, Gim, Z4);
            f2 r0 = fma2(-lo2(Dim), ts[0], lo2(Dre) * tc[0]), i0 = fma2(lo2(Dre), ts[0], lo2(Dim) * tc[0]);
            f2 r1 = fma2(-hi2(Dim), ts[1], hi2(Dre) * tc[1]), i1 = fma2(hi2(Dre), ts[1], hi2(Dim) * tc[1]);
            u4 A = {pk_b(r0[0], i0[0]), pk_b(r0[1], i0[1]), pk_b(r1[0], i1[0]), pk_b(r1[1], i1[1])};
            f4 Ere = mma_b(A, Gre, Z4), Eim = mma_b(A, Gim, Z4);
            u4 o;
#pragma unroll
            for (int i = 0; i < 4; ++i) o[i] = clean(pk_b(Ere[i], Eim[i]));
            *(u4*)(pv + (2 * w + e) * 1024 + ((m * 4 + g) ^ ((w >> 1) * 4)) * 16) = o;
        }
        BARRIER();
        ST(7);
#pragma unroll
        for (int it = 0; it < 2; ++it) {
            const int ch = tid + 256 * it;
            const u32* q = (const u32*)(pv + (ch & 1) * 4096 + (((ch >> 1) * 4) ^ ((ch & 1) * 64)));
            bst4<SC1>((u4){q[0], q[256], q[512], q[768]}, rw, ch * 16, WS_V + b * 8192);
        }
        WAITV(0);
        BARRIER();
        ST(8);
        const int vo = (2 * m + (g >> 1)) * 8192 + (g & 1) * 16, vs = WS_V + w * 64 * 8192 + b * 32;
        u4 v0 = bld4<SC1>(rw, vo, vs), v1 = bld4<SC1>(rw, vo, vs + 32 * 8192);
        poll2(v0, v1, rw, vo, vs, vs + 32 * 8192);
        ST(9);
        char* sz = scr + SCR_X + g * 2048 + w * 256 + m * 8;
#pragma unroll
        for (int hh = 0; hh < 2; ++hh) {
            f4 D0 = mma_b(N0, hh ? v1 : v0, Z4), D1 = mma_b(N1, hh ? v1 : v0, Z4);
            *(u2*)(sz + hh * 128) = (u2){pk_b(D0[0], D0[1]), pk_b(D1[0], D1[1])};
            *(u2*)(sz + 1024 + hh * 128) = (u2){pk_b(D0[2], D0[3]), pk_b(D1[2], D1[3])};
        }
    } else {
        auto announce = [&](int which) {
            if (l < 8) __hip_atomic_store((GAS u32*)(ws + WS_FA) + which * 2048 + l * 256 + b, tag, __ATOMIC_RELAXED, __HIP_MEMORY_SCOPE_AGENT);
            FENCE();
        };
        auto all_announced = [&](int which) {
#pragma nounroll
            for (int sp = 0; sp < SPIN_MAX; ++sp) {
                FENCE();
                u4 f = bld4<SC1>(rw, l * 16, WS_FA + which * 8192 + (b >> 5) * 1024);
                if (__builtin_amdgcn_ballot_w64(f[0] != tag || f[1] != tag || f[2] != tag || f[3] != tag) == 0) break;
                __builtin_amdgcn_s_sleep(4);
            }
        };
        FENCE();
        ST(2);
#pragma unroll
        for (int jj = 0; jj < 7; ++jj) {
            const int cj_ = (4 * jj) % 14, rj = 18 * jj + (4 * jj) / 14;
            w1o[jj] = (row0 + rj) * 3584 + (cch0 + cj_) * 16 + (cch0 + cj_ >= 14 ? 3584 - 224 : 0);
            PIN(w1o[jj]);
        }
        tag = __builtin_amdgcn_readfirstlane(epoch) + 1u;
        auto count_in = [&](int word, u32 last, int which) {
            u32 prev = 0;
            if (l == 0) prev = __hip_atomic_fetch_add(hw + word, 1u, __ATOMIC_RELAXED, __HIP_MEMORY_SCOPE_WORKGROUP);
            if (__builtin_amdgcn_readfirstlane(prev) == last) announce(which);
        };
        WAITV(0);
        if (l < 2) __hip_atomic_store((GAS u32*)(ws + WS_FA) + (2 * (w - 4) + l) * 256 + b, tag, __ATOMIC_RELAXED, __HIP_MEMORY_SCOPE_AGENT);
        FENCE();
        if (!W_EARLY) BARRIER();
        if (w < 6) {
            const int wo = p4 * 2048 + j7 * 32 + ee * 16, wso = WS_WT + b * 8192 + (w - 4) * 256;
#pragma unroll
            for (int c = 0; c < 4; ++c) {
                u4 o;
#pragma unroll
                for (int i = 0; i < 4; ++i)
                    o[i] = w == 4 ? __builtin_amdgcn_perm(fi[i][c >> 1], fr[i][c >> 1], (c & 1) ? 0x07060302u : 0x05040100u)
                                  : __builtin_amdgcn_perm(fi[i][1 - (c >> 1)], fr[i][1 - (c >> 1)], (c & 1) ? 0x05040100u : 0x07060302u) ^ 0x80000000u;
                bst4<SC1>(o, rw, wo + c * 512, wso);
            }
            if (w == 5) __builtin_amdgcn_raw_buffer_store_b32(sr_ | (si_ << 16), rw, l * 4, wso, SC1);
        } else if (w == 6) {
            float re = bf_lo(sr_), im = bf_lo(si_);
            re += dpp<0x128>(re); im -= dpp<0x128>(im);
            u32 a = __builtin_bit_cast(u32, re), a2 = a, c = __builtin_bit_cast(u32, im), c2 = c;
            asm("" : "+v"(a2), "+v"(c2));
            auto pr = __builtin_amdgcn_permlane16_swap(a, a2, false, false);
            auto pi = __builtin_amdgcn_permlane16_swap(c, c2, false, false);
            const float ar = __builtin_bit_cast(float, (u32)pr[0]), br = __builtin_bit_cast(float, (u32)pr[1]);
            const float ai = __builtin_bit_cast(float, (u32)pi[0]), bi = __builtin_bit_cast(float, (u32)pi[1]);
            if (l < 8) {
                __builtin_amdgcn_raw_buffer_store_b32(pk_b(0.25f * (ar + br), 0.25f * (ai + bi)), rw, l * 4, WS_WA + b * 32, SC1);
                __builtin_amdgcn_raw_buffer_store_b32(pk_b(0.25f * (ar - br), 0.25f * (ai - bi)), rw, l * 4, WS_WB + b * 32, SC1);
            }
        }
        ST(18);
        if (W_EARLY) BARRIER();
        BARRIER();
        {
            const char* up = scr + SCR_S + (w - 4) * 2048;
            bst4<SC1>(*(const u4*)(up + (l ^ ((l >> 4) & 7)) * 16), rw, l * 16, WS_U + b * 8192 + (w - 4) * 2048);
            bst4<SC1>(*(const u4*)(up + 1024 + (l ^ (((l >> 4) + 4) & 7)) * 16), rw, l * 16 + 1024, WS_U + b * 8192 + (w - 4) * 2048);
        }
        if (w < 7) { WAITV(2); count_in(13, 2u, 2); }
        else all_announced(0);
        WAITV(0);
        ST(19);
        ST(4);
        BARRIER();
        xl[0] = *(const u2*)((const char*)x + xoff); xl[1] = *(const u2*)((const char*)x + xoff + 512);
        FENCE();
        __builtin_amdgcn_s_sleep(2);
#pragma unroll
        for (int i = 0; i < 2; ++i) bst4<SC1>(SENT4, rw, l * 16 + i * 1024, WS_V + b * 8192 + (w - 4) * 2048);
        FENCE();
        consts0();
        consts3();
        WAITV(0);
        if (w == 7) all_announced(2);
        ST(5);
        BARRIER();
        if (w == 7) announce(1);
#pragma unroll
        for (int i = 0; i < 2; ++i) bst4<SC1>(SENT4, rw, (l >> 3) * 256 + (l & 7) * 16 + i * 2048, WS_LN2 + (b >> 1) * 16384 + (b & 1) * 128 + (w - 4) * 4096);
        bg(0, BG4);
        BARRIER();
        bg(BG4, BG5);
        WAITV(BG5);
        ST(6);
        BARRIER();
        if (w == 7) all_announced(1);
        else __builtin_amdgcn_s_sleep(3);
        bg(BG5, BG6);
        ST(7);
        BARRIER();
    }
    BARRIER();
    {
        u4 A = *(const u4*)(scr + SCR_X + w * 1024 + m * 64 + g * 16);
        f4 Dre = mma_b(A, Gre3, Z4), Dim = mma_b(A, Gim3, Z4), Hre = mma_b(A, SGre, Z4), Him = mma_b(A, SGim, Z4);
        if (g == 0) { Dre[0] = Hre[0]; Dim[0] = Him[0]; }
        f2 r0 = fma2(-lo2(Dim), tws[0], lo2(Dre) * twc[0]), i0 = fma2(lo2(Dre), tws[0], lo2(Dim) * twc[0]);
        f2 r1 = fma2(-hi2(Dim), tws[1], hi2(Dre) * twc[1]), i1 = fma2(hi2(Dre), tws[1], hi2(Dim) * twc[1]);
        u4 A2 = {pk_b(r0[0], i0[0]), pk_b(r0[1], i0[1]), pk_b(r1[0], i1[0]), pk_b(r1[1], i1[1])};
        f2 h[4]; u2 hp[2];
#pragma unroll
        for (int t = 0; t < 2; ++t) {
            f4 Yv = mma_b(A2, GA[t], Z4);
            u32 h0 = pk_b2(bf_2(xl[t][0]) + bf_2(pk_b(Yv[0], Yv[1]))), h1 = pk_b2(bf_2(xl[t][1]) + bf_2(pk_b(Yv[2], Yv[3])));
            hp[t] = (u2){h0, h1};
            h[2 * t] = bf_2(h0); h[2 * t + 1] = bf_2(h1);
        }
        f2 s2 = (h[0] + h[1]) + (h[2] + h[3]);
        const float mean = red_lanes<true>(s2[0] + s2[1]) * (1.0f / 512);
        f2 q2 = {0.f, 0.f};
#pragma unroll
        for (int j = 0; j < 4; ++j) { f2 d = h[j] - mean; q2 = fma2(d, d, q2); }
        const float inv = ln_inv(red_lanes<true>(q2[0] + q2[1]) * (1.0f / 512));
        const float mb = rbf(mean);
#pragma unroll
        for (int t = 0; t < 2; ++t)
            *(u2*)(scr + ((8 * t + (m >> 1)) * 4 + g) * 128 + (w ^ (m >> 1)) * 16 + (m & 1) * 8) =
                (u2){clean(pk_b2(bf_2(pk_b2(h[2 * t] - mb)) * inv)), clean(pk_b2(bf_2(pk_b2(h[2 * t + 1] - mb)) * inv))};
        WAITV(0);
        BARRIER();
        u4 lo = *(const u4*)(scr + (tid ^ ((tid >> 5) & 7)) * 16);
        bst4<SC1>(lo, rw, (tid >> 3) * 256 + (tid & 7) * 16, WS_LN2 + (b >> 1) * 16384 + (b & 1) * 128);
        FENCE();
        if (aw) WAITV(0); else __builtin_amdgcn_s_sleep(8);
        bst2<SC1>(hp[0], ro, xoff, 0); bst2<SC1>(hp[1], ro, xoff + 512, 0);
        FENCE();
        ST(10);
    }
    auto stage_out = [&](int r) {
        w2_read(W[r], LDS_SCR + (w & 3) * 8192 + (r & 1) * 4096);
        asm volatile("s_waitcnt lgkmcnt(0)" ::: "memory");
        PIN(W[r][0]); PIN(W[r][1]); PIN(W[r][2]); PIN(W[r][3]);
        if (r + 2 < 7) w2_glds(pw, r + 2, lds0 + LDS_SCR + (w & 3) * 8192 + (r & 1) * 4096);
    };
    auto publish = [&]() {
        u32 prev = 0;
        if (l == 0) prev = __hip_atomic_fetch_add(hw + 4, 1u, __ATOMIC_RELAXED, __HIP_MEMORY_SCOPE_WORKGROUP);
        if (__builtin_amdgcn_readfirstlane(prev) == 7u && l < 2)
            __hip_atomic_store(fl + (l ? 4096 + (b >> 1) * 32 + (b & 1) : (mt1 * 8 + (nt1 >> 1)) * 32 + (nt1 & 1)), tag, __ATOMIC_RELAXED, __HIP_MEMORY_SCOPE_AGENT);
        FENCE();
    };
    {
        const int tg = w & 3;
        char* xch = smem + XCH + tg * 3072;
        const int hoff = WS_H1 + (mt1 * 8 + 2 * tg) * (56 * 1024) + (nt1 >> 1) * 7168;
        if (__builtin_expect(aw, 1)) {
            const int toff = WS_LN2 + (mt1 * 8 + 2 * tg) * 16384;
            LAS char* wb0 = (LAS char*)smem + (4 * g + (m >> 2)) * 224 + (m & 3) * 8;
            LAS char* wb[4];
            int tq[4];
#pragma unroll
            for (int q = 0; q < 4; ++q) { const int qq = (q + rq) & 3; wb[q] = wb0 + qq * 28672; tq[q] = toff + qq * 4096; }
            u4 T[2][16], A[2][7];
            auto ldt = [&](int kk) {
#pragma unroll
                for (int tt = 0; tt < 2; ++tt) T[tt][kk] = bld4<SC1>(rw, l * 16 + (kk & 3) * 1024, tq[kk >> 2] + tt * 16384);
            };
#pragma unroll
            for (int i = 0; i < 7; ++i) A[0][i] = ldw1(wb[0], 0, i);
            if (l == 0) __hip_atomic_fetch_add(hw + 5, 1u, __ATOMIC_RELAXED, __HIP_MEMORY_SCOPE_WORKGROUP);
            ST(11);
#pragma unroll
            for (int kk = 0; kk < G1_DEPTH; ++kk) { ldt(kk); SCHED(); }
            f4 acc[2][7];
#pragma unroll
            for (int tt = 0; tt < 2; ++tt)
#pragma unroll
                for (int i = 0; i < 7; ++i) acc[tt][i] = Z4;
#pragma unroll
            for (int kk = 0; kk < 16; ++kk) {
                if ((kk & 1) == 0) {
                    if (__builtin_expect(any_sent(umax(umax(max4(T[0][kk]), max4(T[1][kk])), umax(max4(T[0][kk + 1]), max4(T[1][kk + 1])))), 0)) {
                        wait_tok(T[0][kk], T[1][kk], rw, l * 16, tq[kk >> 2] + (kk & 3) * 1024);
                        wait_tok(T[0][kk + 1], T[1][kk + 1], rw, l * 16, tq[kk >> 2] + ((kk + 1) & 3) * 1024);
                    }
                    if (kk == 0) ST(12);
                    if (kk == (BG6 / 7) * 4 - 2) lds_wait(hw + 5, 8u);
                }
                if (kk + G1_DEPTH < 16) ldt(kk + G1_DEPTH);
                if (kk < 15) {
#pragma unroll
                    for (int i = 0; i < 7; ++i) A[(kk + 1) & 1][i] = ldw1(wb[(kk + 1) >> 2], (kk + 1) & 3, i);
                }
#pragma unroll
                for (int i = 0; i < 7; ++i)
#pragma unroll
                    for (int tt = 0; tt < 2; ++tt) acc[tt][i] = mma_b(A[kk & 1][i], T[tt][kk], acc[tt][i]);
#pragma unroll
                for (int i = 0; i < 14; ++i) {
                    __builtin_amdgcn_sched_group_barrier(0x8, 1, 0);
                    if (i == 1 || i == 7) __builtin_amdgcn_sched_group_barrier(0x20, 1, 0);
                    __builtin_amdgcn_sched_group_barrier(0x100, 1, 0);
                    if ((kk & 1) && i >= 3) __builtin_amdgcn_sched_group_barrier(0x2, 1, 0);
                }
                if (kk & 1) {} else SCHED();
            }
#pragma unroll
            for (int q = 0; q < 3; ++q) {
                u2 p0 = pk4(acc[1][2 * q]), p1 = pk4(acc[1][2 * q + 1]);
                *(u4*)(xch + q * 1024 + l * 16) = (u4){p0[0], p0[1], p1[0], p1[1]};
            }
            lds_set(hw + tg, 1u);
            ST(13);
#pragma unroll
            for (int q = 0; q < 3; ++q) {
                u2 p0 = pk4(acc[0][2 * q]), p1 = pk4(acc[0][2 * q + 1]);
                bst4<SC1>(silu_pk4((u4){p0[0], p0[1], p1[0], p1[1]}), rw, l * 16, hoff + ((nt1 & 1) * 3 + q) * 1024);
            }
#pragma unroll
            for (int tt = 0; tt < 2; ++tt) { u2 p = pk4(acc[tt][6]); bst2<SC1>(silu_pk2(p[0], p[1]), rw, l * 8, hoff + tt * (56 * 1024) + 6144 + (nt1 & 1) * 512); }
        } else {
            bg(BG6, 28);
            WAITV(0);
            if (l == 0) __hip_atomic_fetch_add(hw + 5, 1u, __ATOMIC_RELAXED, __HIP_MEMORY_SCOPE_WORKGROUP);
            __builtin_amdgcn_s_sleep(8);
            lds_wait(hw + 5, 8u);
            w2_glds(pw, 0, lds0 + LDS_SCR + tg * 8192);
            w2_glds(pw, 1, lds0 + LDS_SCR + tg * 8192 + 4096);
#pragma unroll
            for (int r = 0; r < B_R; ++r) { WAITV(4); stage_out(r); }
            ST(12);
            lds_wait(hw + tg, 1u);
            ST(13);
#pragma unroll
            for (int q = 0; q < 3; ++q)
                bst4<SC1>(silu_pk4(*(const u4*)(xch + q * 1024 + l * 16)), rw, l * 16, hoff + 56 * 1024 + ((nt1 & 1) * 3 + q) * 1024);
        }
        ST(14);
        WAITV(0);
        publish();
        ST(21);
        if (__builtin_expect(aw, 1)) {
            lds_wait(hw + 4, 8u);
#pragma unroll
            for (int ks = 0; ks < 7; ++ks) w2_glds(pw, ks, lds0 + tg * 28672 + ks * 4096);
        } else {
            stage_out(B_R);
#pragma unroll
            for (int r = B_R + 1; r < 7; ++r) { if (r + 1 < 7) WAITV(4); else WAITV(0); stage_out(r); }
        }
    }
    {
        ST(15);
        const int rot = w & 3;
        if (aw) {
            flag_wait(fl + ((l & 4) ? 4096 + (grp * 8 + th * 4 + rot) * 32 : (grp * 8 + (pw ^ ((l & 2) * 2))) * 32) + (l & 1), tag);
            lds_set(hw + 8 + rot, tag);
        } else {
#pragma nounroll
            for (int sp = 0; sp < SPIN_MAX; ++sp) {
                if (__builtin_amdgcn_readfirstlane(__hip_atomic_load(hw + 8 + rot, __ATOMIC_RELAXED, __HIP_MEMORY_SCOPE_WORKGROUP)) == tag) break;
                __builtin_amdgcn_s_sleep(1);
            }
            FENCE();
        }
        ST(16);
        const int toff = WS_H1 + (grp * 8 + th * 4) * (56 * 1024) + pw * 7168;
        int to[4];
#pragma unroll
        for (int tt = 0; tt < 4; ++tt) to[tt] = toff + ((tt + rot) & 3) * (56 * 1024);
        u4 T[7][4];
        auto ldtok = [&](int ks) {
#pragma unroll
            for (int tt = 0; tt < 4; ++tt) {
                if (ks < 6) T[ks][tt] = bld4<SC1>(rw, l * 16, to[tt] + ko[ks] * 1024);
                else {
                    u2 a = bld2<SC1>(rw, l * 8 + 2048, to[tt] + 4096), c = bld2<SC1>(rw, l * 8 + 2560, to[tt] + 4096);
                    T[ks][tt] = (u4){a[0], a[1], c[0], c[1]};
                }
            }
            SCHED();
        };
#pragma unroll
        for (int ks = 0; ks < G2_DEPTH; ++ks) ldtok(ks);
        if (aw) {
#pragma unroll
            for (int ks = 0; ks < 7; ++ks) w2_read(W[ks], rot * 28672 + ks * 4096);
            asm volatile("s_waitcnt lgkmcnt(0)" ::: "memory");
        }
        if (l == 0) __hip_atomic_fetch_add(hw + 7, 1u, __ATOMIC_RELAXED, __HIP_MEMORY_SCOPE_WORKGROUP);
        const int rho = grp * 128 + th * 64 + rot * 16 + m;
        const int ooff = (256 * (rho & 7) + (rho >> 3)) * 1024 + fb * 128 + (w >> 2) * 64 + g * 16;
        const u4 hres = bld4<SC1>(ro, ooff, 0);
        SCHED();
        f4 acc[4][4];
#pragma unroll
        for (int tt = 0; tt < 4; ++tt)
#pragma unroll
            for (int t = 0; t < 4; ++t) acc[tt][t] = Z4;
#pragma unroll
        for (int ks = 0; ks < 7; ++ks) {
            if (ks + G2_DEPTH < 7) { PIN(T[ks][0]); ldtok(ks + G2_DEPTH); }
#pragma unroll
            for (int tt = 0; tt < 4; ++tt)
#pragma unroll
                for (int t = 0; t < 4; ++t) {
                    acc[tt][t] = mma_b(W[ks][t], T[ks][tt], acc[tt][t]);
                }
            SCHED();
        }
        ST(17);
        lds_wait(hw + 7, 8u);
        char* dp = smem + w * 16384 + l * 16;
#pragma unroll
        for (int tt = 1; tt < 4; ++tt)
#pragma unroll
            for (int t = 0; t < 4; ++t) *(f4*)(dp + (((tt + rot) & 3) * 4 + t) * 1024) = acc[tt][t];
        f4 s0, s1;
        if (aw) { *(f4*)(dp + (rot * 4 + 2) * 1024) = acc[0][2]; *(f4*)(dp + (rot * 4 + 3) * 1024) = acc[0][3]; s0 = acc[0][0]; s1 = acc[0][1]; }
        else { *(f4*)(dp + (rot * 4) * 1024) = acc[0][0]; *(f4*)(dp + (rot * 4 + 1) * 1024) = acc[0][1]; s0 = acc[0][2]; s1 = acc[0][3]; }
        BARRIER();
        f4 r0[7], r1[7];
#pragma unroll
        for (int j = 1; j < 8; ++j) {
            const char* rp = smem + (((w + j) & 7) * 16 + rot * 4 + (w >> 2) * 2) * 1024 + l * 16;
            r0[j - 1] = *(const f4*)rp; r1[j - 1] = *(const f4*)(rp + 1024);
        }
        SCHED();
        s0 = ((s0 + r0[0]) + (r0[1] + r0[2])) + ((r0[3] + r0[4]) + (r0[5] + r0[6]));
        s1 = ((s1 + r1[0]) + (r1[1] + r1[2])) + ((r1[3] + r1[4]) + (r1[5] + r1[6]));
        u2 p0 = pk4(s0), p1 = pk4(s1);
        bst4<NT>((u4){pk_b2(bf_2(hres[0]) + bf_2(p0[0])), pk_b2(bf_2(hres[1]) + bf_2(p0[1])),
                           pk_b2(bf_2(hres[2]) + bf_2(p1[0])), pk_b2(bf_2(hres[3]) + bf_2(p1[1]))}, ro, ooff, 0);
    }
    ST_END(ws, 0);
}

extern "C" void kernel_launch(void* const* d_in, const int* in_sizes, int n_in, void* d_out, int out_size,
                              void* d_ws, size_t ws_size, hipStream_t stream) {
    (void)hipFuncSetAttribute((const void*)k_fused, hipFuncAttributeMaxDynamicSharedMemorySize, LDS_B);
    k_fused<<<256, 512, LDS_B, stream>>>((const uint16_t*)d_in[0], (const uint16_t*)d_in[1], (const uint16_t*)d_in[2],
                                         (const uint16_t*)d_in[4], (const uint16_t*)d_in[5], (uint16_t*)d_out, (char*)d_ws);
}
```

```cpp
#define PRO_SLEEP 4
#include <hip/hip_runtime.h>
#include <hip/hip_bf16.h>
#include <stdint.h>

typedef _Float16 h8 __attribute__((ext_vector_type(8)));
typedef _Float16 h4 __attribute__((ext_vector_type(4)));
typedef _Float16 h2 __attribute__((ext_vector_type(2)));
typedef __bf16 b8 __attribute__((ext_vector_type(8)));
typedef __bf16 b2 __attribute__((ext_vector_type(2)));
typedef short s4 __attribute__((ext_vector_type(4)));
typedef float f4 __attribute__((ext_vector_type(4)));
typedef float f2 __attribute__((ext_vector_type(2)));
typedef unsigned u32;
typedef u32 u4 __attribute__((ext_vector_type(4)));
typedef u32 u2 __attribute__((ext_vector_type(2)));
#define LAS __attribute__((address_space(3)))
#define GAS __attribute__((address_space(1)))
#define DI __device__ __forceinline__
#define FENCE() asm volatile("" ::: "memory")
#define PIN(x) asm volatile("" : "+v"(x))
#define SCHED() __builtin_amdgcn_sched_barrier(0)
#define WAITV(n) asm volatile("s_waitcnt vmcnt(%0)" :: "n"(n) : "memory")
#define BARRIER() do { asm volatile("s_waitcnt lgkmcnt(0)" ::: "memory"); __builtin_amdgcn_s_barrier(); FENCE(); } while (0)
constexpr int SC1 = 16, NT = 2;
constexpr u32 SENT = 0xffffffffu;
constexpr int SPIN_MAX = 1 << 20;

DI u32 pk_h(float a, float b) { f2 v = {a, b}; h2 r = __builtin_convertvector(v, h2); return __builtin_bit_cast(u32, r); }
DI u32 pk_b(float a, float b) { f2 v = {a, b}; b2 r = __builtin_convertvector(v, b2); return __builtin_bit_cast(u32, r); }
DI float bf_lo(u32 v) { return __builtin_bit_cast(float, v << 16); }
DI float bf_hi(u32 v) { return __builtin_bit_cast(float, v & 0xffff0000u); }
DI f2 bf_2(u32 v) { return (f2){bf_lo(v), bf_hi(v)}; }
DI u32 pk_b2(f2 v) { return pk_b(v[0], v[1]); }
DI float rbf(float a) { return bf_lo(pk_b(a, a)); }
DI float cosr(float rev) { return __builtin_amdgcn_cosf(rev); }
DI float sinr(float rev) { return __builtin_amdgcn_sinf(rev); }
DI f2 lo2(f4 v) { return __builtin_shufflevector(v, v, 0, 1); }
DI f2 hi2(f4 v) { return __builtin_shufflevector(v, v, 2, 3); }
DI f2 fma2(f2 a, f2 b, f2 c) { return __builtin_elementwise_fma(a, b, c); }
DI u32 umin(u32 a, u32 b) { return a < b ? a : b; }
DI u32 umax(u32 a, u32 b) { return a > b ? a : b; }
DI u32 clean(u32 v) { return umin(v, SENT - 1); }
DI u32 max4(u4 v) { return umax(umax(umax(v[0], v[1]), v[2]), v[3]); }
#define Z4 ((f4){0, 0, 0, 0})
DI f4 mma_h(u4 a, u4 b, f4 c) { return __builtin_amdgcn_mfma_f32_16x16x32_f16(__builtin_bit_cast(h8, a), __builtin_bit_cast(h8, b), c, 0, 0, 0); }
DI f4 mma_b(u4 a, u4 b, f4 c) { return __builtin_amdgcn_mfma_f32_16x16x32_bf16(__builtin_bit_cast(b8, a), __builtin_bit_cast(b8, b), c, 0, 0, 0); }
DI f4 mma16_b(u2 a, u2 b, f4 c) { return __builtin_amdgcn_mfma_f32_16x16x16bf16_1k(__builtin_bit_cast(s4, a), __builtin_bit_cast(s4, b), c, 0, 0, 0); }
DI f4 mma16_h(u2 a, u2 b, f4 c) { return __builtin_amdgcn_mfma_f32_16x16x16f16(__builtin_bit_cast(h4, a), __builtin_bit_cast(h4, b), c, 0, 0, 0); }
typedef __amdgpu_buffer_rsrc_t rsrc_t;
DI rsrc_t mkrs(const void* p, u32 bytes) { return __builtin_amdgcn_make_buffer_rsrc((void*)p, 0, bytes, 0x00020000); }
template <int AUX> DI u4 bld4(rsrc_t r, int v, int s) { return __builtin_amdgcn_raw_buffer_load_b128(r, v, s, AUX); }
template <int AUX> DI u2 bld2(rsrc_t r, int v, int s) { return __builtin_amdgcn_raw_buffer_load_b64(r, v, s, AUX); }
template <int AUX> DI void bst4(u4 d, rsrc_t r, int v, int s) {
    __builtin_amdgcn_raw_buffer_store_b128(d, r, v, s, AUX);
    asm volatile("s_nop 1" :: "v"(d));
}
template <int AUX> DI void bst2(u2 d, rsrc_t r, int v, int s) { __builtin_amdgcn_raw_buffer_store_b64(d, r, v, s, AUX); }

template <int CTRL> DI float dpp(float v) {
    return __builtin_bit_cast(float, __builtin_amdgcn_update_dpp(0, __builtin_bit_cast(int, v), CTRL, 0xf, 0xf, true));
}
template <bool ALL> DI float red_lanes(float v) {
    v += dpp<0xB1>(v);
    if (ALL) v += dpp<0x4E>(v);
    v += dpp<0x124>(v); v += dpp<0x128>(v);
    u32 x = __builtin_bit_cast(u32, v), y = x;
    asm("" : "+v"(y));
    auto r = __builtin_amdgcn_permlane16_swap(x, y, false, false);
    v = __builtin_bit_cast(float, (u32)r[0]) + __builtin_bit_cast(float, (u32)r[1]);
    x = __builtin_bit_cast(u32, v); y = x;
    asm("" : "+v"(y));
    auto q = __builtin_amdgcn_permlane32_swap(x, y, false, false);
    return __builtin_bit_cast(float, (u32)q[0]) + __builtin_bit_cast(float, (u32)q[1]);
}
DI float ln_inv(float var) { return __builtin_amdgcn_rcpf(rbf(__builtin_amdgcn_sqrtf(rbf(rbf(var) + 1e-5f)))); }

#define ST_DECL
#define ST(i)
#define ST_END(ws, kid)

constexpr u32 WS_U = 0;
constexpr u32 WS_V = WS_U + (2u << 20);
constexpr u32 WS_LN2 = WS_V + (2u << 20);
constexpr u32 WS_WA = WS_LN2 + (2u << 20);
constexpr u32 WS_WB = WS_WA + 8192u;
constexpr u32 WS_FA = WS_WB + 8192u;
constexpr u32 WS_FLAG = WS_FA + 16384u;
constexpr u32 WS_EP = WS_FLAG + 32768u;
constexpr u32 WS_H1 = WS_WA + (128u << 10);
constexpr u32 WS_END = WS_H1 + 7340032u;

#define BGA 9
#define BGB 15
#define BGC 21
#define G_SLEEP 16
#define BG_DELAY 10
constexpr int BG5 = BGA, BG5b = BGB, BG6 = BGC;
constexpr int G1_DEPTH = 10, G2_DEPTH = 3;
constexpr int B_R = 5;
constexpr int CODE_LINES = 290, CODE_PF = 10;
constexpr int LDS_SCR = 114688;
constexpr int SCR_X = 8448;
constexpr int SCR_S = SCR_X + 20480;
constexpr int SCR_F = 49024;
constexpr int XCH = 147456;
constexpr int LDS_B = 163840;

DI void glds16(const void* sbase, u32 voff, u32 lds_dst) {
    asm volatile("s_mov_b32 m0, %2\n\ts_nop 0\n\tglobal_load_lds_dwordx4 %0, %1" :: "v"(voff), "s"(sbase), "s"(lds_dst) : "memory");
}
DI bool any_sent(u32 v) { return __builtin_amdgcn_ballot_w64(v == SENT) != 0; }
DI void poll2(u4& a, u4& c, rsrc_t rw, int vo, int sa, int sc) {
#pragma nounroll
    for (int sp = 0; sp < SPIN_MAX; ++sp) {
        const bool ba = max4(a) == SENT, bc = max4(c) == SENT;
        if (__builtin_amdgcn_ballot_w64(ba || bc) == 0) break;
        __builtin_amdgcn_s_sleep(1);
        FENCE();
        if (ba) a = bld4<SC1>(rw, vo, sa);
        if (bc) c = bld4<SC1>(rw, vo, sc);
    }
}
DI void wait_tok(u4& t0, u4& t1, rsrc_t rw, int vo, int so) {
    if (any_sent(umax(max4(t0), max4(t1)))) {
#pragma nounroll
        for (int sp = 0; sp < SPIN_MAX; ++sp) {
            FENCE();
            t0 = bld4<SC1>(rw, vo, so); t1 = bld4<SC1>(rw, vo, so + 16384);
            if (!any_sent(umax(max4(t0), max4(t1)))) break;
        }
    }
}
DI u4 ldw1(LAS char* wb, int kk, int ft) {
    s4 a0 = __builtin_amdgcn_ds_read_tr16_b64_v4i16((LAS s4*)(wb + kk * 32 * 224 + ft * 32));
    s4 a1 = __builtin_amdgcn_ds_read_tr16_b64_v4i16((LAS s4*)(wb + (kk * 32 + 16) * 224 + ft * 32));
    u2 x0 = __builtin_bit_cast(u2, a0), x1 = __builtin_bit_cast(u2, a1);
    return (u4){x0[0], x0[1], x1[0], x1[1]};
}
DI u2 pk4(f4 a) { return (u2){pk_b(a[0], a[1]), pk_b(a[2], a[3])}; }
DI u2 silu_pk2(u32 p0, u32 p1) {
    u32 o0, o1; float a0, a1, a2, a3, t0, t1, t2, t3;
    asm("v_lshlrev_b32 %2, 16, %10\n\tv_and_b32 %3, 0xffff0000, %10\n\tv_lshlrev_b32 %4, 16, %11\n\tv_and_b32 %5, 0xffff0000, %11\n\t"
        "v_mul_f32 %6, %12, %2\n\tv_mul_f32 %7, %12, %3\n\tv_mul_f32 %8, %12, %4\n\tv_mul_f32 %9, %12, %5\n\t"
        "v_exp_f32 %6, %6\n\tv_exp_f32 %7, %7\n\tv_exp_f32 %8, %8\n\tv_exp_f32 %9, %9\n\t"
        "v_add_f32 %6, 1.0, %6\n\tv_add_f32 %7, 1.0, %7\n\tv_add_f32 %8, 1.0, %8\n\tv_add_f32 %9, 1.0, %9\n\t"
        "v_rcp_f32 %6, %6\n\tv_rcp_f32 %7, %7\n\tv_rcp_f32 %8, %8\n\tv_rcp_f32 %9, %9\n\t"
        "v_mul_f32 %2, %2, %6\n\tv_mul_f32 %3, %3, %7\n\tv_mul_f32 %4, %4, %8\n\tv_mul_f32 %5, %5, %9\n\t"
        "v_cvt_pk_bf16_f32 %0, %2, %3\n\tv_cvt_pk_bf16_f32 %1, %4, %5"
        : "=&v"(o0), "=&v"(o1), "=&v"(a0), "=&v"(a1), "=&v"(a2), "=&v"(a3), "=&v"(t0), "=&v"(t1), "=&v"(t2), "=&v"(t3)
        : "v"(p0), "v"(p1), "s"(-1.4426950408889634f));
    return (u2){o0, o1};
}
DI u4 silu_pk4(u4 p) { u2 a = silu_pk2(p[0], p[1]), b = silu_pk2(p[2], p[3]); return (u4){a[0], a[1], b[0], b[1]}; }
DI void lds_set(LAS u32* p, u32 v) {
    asm volatile("s_waitcnt lgkmcnt(0)" ::: "memory");
    __hip_atomic_store(p, v, __ATOMIC_RELAXED, __HIP_MEMORY_SCOPE_WORKGROUP);
}
DI void lds_wait(LAS u32* p, u32 v) {
#pragma nounroll
    for (int sp = 0; sp < SPIN_MAX; ++sp) {
        if (__builtin_amdgcn_readfirstlane(__hip_atomic_load(p, __ATOMIC_RELAXED, __HIP_MEMORY_SCOPE_WORKGROUP)) >= v) break;
        __builtin_amdgcn_s_sleep(1);
    }
    FENCE();
}
DI void flag_wait(GAS u32* p, u32 tag) {
#pragma nounroll
    for (int sp = 0; sp < SPIN_MAX; ++sp) {
        if (__builtin_amdgcn_ballot_w64(__hip_atomic_load(p, __ATOMIC_RELAXED, __HIP_MEMORY_SCOPE_AGENT) != tag) == 0) break;
        __builtin_amdgcn_s_sleep(2);
    }
    FENCE();
}
#define SENT4 ((u4){SENT, SENT, SENT, SENT})

__global__ __launch_bounds__(512, 2) void k_fused(const uint16_t* x, const uint16_t* wr, const uint16_t* wi, const uint16_t* w1,
                                                  const uint16_t* w2, uint16_t* out, char* ws) {
    extern __shared__ __attribute__((aligned(16))) char smem[];
    const int tid = threadIdx.x, l = tid & 63, g = l >> 4, m = l & 15;
    const int w = __builtin_amdgcn_readfirstlane(tid >> 6);
    const int b = (blockIdx.x & 7) * 32 + (blockIdx.x >> 3);
    const int mt1 = (b >> 6) * 4 + ((b >> 3) & 3), nt1 = ((b >> 5) & 1) * 8 + (b & 7);
    const int grp = b >> 4, th = (b >> 3) & 1, fb = b & 7;
    const rsrc_t rw = mkrs(ws, WS_END), ro = mkrs(out, 2097152);
    char* scr = smem + LDS_SCR;
    LAS u32* hw = (LAS u32*)((LAS char*)smem + LDS_SCR + SCR_F);
    const u32 lds0 = (u32)(uintptr_t)(LAS char*)smem;
    const float mf = (float)m, gf = (float)g, sg = (m & 1) ? -1.0f : 1.0f;
    const bool aw = w < 4;
    ST_DECL;
    unsigned long long pc0;
    asm volatile("s_getpc_b64 %0" : "=s"(pc0));
    if (tid >= 448 && l < CODE_PF)
        asm volatile("s_mov_b32 m0, %2\n\ts_nop 0\n\tglobal_load_lds_dword %0, %1"
                     :: "v"(umin((blockIdx.x >> 3) * CODE_PF + l, CODE_LINES - 1) * 128), "s"(pc0), "s"(lds0 + LDS_SCR + SCR_F + 64) : "memory");
    const u32 epoch = __hip_atomic_load((GAS u32*)(ws + WS_EP), __ATOMIC_RELAXED, __HIP_MEMORY_SCOPE_AGENT);
    FENCE();
    u32 xd[8];
    const int f1 = b >> 5, cg = b & 31;
    const bool mir = cg >= 16;
    const rsrc_t rr = mkrs(wr, 1052672), ri = mkrs(wi, 1052672);
    u4 fr, fi; u32 sr_, si_;
    if (aw) {
        const u32* xr = (const u32*)(x + (256 * (2 * w + ((m >> 1) & 1)) + b) * 512) + 8 * g + 2 * (m >> 2) + (m & 1);
#pragma unroll
        for (int j = 0; j < 8; ++j) xd[j] = xr[32 * j];
    } else {
        __builtin_amdgcn_s_sleep(PRO_SLEEP);
        bst4<SC1>(SENT4, rw, l * 16, WS_U + b * 8192 + (w - 4) * 2048); bst4<SC1>(SENT4, rw, l * 16 + 1024, WS_U + b * 8192 + (w - 4) * 2048);
        __builtin_amdgcn_raw_buffer_store_b32(SENT, rw, l < 4 ? (l >> 1) * 8192 + (l & 1) * 1024 : 0x7ffffff0,
                                              WS_WA + (w - 4) * 2048 + ((b >> 6) * 16 + (b & 15)) * 16 + ((b >> 4) & 3) * 4, SC1);
        FENCE();
        if (w == 6) {
            const int c = (l >> 3) & 3, f_ = 8 * b + (l & 7);
            const int e1 = ((c & 1) ? (2048 - f_) & 2047 : f_) * 514 + (c >> 1) * 512;
            sr_ = __builtin_amdgcn_raw_buffer_load_b16(rr, e1, 0, 0); si_ = __builtin_amdgcn_raw_buffer_load_b16(ri, e1, 0, 0);
        }
    }
    FENCE();
    const int zo = (64 * g + (g < 2 ? m : (m + 15) & 15)) * 4;
    const int rq = ((b >> 3) + b) & 3;
    const int row0 = (tid & 255) / 14, cch0 = (tid & 255) - row0 * 14;
    const char* wsrc = (const char*)w1 + nt1 * 224;
    u32 w1o[7];
    auto w1_burst = [&](int i0, int n) {
#pragma unroll
        for (int i = i0; i < i0 + n; ++i) {
            const int q = (i / 7 + rq) & 3, j = i % 7;
            glds16(wsrc + q * (128 * 3584), w1o[j], lds0 + (w - 4) * 1024 + q * 28672 + j * 4096);
        }
    };
    const int xoff = (256 * w + b) * 1024 + m * 32 + g * 8;
    u2 xl[2];
    float c16[4], s16[4];
    f2 twc[2], tws[2];
    u4 Gre3, Gim3, SGre, SGim, GA[2];
    auto consts0 = [&]() {
#pragma unroll
        for (int u = 0; u < 4; ++u) {
            float a = (4.0f * gf + u) * (mf * (1.0f / 16));
            c16[u] = cosr(a); s16[u] = sinr(a);
            twc[u >> 1][u & 1] = cosr(a * (1.0f / 32)); tws[u >> 1][u & 1] = sinr(a * (1.0f / 32));
        }
    };
    auto consts3 = [&]() {
#pragma unroll
        for (int u = 0; u < 4; ++u) {
            const float k = 4.0f * gf + u;
            const bool k0 = u == 0 && g == 0;
            float cc = c16[u], ss = s16[u];
            Gre3[u] = pk_b(cc, -ss); Gim3[u] = pk_b(ss, cc);
            float a2 = (2.0f * k + 1.0f) * (mf * (1.0f / 32));
            u32 sp = pk_b(g < 2 ? 2.f * cc : 2.f * cosr(a2), g < 2 ? -2.f * ss : -2.f * sinr(a2));
            SGre[u] = k0 ? pk_b(1.0f, sg) : (g < 2 ? sp : 0u);
            SGim[u] = g < 2 ? 0u : sp;
            float a4 = k * (mf * (1.0f / 32)), c4 = 2.f * cosr(a4), s4_ = -2.f * sinr(a4);
            GA[0][u] = k0 ? pk_b(1.0f, sg) : pk_b(c4, s4_);
            GA[1][u] = k0 ? pk_b(1.0f, sg) : ((u & 1) ? pk_b(-c4, -s4_) : pk_b(c4, s4_));
        }
        PIN(Gre3); PIN(Gim3); PIN(SGre); PIN(SGim); PIN(GA[0]); PIN(GA[1]); PIN(twc[0]); PIN(twc[1]); PIN(tws[0]); PIN(tws[1]);
    };
    const int pw = (w + 4 * th) & 7;
    GAS u32* fl = (GAS u32*)(ws + WS_FLAG);
    int ko[7];
    {
        const int r6 = (fb < 6 ? fb : fb - 5) + 3 * (grp & 1);
#pragma unroll
        for (int ks = 0; ks < 6; ++ks) { int k = ks + r6; k = k >= 6 ? k - 6 : k; ko[ks] = k >= 6 ? k - 6 : k; }
        ko[6] = 6;
    }
    u4 W[7][4];
    const u32 w2vo = (l >> 3) * 1024 + (((l & 7) ^ (((l >> 4) & 1) << 2)) * 16);
    auto w2_glds = [&](int p_, int ks, u32 dst) {
        const int q = ko[ks];
        const char* src = (const char*)w2 + (224 * p_ + (ks == 6 ? 96 : (q < 3 ? 32 * q : 16 + 32 * q))) * 1024 + fb * 128;
        glds16(src, w2vo, dst); glds16(src + 8192, w2vo, dst + 1024);
        glds16(src + (ks == 6 ? 112 : 16) * 1024, w2vo, dst + 2048); glds16(src + (ks == 6 ? 120 : 24) * 1024, w2vo, dst + 3072);
    };
    LAS char* w2rd = (LAS char*)smem + (4 * g + (m >> 2)) * 128 + (((m & 3) ^ (((m >> 3) & 1) << 2)) * 16);
    auto w2_read = [&](u4 (&f)[4], int off) {
#pragma unroll
        for (int t = 0; t < 4; ++t) {
            LAS char* p = (LAS char*)((u32)(uintptr_t)w2rd ^ ((t >> 1) * 64)) + off + (t & 1) * 8;
            u2 x0 = __builtin_bit_cast(u2, __builtin_amdgcn_ds_read_tr16_b64_v4i16((LAS s4*)p));
            u2 x1 = __builtin_bit_cast(u2, __builtin_amdgcn_ds_read_tr16_b64_v4i16((LAS s4*)(p + 2048)));
            f[t] = (u4){x0[0], x0[1], x1[0], x1[1]};
        }
    };
    auto bg = [&](int i0, int i1) { w1_burst(i0, i1 - i0); };
    u32 tag;
    if (__builtin_expect(aw, 1)) {
        if (tid < 8) hw[tid] = 0;
        FENCE();
        ST(2);
        {
            u4 FAre, FAim, Fre, Fim, Sre, Sim; u2 Mb; f2 pc[2], ps[2];
            consts0();
            float c0[8], s0[8];
#pragma unroll
            for (int j = 0; j < 8; ++j) {
                float a = (gf + 4.0f * j) * (mf * (1.0f / 32));
                c0[j] = cosr(a); s0[j] = m ? -sinr(a) : ((g & 1) ? -1.0f : 1.0f);
            }
            const float mo = (2.0f * mf + 1.0f) * (1.0f / 32);
#pragma unroll
            for (int u = 0; u < 4; ++u) {
                FAre[u] = pk_h(c0[2 * u], c0[2 * u + 1]); FAim[u] = pk_h(s0[2 * u], s0[2 * u + 1]);
                float cc = c16[u], ss = s16[u];
                Fre[u] = pk_h(cc, ss); Fim[u] = pk_h(-ss, cc);
                float a2 = (4.0f * gf + u) * mo;
                u32 re = pk_h(m < 8 ? cc : cosr(a2), 0.f), im = pk_h(m < 8 ? (m ? -ss : ((u & 1) ? -1.0f : 1.0f)) : -sinr(a2), 0.f);
                Sre[u] = m < 8 ? re : re << 16; Sim[u] = m < 8 ? im : im << 16;
                pc[u & 1][u >> 1] = twc[u >> 1][u & 1]; ps[u & 1][u >> 1] = tws[u >> 1][u & 1];
            }
#pragma unroll
            for (int e = 0; e < 2; ++e) {
                float a = (float)(((m >> 1) * (256 * (2 * g + e) + b)) & 2047) * (1.0f / 2048);
                float cc = cosr(a), ss = sinr(a);
                Mb[e] = (m & 1) ? pk_h(-ss, cc) : pk_h(cc, ss);
            }
            PIN(FAre); PIN(FAim); PIN(Fre); PIN(Fim); PIN(Sre); PIN(Sim); PIN(Mb); PIN(pc[0]); PIN(pc[1]); PIN(ps[0]); PIN(ps[1]);
            ST(3);
            f2 v[8];
#pragma unroll
            for (int j = 0; j < 8; ++j) v[j] = bf_2(xd[j]);
            f2 s2 = ((v[0] + v[1]) + (v[2] + v[3])) + ((v[4] + v[5]) + (v[6] + v[7]));
            const float mean = red_lanes<false>(s2[0] + s2[1]) * (1.0f / 512);
            f2 q0 = {0.f, 0.f}, q1 = {0.f, 0.f};
#pragma unroll
            for (int j = 0; j < 8; ++j) { v[j] -= mean; if (j & 1) q1 = fma2(v[j], v[j], q1); else q0 = fma2(v[j], v[j], q0); }
            q0 += q1;
            const float inv = ln_inv(red_lanes<false>(q0[0] + q0[1]) * (1.0f / 512));
            const float off = (mean - rbf(mean)) * inv;
#pragma unroll
            for (int j = 0; j < 8; ++j) v[j] = fma2(v[j], (f2){inv, inv}, (f2){off, off});
            u4 B[2];
#pragma unroll
            for (int p = 0; p < 2; ++p) {
                u4 A = {pk_h(v[0][p], v[1][p]), pk_h(v[2][p], v[3][p]), pk_h(v[4][p], v[5][p]), pk_h(v[6][p], v[7][p])};
                f4 Dre = mma_h(A, FAre, Z4), Dim = mma_h(A, FAim, Z4);
#pragma unroll
                for (int r = 0; r < 2; ++r) {
                    f2 re = r ? hi2(Dre) : lo2(Dre), im = r ? hi2(Dim) : lo2(Dim);
                    f2 tr = fma2(im, ps[p], re * pc[p]), ti = fma2(-re, ps[p], im * pc[p]);
                    B[r][p] = pk_h(tr[0], ti[0]); B[r][2 + p] = pk_h(tr[1], ti[1]);
                }
            }
#pragma unroll
            for (int r = 0; r < 2; ++r) {
                f4 Gre = mma_h(Fre, B[r], Z4), Gim = mma_h(Fim, B[r], Z4), Hre = mma_h(Sre, B[r], Z4), Him = mma_h(Sim, B[r], Z4);
#pragma unroll
                for (int i = 0; i < 4; ++i) *(u32*)(scr + (2 * w + r) * 1024 + zo + i * 64) = pk_h(m ? Gre[i] : Hre[i], m ? Gim[i] : Him[i]);
            }
            BARRIER();
            ST(18);
            u4 r0 = *(const u4*)(scr + (2 * g) * 1024 + w * 256 + m * 16);
            u4 r1 = *(const u4*)(scr + (2 * g + 1) * 1024 + w * 256 + m * 16);
            u4 o0, o1;
#pragma unroll
            for (int t = 0; t < 4; ++t) {
                f4 D = mma16_h(Mb, (u2){r0[t], r1[t]}, Z4);
                o0[t] = clean(pk_h(D[0], D[1])); o1[t] = clean(pk_h(D[2], D[3]));
            }
            bst4<SC1>(o0, rw, g * 2048 + m * 16, WS_U + b * 8192 + w * 256);
            bst4<SC1>(o1, rw, g * 2048 + m * 16 + 1024, WS_U + b * 8192 + w * 256);
        }
        ST(4);
        u4 Fre, Fim, Gre, Gim; u2 Nb; f2 tc[2], ts[2];
#pragma unroll
        for (int u = 0; u < 4; ++u) {
            float cc = c16[u], ss = s16[u];
            Fre[u] = pk_h(0.25f * cc, 0.25f * ss); Fim[u] = pk_h(-0.25f * ss, 0.25f * cc);
            Gre[u] = pk_b(cc * (1.0f / 256), -ss * (1.0f / 256)); Gim[u] = pk_b(ss * (1.0f / 256), cc * (1.0f / 256));
            float a3 = (4.0f * gf + u) * (mf * (1.0f / 256));
            tc[u >> 1][u & 1] = cosr(a3); ts[u >> 1][u & 1] = sinr(a3);
        }
        PIN(Fre); PIN(Fim); PIN(Gre); PIN(Gim); PIN(tc[0]); PIN(tc[1]); PIN(ts[0]); PIN(ts[1]);
        xl[0] = *(const u2*)((const char*)x + xoff); xl[1] = *(const u2*)((const char*)x + xoff + 512);
        FENCE();
        tag = __builtin_amdgcn_readfirstlane(epoch) + 1u;
        consts3();
#pragma unroll
        for (int e = 0; e < 2; ++e) {
            float a1 = (float)(((2 * g + e) * (256 * (m >> 1) + b)) & 2047) * (1.0f / 2048);
            float c1 = cosr(a1), s1 = sinr(a1);
            Nb[e] = (m & 1) ? pk_b(s1, c1) : pk_b(c1, -s1);
        }
        PIN(Nb);
        WAITV(0);
        ST(19);
        BARRIER();
        ST(5);
        u4 d[2], wa, wb;
        const int uo = (tid >> 1) * 8192 + (tid & 1) * 16, us = WS_U + f1 * 1024 + cg * 32;
        d[0] = bld4<SC1>(rw, uo, us); d[1] = bld4<SC1>(rw, uo, us + 128 * 8192);
        if (cg == 0) {
#pragma nounroll
            for (int sp = 0; sp < SPIN_MAX; ++sp) {
                FENCE();
                const u32 ux = __builtin_amdgcn_raw_buffer_load_b32(rw, tid * 8192, WS_U + ((8 - f1) & 7) * 1024, SC1);
                wa = bld4<SC1>(rw, l * 16, WS_WA + f1 * 1024); wb = bld4<SC1>(rw, l * 16, WS_WB + f1 * 1024);
                *(u32*)(scr + SCR_X + tid * 4) = ux;
                if (!any_sent(umax(ux, umax(max4(wa), max4(wb))))) break;
            }
        }
        poll2(d[0], d[1], rw, uo, us, us + 128 * 8192);
        char* sp = scr + SCR_S + (tid ^ ((tid >> 4) & 1)) * 16;
        *(u4*)sp = d[0]; *(u4*)(sp + 4096) = d[1];
        BARRIER();
        if (b == 0 && tid == 0) __hip_atomic_store((GAS u32*)(ws + WS_EP), tag, __ATOMIC_RELAXED, __HIP_MEMORY_SCOPE_AGENT);
        ST(6);
        u2 ld[4], wl[4];
        const int so = (64 * g + m) * 32 + (((w >> 1) ^ (m >> 3)) * 16) + (w & 1) * 8;
#pragma unroll
        for (int q = 0; q < 4; ++q) ld[q] = *(const u2*)(scr + SCR_S + so + 512 * q);
#pragma unroll
        for (int q = 0; q < 4; ++q) wl[q] = *(const u2*)(scr + SCR_S + so + 8192 + 512 * q);
        FENCE();
        auto fwd = [&](u4 A, f4& Xr, f4& Xi) {
            f4 Dre = mma_h(A, Fre, Z4), Dim = mma_h(A, Fim, Z4);
            f2 r0 = fma2(lo2(Dim), ts[0], lo2(Dre) * tc[0]), i0 = fma2(-lo2(Dre), ts[0], lo2(Dim) * tc[0]);
            f2 r1 = fma2(hi2(Dim), ts[1], hi2(Dre) * tc[1]), i1 = fma2(-hi2(Dre), ts[1], hi2(Dim) * tc[1]);
            u4 B = {pk_h(r0[0], i0[0]), pk_h(r0[1], i0[1]), pk_h(r1[0], i1[0]), pk_h(r1[1], i1[1])};
            Xr = mma_h(Fre, B, Z4); Xi = mma_h(Fim, B, Z4);
        };
        f4 Xre[2], Xim[2];
#pragma unroll
        for (int e = 0; e < 2; ++e) fwd((u4){ld[0][e], ld[1][e], ld[2][e], ld[3][e]}, Xre[e], Xim[e]);
        f2 yr[2][2], yi[2][2];
#pragma unroll
        for (int e = 0; e < 2; ++e)
#pragma unroll
            for (int hh = 0; hh < 2; ++hh) {
                f2 wre = {bf_lo(wl[2 * hh][e]), bf_lo(wl[2 * hh + 1][e])}, wim = {bf_hi(wl[2 * hh][e]), bf_hi(wl[2 * hh + 1][e])};
                f2 xr = hh ? hi2(Xre[e]) : lo2(Xre[e]), xi = hh ? hi2(Xim[e]) : lo2(Xim[e]);
                yr[e][hh] = fma2(-xi, wim, xr * wre); yi[e][hh] = fma2(xr, wim, xi * wre);
            }
        if (cg == 0 && w == 0) {
            const u32* up = (const u32*)(scr + SCR_X) + 64 * g + m;
            f4 Mr, Mi;
            fwd((u4){up[0], up[16], up[32], up[48]}, Mr, Mi);
            f2* sx = (f2*)(scr + SCR_X + 1024);
#pragma unroll
            for (int i = 0; i < 4; ++i) sx[m + 16 * (4 * g + i)] = (f2){Mr[i], Mi[i]};
#pragma unroll
            for (int i = 0; i < 4; ++i) {
                f2 xm = sx[((f1 ? 255 : 256) - (m + 16 * (4 * g + i))) & 255];
                float ar = bf_lo(wa[i]), ai = bf_hi(wa[i]), br = bf_lo(wb[i]), bi = bf_hi(wb[i]);
                yr[0][i >> 1][i & 1] = Xre[0][i] * ar - Xim[0][i] * ai + xm[0] * br + xm[1] * bi;
                yi[0][i >> 1][i & 1] = Xre[0][i] * ai + Xim[0][i] * ar + xm[0] * bi - xm[1] * br;
            }
        }
        char* pv = scr;
#pragma unroll
        for (int e = 0; e < 2; ++e) {
            u4 Y = {pk_b(yr[e][0][0], yi[e][0][0]), pk_b(yr[e][0][1], yi[e][0][1]), pk_b(yr[e][1][0], yi[e][1][0]), pk_b(yr[e][1][1], yi[e][1][1])};
            f4 Dre = mma_b(Y, Gre, Z4), Dim = mma_b(Y, Gim, Z4);
            f2 r0 = fma2(-lo2(Dim), ts[0], lo2(Dre) * tc[0]), i0 = fma2(lo2(Dre), ts[0], lo2(Dim) * tc[0]);
            f2 r1 = fma2(-hi2(Dim), ts[1], hi2(Dre) * tc[1]), i1 = fma2(hi2(Dre), ts[1], hi2(Dim) * tc[1]);
            u4 A = {pk_b(r0[0], i0[0]), pk_b(r0[1], i0[1]), pk_b(r1[0], i1[0]), pk_b(r1[1], i1[1])};
            f4 Ere = mma_b(A, Gre, Z4), Eim = mma_b(A, Gim, Z4);
            u4 o;
#pragma unroll
            for (int i = 0; i < 4; ++i) o[i] = clean(pk_b(Ere[i], Eim[i]));
            *(u4*)(pv + (2 * w + e) * 1024 + ((m * 4 + g) ^ ((w >> 1) * 4)) * 16) = o;
        }
        BARRIER();
        ST(7);
#pragma unroll
        for (int it = 0; it < 2; ++it) {
            const int ch = tid + 256 * it;
            const u32* q = (const u32*)(pv + (ch & 1) * 4096 + (((ch >> 1) * 4) ^ ((ch & 1) * 64)));
            bst4<SC1>((u4){q[0], q[256], q[512], q[768]}, rw, ch * 16, WS_V + b * 8192);
        }
        WAITV(0);
        BARRIER();
        ST(8);
        const int vo = (g * 64 + (m >> 1)) * 8192 + (m & 1) * 16, vs = WS_V + w * 8 * 8192 + b * 32;
        u4 v0 = bld4<SC1>(rw, vo, vs), v1 = bld4<SC1>(rw, vo, vs + 32 * 8192);
        poll2(v0, v1, rw, vo, vs, vs + 32 * 8192);
        ST(9);
        u4 z0, z1;
#pragma unroll
        for (int t = 0; t < 4; ++t) {
            f4 D = mma16_b(Nb, (u2){v0[t], v1[t]}, Z4);
            z0[t] = pk_b(D[0], D[1]); z1[t] = pk_b(D[2], D[3]);
        }
        *(u4*)(scr + SCR_X + g * 2048 + w * 256 + m * 16) = z0;
        *(u4*)(scr + SCR_X + g * 2048 + 1024 + w * 256 + m * 16) = z1;
    } else {
        auto announce = [&](int which) {
            if (l < 8) __hip_atomic_store((GAS u32*)(ws + WS_FA) + which * 2048 + l * 256 + b, tag, __ATOMIC_RELAXED, __HIP_MEMORY_SCOPE_AGENT);
            FENCE();
        };
        auto all_announced = [&](int which) {
#pragma nounroll
            for (int sp = 0; sp < SPIN_MAX; ++sp) {
                FENCE();
                u4 f = bld4<SC1>(rw, l * 16, WS_FA + which * 8192 + (b >> 5) * 1024);
                if (__builtin_amdgcn_ballot_w64(f[0] != tag || f[1] != tag || f[2] != tag || f[3] != tag) == 0) break;
                __builtin_amdgcn_s_sleep(4);
            }
        };
        FENCE();
        ST(2);
#pragma unroll
        for (int jj = 0; jj < 7; ++jj) {
            const int cj_ = (4 * jj) % 14, rj = 18 * jj + (4 * jj) / 14;
            w1o[jj] = (row0 + rj) * 3584 + (cch0 + cj_) * 16 + (cch0 + cj_ >= 14 ? 3584 - 224 : 0);
            PIN(w1o[jj]);
        }
        tag = __builtin_amdgcn_readfirstlane(epoch) + 1u;
        if (w == 6) WAITV(2); else WAITV(0);
        if (l < 2) __hip_atomic_store((GAS u32*)(ws + WS_FA) + (2 * (w - 4) + l) * 256 + b, tag, __ATOMIC_RELAXED, __HIP_MEMORY_SCOPE_AGENT);
        FENCE();
        ST(18);
        BARRIER();
        if (w == 6) {
            float re = bf_lo(sr_), im = bf_lo(si_);
            re += dpp<0x128>(re); im -= dpp<0x128>(im);
            u32 a = __builtin_bit_cast(u32, re), a2 = a, c = __builtin_bit_cast(u32, im), c2 = c;
            asm("" : "+v"(a2), "+v"(c2));
            auto pr = __builtin_amdgcn_permlane16_swap(a, a2, false, false);
            auto pi = __builtin_amdgcn_permlane16_swap(c, c2, false, false);
            const float ar = __builtin_bit_cast(float, (u32)pr[0]), br = __builtin_bit_cast(float, (u32)pr[1]);
            const float ai = __builtin_bit_cast(float, (u32)pi[0]), bi = __builtin_bit_cast(float, (u32)pi[1]);
            const int wo = WS_WA + ((b >> 6) * 16 + (b & 15)) * 16 + ((b >> 4) & 3) * 4;
            if (l < 8) {
                __builtin_amdgcn_raw_buffer_store_b32(clean(pk_b(0.25f * (ar + br), 0.25f * (ai + bi))), rw, l * 1024, wo, SC1);
                __builtin_amdgcn_raw_buffer_store_b32(clean(pk_b(0.25f * (ar - br), 0.25f * (ai - bi))), rw, l * 1024, wo + 8192, SC1);
            }
        }
        consts0();
        __builtin_amdgcn_s_sleep(G_SLEEP);
        if (w == 7) all_announced(0);
        const int f = f1 + 8 * (tid & 255), k0 = 16 * cg - 8 * (cg & 1);
        const int e = mir ? ((2048 - f) & 2047) * 514 + (504 - k0) * 2 : f * 514 + k0 * 2;
        fr = bld4<0>(rr, e, 0); fi = bld4<0>(ri, e, 0);
        xl[0] = *(const u2*)((const char*)x + xoff); xl[1] = *(const u2*)((const char*)x + xoff + 512);
        FENCE();
        ST(4);
        BARRIER();
        {
            u32 o[8];
#pragma unroll
            for (int c = 0; c < 8; ++c)
                o[c] = mir ? __builtin_amdgcn_perm(fi[3 - (c >> 1)], fr[3 - (c >> 1)], (c & 1) ? 0x05040100u : 0x07060302u) ^ 0x80000000u
                           : __builtin_amdgcn_perm(fi[c >> 1], fr[c >> 1], (c & 1) ? 0x07060302u : 0x05040100u);
            char* wp = scr + SCR_S + 8192 + (tid & 255) * 32;
            *(u4*)(wp + (m & 8) * 2) = (u4){o[0], o[1], o[2], o[3]};
            *(u4*)(wp + ((m & 8) * 2 ^ 16)) = (u4){o[4], o[5], o[6], o[7]};
        }
#pragma unroll
        for (int i = 0; i < 2; ++i) bst4<SC1>(SENT4, rw, l * 16 + i * 1024, WS_V + b * 8192 + (w - 4) * 2048);
        FENCE();
        consts3();
        WAITV(0);
        ST(5);
        BARRIER();
        if (w == 7) announce(1);
#pragma unroll
        for (int i = 0; i < 2; ++i) bst4<SC1>(SENT4, rw, (l >> 3) * 256 + (l & 7) * 16 + i * 2048, WS_LN2 + (b >> 1) * 16384 + (b & 1) * 128 + (w - 4) * 4096);
        bg(0, BG5);
        WAITV(BG5);
        ST(6);
        BARRIER();
        if (w == 7) all_announced(1);
        else __builtin_amdgcn_s_sleep(3);
        bg(BG5, BG5b);
        ST(7);
        BARRIER();
        __builtin_amdgcn_s_sleep(BG_DELAY);
        bg(BG5b, BG6);
    }
    BARRIER();
    {
        const u32* zp = (const u32*)(scr + SCR_X + w * 1024 + zo);
        u4 A = {zp[0], zp[16], zp[32], zp[48]};
        f4 Dre = mma_b(A, Gre3, Z4), Dim = mma_b(A, Gim3, Z4), Hre = mma_b(A, SGre, Z4), Him = mma_b(A, SGim, Z4);
        if (g == 0) { Dre[0] = Hre[0]; Dim[0] = Him[0]; }
        f2 r0 = fma2(-lo2(Dim), tws[0], lo2(Dre) * twc[0]), i0 = fma2(lo2(Dre), tws[0], lo2(Dim) * twc[0]);
        f2 r1 = fma2(-hi2(Dim), tws[1], hi2(Dre) * twc[1]), i1 = fma2(hi2(Dre), tws[1], hi2(Dim) * twc[1]);
        u4 A2 = {pk_b(r0[0], i0[0]), pk_b(r0[1], i0[1]), pk_b(r1[0], i1[0]), pk_b(r1[1], i1[1])};
        f2 h[4]; u2 hp[2];
#pragma unroll
        for (int t = 0; t < 2; ++t) {
            f4 Yv = mma_b(A2, GA[t], Z4);
            u32 h0 = pk_b2(bf_2(xl[t][0]) + bf_2(pk_b(Yv[0], Yv[1]))), h1 = pk_b2(bf_2(xl[t][1]) + bf_2(pk_b(Yv[2], Yv[3])));
            hp[t] = (u2){h0, h1};
            h[2 * t] = bf_2(h0); h[2 * t + 1] = bf_2(h1);
        }
        f2 s2 = (h[0] + h[1]) + (h[2] + h[3]);
        const float mean = red_lanes<true>(s2[0] + s2[1]) * (1.0f / 512);
        f2 q2 = {0.f, 0.f};
#pragma unroll
        for (int j = 0; j < 4; ++j) { f2 d = h[j] - mean; q2 = fma2(d, d, q2); }
        const float inv = ln_inv(red_lanes<true>(q2[0] + q2[1]) * (1.0f / 512));
        const float mb = rbf(mean);
#pragma unroll
        for (int t = 0; t < 2; ++t)
            *(u2*)(scr + ((8 * t + (m >> 1)) * 4 + g) * 128 + (w ^ (m >> 1)) * 16 + (m & 1) * 8) =
                (u2){clean(pk_b2(bf_2(pk_b2(h[2 * t] - mb)) * inv)), clean(pk_b2(bf_2(pk_b2(h[2 * t + 1] - mb)) * inv))};
        WAITV(0);
        BARRIER();
        u4 lo = *(const u4*)(scr + (tid ^ ((tid >> 5) & 7)) * 16);
        bst4<SC1>(lo, rw, (tid >> 3) * 256 + (tid & 7) * 16, WS_LN2 + (b >> 1) * 16384 + (b & 1) * 128);
        FENCE();
        if (aw) WAITV(0); else __builtin_amdgcn_s_sleep(8);
        bst2<SC1>(hp[0], ro, xoff, 0); bst2<SC1>(hp[1], ro, xoff + 512, 0);
        FENCE();
        ST(10);
    }
    auto stage_out = [&](int r) {
        w2_read(W[r], LDS_SCR + (w & 3) * 8192 + (r & 1) * 4096);
        asm volatile("s_waitcnt lgkmcnt(0)" ::: "memory");
        PIN(W[r][0]); PIN(W[r][1]); PIN(W[r][2]); PIN(W[r][3]);
        if (r + 2 < 7) w2_glds(pw, r + 2, lds0 + LDS_SCR + (w & 3) * 8192 + (r & 1) * 4096);
    };
    auto publish = [&]() {
        u32 prev = 0;
        if (l == 0) prev = __hip_atomic_fetch_add(hw + 4, 1u, __ATOMIC_RELAXED, __HIP_MEMORY_SCOPE_WORKGROUP);
        if (__builtin_amdgcn_readfirstlane(prev) == 7u && l < 2)
            __hip_atomic_store(fl + (l ? 4096 + (b >> 1) * 32 + (b & 1) : (mt1 * 8 + (nt1 >> 1)) * 32 + (nt1 & 1)), tag, __ATOMIC_RELAXED, __HIP_MEMORY_SCOPE_AGENT);
        FENCE();
    };
    {
        const int tg = w & 3;
        char* xch = smem + XCH + tg * 3072;
        const int hoff = WS_H1 + (mt1 * 8 + 2 * tg) * (56 * 1024) + (nt1 >> 1) * 7168;
        if (__builtin_expect(aw, 1)) {
            const int toff = WS_LN2 + (mt1 * 8 + 2 * tg) * 16384;
            LAS char* wb0 = (LAS char*)smem + (4 * g + (m >> 2)) * 224 + (m & 3) * 8;
            LAS char* wb[4];
            int tq[4];
#pragma unroll
            for (int q = 0; q < 4; ++q) { const int qq = (q + rq) & 3; wb[q] = wb0 + qq * 28672; tq[q] = toff + qq * 4096; }
            u4 T[2][16], A[2][7];
            auto ldt = [&](int kk) {
#pragma unroll
                for (int tt = 0; tt < 2; ++tt) T[tt][kk] = bld4<SC1>(rw, l * 16 + (kk & 3) * 1024, tq[kk >> 2] + tt * 16384);
            };
#pragma unroll
            for (int i = 0; i < 7; ++i) A[0][i] = ldw1(wb[0], 0, i);
            if (l == 0) __hip_atomic_fetch_add(hw + 5, 1u, __ATOMIC_RELAXED, __HIP_MEMORY_SCOPE_WORKGROUP);
            ST(11);
#pragma unroll
            for (int kk = 0; kk < G1_DEPTH; ++kk) { ldt(kk); SCHED(); }
            f4 acc[2][7];
#pragma unroll
            for (int tt = 0; tt < 2; ++tt)
#pragma unroll
                for (int i = 0; i < 7; ++i) acc[tt][i] = Z4;
#pragma unroll
            for (int kk = 0; kk < 16; ++kk) {
                if ((kk & 1) == 0) {
                    if (__builtin_expect(any_sent(umax(umax(max4(T[0][kk]), max4(T[1][kk])), umax(max4(T[0][kk + 1]), max4(T[1][kk + 1])))), 0)) {
                        wait_tok(T[0][kk], T[1][kk], rw, l * 16, tq[kk >> 2] + (kk & 3) * 1024);
                        wait_tok(T[0][kk + 1], T[1][kk + 1], rw, l * 16, tq[kk >> 2] + ((kk + 1) & 3) * 1024);
                    }
                    if (kk == 0) ST(12);
                    if (kk == (BG6 / 7) * 4 - 2) lds_wait(hw + 5, 8u);
                }
                if (kk + G1_DEPTH < 16) ldt(kk + G1_DEPTH);
                if (kk < 15) {
#pragma unroll
                    for (int i = 0; i < 7; ++i) A[(kk + 1) & 1][i] = ldw1(wb[(kk + 1) >> 2], (kk + 1) & 3, i);
                }
#pragma unroll
                for (int i = 0; i < 7; ++i)
#pragma unroll
                    for (int tt = 0; tt < 2; ++tt) acc[tt][i] = mma_b(A[kk & 1][i], T[tt][kk], acc[tt][i]);
#pragma unroll
                for (int i = 0; i < 14; ++i) {
                    __builtin_amdgcn_sched_group_barrier(0x8, 1, 0);
                    if (i == 1 || i == 7) __builtin_amdgcn_sched_group_barrier(0x20, 1, 0);
                    __builtin_amdgcn_sched_group_barrier(0x100, 1, 0);
                    if ((kk & 1) && i >= 3) __builtin_amdgcn_sched_group_barrier(0x2, 1, 0);
                }
                if (kk & 1) {} else SCHED();
            }
#pragma unroll
            for (int q = 0; q < 3; ++q) {
                u2 p0 = pk4(acc[1][2 * q]), p1 = pk4(acc[1][2 * q + 1]);
                *(u4*)(xch + q * 1024 + l * 16) = (u4){p0[0], p0[1], p1[0], p1[1]};
            }
            lds_set(hw + tg, 1u);
            ST(13);
#pragma unroll
            for (int q = 0; q < 3; ++q) {
                u2 p0 = pk4(acc[0][2 * q]), p1 = pk4(acc[0][2 * q + 1]);
                bst4<SC1>(silu_pk4((u4){p0[0], p0[1], p1[0], p1[1]}), rw, l * 16, hoff + ((nt1 & 1) * 3 + q) * 1024);
            }
#pragma unroll
            for (int tt = 0; tt < 2; ++tt) { u2 p = pk4(acc[tt][6]); bst2<SC1>(silu_pk2(p[0], p[1]), rw, l * 8, hoff + tt * (56 * 1024) + 6144 + (nt1 & 1) * 512); }
        } else {
            bg(BG6, 28);
            WAITV(0);
            if (l == 0) __hip_atomic_fetch_add(hw + 5, 1u, __ATOMIC_RELAXED, __HIP_MEMORY_SCOPE_WORKGROUP);
            __builtin_amdgcn_s_sleep(8);
            lds_wait(hw + 5, 8u);
            w2_glds(pw, 0, lds0 + LDS_SCR + tg * 8192);
            w2_glds(pw, 1, lds0 + LDS_SCR + tg * 8192 + 4096);
#pragma unroll
            for (int r = 0; r < B_R; ++r) { WAITV(4); stage_out(r); }
            ST(12);
            lds_wait(hw + tg, 1u);
            ST(13);
#pragma unroll
            for (int q = 0; q < 3; ++q)
                bst4<SC1>(silu_pk4(*(const u4*)(xch + q * 1024 + l * 16)), rw, l * 16, hoff + 56 * 1024 + ((nt1 & 1) * 3 + q) * 1024);
        }
        ST(14);
        WAITV(0);
        publish();
        ST(21);
        if (__builtin_expect(aw, 1)) {
            lds_wait(hw + 4, 8u);
#pragma unroll
            for (int ks = 0; ks < 7; ++ks) w2_glds(pw, ks, lds0 + tg * 28672 + ks * 4096);
        } else {
            stage_out(B_R);
#pragma unroll
            for (int r = B_R + 1; r < 7; ++r) { if (r + 1 < 7) WAITV(4); else WAITV(0); stage_out(r); }
        }
    }
    {
        ST(15);
        const int rot = w & 3;
        if (aw) {
            flag_wait(fl + ((l & 4) ? 4096 + (grp * 8 + th * 4 + rot) * 32 : (grp * 8 + (pw ^ ((l & 2) * 2))) * 32) + (l & 1), tag);
            lds_set(hw + 8 + rot, tag);
        } else {
#pragma nounroll
            for (int sp = 0; sp < SPIN_MAX; ++sp) {
                if (__builtin_amdgcn_readfirstlane(__hip_atomic_load(hw + 8 + rot, __ATOMIC_RELAXED, __HIP_MEMORY_SCOPE_WORKGROUP)) == tag) break;
                __builtin_amdgcn_s_sleep(1);
            }
            FENCE();
        }
        ST(16);
        const int toff = WS_H1 + (grp * 8 + th * 4) * (56 * 1024) + pw * 7168;
        int to[4];
#pragma unroll
        for (int tt = 0; tt < 4; ++tt) to[tt] = toff + ((tt + rot) & 3) * (56 * 1024);
        u4 T[7][4];
        auto ldtok = [&](int ks) {
#pragma unroll
            for (int tt = 0; tt < 4; ++tt) {
                if (ks < 6) T[ks][tt] = bld4<SC1>(rw, l * 16, to[tt] + ko[ks] * 1024);
                else {
                    u2 a = bld2<SC1>(rw, l * 8 + 2048, to[tt] + 4096), c = bld2<SC1>(rw, l * 8 + 2560, to[tt] + 4096);
                    T[ks][tt] = (u4){a[0], a[1], c[0], c[1]};
                }
            }
            SCHED();
        };
#pragma unroll
        for (int ks = 0; ks < G2_DEPTH; ++ks) ldtok(ks);
        if (aw) {
#pragma unroll
            for (int ks = 0; ks < 7; ++ks) w2_read(W[ks], rot * 28672 + ks * 4096);
            asm volatile("s_waitcnt lgkmcnt(0)" ::: "memory");
        }
        if (l == 0) __hip_atomic_fetch_add(hw + 7, 1u, __ATOMIC_RELAXED, __HIP_MEMORY_SCOPE_WORKGROUP);
        const int rho = grp * 128 + th * 64 + rot * 16 + m;
        const int ooff = (256 * (rho & 7) + (rho >> 3)) * 1024 + fb * 128 + (w >> 2) * 64 + g * 16;
        const u4 hres = bld4<SC1>(ro, ooff, 0);
        SCHED();
        f4 acc[4][4];
#pragma unroll
        for (int tt = 0; tt < 4; ++tt)
#pragma unroll
            for (int t = 0; t < 4; ++t) acc[tt][t] = Z4;
#pragma unroll
        for (int ks = 0; ks < 7; ++ks) {
            if (ks + G2_DEPTH < 7) { PIN(T[ks][0]); ldtok(ks + G2_DEPTH); }
#pragma unroll
            for (int tt = 0; tt < 4; ++tt)
#pragma unroll
                for (int t = 0; t < 4; ++t) {
                    acc[tt][t] = mma_b(W[ks][t], T[ks][tt], acc[tt][t]);
                }
            SCHED();
        }
        ST(17);
        lds_wait(hw + 7, 8u);
        char* dp = smem + w * 16384 + l * 16;
#pragma unroll
        for (int tt = 1; tt < 4; ++tt)
#pragma unroll
            for (int t = 0; t < 4; ++t) *(f4*)(dp + (((tt + rot) & 3) * 4 + t) * 1024) = acc[tt][t];
        f4 s0, s1;
        if (aw) { *(f4*)(dp + (rot * 4 + 2) * 1024) = acc[0][2]; *(f4*)(dp + (rot * 4 + 3) * 1024) = acc[0][3]; s0 = acc[0][0]; s1 = acc[0][1]; }
        else { *(f4*)(dp + (rot * 4) * 1024) = acc[0][0]; *(f4*)(dp + (rot * 4 + 1) * 1024) = acc[0][1]; s0 = acc[0][2]; s1 = acc[0][3]; }
        BARRIER();
        f4 r0[7], r1[7];
#pragma unroll
        for (int j = 1; j < 8; ++j) {
            const char* rp = smem + (((w + j) & 7) * 16 + rot * 4 + (w >> 2) * 2) * 1024 + l * 16;
            r0[j - 1] = *(const f4*)rp; r1[j - 1] = *(const f4*)(rp + 1024);
        }
        SCHED();
        s0 = ((s0 + r0[0]) + (r0[1] + r0[2])) + ((r0[3] + r0[4]) + (r0[5] + r0[6]));
        s1 = ((s1 + r1[0]) + (r1[1] + r1[2])) + ((r1[3] + r1[4]) + (r1[5] + r1[6]));
        u2 p0 = pk4(s0), p1 = pk4(s1);
        bst4<NT>((u4){pk_b2(bf_2(hres[0]) + bf_2(p0[0])), pk_b2(bf_2(hres[1]) + bf_2(p0[1])),
                           pk_b2(bf_2(hres[2]) + bf_2(p1[0])), pk_b2(bf_2(hres[3]) + bf_2(p1[1]))}, ro, ooff, 0);
    }
    ST_END(ws, 0);
}

extern "C" void kernel_launch(void* const* d_in, const int* in_sizes, int n_in, void* d_out, int out_size,
                              void* d_ws, size_t ws_size, hipStream_t stream) {
    (void)hipFuncSetAttribute((const void*)k_fused, hipFuncAttributeMaxDynamicSharedMemorySize, LDS_B);
    k_fused<<<256, 512, LDS_B, stream>>>((const uint16_t*)d_in[0], (const uint16_t*)d_in[1], (const uint16_t*)d_in[2],
                                         (const uint16_t*)d_in[4], (const uint16_t*)d_in[5], (uint16_t*)d_out, (char*)d_ws);
}
```

```cpp
#define FS 1
#include <hip/hip_runtime.h>
#include <hip/hip_bf16.h>
#include <stdint.h>

typedef _Float16 h8 __attribute__((ext_vector_type(8)));
typedef _Float16 h4 __attribute__((ext_vector_type(4)));
typedef _Float16 h2 __attribute__((ext_vector_type(2)));
typedef __bf16 b8 __attribute__((ext_vector_type(8)));
typedef __bf16 b2 __attribute__((ext_vector_type(2)));
typedef short s4 __attribute__((ext_vector_type(4)));
typedef float f4 __attribute__((ext_vector_type(4)));
typedef float f2 __attribute__((ext_vector_type(2)));
typedef unsigned u32;
typedef u32 u4 __attribute__((ext_vector_type(4)));
typedef u32 u2 __attribute__((ext_vector_type(2)));
#define LAS __attribute__((address_space(3)))
#define GAS __attribute__((address_space(1)))
#define DI __device__ __forceinline__
#define FENCE() asm volatile("" ::: "memory")
#define PIN(x) asm volatile("" : "+v"(x))
#define SCHED() __builtin_amdgcn_sched_barrier(0)
#define WAITV(n) asm volatile("s_waitcnt vmcnt(%0)" :: "n"(n) : "memory")
#define BARRIER() do { asm volatile("s_waitcnt lgkmcnt(0)" ::: "memory"); __builtin_amdgcn_s_barrier(); FENCE(); } while (0)
constexpr int SC1 = 16, NT = 2;
constexpr u32 SENT = 0xffffffffu;
constexpr int SPIN_MAX = 1 << 20;

DI u32 pk_h(float a, float b) { f2 v = {a, b}; h2 r = __builtin_convertvector(v, h2); return __builtin_bit_cast(u32, r); }
DI u32 pk_b(float a, float b) { f2 v = {a, b}; b2 r = __builtin_convertvector(v, b2); return __builtin_bit_cast(u32, r); }
DI float bf_lo(u32 v) { return __builtin_bit_cast(float, v << 16); }
DI float bf_hi(u32 v) { return __builtin_bit_cast(float, v & 0xffff0000u); }
DI f2 bf_2(u32 v) { return (f2){bf_lo(v), bf_hi(v)}; }
DI u32 pk_b2(f2 v) { return pk_b(v[0], v[1]); }
DI float rbf(float a) { return bf_lo(pk_b(a, a)); }
DI float cosr(float rev) { return __builtin_amdgcn_cosf(rev); }
DI float sinr(float rev) { return __builtin_amdgcn_sinf(rev); }
DI f2 lo2(f4 v) { return __builtin_shufflevector(v, v, 0, 1); }
DI f2 hi2(f4 v) { return __builtin_shufflevector(v, v, 2, 3); }
DI f2 fma2(f2 a, f2 b, f2 c) { return __builtin_elementwise_fma(a, b, c); }
DI u32 umin(u32 a, u32 b) { return a < b ? a : b; }
DI u32 umax(u32 a, u32 b) { return a > b ? a : b; }
DI u32 clean(u32 v) { return umin(v, SENT - 1); }
DI u32 max4(u4 v) { return umax(umax(umax(v[0], v[1]), v[2]), v[3]); }
#define Z4 ((f4){0, 0, 0, 0})
DI f4 mma_h(u4 a, u4 b, f4 c) { return __builtin_amdgcn_mfma_f32_16x16x32_f16(__builtin_bit_cast(h8, a), __builtin_bit_cast(h8, b), c, 0, 0, 0); }
DI f4 mma_b(u4 a, u4 b, f4 c) { return __builtin_amdgcn_mfma_f32_16x16x32_bf16(__builtin_bit_cast(b8, a), __builtin_bit_cast(b8, b), c, 0, 0, 0); }
DI f4 mma16_b(u2 a, u2 b, f4 c) { return __builtin_amdgcn_mfma_f32_16x16x16bf16_1k(__builtin_bit_cast(s4, a), __builtin_bit_cast(s4, b), c, 0, 0, 0); }
DI f4 mma16_h(u2 a, u2 b, f4 c) { return __builtin_amdgcn_mfma_f32_16x16x16f16(__builtin_bit_cast(h4, a), __builtin_bit_cast(h4, b), c, 0, 0, 0); }
typedef __amdgpu_buffer_rsrc_t rsrc_t;
DI rsrc_t mkrs(const void* p, u32 bytes) { return __builtin_amdgcn_make_buffer_rsrc((void*)p, 0, bytes, 0x00020000); }
template <int AUX> DI u4 bld4(rsrc_t r, int v, int s) { return __builtin_amdgcn_raw_buffer_load_b128(r, v, s, AUX); }
template <int AUX> DI u2 bld2(rsrc_t r, int v, int s) { return __builtin_amdgcn_raw_buffer_load_b64(r, v, s, AUX); }
template <int AUX> DI void bst4(u4 d, rsrc_t r, int v, int s) {
    __builtin_amdgcn_raw_buffer_store_b128(d, r, v, s, AUX);
    asm volatile("s_nop 1" :: "v"(d));
}
template <int AUX> DI void bst2(u2 d, rsrc_t r, int v, int s) { __builtin_amdgcn_raw_buffer_store_b64(d, r, v, s, AUX); }

template <int CTRL> DI float dpp(float v) {
    return __builtin_bit_cast(float, __builtin_amdgcn_update_dpp(0, __builtin_bit_cast(int, v), CTRL, 0xf, 0xf, true));
}
template <bool ALL> DI float red_lanes(float v) {
    v += dpp<0xB1>(v);
    if (ALL) v += dpp<0x4E>(v);
    v += dpp<0x124>(v); v += dpp<0x128>(v);
    u32 x = __builtin_bit_cast(u32, v), y = x;
    asm("" : "+v"(y));
    auto r = __builtin_amdgcn_permlane16_swap(x, y, false, false);
    v = __builtin_bit_cast(float, (u32)r[0]) + __builtin_bit_cast(float, (u32)r[1]);
    x = __builtin_bit_cast(u32, v); y = x;
    asm("" : "+v"(y));
    auto q = __builtin_amdgcn_permlane32_swap(x, y, false, false);
    return __builtin_bit_cast(float, (u32)q[0]) + __builtin_bit_cast(float, (u32)q[1]);
}
DI float ln_inv(float var) { return __builtin_amdgcn_rcpf(rbf(__builtin_amdgcn_sqrtf(rbf(rbf(var) + 1e-5f)))); }

#define ST_DECL
#define ST(i)
#define ST_END(ws, kid)

constexpr u32 WS_U = 0;
constexpr u32 WS_V = WS_U + (2u << 20);
constexpr u32 WS_LN2 = WS_V + (2u << 20);
constexpr u32 WS_WA = WS_LN2 + (2u << 20);
constexpr u32 WS_WB = WS_WA + 8192u;
constexpr u32 WS_FA = WS_WB + 8192u;
constexpr u32 WS_FLAG = WS_FA + 16384u * FS;
constexpr u32 WS_EP = WS_FLAG + 32768u;
constexpr u32 WS_H1 = WS_WA + (512u << 10);
constexpr u32 WS_END = WS_H1 + 7340032u;

#define BGA 9
#define BGB 15
#define BGC 21
#define G_SLEEP 16
#define GA_SLEEP 4
#define GB_SLEEP 14
#define A_SLEEP 0
#define BG_DELAY 10
#define PRO_SLEEP 4
constexpr int BG5 = BGA, BG5b = BGB, BG6 = BGC;
constexpr int G1_DEPTH = 10, G2_DEPTH = 3;
constexpr int B_R = 5;
constexpr int CODE_LINES = 290, CODE_PF = 10;
constexpr int LDS_SCR = 114688;
constexpr int SCR_X = 8448;
constexpr int SCR_S = SCR_X + 20480;
constexpr int SCR_F = 49024;
constexpr int XCH = 147456;
constexpr int LDS_B = 163840;

DI void glds16(const void* sbase, u32 voff, u32 lds_dst) {
    asm volatile("s_mov_b32 m0, %2\n\ts_nop 0\n\tglobal_load_lds_dwordx4 %0, %1" :: "v"(voff), "s"(sbase), "s"(lds_dst) : "memory");
}
DI bool any_sent(u32 v) { return __builtin_amdgcn_ballot_w64(v == SENT) != 0; }
DI void poll2(u4& a, u4& c, rsrc_t rw, int vo, int sa, int sc) {
#pragma nounroll
    for (int sp = 0; sp < SPIN_MAX; ++sp) {
        const bool ba = max4(a) == SENT, bc = max4(c) == SENT;
        if (__builtin_amdgcn_ballot_w64(ba || bc) == 0) break;
        __builtin_amdgcn_s_sleep(1);
        FENCE();
        if (ba) a = bld4<SC1>(rw, vo, sa);
        if (bc) c = bld4<SC1>(rw, vo, sc);
    }
}
DI void wait_tok(u4& t0, u4& t1, rsrc_t rw, int vo, int so) {
    if (any_sent(umax(max4(t0), max4(t1)))) {
#pragma nounroll
        for (int sp = 0; sp < SPIN_MAX; ++sp) {
            FENCE();
            t0 = bld4<SC1>(rw, vo, so); t1 = bld4<SC1>(rw, vo, so + 16384);
            if (!any_sent(umax(max4(t0), max4(t1)))) break;
        }
    }
}
DI u4 ldw1(LAS char* wb, int kk, int ft) {
    s4 a0 = __builtin_amdgcn_ds_read_tr16_b64_v4i16((LAS s4*)(wb + kk * 32 * 224 + ft * 32));
    s4 a1 = __builtin_amdgcn_ds_read_tr16_b64_v4i16((LAS s4*)(wb + (kk * 32 + 16) * 224 + ft * 32));
    u2 x0 = __builtin_bit_cast(u2, a0), x1 = __builtin_bit_cast(u2, a1);
    return (u4){x0[0], x0[1], x1[0], x1[1]};
}
DI u2 pk4(f4 a) { return (u2){pk_b(a[0], a[1]), pk_b(a[2], a[3])}; }
DI u2 silu_pk2(u32 p0, u32 p1) {
    u32 o0, o1; float a0, a1, a2, a3, t0, t1, t2, t3;
    asm("v_lshlrev_b32 %2, 16, %10\n\tv_and_b32 %3, 0xffff0000, %10\n\tv_lshlrev_b32 %4, 16, %11\n\tv_and_b32 %5, 0xffff0000, %11\n\t"
        "v_mul_f32 %6, %12, %2\n\tv_mul_f32 %7, %12, %3\n\tv_mul_f32 %8, %12, %4\n\tv_mul_f32 %9, %12, %5\n\t"
        "v_exp_f32 %6, %6\n\tv_exp_f32 %7, %7\n\tv_exp_f32 %8, %8\n\tv_exp_f32 %9, %9\n\t"
        "v_add_f32 %6, 1.0, %6\n\tv_add_f32 %7, 1.0, %7\n\tv_add_f32 %8, 1.0, %8\n\tv_add_f32 %9, 1.0, %9\n\t"
        "v_rcp_f32 %6, %6\n\tv_rcp_f32 %7, %7\n\tv_rcp_f32 %8, %8\n\tv_rcp_f32 %9, %9\n\t"
        "v_mul_f32 %2, %2, %6\n\tv_mul_f32 %3, %3, %7\n\tv_mul_f32 %4, %4, %8\n\tv_mul_f32 %5, %5, %9\n\t"
        "v_cvt_pk_bf16_f32 %0, %2, %3\n\tv_cvt_pk_bf16_f32 %1, %4, %5"
        : "=&v"(o0), "=&v"(o1), "=&v"(a0), "=&v"(a1), "=&v"(a2), "=&v"(a3), "=&v"(t0), "=&v"(t1), "=&v"(t2), "=&v"(t3)
        : "v"(p0), "v"(p1), "s"(-1.4426950408889634f));
    return (u2){o0, o1};
}
DI u4 silu_pk4(u4 p) { u2 a = silu_pk2(p[0], p[1]), b = silu_pk2(p[2], p[3]); return (u4){a[0], a[1], b[0], b[1]}; }
DI void lds_set(LAS u32* p, u32 v) {
    asm volatile("s_waitcnt lgkmcnt(0)" ::: "memory");
    __hip_atomic_store(p, v, __ATOMIC_RELAXED, __HIP_MEMORY_SCOPE_WORKGROUP);
}
DI void lds_wait(LAS u32* p, u32 v) {
#pragma nounroll
    for (int sp = 0; sp < SPIN_MAX; ++sp) {
        if (__builtin_amdgcn_readfirstlane(__hip_atomic_load(p, __ATOMIC_RELAXED, __HIP_MEMORY_SCOPE_WORKGROUP)) >= v) break;
        __builtin_amdgcn_s_sleep(1);
    }
    FENCE();
}
DI void flag_wait(GAS u32* p, u32 tag) {
#pragma nounroll
    for (int sp = 0; sp < SPIN_MAX; ++sp) {
        if (__builtin_amdgcn_ballot_w64(__hip_atomic_load(p, __ATOMIC_RELAXED, __HIP_MEMORY_SCOPE_AGENT) != tag) == 0) break;
        __builtin_amdgcn_s_sleep(2);
    }
    FENCE();
}
#define SENT4 ((u4){SENT, SENT, SENT, SENT})

__global__ __launch_bounds__(512, 2) void k_fused(const uint16_t* x, const uint16_t* wr, const uint16_t* wi, const uint16_t* w1,
                                                  const uint16_t* w2, uint16_t* out, char* ws) {
    extern __shared__ __attribute__((aligned(16))) char smem[];
    const int tid = threadIdx.x, l = tid & 63, g = l >> 4, m = l & 15;
    const int w = __builtin_amdgcn_readfirstlane(tid >> 6);
    const int b = (blockIdx.x & 7) * 32 + (blockIdx.x >> 3);
    const int mt1 = (b >> 6) * 4 + ((b >> 3) & 3), nt1 = ((b >> 5) & 1) * 8 + (b & 7);
    const int grp = b >> 4, th = (b >> 3) & 1, fb = b & 7;
    const rsrc_t rw = mkrs(ws, WS_END), ro = mkrs(out, 2097152);
    char* scr = smem + LDS_SCR;
    LAS u32* hw = (LAS u32*)((LAS char*)smem + LDS_SCR + SCR_F);
    const u32 lds0 = (u32)(uintptr_t)(LAS char*)smem;
    const float mf = (float)m, gf = (float)g, sg = (m & 1) ? -1.0f : 1.0f;
    const bool aw = w < 4;
    ST_DECL;
    unsigned long long pc0;
    asm volatile("s_getpc_b64 %0" : "=s"(pc0));
    if (tid >= 448 && l < CODE_PF)
        asm volatile("s_mov_b32 m0, %2\n\ts_nop 0\n\tglobal_load_lds_dword %0, %1"
                     :: "v"(umin((blockIdx.x >> 3) * CODE_PF + l, CODE_LINES - 1) * 128), "s"(pc0), "s"(lds0 + LDS_SCR + SCR_F + 64) : "memory");
    const u32 epoch = __hip_atomic_load((GAS u32*)(ws + WS_EP), __ATOMIC_RELAXED, __HIP_MEMORY_SCOPE_AGENT);
    FENCE();
    u32 xd[8];
    const int f1 = b >> 5, cg = b & 31;
    const bool mir = cg >= 16;
    const rsrc_t rr = mkrs(wr, 1052672), ri = mkrs(wi, 1052672);
    u4 fr, fi; u32 sr_, si_;
    if (aw) {
        __builtin_amdgcn_s_sleep(A_SLEEP);
        const u32* xr = (const u32*)(x + (256 * (2 * w + ((m >> 1) & 1)) + b) * 512) + 8 * g + 2 * (m >> 2) + (m & 1);
#pragma unroll
        for (int j = 0; j < 8; ++j) xd[j] = xr[32 * j];
    } else {
        bst4<SC1>(SENT4, rw, l * 16, WS_U + b * 8192 + (w - 4) * 2048); bst4<SC1>(SENT4, rw, l * 16 + 1024, WS_U + b * 8192 + (w - 4) * 2048);
        __builtin_amdgcn_raw_buffer_store_b32(SENT, rw, l < 4 ? (l >> 1) * 8192 + (l & 1) * 1024 : 0x7ffffff0,
                                              WS_WA + (w - 4) * 2048 + ((b >> 6) * 16 + (b & 15)) * 16 + ((b >> 4) & 3) * 4, SC1);
        FENCE();
        if (w == 6) {
            const int c = (l >> 3) & 3, f_ = 8 * b + (l & 7);
            const int e1 = ((c & 1) ? (2048 - f_) & 2047 : f_) * 514 + (c >> 1) * 512;
            sr_ = __builtin_amdgcn_raw_buffer_load_b16(rr, e1, 0, 0); si_ = __builtin_amdgcn_raw_buffer_load_b16(ri, e1, 0, 0);
        }
    }
    FENCE();
    const int zo = (64 * g + (g < 2 ? m : (m + 15) & 15)) * 4;
    const int rq = ((b >> 3) + b) & 3;
    const int row0 = (tid & 255) / 14, cch0 = (tid & 255) - row0 * 14;
    const char* wsrc = (const char*)w1 + nt1 * 224;
    u32 w1o[7];
    auto w1_burst = [&](int i0, int n) {
#pragma unroll
        for (int i = i0; i < i0 + n; ++i) {
            const int q = (i / 7 + rq) & 3, j = i % 7;
            glds16(wsrc + q * (128 * 3584), w1o[j], lds0 + (w - 4) * 1024 + q * 28672 + j * 4096);
        }
    };
    const int xoff = (256 * w + b) * 1024 + m * 32 + g * 8;
    u2 xl[2];
    float c16[4], s16[4];
    f2 twc[2], tws[2];
    u4 Gre3, Gim3, SGre, SGim, GA[2];
    auto consts0 = [&]() {
#pragma unroll
        for (int u = 0; u < 4; ++u) {
            float a = (4.0f * gf + u) * (mf * (1.0f / 16));
            c16[u] = cosr(a); s16[u] = sinr(a);
            twc[u >> 1][u & 1] = cosr(a * (1.0f / 32)); tws[u >> 1][u & 1] = sinr(a * (1.0f / 32));
        }
    };
    auto consts3 = [&]() {
#pragma unroll
        for (int u = 0; u < 4; ++u) {
            const float k = 4.0f * gf + u;
            const bool k0 = u == 0 && g == 0;
            float cc = c16[u], ss = s16[u];
            Gre3[u] = pk_b(cc, -ss); Gim3[u] = pk_b(ss, cc);
            float a2 = (2.0f * k + 1.0f) * (mf * (1.0f / 32));
            u32 sp = pk_b(g < 2 ? 2.f * cc : 2.f * cosr(a2), g < 2 ? -2.f * ss : -2.f * sinr(a2));
            SGre[u] = k0 ? pk_b(1.0f, sg) : (g < 2 ? sp : 0u);
            SGim[u] = g < 2 ? 0u : sp;
            float a4 = k * (mf * (1.0f / 32)), c4 = 2.f * cosr(a4), s4_ = -2.f * sinr(a4);
            GA[0][u] = k0 ? pk_b(1.0f, sg) : pk_b(c4, s4_);
            GA[1][u] = k0 ? pk_b(1.0f, sg) : ((u & 1) ? pk_b(-c4, -s4_) : pk_b(c4, s4_));
        }
        PIN(Gre3); PIN(Gim3); PIN(SGre); PIN(SGim); PIN(GA[0]); PIN(GA[1]); PIN(twc[0]); PIN(twc[1]); PIN(tws[0]); PIN(tws[1]);
    };
    const int pw = (w + 4 * th) & 7;
    GAS u32* fl = (GAS u32*)(ws + WS_FLAG);
    int ko[7];
    {
        const int r6 = (fb < 6 ? fb : fb - 5) + 3 * (grp & 1);
#pragma unroll
        for (int ks = 0; ks < 6; ++ks) { int k = ks + r6; k = k >= 6 ? k - 6 : k; ko[ks] = k >= 6 ? k - 6 : k; }
        ko[6] = 6;
    }
    u4 W[7][4];
    const u32 w2vo = (l >> 3) * 1024 + (((l & 7) ^ (((l >> 4) & 1) << 2)) * 16);
    auto w2_glds = [&](int p_, int ks, u32 dst) {
        const int q = ko[ks];
        const char* src = (const char*)w2 + (224 * p_ + (ks == 6 ? 96 : (q < 3 ? 32 * q : 16 + 32 * q))) * 1024 + fb * 128;
        glds16(src, w2vo, dst); glds16(src + 8192, w2vo, dst + 1024);
        glds16(src + (ks == 6 ? 112 : 16) * 1024, w2vo, dst + 2048); glds16(src + (ks == 6 ? 120 : 24) * 1024, w2vo, dst + 3072);
    };
    LAS char* w2rd = (LAS char*)smem + (4 * g + (m >> 2)) * 128 + (((m & 3) ^ (((m >> 3) & 1) << 2)) * 16);
    auto w2_read = [&](u4 (&f)[4], int off) {
#pragma unroll
        for (int t = 0; t < 4; ++t) {
            LAS char* p = (LAS char*)((u32)(uintptr_t)w2rd ^ ((t >> 1) * 64)) + off + (t & 1) * 8;
            u2 x0 = __builtin_bit_cast(u2, __builtin_amdgcn_ds_read_tr16_b64_v4i16((LAS s4*)p));
            u2 x1 = __builtin_bit_cast(u2, __builtin_amdgcn_ds_read_tr16_b64_v4i16((LAS s4*)(p + 2048)));
            f[t] = (u4){x0[0], x0[1], x1[0], x1[1]};
        }
    };
    auto bg = [&](int i0, int i1) { w1_burst(i0, i1 - i0); };
    u32 tag;
    if (__builtin_expect(aw, 1)) {
        if (tid < 8) hw[tid] = 0;
        FENCE();
        ST(2);
        {
            u4 FAre, FAim, Fre, Fim, Sre, Sim; u2 Mb; f2 pc[2], ps[2];
            consts0();
            float c0[8], s0[8];
#pragma unroll
            for (int j = 0; j < 8; ++j) {
                float a = (gf + 4.0f * j) * (mf * (1.0f / 32));
                c0[j] = cosr(a); s0[j] = m ? -sinr(a) : ((g & 1) ? -1.0f : 1.0f);
            }
            const float mo = (2.0f * mf + 1.0f) * (1.0f / 32);
#pragma unroll
            for (int u = 0; u < 4; ++u) {
                FAre[u] = pk_h(c0[2 * u], c0[2 * u + 1]); FAim[u] = pk_h(s0[2 * u], s0[2 * u + 1]);
                float cc = c16[u], ss = s16[u];
                Fre[u] = pk_h(cc, ss); Fim[u] = pk_h(-ss, cc);
                float a2 = (4.0f * gf + u) * mo;
                u32 re = pk_h(m < 8 ? cc : cosr(a2), 0.f), im = pk_h(m < 8 ? (m ? -ss : ((u & 1) ? -1.0f : 1.0f)) : -sinr(a2), 0.f);
                Sre[u] = m < 8 ? re : re << 16; Sim[u] = m < 8 ? im : im << 16;
                pc[u & 1][u >> 1] = twc[u >> 1][u & 1]; ps[u & 1][u >> 1] = tws[u >> 1][u & 1];
            }
#pragma unroll
            for (int e = 0; e < 2; ++e) {
                float a = (float)(((m >> 1) * (256 * (2 * g + e) + b)) & 2047) * (1.0f / 2048);
                float cc = cosr(a), ss = sinr(a);
                Mb[e] = (m & 1) ? pk_h(-ss, cc) : pk_h(cc, ss);
            }
            PIN(FAre); PIN(FAim); PIN(Fre); PIN(Fim); PIN(Sre); PIN(Sim); PIN(Mb); PIN(pc[0]); PIN(pc[1]); PIN(ps[0]); PIN(ps[1]);
            ST(3);
            f2 v[8];
#pragma unroll
            for (int j = 0; j < 8; ++j) v[j] = bf_2(xd[j]);
            f2 s2 = ((v[0] + v[1]) + (v[2] + v[3])) + ((v[4] + v[5]) + (v[6] + v[7]));
            const float mean = red_lanes<false>(s2[0] + s2[1]) * (1.0f / 512);
            f2 q0 = {0.f, 0.f}, q1 = {0.f, 0.f};
#pragma unroll
            for (int j = 0; j < 8; ++j) { v[j] -= mean; if (j & 1) q1 = fma2(v[j], v[j], q1); else q0 = fma2(v[j], v[j], q0); }
            q0 += q1;
            const float inv = ln_inv(red_lanes<false>(q0[0] + q0[1]) * (1.0f / 512));
            const float off = (mean - rbf(mean)) * inv;
#pragma unroll
            for (int j = 0; j < 8; ++j) v[j] = fma2(v[j], (f2){inv, inv}, (f2){off, off});
            u4 B[2];
#pragma unroll
            for (int p = 0; p < 2; ++p) {
                u4 A = {pk_h(v[0][p], v[1][p]), pk_h(v[2][p], v[3][p]), pk_h(v[4][p], v[5][p]), pk_h(v[6][p], v[7][p])};
                f4 Dre = mma_h(A, FAre, Z4), Dim = mma_h(A, FAim, Z4);
#pragma unroll
                for (int r = 0; r < 2; ++r) {
                    f2 re = r ? hi2(Dre) : lo2(Dre), im = r ? hi2(Dim) : lo2(Dim);
                    f2 tr = fma2(im, ps[p], re * pc[p]), ti = fma2(-re, ps[p], im * pc[p]);
                    B[r][p] = pk_h(tr[0], ti[0]); B[r][2 + p] = pk_h(tr[1], ti[1]);
                }
            }
#pragma unroll
            for (int r = 0; r < 2; ++r) {
                f4 Gre = mma_h(Fre, B[r], Z4), Gim = mma_h(Fim, B[r], Z4), Hre = mma_h(Sre, B[r], Z4), Him = mma_h(Sim, B[r], Z4);
#pragma unroll
                for (int i = 0; i < 4; ++i) *(u32*)(scr + (2 * w + r) * 1024 + zo + i * 64) = pk_h(m ? Gre[i] : Hre[i], m ? Gim[i] : Him[i]);
            }
            BARRIER();
            ST(18);
            u4 r0 = *(const u4*)(scr + (2 * g) * 1024 + w * 256 + m * 16);
            u4 r1 = *(const u4*)(scr + (2 * g + 1) * 1024 + w * 256 + m * 16);
            u4 o0, o1;
#pragma unroll
            for (int t = 0; t < 4; ++t) {
                f4 D = mma16_h(Mb, (u2){r0[t], r1[t]}, Z4);
                o0[t] = clean(pk_h(D[0], D[1])); o1[t] = clean(pk_h(D[2], D[3]));
            }
            bst4<SC1>(o0, rw, g * 2048 + m * 16, WS_U + b * 8192 + w * 256);
            bst4<SC1>(o1, rw, g * 2048 + m * 16 + 1024, WS_U + b * 8192 + w * 256);
        }
        ST(4);
        u4 Fre, Fim, Gre, Gim; u2 Nb; f2 tc[2], ts[2];
#pragma unroll
        for (int u = 0; u < 4; ++u) {
            float cc = c16[u], ss = s16[u];
            Fre[u] = pk_h(0.25f * cc, 0.25f * ss); Fim[u] = pk_h(-0.25f * ss, 0.25f * cc);
            Gre[u] = pk_b(cc * (1.0f / 256), -ss * (1.0f / 256)); Gim[u] = pk_b(ss * (1.0f / 256), cc * (1.0f / 256));
            float a3 = (4.0f * gf + u) * (mf * (1.0f / 256));
            tc[u >> 1][u & 1] = cosr(a3); ts[u >> 1][u & 1] = sinr(a3);
        }
        PIN(Fre); PIN(Fim); PIN(Gre); PIN(Gim); PIN(tc[0]); PIN(tc[1]); PIN(ts[0]); PIN(ts[1]);
        xl[0] = *(const u2*)((const char*)x + xoff); xl[1] = *(const u2*)((const char*)x + xoff + 512);
        FENCE();
        tag = __builtin_amdgcn_readfirstlane(epoch) + 1u;
        consts3();
#pragma unroll
        for (int e = 0; e < 2; ++e) {
            float a1 = (float)(((2 * g + e) * (256 * (m >> 1) + b)) & 2047) * (1.0f / 2048);
            float c1 = cosr(a1), s1 = sinr(a1);
            Nb[e] = (m & 1) ? pk_b(s1, c1) : pk_b(c1, -s1);
        }
        PIN(Nb);
        WAITV(0);
        ST(19);
        BARRIER();
        ST(5);
        u4 d[2], wa, wb;
        const int uo = (tid >> 1) * 8192 + (tid & 1) * 16, us = WS_U + f1 * 1024 + cg * 32;
        d[0] = bld4<SC1>(rw, uo, us); d[1] = bld4<SC1>(rw, uo, us + 128 * 8192);
        if (cg == 0) {
#pragma nounroll
            for (int sp = 0; sp < SPIN_MAX; ++sp) {
                FENCE();
                const u32 ux = __builtin_amdgcn_raw_buffer_load_b32(rw, tid * 8192, WS_U + ((8 - f1) & 7) * 1024, SC1);
                wa = bld4<SC1>(rw, l * 16, WS_WA + f1 * 1024); wb = bld4<SC1>(rw, l * 16, WS_WB + f1 * 1024);
                *(u32*)(scr + SCR_X + tid * 4) = ux;
                if (!any_sent(umax(ux, umax(max4(wa), max4(wb))))) break;
            }
        }
        poll2(d[0], d[1], rw, uo, us, us + 128 * 8192);
        char* sp = scr + SCR_S + (tid ^ ((tid >> 4) & 1)) * 16;
        *(u4*)sp = d[0]; *(u4*)(sp + 4096) = d[1];
        BARRIER();
        if (b == 0 && tid == 0) __hip_atomic_store((GAS u32*)(ws + WS_EP), tag, __ATOMIC_RELAXED, __HIP_MEMORY_SCOPE_AGENT);
        ST(6);
        u2 ld[4], wl[4];
        const int so = (64 * g + m) * 32 + (((w >> 1) ^ (m >> 3)) * 16) + (w & 1) * 8;
#pragma unroll
        for (int q = 0; q < 4; ++q) ld[q] = *(const u2*)(scr + SCR_S + so + 512 * q);
#pragma unroll
        for (int q = 0; q < 4; ++q) wl[q] = *(const u2*)(scr + SCR_S + so + 8192 + 512 * q);
        FENCE();
        auto fwd = [&](u4 A, f4& Xr, f4& Xi) {
            f4 Dre = mma_h(A, Fre, Z4), Dim = mma_h(A, Fim, Z4);
            f2 r0 = fma2(lo2(Dim), ts[0], lo2(Dre) * tc[0]), i0 = fma2(-lo2(Dre), ts[0], lo2(Dim) * tc[0]);
            f2 r1 = fma2(hi2(Dim), ts[1], hi2(Dre) * tc[1]), i1 = fma2(-hi2(Dre), ts[1], hi2(Dim) * tc[1]);
            u4 B = {pk_h(r0[0], i0[0]), pk_h(r0[1], i0[1]), pk_h(r1[0], i1[0]), pk_h(r1[1], i1[1])};
            Xr = mma_h(Fre, B, Z4); Xi = mma_h(Fim, B, Z4);
        };
        f4 Xre[2], Xim[2];
#pragma unroll
        for (int e = 0; e < 2; ++e) fwd((u4){ld[0][e], ld[1][e], ld[2][e], ld[3][e]}, Xre[e], Xim[e]);
        f2 yr[2][2], yi[2][2];
#pragma unroll
        for (int e = 0; e < 2; ++e)
#pragma unroll
            for (int hh = 0; hh < 2; ++hh) {
                f2 wre = {bf_lo(wl[2 * hh][e]), bf_lo(wl[2 * hh + 1][e])}, wim = {bf_hi(wl[2 * hh][e]), bf_hi(wl[2 * hh + 1][e])};
                f2 xr = hh ? hi2(Xre[e]) : lo2(Xre[e]), xi = hh ? hi2(Xim[e]) : lo2(Xim[e]);
                yr[e][hh] = fma2(-xi, wim, xr * wre); yi[e][hh] = fma2(xr, wim, xi * wre);
            }
        if (cg == 0 && w == 0) {
            const u32* up = (const u32*)(scr + SCR_X) + 64 * g + m;
            f4 Mr, Mi;
            fwd((u4){up[0], up[16], up[32], up[48]}, Mr, Mi);
            f2* sx = (f2*)(scr + SCR_X + 1024);
#pragma unroll
            for (int i = 0; i < 4; ++i) sx[m + 16 * (4 * g + i)] = (f2){Mr[i], Mi[i]};
#pragma unroll
            for (int i = 0; i < 4; ++i) {
                f2 xm = sx[((f1 ? 255 : 256) - (m + 16 * (4 * g + i))) & 255];
                float ar = bf_lo(wa[i]), ai = bf_hi(wa[i]), br = bf_lo(wb[i]), bi = bf_hi(wb[i]);
                yr[0][i >> 1][i & 1] = Xre[0][i] * ar - Xim[0][i] * ai + xm[0] * br + xm[1] * bi;
                yi[0][i >> 1][i & 1] = Xre[0][i] * ai + Xim[0][i] * ar + xm[0] * bi - xm[1] * br;
            }
        }
        char* pv = scr;
#pragma unroll
        for (int e = 0; e < 2; ++e) {
            u4 Y = {pk_b(yr[e][0][0], yi[e][0][0]), pk_b(yr[e][0][1], yi[e][0][1]), pk_b(yr[e][1][0], yi[e][1][0]), pk_b(yr[e][1][1], yi[e][1][1])};
            f4 Dre = mma_b(Y, Gre, Z4), Dim = mma_b(Y, Gim, Z4);
            f2 r0 = fma2(-lo2(Dim), ts[0], lo2(Dre) * tc[0]), i0 = fma2(lo2(Dre), ts[0], lo2(Dim) * tc[0]);
            f2 r1 = fma2(-hi2(Dim), ts[1], hi2(Dre) * tc[1]), i1 = fma2(hi2(Dre), ts[1], hi2(Dim) * tc[1]);
            u4 A = {pk_b(r0[0], i0[0]), pk_b(r0[1], i0[1]), pk_b(r1[0], i1[0]), pk_b(r1[1], i1[1])};
            f4 Ere = mma_b(A, Gre, Z4), Eim = mma_b(A, Gim, Z4);
            u4 o;
#pragma unroll
            for (int i = 0; i < 4; ++i) o[i] = clean(pk_b(Ere[i], Eim[i]));
            *(u4*)(pv + (2 * w + e) * 1024 + ((m * 4 + g) ^ ((w >> 1) * 4)) * 16) = o;
        }
        BARRIER();
        ST(7);
#pragma unroll
        for (int it = 0; it < 2; ++it) {
            const int ch = tid + 256 * it;
            const u32* q = (const u32*)(pv + (ch & 1) * 4096 + (((ch >> 1) * 4) ^ ((ch & 1) * 64)));
            bst4<SC1>((u4){q[0], q[256], q[512], q[768]}, rw, ch * 16, WS_V + b * 8192);
        }
        WAITV(0);
        BARRIER();
        ST(8);
        const int vo = (g * 64 + (m >> 1)) * 8192 + (m & 1) * 16, vs = WS_V + w * 8 * 8192 + b * 32;
        u4 v0 = bld4<SC1>(rw, vo, vs), v1 = bld4<SC1>(rw, vo, vs + 32 * 8192);
        poll2(v0, v1, rw, vo, vs, vs + 32 * 8192);
        ST(9);
        u4 z0, z1;
#pragma unroll
        for (int t = 0; t < 4; ++t) {
            f4 D = mma16_b(Nb, (u2){v0[t], v1[t]}, Z4);
            z0[t] = pk_b(D[0], D[1]); z1[t] = pk_b(D[2], D[3]);
        }
        *(u4*)(scr + SCR_X + g * 2048 + w * 256 + m * 16) = z0;
        *(u4*)(scr + SCR_X + g * 2048 + 1024 + w * 256 + m * 16) = z1;
    } else {
        auto announce = [&](int which) {
            if (l < 8) __hip_atomic_store((GAS u32*)(ws + WS_FA) + (which * 2048 + l * 256 + b) * FS, tag, __ATOMIC_RELAXED, __HIP_MEMORY_SCOPE_AGENT);
            FENCE();
        };
        auto all_announced = [&](int which) {
#pragma nounroll
            for (int sp = 0; sp < SPIN_MAX; ++sp) {
                FENCE();
                u4 f;
#pragma unroll
                for (int i = 0; i < 4; ++i) f[i] = __builtin_amdgcn_raw_buffer_load_b32(rw, (l + 64 * i) * 4 * FS, WS_FA + (which * 8192 + (b >> 5) * 1024) * FS, SC1);
                if (__builtin_amdgcn_ballot_w64(f[0] != tag || f[1] != tag || f[2] != tag || f[3] != tag) == 0) break;
                __builtin_amdgcn_s_sleep(4);
            }
        };
        FENCE();
        ST(2);
        tag = __builtin_amdgcn_readfirstlane(epoch) + 1u;
        if (w == 6) WAITV(2); else WAITV(0);
        if (l < 2) __hip_atomic_store((GAS u32*)(ws + WS_FA) + ((2 * (w - 4) + l) * 256 + b) * FS, tag, __ATOMIC_RELAXED, __HIP_MEMORY_SCOPE_AGENT);
        FENCE();
        ST(18);
#pragma unroll
        for (int jj = 0; jj < 7; ++jj) {
            const int cj_ = (4 * jj) % 14, rj = 18 * jj + (4 * jj) / 14;
            w1o[jj] = (row0 + rj) * 3584 + (cch0 + cj_) * 16 + (cch0 + cj_ >= 14 ? 3584 - 224 : 0);
            PIN(w1o[jj]);
        }
        BARRIER();
        if (w == 6) {
            float re = bf_lo(sr_), im = bf_lo(si_);
            re += dpp<0x128>(re); im -= dpp<0x128>(im);
            u32 a = __builtin_bit_cast(u32, re), a2 = a, c = __builtin_bit_cast(u32, im), c2 = c;
            asm("" : "+v"(a2), "+v"(c2));
            auto pr = __builtin_amdgcn_permlane16_swap(a, a2, false, false);
            auto pi = __builtin_amdgcn_permlane16_swap(c, c2, false, false);
            const float ar = __builtin_bit_cast(float, (u32)pr[0]), br = __builtin_bit_cast(float, (u32)pr[1]);
            const float ai = __builtin_bit_cast(float, (u32)pi[0]), bi = __builtin_bit_cast(float, (u32)pi[1]);
            const int wo = WS_WA + ((b >> 6) * 16 + (b & 15)) * 16 + ((b >> 4) & 3) * 4;
            if (l < 8) {
                __builtin_amdgcn_raw_buffer_store_b32(clean(pk_b(0.25f * (ar + br), 0.25f * (ai + bi))), rw, l * 1024, wo, SC1);
                __builtin_amdgcn_raw_buffer_store_b32(clean(pk_b(0.25f * (ar - br), 0.25f * (ai - bi))), rw, l * 1024, wo + 8192, SC1);
            }
        }
        const int f = f1 + 8 * (tid & 255), k0 = 16 * cg - 8 * (cg & 1);
        const int e = mir ? ((2048 - f) & 2047) * 514 + (504 - k0) * 2 : f * 514 + k0 * 2;
        consts0();
        __builtin_amdgcn_s_sleep(GA_SLEEP);
        fr = bld4<0>(rr, e, 0);
        FENCE();
        __builtin_amdgcn_s_sleep(GB_SLEEP);
        if (w == 7) all_announced(0);
        fi = bld4<0>(ri, e, 0);
        xl[0] = *(const u2*)((const char*)x + xoff); xl[1] = *(const u2*)((const char*)x + xoff + 512);
        FENCE();
        ST(4);
        BARRIER();
        {
            u32 o[8];
#pragma unroll
            for (int c = 0; c < 8; ++c)
                o[c] = mir ? __builtin_amdgcn_perm(fi[3 - (c >> 1)], fr[3 - (c >> 1)], (c & 1) ? 0x05040100u : 0x07060302u) ^ 0x80000000u
                           : __builtin_amdgcn_perm(fi[c >> 1], fr[c >> 1], (c & 1) ? 0x07060302u : 0x05040100u);
            char* wp = scr + SCR_S + 8192 + (tid & 255) * 32;
            *(u4*)(wp + (m & 8) * 2) = (u4){o[0], o[1], o[2], o[3]};
            *(u4*)(wp + ((m & 8) * 2 ^ 16)) = (u4){o[4], o[5], o[6], o[7]};
        }
#pragma unroll
        for (int i = 0; i < 2; ++i) bst4<SC1>(SENT4, rw, l * 16 + i * 1024, WS_V + b * 8192 + (w - 4) * 2048);
        FENCE();
        consts3();
        WAITV(0);
        ST(5);
        BARRIER();
        if (w == 7) announce(1);
#pragma unroll
        for (int i = 0; i < 2; ++i) bst4<SC1>(SENT4, rw, (l >> 3) * 256 + (l & 7) * 16 + i * 2048, WS_LN2 + (b >> 1) * 16384 + (b & 1) * 128 + (w - 4) * 4096);
        bg(0, BG5);
        WAITV(BG5);
        ST(6);
        BARRIER();
        if (w == 7) all_announced(1);
        else __builtin_amdgcn_s_sleep(3);
        bg(BG5, BG5b);
        ST(7);
        BARRIER();
        __builtin_amdgcn_s_sleep(BG_DELAY);
        bg(BG5b, BG6);
    }
    BARRIER();
    {
        const u32* zp = (const u32*)(scr + SCR_X + w * 1024 + zo);
        u4 A = {zp[0], zp[16], zp[32], zp[48]};
        f4 Dre = mma_b(A, Gre3, Z4), Dim = mma_b(A, Gim3, Z4), Hre = mma_b(A, SGre, Z4), Him = mma_b(A, SGim, Z4);
        if (g == 0) { Dre[0] = Hre[0]; Dim[0] = Him[0]; }
        f2 r0 = fma2(-lo2(Dim), tws[0], lo2(Dre) * twc[0]), i0 = fma2(lo2(Dre), tws[0], lo2(Dim) * twc[0]);
        f2 r1 = fma2(-hi2(Dim), tws[1], hi2(Dre) * twc[1]), i1 = fma2(hi2(Dre), tws[1], hi2(Dim) * twc[1]);
        u4 A2 = {pk_b(r0[0], i0[0]), pk_b(r0[1], i0[1]), pk_b(r1[0], i1[0]), pk_b(r1[1], i1[1])};
        f2 h[4]; u2 hp[2];
#pragma unroll
        for (int t = 0; t < 2; ++t) {
            f4 Yv = mma_b(A2, GA[t], Z4);
            u32 h0 = pk_b2(bf_2(xl[t][0]) + bf_2(pk_b(Yv[0], Yv[1]))), h1 = pk_b2(bf_2(xl[t][1]) + bf_2(pk_b(Yv[2], Yv[3])));
            hp[t] = (u2){h0, h1};
            h[2 * t] = bf_2(h0); h[2 * t + 1] = bf_2(h1);
        }
        f2 s2 = (h[0] + h[1]) + (h[2] + h[3]);
        const float mean = red_lanes<true>(s2[0] + s2[1]) * (1.0f / 512);
        f2 q2 = {0.f, 0.f};
#pragma unroll
        for (int j = 0; j < 4; ++j) { f2 d = h[j] - mean; q2 = fma2(d, d, q2); }
        const float inv = ln_inv(red_lanes<true>(q2[0] + q2[1]) * (1.0f / 512));
        const float mb = rbf(mean);
#pragma unroll
        for (int t = 0; t < 2; ++t)
            *(u2*)(scr + ((8 * t + (m >> 1)) * 4 + g) * 128 + (w ^ (m >> 1)) * 16 + (m & 1) * 8) =
                (u2){clean(pk_b2(bf_2(pk_b2(h[2 * t] - mb)) * inv)), clean(pk_b2(bf_2(pk_b2(h[2 * t + 1] - mb)) * inv))};
        WAITV(0);
        BARRIER();
        u4 lo = *(const u4*)(scr + (tid ^ ((tid >> 5) & 7)) * 16);
        bst4<SC1>(lo, rw, (tid >> 3) * 256 + (tid & 7) * 16, WS_LN2 + (b >> 1) * 16384 + (b & 1) * 128);
        FENCE();
        if (aw) WAITV(0); else __builtin_amdgcn_s_sleep(8);
        bst2<SC1>(hp[0], ro, xoff, 0); bst2<SC1>(hp[1], ro, xoff + 512, 0);
        FENCE();
        ST(10);
    }
    auto stage_out = [&](int r) {
        w2_read(W[r], LDS_SCR + (w & 3) * 8192 + (r & 1) * 4096);
        asm volatile("s_waitcnt lgkmcnt(0)" ::: "memory");
        PIN(W[r][0]); PIN(W[r][1]); PIN(W[r][2]); PIN(W[r][3]);
        if (r + 2 < 7) w2_glds(pw, r + 2, lds0 + LDS_SCR + (w & 3) * 8192 + (r & 1) * 4096);
    };
    auto publish = [&]() {
        u32 prev = 0;
        if (l == 0) prev = __hip_atomic_fetch_add(hw + 4, 1u, __ATOMIC_RELAXED, __HIP_MEMORY_SCOPE_WORKGROUP);
        if (__builtin_amdgcn_readfirstlane(prev) == 7u && l < 2)
            __hip_atomic_store(fl + (l ? 4096 + (b >> 1) * 32 + (b & 1) : (mt1 * 8 + (nt1 >> 1)) * 32 + (nt1 & 1)), tag, __ATOMIC_RELAXED, __HIP_MEMORY_SCOPE_AGENT);
        FENCE();
    };
    {
        const int tg = w & 3;
        char* xch = smem + XCH + tg * 3072;
        const int hoff = WS_H1 + (mt1 * 8 + 2 * tg) * (56 * 1024) + (nt1 >> 1) * 7168;
        if (__builtin_expect(aw, 1)) {
            const int toff = WS_LN2 + (mt1 * 8 + 2 * tg) * 16384;
            LAS char* wb0 = (LAS char*)smem + (4 * g + (m >> 2)) * 224 + (m & 3) * 8;
            LAS char* wb[4];
            int tq[4];
#pragma unroll
            for (int q = 0; q < 4; ++q) { const int qq = (q + rq) & 3; wb[q] = wb0 + qq * 28672; tq[q] = toff + qq * 4096; }
            u4 T[2][16], A[2][7];
            auto ldt = [&](int kk) {
#pragma unroll
                for (int tt = 0; tt < 2; ++tt) T[tt][kk] = bld4<SC1>(rw, l * 16 + (kk & 3) * 1024, tq[kk >> 2] + tt * 16384);
            };
#pragma unroll
            for (int i = 0; i < 7; ++i) A[0][i] = ldw1(wb[0], 0, i);
            if (l == 0) __hip_atomic_fetch_add(hw + 5, 1u, __ATOMIC_RELAXED, __HIP_MEMORY_SCOPE_WORKGROUP);
            ST(11);
#pragma unroll
            for (int kk = 0; kk < G1_DEPTH; ++kk) { ldt(kk); SCHED(); }
            f4 acc[2][7];
#pragma unroll
            for (int tt = 0; tt < 2; ++tt)
#pragma unroll
                for (int i = 0; i < 7; ++i) acc[tt][i] = Z4;
#pragma unroll
            for (int kk = 0; kk < 16; ++kk) {
                if ((kk & 1) == 0) {
                    if (__builtin_expect(any_sent(umax(umax(max4(T[0][kk]), max4(T[1][kk])), umax(max4(T[0][kk + 1]), max4(T[1][kk + 1])))), 0)) {
                        wait_tok(T[0][kk], T[1][kk], rw, l * 16, tq[kk >> 2] + (kk & 3) * 1024);
                        wait_tok(T[0][kk + 1], T[1][kk + 1], rw, l * 16, tq[kk >> 2] + ((kk + 1) & 3) * 1024);
                    }
                    if (kk == 0) ST(12);
                    if (kk == (BG6 / 7) * 4 - 2) lds_wait(hw + 5, 8u);
                }
                if (kk + G1_DEPTH < 16) ldt(kk + G1_DEPTH);
                if (kk < 15) {
#pragma unroll
                    for (int i = 0; i < 7; ++i) A[(kk + 1) & 1][i] = ldw1(wb[(kk + 1) >> 2], (kk + 1) & 3, i);
                }
#pragma unroll
                for (int i = 0; i < 7; ++i)
#pragma unroll
                    for (int tt = 0; tt < 2; ++tt) acc[tt][i] = mma_b(A[kk & 1][i], T[tt][kk], acc[tt][i]);
#pragma unroll
                for (int i = 0; i < 14; ++i) {
                    __builtin_amdgcn_sched_group_barrier(0x8, 1, 0);
                    if (i == 1 || i == 7) __builtin_amdgcn_sched_group_barrier(0x20, 1, 0);
                    __builtin_amdgcn_sched_group_barrier(0x100, 1, 0);
                    if ((kk & 1) && i >= 3) __builtin_amdgcn_sched_group_barrier(0x2, 1, 0);
                }
                if (kk & 1) {} else SCHED();
            }
#pragma unroll
            for (int q = 0; q < 3; ++q) {
                u2 p0 = pk4(acc[1][2 * q]), p1 = pk4(acc[1][2 * q + 1]);
                *(u4*)(xch + q * 1024 + l * 16) = (u4){p0[0], p0[1], p1[0], p1[1]};
            }
            lds_set(hw + tg, 1u);
            ST(13);
#pragma unroll
            for (int q = 0; q < 3; ++q) {
                u2 p0 = pk4(acc[0][2 * q]), p1 = pk4(acc[0][2 * q + 1]);
                bst4<SC1>(silu_pk4((u4){p0[0], p0[1], p1[0], p1[1]}), rw, l * 16, hoff + ((nt1 & 1) * 3 + q) * 1024);
            }
#pragma unroll
            for (int tt = 0; tt < 2; ++tt) { u2 p = pk4(acc[tt][6]); bst2<SC1>(silu_pk2(p[0], p[1]), rw, l * 8, hoff + tt * (56 * 1024) + 6144 + (nt1 & 1) * 512); }
        } else {
            bg(BG6, 28);
            WAITV(0);
            if (l == 0) __hip_atomic_fetch_add(hw + 5, 1u, __ATOMIC_RELAXED, __HIP_MEMORY_SCOPE_WORKGROUP);
            __builtin_amdgcn_s_sleep(8);
            lds_wait(hw + 5, 8u);
            w2_glds(pw, 0, lds0 + LDS_SCR + tg * 8192);
            w2_glds(pw, 1, lds0 + LDS_SCR + tg * 8192 + 4096);
#pragma unroll
            for (int r = 0; r < B_R; ++r) { WAITV(4); stage_out(r); }
            ST(12);
            lds_wait(hw + tg, 1u);
            ST(13);
#pragma unroll
            for (int q = 0; q < 3; ++q)
                bst4<SC1>(silu_pk4(*(const u4*)(xch + q * 1024 + l * 16)), rw, l * 16, hoff + 56 * 1024 + ((nt1 & 1) * 3 + q) * 1024);
        }
        ST(14);
        WAITV(0);
        publish();
        ST(21);
        if (__builtin_expect(aw, 1)) {
            lds_wait(hw + 4, 8u);
#pragma unroll
            for (int ks = 0; ks < 7; ++ks) w2_glds(pw, ks, lds0 + tg * 28672 + ks * 4096);
        } else {
            stage_out(B_R);
#pragma unroll
            for (int r = B_R + 1; r < 7; ++r) { if (r + 1 < 7) WAITV(4); else WAITV(0); stage_out(r); }
        }
    }
    {
        ST(15);
        const int rot = w & 3;
        if (aw) {
            flag_wait(fl + ((l & 4) ? 4096 + (grp * 8 + th * 4 + rot) * 32 : (grp * 8 + (pw ^ ((l & 2) * 2))) * 32) + (l & 1), tag);
            lds_set(hw + 8 + rot, tag);
        } else {
#pragma nounroll
            for (int sp = 0; sp < SPIN_MAX; ++sp) {
                if (__builtin_amdgcn_readfirstlane(__hip_atomic_load(hw + 8 + rot, __ATOMIC_RELAXED, __HIP_MEMORY_SCOPE_WORKGROUP)) == tag) break;
                __builtin_amdgcn_s_sleep(1);
            }
            FENCE();
        }
        ST(16);
        const int toff = WS_H1 + (grp * 8 + th * 4) * (56 * 1024) + pw * 7168;
        int to[4];
#pragma unroll
        for (int tt = 0; tt < 4; ++tt) to[tt] = toff + ((tt + rot) & 3) * (56 * 1024);
        u4 T[7][4];
        auto ldtok = [&](int ks) {
#pragma unroll
            for (int tt = 0; tt < 4; ++tt) {
                if (ks < 6) T[ks][tt] = bld4<SC1>(rw, l * 16, to[tt] + ko[ks] * 1024);
                else {
                    u2 a = bld2<SC1>(rw, l * 8 + 2048, to[tt] + 4096), c = bld2<SC1>(rw, l * 8 + 2560, to[tt] + 4096);
                    T[ks][tt] = (u4){a[0], a[1], c[0], c[1]};
                }
            }
            SCHED();
        };
#pragma unroll
        for (int ks = 0; ks < G2_DEPTH; ++ks) ldtok(ks);
        if (aw) {
#pragma unroll
            for (int ks = 0; ks < 7; ++ks) w2_read(W[ks], rot * 28672 + ks * 4096);
            asm volatile("s_waitcnt lgkmcnt(0)" ::: "memory");
        }
        if (l == 0) __hip_atomic_fetch_add(hw + 7, 1u, __ATOMIC_RELAXED, __HIP_MEMORY_SCOPE_WORKGROUP);
        const int rho = grp * 128 + th * 64 + rot * 16 + m;
        const int ooff = (256 * (rho & 7) + (rho >> 3)) * 1024 + fb * 128 + (w >> 2) * 64 + g * 16;
        const u4 hres = bld4<SC1>(ro, ooff, 0);
        SCHED();
        f4 acc[4][4];
#pragma unroll
        for (int tt = 0; tt < 4; ++tt)
#pragma unroll
            for (int t = 0; t < 4; ++t) acc[tt][t] = Z4;
#pragma unroll
        for (int ks = 0; ks < 7; ++ks) {
            if (ks + G2_DEPTH < 7) { PIN(T[ks][0]); ldtok(ks + G2_DEPTH); }
#pragma unroll
            for (int tt = 0; tt < 4; ++tt)
#pragma unroll
                for (int t = 0; t < 4; ++t) {
                    acc[tt][t] = mma_b(W[ks][t], T[ks][tt], acc[tt][t]);
                }
            SCHED();
        }
        ST(17);
        lds_wait(hw + 7, 8u);
        char* dp = smem + w * 16384 + l * 16;
#pragma unroll
        for (int tt = 1; tt < 4; ++tt)
#pragma unroll
            for (int t = 0; t < 4; ++t) *(f4*)(dp + (((tt + rot) & 3) * 4 + t) * 1024) = acc[tt][t];
        f4 s0, s1;
        if (aw) { *(f4*)(dp + (rot * 4 + 2) * 1024) = acc[0][2]; *(f4*)(dp + (rot * 4 + 3) * 1024) = acc[0][3]; s0 = acc[0][0]; s1 = acc[0][1]; }
        else { *(f4*)(dp + (rot * 4) * 1024) = acc[0][0]; *(f4*)(dp + (rot * 4 + 1) * 1024) = acc[0][1]; s0 = acc[0][2]; s1 = acc[0][3]; }
        BARRIER();
        f4 r0[7], r1[7];
#pragma unroll
        for (int j = 1; j < 8; ++j) {
            const char* rp = smem + (((w + j) & 7) * 16 + rot * 4 + (w >> 2) * 2) * 1024 + l * 16;
            r0[j - 1] = *(const f4*)rp; r1[j - 1] = *(const f4*)(rp + 1024);
        }
        SCHED();
        s0 = ((s0 + r0[0]) + (r0[1] + r0[2])) + ((r0[3] + r0[4]) + (r0[5] + r0[6]));
        s1 = ((s1 + r1[0]) + (r1[1] + r1[2])) + ((r1[3] + r1[4]) + (r1[5] + r1[6]));
        u2 p0 = pk4(s0), p1 = pk4(s1);
        bst4<NT>((u4){pk_b2(bf_2(hres[0]) + bf_2(p0[0])), pk_b2(bf_2(hres[1]) + bf_2(p0[1])),
                           pk_b2(bf_2(hres[2]) + bf_2(p1[0])), pk_b2(bf_2(hres[3]) + bf_2(p1[1]))}, ro, ooff, 0);
    }
    ST_END(ws, 0);
}

extern "C" void kernel_launch(void* const* d_in, const int* in_sizes, int n_in, void* d_out, int out_size,
                              void* d_ws, size_t ws_size, hipStream_t stream) {
    (void)hipFuncSetAttribute((const void*)k_fused, hipFuncAttributeMaxDynamicSharedMemorySize, LDS_B);
    k_fused<<<256, 512, LDS_B, stream>>>((const uint16_t*)d_in[0], (const uint16_t*)d_in[1], (const uint16_t*)d_in[2],
                                         (const uint16_t*)d_in[4], (const uint16_t*)d_in[5], (uint16_t*)d_out, (char*)d_ws);
}
```

```cpp
#define DIAG_PT 4
#include <hip/hip_runtime.h>
#include <hip/hip_bf16.h>
#include <stdint.h>

typedef _Float16 h8 __attribute__((ext_vector_type(8)));
typedef _Float16 h4 __attribute__((ext_vector_type(4)));
typedef _Float16 h2 __attribute__((ext_vector_type(2)));
typedef __bf16 b8 __attribute__((ext_vector_type(8)));
typedef __bf16 b2 __attribute__((ext_vector_type(2)));
typedef short s4 __attribute__((ext_vector_type(4)));
typedef float f4 __attribute__((ext_vector_type(4)));
typedef float f2 __attribute__((ext_vector_type(2)));
typedef unsigned u32;
typedef u32 u4 __attribute__((ext_vector_type(4)));
typedef u32 u2 __attribute__((ext_vector_type(2)));
#define LAS __attribute__((address_space(3)))
#define GAS __attribute__((address_space(1)))
#define DI __device__ __forceinline__
#define FENCE() asm volatile("" ::: "memory")
#define PIN(x) asm volatile("" : "+v"(x))
#define SCHED() __builtin_amdgcn_sched_barrier(0)
#define WAITV(n) asm volatile("s_waitcnt vmcnt(%0)" :: "n"(n) : "memory")
#define BARRIER() do { asm volatile("s_waitcnt lgkmcnt(0)" ::: "memory"); __builtin_amdgcn_s_barrier(); FENCE(); } while (0)
constexpr int SC1 = 16, NT = 2;
constexpr u32 SENT = 0xffffffffu;
constexpr int SPIN_MAX = 1 << 20;

DI u32 pk_h(float a, float b) { f2 v = {a, b}; h2 r = __builtin_convertvector(v, h2); return __builtin_bit_cast(u32, r); }
DI u32 pk_b(float a, float b) { f2 v = {a, b}; b2 r = __builtin_convertvector(v, b2); return __builtin_bit_cast(u32, r); }
DI float bf_lo(u32 v) { return __builtin_bit_cast(float, v << 16); }
DI float bf_hi(u32 v) { return __builtin_bit_cast(float, v & 0xffff0000u); }
DI f2 bf_2(u32 v) { return (f2){bf_lo(v), bf_hi(v)}; }
DI u32 pk_b2(f2 v) { return pk_b(v[0], v[1]); }
DI float rbf(float a) { return bf_lo(pk_b(a, a)); }
DI float cosr(float rev) { return __builtin_amdgcn_cosf(rev); }
DI float sinr(float rev) { return __builtin_amdgcn_sinf(rev); }
DI f2 lo2(f4 v) { return __builtin_shufflevector(v, v, 0, 1); }
DI f2 hi2(f4 v) { return __builtin_shufflevector(v, v, 2, 3); }
DI f2 fma2(f2 a, f2 b, f2 c) { return __builtin_elementwise_fma(a, b, c); }
DI u32 umin(u32 a, u32 b) { return a < b ? a : b; }
DI u32 umax(u32 a, u32 b) { return a > b ? a : b; }
DI u32 clean(u32 v) { return umin(v, SENT - 1); }
DI u32 max4(u4 v) { return umax(umax(umax(v[0], v[1]), v[2]), v[3]); }
#define Z4 ((f4){0, 0, 0, 0})
DI f4 mma_h(u4 a, u4 b, f4 c) { return __builtin_amdgcn_mfma_f32_16x16x32_f16(__builtin_bit_cast(h8, a), __builtin_bit_cast(h8, b), c, 0, 0, 0); }
DI f4 mma_b(u4 a, u4 b, f4 c) { return __builtin_amdgcn_mfma_f32_16x16x32_bf16(__builtin_bit_cast(b8, a), __builtin_bit_cast(b8, b), c, 0, 0, 0); }
DI f4 mma16_b(u2 a, u2 b, f4 c) { return __builtin_amdgcn_mfma_f32_16x16x16bf16_1k(__builtin_bit_cast(s4, a), __builtin_bit_cast(s4, b), c, 0, 0, 0); }
DI f4 mma16_h(u2 a, u2 b, f4 c) { return __builtin_amdgcn_mfma_f32_16x16x16f16(__builtin_bit_cast(h4, a), __builtin_bit_cast(h4, b), c, 0, 0, 0); }
typedef __amdgpu_buffer_rsrc_t rsrc_t;
DI rsrc_t mkrs(const void* p, u32 bytes) { return __builtin_amdgcn_make_buffer_rsrc((void*)p, 0, bytes, 0x00020000); }
template <int AUX> DI u4 bld4(rsrc_t r, int v, int s) { return __builtin_amdgcn_raw_buffer_load_b128(r, v, s, AUX); }
template <int AUX> DI u2 bld2(rsrc_t r, int v, int s) { return __builtin_amdgcn_raw_buffer_load_b64(r, v, s, AUX); }
template <int AUX> DI void bst4(u4 d, rsrc_t r, int v, int s) {
    __builtin_amdgcn_raw_buffer_store_b128(d, r, v, s, AUX);
    asm volatile("s_nop 1" :: "v"(d));
}
template <int AUX> DI void bst2(u2 d, rsrc_t r, int v, int s) { __builtin_amdgcn_raw_buffer_store_b64(d, r, v, s, AUX); }

template <int CTRL> DI float dpp(float v) {
    return __builtin_bit_cast(float, __builtin_amdgcn_update_dpp(0, __builtin_bit_cast(int, v), CTRL, 0xf, 0xf, true));
}
template <bool ALL> DI float red_lanes(float v) {
    v += dpp<0xB1>(v);
    if (ALL) v += dpp<0x4E>(v);
    v += dpp<0x124>(v); v += dpp<0x128>(v);
    u32 x = __builtin_bit_cast(u32, v), y = x;
    asm("" : "+v"(y));
    auto r = __builtin_amdgcn_permlane16_swap(x, y, false, false);
    v = __builtin_bit_cast(float, (u32)r[0]) + __builtin_bit_cast(float, (u32)r[1]);
    x = __builtin_bit_cast(u32, v); y = x;
    asm("" : "+v"(y));
    auto q = __builtin_amdgcn_permlane32_swap(x, y, false, false);
    return __builtin_bit_cast(float, (u32)q[0]) + __builtin_bit_cast(float, (u32)q[1]);
}
DI float ln_inv(float var) { return __builtin_amdgcn_rcpf(rbf(__builtin_amdgcn_sqrtf(rbf(rbf(var) + 1e-5f)))); }

#define ST_DECL unsigned long long dg0_ = __builtin_amdgcn_s_memrealtime(), dg1_ = dg0_
#define ST(i) do { if (i == DIAG_PT) dg1_ = __builtin_amdgcn_s_memrealtime(); } while (0)
#define ST_END(ws, kid) do { WAITV(0); ST(22); unsigned long long e_ = __builtin_amdgcn_s_memrealtime() + (dg1_ - dg0_); if (aw) while (__builtin_amdgcn_s_memrealtime() < e_) __builtin_amdgcn_s_sleep(1); } while (0)

#define FS 1
constexpr u32 WS_U = 0;
constexpr u32 WS_V = WS_U + (2u << 20);
constexpr u32 WS_LN2 = WS_V + (2u << 20);
constexpr u32 WS_WA = WS_LN2 + (2u << 20);
constexpr u32 WS_WB = WS_WA + 8192u;
constexpr u32 WS_FA = WS_WB + 8192u;
constexpr u32 WS_FLAG = WS_FA + 16384u * FS;
constexpr u32 WS_EP = WS_FLAG + 32768u;
constexpr u32 WS_H1 = WS_WA + (512u << 10);
constexpr u32 WS_END = WS_H1 + 7340032u;

#define BGA 9
#define BGB 15
#define BGC 21
#define G_SLEEP 16
#define GA_SLEEP 4
#define GB_SLEEP 8
#define A_SLEEP 0
#define BG_DELAY 10
#define PRO_SLEEP 4
constexpr int BG5 = BGA, BG5b = BGB, BG6 = BGC;
constexpr int G1_DEPTH = 10, G2_DEPTH = 3;
constexpr int B_R = 5;
constexpr int CODE_LINES = 290, CODE_PF = 10;
constexpr int LDS_SCR = 114688;
constexpr int SCR_X = 8448;
constexpr int SCR_S = SCR_X + 20480;
constexpr int SCR_F = 49024;
constexpr int XCH = 147456;
constexpr int LDS_B = 163840;

DI void glds16(const void* sbase, u32 voff, u32 lds_dst) {
    asm volatile("s_mov_b32 m0, %2\n\ts_nop 0\n\tglobal_load_lds_dwordx4 %0, %1" :: "v"(voff), "s"(sbase), "s"(lds_dst) : "memory");
}
DI bool any_sent(u32 v) { return __builtin_amdgcn_ballot_w64(v == SENT) != 0; }
DI void poll2(u4& a, u4& c, rsrc_t rw, int vo, int sa, int sc) {
#pragma nounroll
    for (int sp = 0; sp < SPIN_MAX; ++sp) {
        const bool ba = max4(a) == SENT, bc = max4(c) == SENT;
        if (__builtin_amdgcn_ballot_w64(ba || bc) == 0) break;
        __builtin_amdgcn_s_sleep(1);
        FENCE();
        if (ba) a = bld4<SC1>(rw, vo, sa);
        if (bc) c = bld4<SC1>(rw, vo, sc);
    }
}
DI void wait_tok(u4& t0, u4& t1, rsrc_t rw, int vo, int so) {
    if (any_sent(umax(max4(t0), max4(t1)))) {
#pragma nounroll
        for (int sp = 0; sp < SPIN_MAX; ++sp) {
            FENCE();
            t0 = bld4<SC1>(rw, vo, so); t1 = bld4<SC1>(rw, vo, so + 16384);
            if (!any_sent(umax(max4(t0), max4(t1)))) break;
        }
    }
}
DI u4 ldw1(LAS char* wb, int kk, int ft) {
    s4 a0 = __builtin_amdgcn_ds_read_tr16_b64_v4i16((LAS s4*)(wb + kk * 32 * 224 + ft * 32));
    s4 a1 = __builtin_amdgcn_ds_read_tr16_b64_v4i16((LAS s4*)(wb + (kk * 32 + 16) * 224 + ft * 32));
    u2 x0 = __builtin_bit_cast(u2, a0), x1 = __builtin_bit_cast(u2, a1);
    return (u4){x0[0], x0[1], x1[0], x1[1]};
}
DI u2 pk4(f4 a) { return (u2){pk_b(a[0], a[1]), pk_b(a[2], a[3])}; }
DI u2 silu_pk2(u32 p0, u32 p1) {
    u32 o0, o1; float a0, a1, a2, a3, t0, t1, t2, t3;
    asm("v_lshlrev_b32 %2, 16, %10\n\tv_and_b32 %3, 0xffff0000, %10\n\tv_lshlrev_b32 %4, 16, %11\n\tv_and_b32 %5, 0xffff0000, %11\n\t"
        "v_mul_f32 %6, %12, %2\n\tv_mul_f32 %7, %12, %3\n\tv_mul_f32 %8, %12, %4\n\tv_mul_f32 %9, %12, %5\n\t"
        "v_exp_f32 %6, %6\n\tv_exp_f32 %7, %7\n\tv_exp_f32 %8, %8\n\tv_exp_f32 %9, %9\n\t"
        "v_add_f32 %6, 1.0, %6\n\tv_add_f32 %7, 1.0, %7\n\tv_add_f32 %8, 1.0, %8\n\tv_add_f32 %9, 1.0, %9\n\t"
        "v_rcp_f32 %6, %6\n\tv_rcp_f32 %7, %7\n\tv_rcp_f32 %8, %8\n\tv_rcp_f32 %9, %9\n\t"
        "v_mul_f32 %2, %2, %6\n\tv_mul_f32 %3, %3, %7\n\tv_mul_f32 %4, %4, %8\n\tv_mul_f32 %5, %5, %9\n\t"
        "v_cvt_pk_bf16_f32 %0, %2, %3\n\tv_cvt_pk_bf16_f32 %1, %4, %5"
        : "=&v"(o0), "=&v"(o1), "=&v"(a0), "=&v"(a1), "=&v"(a2), "=&v"(a3), "=&v"(t0), "=&v"(t1), "=&v"(t2), "=&v"(t3)
        : "v"(p0), "v"(p1), "s"(-1.4426950408889634f));
    return (u2){o0, o1};
}
DI u4 silu_pk4(u4 p) { u2 a = silu_pk2(p[0], p[1]), b = silu_pk2(p[2], p[3]); return (u4){a[0], a[1], b[0], b[1]}; }
DI void lds_set(LAS u32* p, u32 v) {
    asm volatile("s_waitcnt lgkmcnt(0)" ::: "memory");
    __hip_atomic_store(p, v, __ATOMIC_RELAXED, __HIP_MEMORY_SCOPE_WORKGROUP);
}
DI void lds_wait(LAS u32* p, u32 v) {
#pragma nounroll
    for (int sp = 0; sp < SPIN_MAX; ++sp) {
        if (__builtin_amdgcn_readfirstlane(__hip_atomic_load(p, __ATOMIC_RELAXED, __HIP_MEMORY_SCOPE_WORKGROUP)) >= v) break;
        __builtin_amdgcn_s_sleep(1);
    }
    FENCE();
}
DI void flag_wait(GAS u32* p, u32 tag) {
#pragma nounroll
    for (int sp = 0; sp < SPIN_MAX; ++sp) {
        if (__builtin_amdgcn_ballot_w64(__hip_atomic_load(p, __ATOMIC_RELAXED, __HIP_MEMORY_SCOPE_AGENT) != tag) == 0) break;
        __builtin_amdgcn_s_sleep(2);
    }
    FENCE();
}
#define SENT4 ((u4){SENT, SENT, SENT, SENT})

__global__ __launch_bounds__(512, 2) void k_fused(const uint16_t* x, const uint16_t* wr, const uint16_t* wi, const uint16_t* w1,
                                                  const uint16_t* w2, uint16_t* out, char* ws) {
    extern __shared__ __attribute__((aligned(16))) char smem[];
    const int tid = threadIdx.x, l = tid & 63, g = l >> 4, m = l & 15;
    const int w = __builtin_amdgcn_readfirstlane(tid >> 6);
    const int b = (blockIdx.x & 7) * 32 + (blockIdx.x >> 3);
    const int mt1 = (b >> 6) * 4 + ((b >> 3) & 3), nt1 = ((b >> 5) & 1) * 8 + (b & 7);
    const int grp = b >> 4, th = (b >> 3) & 1, fb = b & 7;
    const rsrc_t rw = mkrs(ws, WS_END), ro = mkrs(out, 2097152);
    char* scr = smem + LDS_SCR;
    LAS u32* hw = (LAS u32*)((LAS char*)smem + LDS_SCR + SCR_F);
    const u32 lds0 = (u32)(uintptr_t)(LAS char*)smem;
    const float mf = (float)m, gf = (float)g, sg = (m & 1) ? -1.0f : 1.0f;
    const bool aw = w < 4;
    ST_DECL;
    unsigned long long pc0;
    asm volatile("s_getpc_b64 %0" : "=s"(pc0));
    if (tid >= 448 && l < CODE_PF)
        asm volatile("s_mov_b32 m0, %2\n\ts_nop 0\n\tglobal_load_lds_dword %0, %1"
                     :: "v"(umin((blockIdx.x >> 3) * CODE_PF + l, CODE_LINES - 1) * 128), "s"(pc0), "s"(lds0 + LDS_SCR + SCR_F + 64) : "memory");
    const u32 epoch = __hip_atomic_load((GAS u32*)(ws + WS_EP), __ATOMIC_RELAXED, __HIP_MEMORY_SCOPE_AGENT);
    FENCE();
    u32 xd[8];
    const int f1 = b >> 5, cg = b & 31;
    const bool mir = cg >= 16;
    const rsrc_t rr = mkrs(wr, 1052672), ri = mkrs(wi, 1052672);
    u4 fr, fi; u32 sr_, si_;
    if (aw) {
        __builtin_amdgcn_s_sleep(A_SLEEP);
        const u32* xr = (const u32*)(x + (256 * (2 * w + ((m >> 1) & 1)) + b) * 512) + 8 * g + 2 * (m >> 2) + (m & 1);
#pragma unroll
        for (int j = 0; j < 8; ++j) xd[j] = xr[32 * j];
    } else {
        bst4<SC1>(SENT4, rw, l * 16, WS_U + b * 8192 + (w - 4) * 2048); bst4<SC1>(SENT4, rw, l * 16 + 1024, WS_U + b * 8192 + (w - 4) * 2048);
        __builtin_amdgcn_raw_buffer_store_b32(SENT, rw, l < 4 ? (l >> 1) * 8192 + (l & 1) * 1024 : 0x7ffffff0,
                                              WS_WA + (w - 4) * 2048 + ((b >> 6) * 16 + (b & 15)) * 16 + ((b >> 4) & 3) * 4, SC1);
        FENCE();
        if (w == 6) {
            const int c = (l >> 3) & 3, f_ = 8 * b + (l & 7);
            const int e1 = ((c & 1) ? (2048 - f_) & 2047 : f_) * 514 + (c >> 1) * 512;
            sr_ = __builtin_amdgcn_raw_buffer_load_b16(rr, e1, 0, 0); si_ = __builtin_amdgcn_raw_buffer_load_b16(ri, e1, 0, 0);
        }
    }
    FENCE();
    const int zo = (64 * g + (g < 2 ? m : (m + 15) & 15)) * 4;
    const int rq = ((b >> 3) + b) & 3;
    const int row0 = (tid & 255) / 14, cch0 = (tid & 255) - row0 * 14;
    const char* wsrc = (const char*)w1 + nt1 * 224;
    u32 w1o[7];
    auto w1_burst = [&](int i0, int n) {
#pragma unroll
        for (int i = i0; i < i0 + n; ++i) {
            const int q = (i / 7 + rq) & 3, j = i % 7;
            glds16(wsrc + q * (128 * 3584), w1o[j], lds0 + (w - 4) * 1024 + q * 28672 + j * 4096);
        }
    };
    const int xoff = (256 * w + b) * 1024 + m * 32 + g * 8;
    u2 xl[2];
    float c16[4], s16[4];
    f2 twc[2], tws[2];
    u4 Gre3, Gim3, SGre, SGim, GA[2];
    auto consts0 = [&]() {
#pragma unroll
        for (int u = 0; u < 4; ++u) {
            float a = (4.0f * gf + u) * (mf * (1.0f / 16));
            c16[u] = cosr(a); s16[u] = sinr(a);
            twc[u >> 1][u & 1] = cosr(a * (1.0f / 32)); tws[u >> 1][u & 1] = sinr(a * (1.0f / 32));
        }
    };
    auto consts3 = [&]() {
#pragma unroll
        for (int u = 0; u < 4; ++u) {
            const float k = 4.0f * gf + u;
            const bool k0 = u == 0 && g == 0;
            float cc = c16[u], ss = s16[u];
            Gre3[u] = pk_b(cc, -ss); Gim3[u] = pk_b(ss, cc);
            float a2 = (2.0f * k + 1.0f) * (mf * (1.0f / 32));
            u32 sp = pk_b(g < 2 ? 2.f * cc : 2.f * cosr(a2), g < 2 ? -2.f * ss : -2.f * sinr(a2));
            SGre[u] = k0 ? pk_b(1.0f, sg) : (g < 2 ? sp : 0u);
            SGim[u] = g < 2 ? 0u : sp;
            float a4 = k * (mf * (1.0f / 32)), c4 = 2.f * cosr(a4), s4_ = -2.f * sinr(a4);
            GA[0][u] = k0 ? pk_b(1.0f, sg) : pk_b(c4, s4_);
            GA[1][u] = k0 ? pk_b(1.0f, sg) : ((u & 1) ? pk_b(-c4, -s4_) : pk_b(c4, s4_));
        }
        PIN(Gre3); PIN(Gim3); PIN(SGre); PIN(SGim); PIN(GA[0]); PIN(GA[1]); PIN(twc[0]); PIN(twc[1]); PIN(tws[0]); PIN(tws[1]);
    };
    const int pw = (w + 4 * th) & 7;
    GAS u32* fl = (GAS u32*)(ws + WS_FLAG);
    int ko[7];
    {
        const int r6 = (fb < 6 ? fb : fb - 5) + 3 * (grp & 1);
#pragma unroll
        for (int ks = 0; ks < 6; ++ks) { int k = ks + r6; k = k >= 6 ? k - 6 : k; ko[ks] = k >= 6 ? k - 6 : k; }
        ko[6] = 6;
    }
    u4 W[7][4];
    const u32 w2vo = (l >> 3) * 1024 + (((l & 7) ^ (((l >> 4) & 1) << 2)) * 16);
    auto w2_glds = [&](int p_, int ks, u32 dst) {
        const int q = ko[ks];
        const char* src = (const char*)w2 + (224 * p_ + (ks == 6 ? 96 : (q < 3 ? 32 * q : 16 + 32 * q))) * 1024 + fb * 128;
        glds16(src, w2vo, dst); glds16(src + 8192, w2vo, dst + 1024);
        glds16(src + (ks == 6 ? 112 : 16) * 1024, w2vo, dst + 2048); glds16(src + (ks == 6 ? 120 : 24) * 1024, w2vo, dst + 3072);
    };
    LAS char* w2rd = (LAS char*)smem + (4 * g + (m >> 2)) * 128 + (((m & 3) ^ (((m >> 3) & 1) << 2)) * 16);
    auto w2_read = [&](u4 (&f)[4], int off) {
#pragma unroll
        for (int t = 0; t < 4; ++t) {
            LAS char* p = (LAS char*)((u32)(uintptr_t)w2rd ^ ((t >> 1) * 64)) + off + (t & 1) * 8;
            u2 x0 = __builtin_bit_cast(u2, __builtin_amdgcn_ds_read_tr16_b64_v4i16((LAS s4*)p));
            u2 x1 = __builtin_bit_cast(u2, __builtin_amdgcn_ds_read_tr16_b64_v4i16((LAS s4*)(p + 2048)));
            f[t] = (u4){x0[0], x0[1], x1[0], x1[1]};
        }
    };
    auto bg = [&](int i0, int i1) { w1_burst(i0, i1 - i0); };
    u32 tag;
    if (__builtin_expect(aw, 1)) {
        if (tid < 8) hw[tid] = 0;
        FENCE();
        ST(2);
        {
            u4 FAre, FAim, Fre, Fim, Sre, Sim; u2 Mb; f2 pc[2], ps[2];
            consts0();
            float c0[8], s0[8];
#pragma unroll
            for (int j = 0; j < 8; ++j) {
                float a = (gf + 4.0f * j) * (mf * (1.0f / 32));
                c0[j] = cosr(a); s0[j] = m ? -sinr(a) : ((g & 1) ? -1.0f : 1.0f);
            }
            const float mo = (2.0f * mf + 1.0f) * (1.0f / 32);
#pragma unroll
            for (int u = 0; u < 4; ++u) {
                FAre[u] = pk_h(c0[2 * u], c0[2 * u + 1]); FAim[u] = pk_h(s0[2 * u], s0[2 * u + 1]);
                float cc = c16[u], ss = s16[u];
                Fre[u] = pk_h(cc, ss); Fim[u] = pk_h(-ss, cc);
                float a2 = (4.0f * gf + u) * mo;
                u32 re = pk_h(m < 8 ? cc : cosr(a2), 0.f), im = pk_h(m < 8 ? (m ? -ss : ((u & 1) ? -1.0f : 1.0f)) : -sinr(a2), 0.f);
                Sre[u] = m < 8 ? re : re << 16; Sim[u] = m < 8 ? im : im << 16;
                pc[u & 1][u >> 1] = twc[u >> 1][u & 1]; ps[u & 1][u >> 1] = tws[u >> 1][u & 1];
            }
#pragma unroll
            for (int e = 0; e < 2; ++e) {
                float a = (float)(((m >> 1) * (256 * (2 * g + e) + b)) & 2047) * (1.0f / 2048);
                float cc = cosr(a), ss = sinr(a);
                Mb[e] = (m & 1) ? pk_h(-ss, cc) : pk_h(cc, ss);
            }
            PIN(FAre); PIN(FAim); PIN(Fre); PIN(Fim); PIN(Sre); PIN(Sim); PIN(Mb); PIN(pc[0]); PIN(pc[1]); PIN(ps[0]); PIN(ps[1]);
            ST(3);
            f2 v[8];
#pragma unroll
            for (int j = 0; j < 8; ++j) v[j] = bf_2(xd[j]);
            f2 s2 = ((v[0] + v[1]) + (v[2] + v[3])) + ((v[4] + v[5]) + (v[6] + v[7]));
            const float mean = red_lanes<false>(s2[0] + s2[1]) * (1.0f / 512);
            f2 q0 = {0.f, 0.f}, q1 = {0.f, 0.f};
#pragma unroll
            for (int j = 0; j < 8; ++j) { v[j] -= mean; if (j & 1) q1 = fma2(v[j], v[j], q1); else q0 = fma2(v[j], v[j], q0); }
            q0 += q1;
            const float inv = ln_inv(red_lanes<false>(q0[0] + q0[1]) * (1.0f / 512));
            const float off = (mean - rbf(mean)) * inv;
#pragma unroll
            for (int j = 0; j < 8; ++j) v[j] = fma2(v[j], (f2){inv, inv}, (f2){off, off});
            u4 B[2];
#pragma unroll
            for (int p = 0; p < 2; ++p) {
                u4 A = {pk_h(v[0][p], v[1][p]), pk_h(v[2][p], v[3][p]), pk_h(v[4][p], v[5][p]), pk_h(v[6][p], v[7][p])};
                f4 Dre = mma_h(A, FAre, Z4), Dim = mma_h(A, FAim, Z4);
#pragma unroll
                for (int r = 0; r < 2; ++r) {
                    f2 re = r ? hi2(Dre) : lo2(Dre), im = r ? hi2(Dim) : lo2(Dim);
                    f2 tr = fma2(im, ps[p], re * pc[p]), ti = fma2(-re, ps[p], im * pc[p]);
                    B[r][p] = pk_h(tr[0], ti[0]); B[r][2 + p] = pk_h(tr[1], ti[1]);
                }
            }
#pragma unroll
            for (int r = 0; r < 2; ++r) {
                f4 Gre = mma_h(Fre, B[r], Z4), Gim = mma_h(Fim, B[r], Z4), Hre = mma_h(Sre, B[r], Z4), Him = mma_h(Sim, B[r], Z4);
#pragma unroll
                for (int i = 0; i < 4; ++i) *(u32*)(scr + (2 * w + r) * 1024 + zo + i * 64) = pk_h(m ? Gre[i] : Hre[i], m ? Gim[i] : Him[i]);
            }
            BARRIER();
            ST(18);
            u4 r0 = *(const u4*)(scr + (2 * g) * 1024 + w * 256 + m * 16);
            u4 r1 = *(const u4*)(scr + (2 * g + 1) * 1024 + w * 256 + m * 16);
            u4 o0, o1;
#pragma unroll
            for (int t = 0; t < 4; ++t) {
                f4 D = mma16_h(Mb, (u2){r0[t], r1[t]}, Z4);
                o0[t] = clean(pk_h(D[0], D[1])); o1[t] = clean(pk_h(D[2], D[3]));
            }
            bst4<SC1>(o0, rw, g * 2048 + m * 16, WS_U + b * 8192 + w * 256);
            bst4<SC1>(o1, rw, g * 2048 + m * 16 + 1024, WS_U + b * 8192 + w * 256);
        }
        ST(4);
        u4 Fre, Fim, Gre, Gim; u2 Nb; f2 tc[2], ts[2];
#pragma unroll
        for (int u = 0; u < 4; ++u) {
            float cc = c16[u], ss = s16[u];
            Fre[u] = pk_h(0.25f * cc, 0.25f * ss); Fim[u] = pk_h(-0.25f * ss, 0.25f * cc);
            Gre[u] = pk_b(cc * (1.0f / 256), -ss * (1.0f / 256)); Gim[u] = pk_b(ss * (1.0f / 256), cc * (1.0f / 256));
            float a3 = (4.0f * gf + u) * (mf * (1.0f / 256));
            tc[u >> 1][u & 1] = cosr(a3); ts[u >> 1][u & 1] = sinr(a3);
        }
        PIN(Fre); PIN(Fim); PIN(Gre); PIN(Gim); PIN(tc[0]); PIN(tc[1]); PIN(ts[0]); PIN(ts[1]);
        xl[0] = *(const u2*)((const char*)x + xoff); xl[1] = *(const u2*)((const char*)x + xoff + 512);
        FENCE();
        tag = __builtin_amdgcn_readfirstlane(epoch) + 1u;
        consts3();
#pragma unroll
        for (int e = 0; e < 2; ++e) {
            float a1 = (float)(((2 * g + e) * (256 * (m >> 1) + b)) & 2047) * (1.0f / 2048);
            float c1 = cosr(a1), s1 = sinr(a1);
            Nb[e] = (m & 1) ? pk_b(s1, c1) : pk_b(c1, -s1);
        }
        PIN(Nb);
        WAITV(0);
        ST(19);
        BARRIER();
        ST(5);
        u4 d[2], wa, wb;
        const int uo = (tid >> 1) * 8192 + (tid & 1) * 16, us = WS_U + f1 * 1024 + cg * 32;
        d[0] = bld4<SC1>(rw, uo, us); d[1] = bld4<SC1>(rw, uo, us + 128 * 8192);
        if (cg == 0) {
#pragma nounroll
            for (int sp = 0; sp < SPIN_MAX; ++sp) {
                FENCE();
                const u32 ux = __builtin_amdgcn_raw_buffer_load_b32(rw, tid * 8192, WS_U + ((8 - f1) & 7) * 1024, SC1);
                wa = bld4<SC1>(rw, l * 16, WS_WA + f1 * 1024); wb = bld4<SC1>(rw, l * 16, WS_WB + f1 * 1024);
                *(u32*)(scr + SCR_X + tid * 4) = ux;
                if (!any_sent(umax(ux, umax(max4(wa), max4(wb))))) break;
            }
        }
        poll2(d[0], d[1], rw, uo, us, us + 128 * 8192);
        char* sp = scr + SCR_S + (tid ^ ((tid >> 4) & 1)) * 16;
        *(u4*)sp = d[0]; *(u4*)(sp + 4096) = d[1];
        BARRIER();
        if (b == 0 && tid == 0) __hip_atomic_store((GAS u32*)(ws + WS_EP), tag, __ATOMIC_RELAXED, __HIP_MEMORY_SCOPE_AGENT);
        ST(6);
        u2 ld[4], wl[4];
        const int so = (64 * g + m) * 32 + (((w >> 1) ^ (m >> 3)) * 16) + (w & 1) * 8;
#pragma unroll
        for (int q = 0; q < 4; ++q) ld[q] = *(const u2*)(scr + SCR_S + so + 512 * q);
#pragma unroll
        for (int q = 0; q < 4; ++q) wl[q] = *(const u2*)(scr + SCR_S + so + 8192 + 512 * q);
        FENCE();
        auto fwd = [&](u4 A, f4& Xr, f4& Xi) {
            f4 Dre = mma_h(A, Fre, Z4), Dim = mma_h(A, Fim, Z4);
            f2 r0 = fma2(lo2(Dim), ts[0], lo2(Dre) * tc[0]), i0 = fma2(-lo2(Dre), ts[0], lo2(Dim) * tc[0]);
            f2 r1 = fma2(hi2(Dim), ts[1], hi2(Dre) * tc[1]), i1 = fma2(-hi2(Dre), ts[1], hi2(Dim) * tc[1]);
            u4 B = {pk_h(r0[0], i0[0]), pk_h(r0[1], i0[1]), pk_h(r1[0], i1[0]), pk_h(r1[1], i1[1])};
            Xr = mma_h(Fre, B, Z4); Xi = mma_h(Fim, B, Z4);
        };
        f4 Xre[2], Xim[2];
#pragma unroll
        for (int e = 0; e < 2; ++e) fwd((u4){ld[0][e], ld[1][e], ld[2][e], ld[3][e]}, Xre[e], Xim[e]);
        f2 yr[2][2], yi[2][2];
#pragma unroll
        for (int e = 0; e < 2; ++e)
#pragma unroll
            for (int hh = 0; hh < 2; ++hh) {
                f2 wre = {bf_lo(wl[2 * hh][e]), bf_lo(wl[2 * hh + 1][e])}, wim = {bf_hi(wl[2 * hh][e]), bf_hi(wl[2 * hh + 1][e])};
                f2 xr = hh ? hi2(Xre[e]) : lo2(Xre[e]), xi = hh ? hi2(Xim[e]) : lo2(Xim[e]);
                yr[e][hh] = fma2(-xi, wim, xr * wre); yi[e][hh] = fma2(xr, wim, xi * wre);
            }
        if (cg == 0 && w == 0) {
            const u32* up = (const u32*)(scr + SCR_X) + 64 * g + m;
            f4 Mr, Mi;
            fwd((u4){up[0], up[16], up[32], up[48]}, Mr, Mi);
            f2* sx = (f2*)(scr + SCR_X + 1024);
#pragma unroll
            for (int i = 0; i < 4; ++i) sx[m + 16 * (4 * g + i)] = (f2){Mr[i], Mi[i]};
#pragma unroll
            for (int i = 0; i < 4; ++i) {
                f2 xm = sx[((f1 ? 255 : 256) - (m + 16 * (4 * g + i))) & 255];
                float ar = bf_lo(wa[i]), ai = bf_hi(wa[i]), br = bf_lo(wb[i]), bi = bf_hi(wb[i]);
                yr[0][i >> 1][i & 1] = Xre[0][i] * ar - Xim[0][i] * ai + xm[0] * br + xm[1] * bi;
                yi[0][i >> 1][i & 1] = Xre[0][i] * ai + Xim[0][i] * ar + xm[0] * bi - xm[1] * br;
            }
        }
        char* pv = scr;
#pragma unroll
        for (int e = 0; e < 2; ++e) {
            u4 Y = {pk_b(yr[e][0][0], yi[e][0][0]), pk_b(yr[e][0][1], yi[e][0][1]), pk_b(yr[e][1][0], yi[e][1][0]), pk_b(yr[e][1][1], yi[e][1][1])};
            f4 Dre = mma_b(Y, Gre, Z4), Dim = mma_b(Y, Gim, Z4);
            f2 r0 = fma2(-lo2(Dim), ts[0], lo2(Dre) * tc[0]), i0 = fma2(lo2(Dre), ts[0], lo2(Dim) * tc[0]);
            f2 r1 = fma2(-hi2(Dim), ts[1], hi2(Dre) * tc[1]), i1 = fma2(hi2(Dre), ts[1], hi2(Dim) * tc[1]);
            u4 A = {pk_b(r0[0], i0[0]), pk_b(r0[1], i0[1]), pk_b(r1[0], i1[0]), pk_b(r1[1], i1[1])};
            f4 Ere = mma_b(A, Gre, Z4), Eim = mma_b(A, Gim, Z4);
            u4 o;
#pragma unroll
            for (int i = 0; i < 4; ++i) o[i] = clean(pk_b(Ere[i], Eim[i]));
            *(u4*)(pv + (2 * w + e) * 1024 + ((m * 4 + g) ^ ((w >> 1) * 4)) * 16) = o;
        }
        BARRIER();
        ST(7);
#pragma unroll
        for (int it = 0; it < 2; ++it) {
            const int ch = tid + 256 * it;
            const u32* q = (const u32*)(pv + (ch & 1) * 4096 + (((ch >> 1) * 4) ^ ((ch & 1) * 64)));
            bst4<SC1>((u4){q[0], q[256], q[512], q[768]}, rw, ch * 16, WS_V + b * 8192);
        }
        WAITV(0);
        BARRIER();
        ST(8);
        const int vo = (g * 64 + (m >> 1)) * 8192 + (m & 1) * 16, vs = WS_V + w * 8 * 8192 + b * 32;
        u4 v0 = bld4<SC1>(rw, vo, vs), v1 = bld4<SC1>(rw, vo, vs + 32 * 8192);
        poll2(v0, v1, rw, vo, vs, vs + 32 * 8192);
        ST(9);
        u4 z0, z1;
#pragma unroll
        for (int t = 0; t < 4; ++t) {
            f4 D = mma16_b(Nb, (u2){v0[t], v1[t]}, Z4);
            z0[t] = pk_b(D[0], D[1]); z1[t] = pk_b(D[2], D[3]);
        }
        *(u4*)(scr + SCR_X + g * 2048 + w * 256 + m * 16) = z0;
        *(u4*)(scr + SCR_X + g * 2048 + 1024 + w * 256 + m * 16) = z1;
    } else {
        auto announce = [&](int which) {
            if (l < 8) __hip_atomic_store((GAS u32*)(ws + WS_FA) + (which * 2048 + l * 256 + b) * FS, tag, __ATOMIC_RELAXED, __HIP_MEMORY_SCOPE_AGENT);
            FENCE();
        };
        auto all_announced = [&](int which) {
#pragma nounroll
            for (int sp = 0; sp < SPIN_MAX; ++sp) {
                FENCE();
                u4 f;
#pragma unroll
                for (int i = 0; i < 4; ++i) f[i] = __builtin_amdgcn_raw_buffer_load_b32(rw, (l + 64 * i) * 4 * FS, WS_FA + (which * 8192 + (b >> 5) * 1024) * FS, SC1);
                if (__builtin_amdgcn_ballot_w64(f[0] != tag || f[1] != tag || f[2] != tag || f[3] != tag) == 0) break;
                __builtin_amdgcn_s_sleep(4);
            }
        };
        FENCE();
        ST(2);
        tag = __builtin_amdgcn_readfirstlane(epoch) + 1u;
        if (w == 6) WAITV(2); else WAITV(0);
        if (l < 2) __hip_atomic_store((GAS u32*)(ws + WS_FA) + ((2 * (w - 4) + l) * 256 + b) * FS, tag, __ATOMIC_RELAXED, __HIP_MEMORY_SCOPE_AGENT);
        FENCE();
        ST(18);
#pragma unroll
        for (int jj = 0; jj < 7; ++jj) {
            const int cj_ = (4 * jj) % 14, rj = 18 * jj + (4 * jj) / 14;
            w1o[jj] = (row0 + rj) * 3584 + (cch0 + cj_) * 16 + (cch0 + cj_ >= 14 ? 3584 - 224 : 0);
            PIN(w1o[jj]);
        }
        BARRIER();
        if (w == 6) {
            float re = bf_lo(sr_), im = bf_lo(si_);
            re += dpp<0x128>(re); im -= dpp<0x128>(im);
            u32 a = __builtin_bit_cast(u32, re), a2 = a, c = __builtin_bit_cast(u32, im), c2 = c;
            asm("" : "+v"(a2), "+v"(c2));
            auto pr = __builtin_amdgcn_permlane16_swap(a, a2, false, false);
            auto pi = __builtin_amdgcn_permlane16_swap(c, c2, false, false);
            const float ar = __builtin_bit_cast(float, (u32)pr[0]), br = __builtin_bit_cast(float, (u32)pr[1]);
            const float ai = __builtin_bit_cast(float, (u32)pi[0]), bi = __builtin_bit_cast(float, (u32)pi[1]);
            const int wo = WS_WA + ((b >> 6) * 16 + (b & 15)) * 16 + ((b >> 4) & 3) * 4;
            if (l < 8) {
                __builtin_amdgcn_raw_buffer_store_b32(clean(pk_b(0.25f * (ar + br), 0.25f * (ai + bi))), rw, l * 1024, wo, SC1);
                __builtin_amdgcn_raw_buffer_store_b32(clean(pk_b(0.25f * (ar - br), 0.25f * (ai - bi))), rw, l * 1024, wo + 8192, SC1);
            }
        }
        const int f = f1 + 8 * (tid & 255), k0 = 16 * cg - 8 * (cg & 1);
        const int e = mir ? ((2048 - f) & 2047) * 514 + (504 - k0) * 2 : f * 514 + k0 * 2;
        consts0();
        __builtin_amdgcn_s_sleep(GA_SLEEP);
        fr = bld4<0>(rr, e, 0);
        FENCE();
        __builtin_amdgcn_s_sleep(GB_SLEEP);
        if (w == 7) all_announced(0);
        fi = bld4<0>(ri, e, 0);
        xl[0] = *(const u2*)((const char*)x + xoff); xl[1] = *(const u2*)((const char*)x + xoff + 512);
        FENCE();
        ST(4);
        BARRIER();
        {
            u32 o[8];
#pragma unroll
            for (int c = 0; c < 8; ++c)
                o[c] = mir ? __builtin_amdgcn_perm(fi[3 - (c >> 1)], fr[3 - (c >> 1)], (c & 1) ? 0x05040100u : 0x07060302u) ^ 0x80000000u
                           : __builtin_amdgcn_perm(fi[c >> 1], fr[c >> 1], (c & 1) ? 0x07060302u : 0x05040100u);
            char* wp = scr + SCR_S + 8192 + (tid & 255) * 32;
            *(u4*)(wp + (m & 8) * 2) = (u4){o[0], o[1], o[2], o[3]};
            *(u4*)(wp + ((m & 8) * 2 ^ 16)) = (u4){o[4], o[5], o[6], o[7]};
        }
#pragma unroll
        for (int i = 0; i < 2; ++i) bst4<SC1>(SENT4, rw, l * 16 + i * 1024, WS_V + b * 8192 + (w - 4) * 2048);
        FENCE();
        consts3();
        WAITV(0);
        ST(5);
        BARRIER();
        if (w == 7) announce(1);
#pragma unroll
        for (int i = 0; i < 2; ++i) bst4<SC1>(SENT4, rw, (l >> 3) * 256 + (l & 7) * 16 + i * 2048, WS_LN2 + (b >> 1) * 16384 + (b & 1) * 128 + (w - 4) * 4096);
        bg(0, BG5);
        WAITV(BG5);
        ST(6);
        BARRIER();
        if (w == 7) all_announced(1);
        else __builtin_amdgcn_s_sleep(3);
        bg(BG5, BG5b);
        ST(7);
        BARRIER();
        __builtin_amdgcn_s_sleep(BG_DELAY);
        bg(BG5b, BG6);
    }
    BARRIER();
    {
        const u32* zp = (const u32*)(scr + SCR_X + w * 1024 + zo);
        u4 A = {zp[0], zp[16], zp[32], zp[48]};
        f4 Dre = mma_b(A, Gre3, Z4), Dim = mma_b(A, Gim3, Z4), Hre = mma_b(A, SGre, Z4), Him = mma_b(A, SGim, Z4);
        if (g == 0) { Dre[0] = Hre[0]; Dim[0] = Him[0]; }
        f2 r0 = fma2(-lo2(Dim), tws[0], lo2(Dre) * twc[0]), i0 = fma2(lo2(Dre), tws[0], lo2(Dim) * twc[0]);
        f2 r1 = fma2(-hi2(Dim), tws[1], hi2(Dre) * twc[1]), i1 = fma2(hi2(Dre), tws[1], hi2(Dim) * twc[1]);
        u4 A2 = {pk_b(r0[0], i0[0]), pk_b(r0[1], i0[1]), pk_b(r1[0], i1[0]), pk_b(r1[1], i1[1])};
        f2 h[4]; u2 hp[2];
#pragma unroll
        for (int t = 0; t < 2; ++t) {
            f4 Yv = mma_b(A2, GA[t], Z4);
            u32 h0 = pk_b2(bf_2(xl[t][0]) + bf_2(pk_b(Yv[0], Yv[1]))), h1 = pk_b2(bf_2(xl[t][1]) + bf_2(pk_b(Yv[2], Yv[3])));
            hp[t] = (u2){h0, h1};
            h[2 * t] = bf_2(h0); h[2 * t + 1] = bf_2(h1);
        }
        f2 s2 = (h[0] + h[1]) + (h[2] + h[3]);
        const float mean = red_lanes<true>(s2[0] + s2[1]) * (1.0f / 512);
        f2 q2 = {0.f, 0.f};
#pragma unroll
        for (int j = 0; j < 4; ++j) { f2 d = h[j] - mean; q2 = fma2(d, d, q2); }
        const float inv = ln_inv(red_lanes<true>(q2[0] + q2[1]) * (1.0f / 512));
        const float mb = rbf(mean);
#pragma unroll
        for (int t = 0; t < 2; ++t)
            *(u2*)(scr + ((8 * t + (m >> 1)) * 4 + g) * 128 + (w ^ (m >> 1)) * 16 + (m & 1) * 8) =
                (u2){clean(pk_b2(bf_2(pk_b2(h[2 * t] - mb)) * inv)), clean(pk_b2(bf_2(pk_b2(h[2 * t + 1] - mb)) * inv))};
        WAITV(0);
        BARRIER();
        u4 lo = *(const u4*)(scr + (tid ^ ((tid >> 5) & 7)) * 16);
        bst4<SC1>(lo, rw, (tid >> 3) * 256 + (tid & 7) * 16, WS_LN2 + (b >> 1) * 16384 + (b & 1) * 128);
        FENCE();
        if (aw) WAITV(0); else __builtin_amdgcn_s_sleep(8);
        bst2<SC1>(hp[0], ro, xoff, 0); bst2<SC1>(hp[1], ro, xoff + 512, 0);
        FENCE();
        ST(10);
    }
    auto stage_out = [&](int r) {
        w2_read(W[r], LDS_SCR + (w & 3) * 8192 + (r & 1) * 4096);
        asm volatile("s_waitcnt lgkmcnt(0)" ::: "memory");
        PIN(W[r][0]); PIN(W[r][1]); PIN(W[r][2]); PIN(W[r][3]);
        if (r + 2 < 7) w2_glds(pw, r + 2, lds0 + LDS_SCR + (w & 3) * 8192 + (r & 1) * 4096);
    };
    auto publish = [&]() {
        u32 prev = 0;
        if (l == 0) prev = __hip_atomic_fetch_add(hw + 4, 1u, __ATOMIC_RELAXED, __HIP_MEMORY_SCOPE_WORKGROUP);
        if (__builtin_amdgcn_readfirstlane(prev) == 7u && l < 2)
            __hip_atomic_store(fl + (l ? 4096 + (b >> 1) * 32 + (b & 1) : (mt1 * 8 + (nt1 >> 1)) * 32 + (nt1 & 1)), tag, __ATOMIC_RELAXED, __HIP_MEMORY_SCOPE_AGENT);
        FENCE();
    };
    {
        const int tg = w & 3;
        char* xch = smem + XCH + tg * 3072;
        const int hoff = WS_H1 + (mt1 * 8 + 2 * tg) * (56 * 1024) + (nt1 >> 1) * 7168;
        if (__builtin_expect(aw, 1)) {
            const int toff = WS_LN2 + (mt1 * 8 + 2 * tg) * 16384;
            LAS char* wb0 = (LAS char*)smem + (4 * g + (m >> 2)) * 224 + (m & 3) * 8;
            LAS char* wb[4];
            int tq[4];
#pragma unroll
            for (int q = 0; q < 4; ++q) { const int qq = (q + rq) & 3; wb[q] = wb0 + qq * 28672; tq[q] = toff + qq * 4096; }
            u4 T[2][16], A[2][7];
            auto ldt = [&](int kk) {
#pragma unroll
                for (int tt = 0; tt < 2; ++tt) T[tt][kk] = bld4<SC1>(rw, l * 16 + (kk & 3) * 1024, tq[kk >> 2] + tt * 16384);
            };
#pragma unroll
            for (int i = 0; i < 7; ++i) A[0][i] = ldw1(wb[0], 0, i);
            if (l == 0) __hip_atomic_fetch_add(hw + 5, 1u, __ATOMIC_RELAXED, __HIP_MEMORY_SCOPE_WORKGROUP);
            ST(11);
#pragma unroll
            for (int kk = 0; kk < G1_DEPTH; ++kk) { ldt(kk); SCHED(); }
            f4 acc[2][7];
#pragma unroll
            for (int tt = 0; tt < 2; ++tt)
#pragma unroll
                for (int i = 0; i < 7; ++i) acc[tt][i] = Z4;
#pragma unroll
            for (int kk = 0; kk < 16; ++kk) {
                if ((kk & 1) == 0) {
                    if (__builtin_expect(any_sent(umax(umax(max4(T[0][kk]), max4(T[1][kk])), umax(max4(T[0][kk + 1]), max4(T[1][kk + 1])))), 0)) {
                        wait_tok(T[0][kk], T[1][kk], rw, l * 16, tq[kk >> 2] + (kk & 3) * 1024);
                        wait_tok(T[0][kk + 1], T[1][kk + 1], rw, l * 16, tq[kk >> 2] + ((kk + 1) & 3) * 1024);
                    }
                    if (kk == 0) ST(12);
                    if (kk == (BG6 / 7) * 4 - 2) lds_wait(hw + 5, 8u);
                }
                if (kk + G1_DEPTH < 16) ldt(kk + G1_DEPTH);
                if (kk < 15) {
#pragma unroll
                    for (int i = 0; i < 7; ++i) A[(kk + 1) & 1][i] = ldw1(wb[(kk + 1) >> 2], (kk + 1) & 3, i);
                }
#pragma unroll
                for (int i = 0; i < 7; ++i)
#pragma unroll
                    for (int tt = 0; tt < 2; ++tt) acc[tt][i] = mma_b(A[kk & 1][i], T[tt][kk], acc[tt][i]);
#pragma unroll
                for (int i = 0; i < 14; ++i) {
                    __builtin_amdgcn_sched_group_barrier(0x8, 1, 0);
                    if (i == 1 || i == 7) __builtin_amdgcn_sched_group_barrier(0x20, 1, 0);
                    __builtin_amdgcn_sched_group_barrier(0x100, 1, 0);
                    if ((kk & 1) && i >= 3) __builtin_amdgcn_sched_group_barrier(0x2, 1, 0);
                }
                if (kk & 1) {} else SCHED();
            }
#pragma unroll
            for (int q = 0; q < 3; ++q) {
                u2 p0 = pk4(acc[1][2 * q]), p1 = pk4(acc[1][2 * q + 1]);
                *(u4*)(xch + q * 1024 + l * 16) = (u4){p0[0], p0[1], p1[0], p1[1]};
            }
            lds_set(hw + tg, 1u);
            ST(13);
#pragma unroll
            for (int q = 0; q < 3; ++q) {
                u2 p0 = pk4(acc[0][2 * q]), p1 = pk4(acc[0][2 * q + 1]);
                bst4<SC1>(silu_pk4((u4){p0[0], p0[1], p1[0], p1[1]}), rw, l * 16, hoff + ((nt1 & 1) * 3 + q) * 1024);
            }
#pragma unroll
            for (int tt = 0; tt < 2; ++tt) { u2 p = pk4(acc[tt][6]); bst2<SC1>(silu_pk2(p[0], p[1]), rw, l * 8, hoff + tt * (56 * 1024) + 6144 + (nt1 & 1) * 512); }
        } else {
            bg(BG6, 28);
            WAITV(0);
            if (l == 0) __hip_atomic_fetch_add(hw + 5, 1u, __ATOMIC_RELAXED, __HIP_MEMORY_SCOPE_WORKGROUP);
            __builtin_amdgcn_s_sleep(8);
            lds_wait(hw + 5, 8u);
            w2_glds(pw, 0, lds0 + LDS_SCR + tg * 8192);
            w2_glds(pw, 1, lds0 + LDS_SCR + tg * 8192 + 4096);
#pragma unroll
            for (int r = 0; r < B_R; ++r) { WAITV(4); stage_out(r); }
            ST(12);
            lds_wait(hw + tg, 1u);
            ST(13);
#pragma unroll
            for (int q = 0; q < 3; ++q)
                bst4<SC1>(silu_pk4(*(const u4*)(xch + q * 1024 + l * 16)), rw, l * 16, hoff + 56 * 1024 + ((nt1 & 1) * 3 + q) * 1024);
        }
        ST(14);
        WAITV(0);
        publish();
        ST(21);
        if (__builtin_expect(aw, 1)) {
            lds_wait(hw + 4, 8u);
#pragma unroll
            for (int ks = 0; ks < 7; ++ks) w2_glds(pw, ks, lds0 + tg * 28672 + ks * 4096);
        } else {
            stage_out(B_R);
#pragma unroll
            for (int r = B_R + 1; r < 7; ++r) { if (r + 1 < 7) WAITV(4); else WAITV(0); stage_out(r); }
        }
    }
    {
        ST(15);
        const int rot = w & 3;
        if (aw) {
            flag_wait(fl + ((l & 4) ? 4096 + (grp * 8 + th * 4 + rot) * 32 : (grp * 8 + (pw ^ ((l & 2) * 2))) * 32) + (l & 1), tag);
            lds_set(hw + 8 + rot, tag);
        } else {
#pragma nounroll
            for (int sp = 0; sp < SPIN_MAX; ++sp) {
                if (__builtin_amdgcn_readfirstlane(__hip_atomic_load(hw + 8 + rot, __ATOMIC_RELAXED, __HIP_MEMORY_SCOPE_WORKGROUP)) == tag) break;
                __builtin_amdgcn_s_sleep(1);
            }
            FENCE();
        }
        ST(16);
        const int toff = WS_H1 + (grp * 8 + th * 4) * (56 * 1024) + pw * 7168;
        int to[4];
#pragma unroll
        for (int tt = 0; tt < 4; ++tt) to[tt] = toff + ((tt + rot) & 3) * (56 * 1024);
        u4 T[7][4];
        auto ldtok = [&](int ks) {
#pragma unroll
            for (int tt = 0; tt < 4; ++tt) {
                if (ks < 6) T[ks][tt] = bld4<SC1>(rw, l * 16, to[tt] + ko[ks] * 1024);
                else {
                    u2 a = bld2<SC1>(rw, l * 8 + 2048, to[tt] + 4096), c = bld2<SC1>(rw, l * 8 + 2560, to[tt] + 4096);
                    T[ks][tt] = (u4){a[0], a[1], c[0], c[1]};
                }
            }
            SCHED();
        };
#pragma unroll
        for (int ks = 0; ks < G2_DEPTH; ++ks) ldtok(ks);
        if (aw) {
#pragma unroll
            for (int ks = 0; ks < 7; ++ks) w2_read(W[ks], rot * 28672 + ks * 4096);
            asm volatile("s_waitcnt lgkmcnt(0)" ::: "memory");
        }
        if (l == 0) __hip_atomic_fetch_add(hw + 7, 1u, __ATOMIC_RELAXED, __HIP_MEMORY_SCOPE_WORKGROUP);
        const int rho = grp * 128 + th * 64 + rot * 16 + m;
        const int ooff = (256 * (rho & 7) + (rho >> 3)) * 1024 + fb * 128 + (w >> 2) * 64 + g * 16;
        const u4 hres = bld4<SC1>(ro, ooff, 0);
        SCHED();
        f4 acc[4][4];
#pragma unroll
        for (int tt = 0; tt < 4; ++tt)
#pragma unroll
            for (int t = 0; t < 4; ++t) acc[tt][t] = Z4;
#pragma unroll
        for (int ks = 0; ks < 7; ++ks) {
            if (ks + G2_DEPTH < 7) { PIN(T[ks][0]); ldtok(ks + G2_DEPTH); }
#pragma unroll
            for (int tt = 0; tt < 4; ++tt)
#pragma unroll
                for (int t = 0; t < 4; ++t) {
                    acc[tt][t] = mma_b(W[ks][t], T[ks][tt], acc[tt][t]);
                }
            SCHED();
        }
        ST(17);
        lds_wait(hw + 7, 8u);
        char* dp = smem + w * 16384 + l * 16;
#pragma unroll
        for (int tt = 1; tt < 4; ++tt)
#pragma unroll
            for (int t = 0; t < 4; ++t) *(f4*)(dp + (((tt + rot) & 3) * 4 + t) * 1024) = acc[tt][t];
        f4 s0, s1;
        if (aw) { *(f4*)(dp + (rot * 4 + 2) * 1024) = acc[0][2]; *(f4*)(dp + (rot * 4 + 3) * 1024) = acc[0][3]; s0 = acc[0][0]; s1 = acc[0][1]; }
        else { *(f4*)(dp + (rot * 4) * 1024) = acc[0][0]; *(f4*)(dp + (rot * 4 + 1) * 1024) = acc[0][1]; s0 = acc[0][2]; s1 = acc[0][3]; }
        BARRIER();
        f4 r0[7], r1[7];
#pragma unroll
        for (int j = 1; j < 8; ++j) {
            const char* rp = smem + (((w + j) & 7) * 16 + rot * 4 + (w >> 2) * 2) * 1024 + l * 16;
            r0[j - 1] = *(const f4*)rp; r1[j - 1] = *(const f4*)(rp + 1024);
        }
        SCHED();
        s0 = ((s0 + r0[0]) + (r0[1] + r0[2])) + ((r0[3] + r0[4]) + (r0[5] + r0[6]));
        s1 = ((s1 + r1[0]) + (r1[1] + r1[2])) + ((r1[3] + r1[4]) + (r1[5] + r1[6]));
        u2 p0 = pk4(s0), p1 = pk4(s1);
        bst4<NT>((u4){pk_b2(bf_2(hres[0]) + bf_2(p0[0])), pk_b2(bf_2(hres[1]) + bf_2(p0[1])),
                           pk_b2(bf_2(hres[2]) + bf_2(p1[0])), pk_b2(bf_2(hres[3]) + bf_2(p1[1]))}, ro, ooff, 0);
    }
    ST_END(ws, 0);
}

extern "C" void kernel_launch(void* const* d_in, const int* in_sizes, int n_in, void* d_out, int out_size,
                              void* d_ws, size_t ws_size, hipStream_t stream) {
    (void)hipFuncSetAttribute((const void*)k_fused, hipFuncAttributeMaxDynamicSharedMemorySize, LDS_B);
    k_fused<<<256, 512, LDS_B, stream>>>((const uint16_t*)d_in[0], (const uint16_t*)d_in[1], (const uint16_t*)d_in[2],
                                         (const uint16_t*)d_in[4], (const uint16_t*)d_in[5], (uint16_t*)d_out, (char*)d_ws);
}
```

```cpp
#include <hip/hip_runtime.h>
#include <hip/hip_bf16.h>
#include <stdint.h>

typedef _Float16 h8 __attribute__((ext_vector_type(8)));
typedef _Float16 h4 __attribute__((ext_vector_type(4)));
typedef _Float16 h2 __attribute__((ext_vector_type(2)));
typedef __bf16 b8 __attribute__((ext_vector_type(8)));
typedef __bf16 b2 __attribute__((ext_vector_type(2)));
typedef short s4 __attribute__((ext_vector_type(4)));
typedef float f4 __attribute__((ext_vector_type(4)));
typedef float f2 __attribute__((ext_vector_type(2)));
typedef unsigned u32;
typedef u32 u4 __attribute__((ext_vector_type(4)));
typedef u32 u2 __attribute__((ext_vector_type(2)));
#define LAS __attribute__((address_space(3)))
#define GAS __attribute__((address_space(1)))
#define DI __device__ __forceinline__
#define FENCE() asm volatile("" ::: "memory")
#define PIN(x) asm volatile("" : "+v"(x))
#define SCHED() __builtin_amdgcn_sched_barrier(0)
#define WAITV(n) asm volatile("s_waitcnt vmcnt(%0)" :: "n"(n) : "memory")
#define BARRIER() do { asm volatile("s_waitcnt lgkmcnt(0)" ::: "memory"); __builtin_amdgcn_s_barrier(); FENCE(); } while (0)
constexpr int SC1 = 16, NT = 2;
constexpr u32 SENT = 0xffffffffu;
constexpr int SPIN_MAX = 1 << 20;

DI u32 pk_h(float a, float b) { f2 v = {a, b}; h2 r = __builtin_convertvector(v, h2); return __builtin_bit_cast(u32, r); }
DI u32 pk_b(float a, float b) { f2 v = {a, b}; b2 r = __builtin_convertvector(v, b2); return __builtin_bit_cast(u32, r); }
DI float bf_lo(u32 v) { return __builtin_bit_cast(float, v << 16); }
DI float bf_hi(u32 v) { return __builtin_bit_cast(float, v & 0xffff0000u); }
DI f2 bf_2(u32 v) { return (f2){bf_lo(v), bf_hi(v)}; }
DI u32 pk_b2(f2 v) { return pk_b(v[0], v[1]); }
DI float rbf(float a) { return bf_lo(pk_b(a, a)); }
DI float cosr(float rev) { return __builtin_amdgcn_cosf(rev); }
DI float sinr(float rev) { return __builtin_amdgcn_sinf(rev); }
DI f2 lo2(f4 v) { return __builtin_shufflevector(v, v, 0, 1); }
DI f2 hi2(f4 v) { return __builtin_shufflevector(v, v, 2, 3); }
DI f2 fma2(f2 a, f2 b, f2 c) { return __builtin_elementwise_fma(a, b, c); }
DI u32 umin(u32 a, u32 b) { return a < b ? a : b; }
DI u32 umax(u32 a, u32 b) { return a > b ? a : b; }
DI u32 clean(u32 v) { return umin(v, SENT - 1); }
DI u32 max4(u4 v) { return umax(umax(umax(v[0], v[1]), v[2]), v[3]); }
#define Z4 ((f4){0, 0, 0, 0})
DI f4 mma_h(u4 a, u4 b, f4 c) { return __builtin_amdgcn_mfma_f32_16x16x32_f16(__builtin_bit_cast(h8, a), __builtin_bit_cast(h8, b), c, 0, 0, 0); }
DI f4 mma_b(u4 a, u4 b, f4 c) { return __builtin_amdgcn_mfma_f32_16x16x32_bf16(__builtin_bit_cast(b8, a), __builtin_bit_cast(b8, b), c, 0, 0, 0); }
DI f4 mma16_b(u2 a, u2 b, f4 c) { return __builtin_amdgcn_mfma_f32_16x16x16bf16_1k(__builtin_bit_cast(s4, a), __builtin_bit_cast(s4, b), c, 0, 0, 0); }
DI f4 mma16_h(u2 a, u2 b, f4 c) { return __builtin_amdgcn_mfma_f32_16x16x16f16(__builtin_bit_cast(h4, a), __builtin_bit_cast(h4, b), c, 0, 0, 0); }
typedef __amdgpu_buffer_rsrc_t rsrc_t;
DI rsrc_t mkrs(const void* p, u32 bytes) { return __builtin_amdgcn_make_buffer_rsrc((void*)p, 0, bytes, 0x00020000); }
template <int AUX> DI u4 bld4(rsrc_t r, int v, int s) { return __builtin_amdgcn_raw_buffer_load_b128(r, v, s, AUX); }
template <int AUX> DI u2 bld2(rsrc_t r, int v, int s) { return __builtin_amdgcn_raw_buffer_load_b64(r, v, s, AUX); }
template <int AUX> DI void bst4(u4 d, rsrc_t r, int v, int s) {
    __builtin_amdgcn_raw_buffer_store_b128(d, r, v, s, AUX);
    asm volatile("s_nop 1" :: "v"(d));
}
template <int AUX> DI void bst2(u2 d, rsrc_t r, int v, int s) { __builtin_amdgcn_raw_buffer_store_b64(d, r, v, s, AUX); }

template <int CTRL> DI float dpp(float v) {
    return __builtin_bit_cast(float, __builtin_amdgcn_update_dpp(0, __builtin_bit_cast(int, v), CTRL, 0xf, 0xf, true));
}
template <bool ALL> DI float red_lanes(float v) {
    v += dpp<0xB1>(v);
    if (ALL) v += dpp<0x4E>(v);
    v += dpp<0x124>(v); v += dpp<0x128>(v);
    u32 x = __builtin_bit_cast(u32, v), y = x;
    asm("" : "+v"(y));
    auto r = __builtin_amdgcn_permlane16_swap(x, y, false, false);
    v = __builtin_bit_cast(float, (u32)r[0]) + __builtin_bit_cast(float, (u32)r[1]);
    x = __builtin_bit_cast(u32, v); y = x;
    asm("" : "+v"(y));
    auto q = __builtin_amdgcn_permlane32_swap(x, y, false, false);
    return __builtin_bit_cast(float, (u32)q[0]) + __builtin_bit_cast(float, (u32)q[1]);
}
DI float ln_inv(float var) { return __builtin_amdgcn_rcpf(rbf(__builtin_amdgcn_sqrtf(rbf(rbf(var) + 1e-5f)))); }

#define ST_DECL
#define ST(i)
#define ST_END(ws, kid)

#define FS 1
constexpr u32 WS_U = 0;
constexpr u32 WS_V = WS_U + (2u << 20);
constexpr u32 WS_LN2 = WS_V + (2u << 20);
constexpr u32 WS_WA = WS_LN2 + (2u << 20);
constexpr u32 WS_WB = WS_WA + 8192u;
constexpr u32 WS_FA = WS_WB + 8192u;
constexpr u32 WS_FLAG = WS_FA + 16384u * FS;
constexpr u32 WS_EP = WS_FLAG + 32768u;
constexpr u32 WS_H1 = WS_WA + (512u << 10);
constexpr u32 WS_END = WS_H1 + 7340032u;

#define BGA 9
#define BGB 15
#define BGC 21
#define G_SLEEP 16
#define GA_SLEEP 4
#define GB_SLEEP 8
#define A_SLEEP 0
#define VEARLY 0
#define BG_DELAY 10
#define PRO_SLEEP 4
constexpr int BG5 = BGA, BG5b = BGB, BG6 = BGC;
constexpr int G1_DEPTH = 10, G2_DEPTH = 3;
constexpr int B_R = 5;
constexpr int CODE_LINES = 290, CODE_PF = 10;
constexpr int LDS_SCR = 114688;
constexpr int SCR_X = 8448;
constexpr int SCR_S = SCR_X + 20480;
constexpr int SCR_F = 49024;
constexpr int XCH = 147456;
constexpr int LDS_B = 163840;

DI void glds16(const void* sbase, u32 voff, u32 lds_dst) {
    asm volatile("s_mov_b32 m0, %2\n\ts_nop 0\n\tglobal_load_lds_dwordx4 %0, %1" :: "v"(voff), "s"(sbase), "s"(lds_dst) : "memory");
}
DI bool any_sent(u32 v) { return __builtin_amdgcn_ballot_w64(v == SENT) != 0; }
DI void poll2(u4& a, u4& c, rsrc_t rw, int vo, int sa, int sc) {
#pragma nounroll
    for (int sp = 0; sp < SPIN_MAX; ++sp) {
        const bool ba = max4(a) == SENT, bc = max4(c) == SENT;
        if (__builtin_amdgcn_ballot_w64(ba || bc) == 0) break;
        __builtin_amdgcn_s_sleep(1);
        FENCE();
        if (ba) a = bld4<SC1>(rw, vo, sa);
        if (bc) c = bld4<SC1>(rw, vo, sc);
    }
}
DI void wait_tok(u4& t0, u4& t1, rsrc_t rw, int vo, int so) {
    if (any_sent(umax(max4(t0), max4(t1)))) {
#pragma nounroll
        for (int sp = 0; sp < SPIN_MAX; ++sp) {
            FENCE();
            t0 = bld4<SC1>(rw, vo, so); t1 = bld4<SC1>(rw, vo, so + 16384);
            if (!any_sent(umax(max4(t0), max4(t1)))) break;
        }
    }
}
DI u4 ldw1(LAS char* wb, int kk, int ft) {
    s4 a0 = __builtin_amdgcn_ds_read_tr16_b64_v4i16((LAS s4*)(wb + kk * 32 * 224 + ft * 32));
    s4 a1 = __builtin_amdgcn_ds_read_tr16_b64_v4i16((LAS s4*)(wb + (kk * 32 + 16) * 224 + ft * 32));
    u2 x0 = __builtin_bit_cast(u2, a0), x1 = __builtin_bit_cast(u2, a1);
    return (u4){x0[0], x0[1], x1[0], x1[1]};
}
DI u2 pk4(f4 a) { return (u2){pk_b(a[0], a[1]), pk_b(a[2], a[3])}; }
DI u2 silu_pk2(u32 p0, u32 p1) {
    u32 o0, o1; float a0, a1, a2, a3, t0, t1, t2, t3;
    asm("v_lshlrev_b32 %2, 16, %10\n\tv_and_b32 %3, 0xffff0000, %10\n\tv_lshlrev_b32 %4, 16, %11\n\tv_and_b32 %5, 0xffff0000, %11\n\t"
        "v_mul_f32 %6, %12, %2\n\tv_mul_f32 %7, %12, %3\n\tv_mul_f32 %8, %12, %4\n\tv_mul_f32 %9, %12, %5\n\t"
        "v_exp_f32 %6, %6\n\tv_exp_f32 %7, %7\n\tv_exp_f32 %8, %8\n\tv_exp_f32 %9, %9\n\t"
        "v_add_f32 %6, 1.0, %6\n\tv_add_f32 %7, 1.0, %7\n\tv_add_f32 %8, 1.0, %8\n\tv_add_f32 %9, 1.0, %9\n\t"
        "v_rcp_f32 %6, %6\n\tv_rcp_f32 %7, %7\n\tv_rcp_f32 %8, %8\n\tv_rcp_f32 %9, %9\n\t"
        "v_mul_f32 %2, %2, %6\n\tv_mul_f32 %3, %3, %7\n\tv_mul_f32 %4, %4, %8\n\tv_mul_f32 %5, %5, %9\n\t"
        "v_cvt_pk_bf16_f32 %0, %2, %3\n\tv_cvt_pk_bf16_f32 %1, %4, %5"
        : "=&v"(o0), "=&v"(o1), "=&v"(a0), "=&v"(a1), "=&v"(a2), "=&v"(a3), "=&v"(t0), "=&v"(t1), "=&v"(t2), "=&v"(t3)
        : "v"(p0), "v"(p1), "s"(-1.4426950408889634f));
    return (u2){o0, o1};
}
DI u4 silu_pk4(u4 p) { u2 a = silu_pk2(p[0], p[1]), b = silu_pk2(p[2], p[3]); return (u4){a[0], a[1], b[0], b[1]}; }
DI void lds_set(LAS u32* p, u32 v) {
    asm volatile("s_waitcnt lgkmcnt(0)" ::: "memory");
    __hip_atomic_store(p, v, __ATOMIC_RELAXED, __HIP_MEMORY_SCOPE_WORKGROUP);
}
DI void lds_wait(LAS u32* p, u32 v) {
#pragma nounroll
    for (int sp = 0; sp < SPIN_MAX; ++sp) {
        if (__builtin_amdgcn_readfirstlane(__hip_atomic_load(p, __ATOMIC_RELAXED, __HIP_MEMORY_SCOPE_WORKGROUP)) >= v) break;
        __builtin_amdgcn_s_sleep(1);
    }
    FENCE();
}
DI void flag_wait(GAS u32* p, u32 tag) {
#pragma nounroll
    for (int sp = 0; sp < SPIN_MAX; ++sp) {
        if (__builtin_amdgcn_ballot_w64(__hip_atomic_load(p, __ATOMIC_RELAXED, __HIP_MEMORY_SCOPE_AGENT) != tag) == 0) break;
        __builtin_amdgcn_s_sleep(2);
    }
    FENCE();
}
#define SENT4 ((u4){SENT, SENT, SENT, SENT})

__global__ __launch_bounds__(512, 2) void k_fused(const uint16_t* x, const uint16_t* wr, const uint16_t* wi, const uint16_t* w1,
                                                  const uint16_t* w2, uint16_t* out, char* ws) {
    extern __shared__ __attribute__((aligned(16))) char smem[];
    const int tid = threadIdx.x, l = tid & 63, g = l >> 4, m = l & 15;
    const int w = __builtin_amdgcn_readfirstlane(tid >> 6);
    const int b = (blockIdx.x & 7) * 32 + (blockIdx.x >> 3);
    const int mt1 = (b >> 6) * 4 + ((b >> 3) & 3), nt1 = ((b >> 5) & 1) * 8 + (b & 7);
    const int grp = b >> 4, th = (b >> 3) & 1, fb = b & 7;
    const rsrc_t rw = mkrs(ws, WS_END), ro = mkrs(out, 2097152);
    char* scr = smem + LDS_SCR;
    LAS u32* hw = (LAS u32*)((LAS char*)smem + LDS_SCR + SCR_F);
    const u32 lds0 = (u32)(uintptr_t)(LAS char*)smem;
    const float mf = (float)m, gf = (float)g, sg = (m & 1) ? -1.0f : 1.0f;
    const bool aw = w < 4;
    ST_DECL;
    unsigned long long pc0;
    asm volatile("s_getpc_b64 %0" : "=s"(pc0));
    if (tid >= 448 && l < CODE_PF)
        asm volatile("s_mov_b32 m0, %2\n\ts_nop 0\n\tglobal_load_lds_dword %0, %1"
                     :: "v"(umin((blockIdx.x >> 3) * CODE_PF + l, CODE_LINES - 1) * 128), "s"(pc0), "s"(lds0 + LDS_SCR + SCR_F + 64) : "memory");
    const u32 epoch = __hip_atomic_load((GAS u32*)(ws + WS_EP), __ATOMIC_RELAXED, __HIP_MEMORY_SCOPE_AGENT);
    FENCE();
    u32 xd[8];
    const int f1 = b >> 5, cg = b & 31;
    const bool mir = cg >= 16;
    const rsrc_t rr = mkrs(wr, 1052672), ri = mkrs(wi, 1052672);
    u4 fr, fi; u32 sr_, si_;
    if (aw) {
        __builtin_amdgcn_s_sleep(A_SLEEP);
        const u32* xr = (const u32*)(x + (256 * (2 * w + ((m >> 1) & 1)) + b) * 512) + 8 * g + 2 * (m >> 2) + (m & 1);
#pragma unroll
        for (int j = 0; j < 8; ++j) xd[j] = xr[32 * j];
    } else {
        bst4<SC1>(SENT4, rw, l * 16, WS_U + b * 8192 + (w - 4) * 2048); bst4<SC1>(SENT4, rw, l * 16 + 1024, WS_U + b * 8192 + (w - 4) * 2048);
        __builtin_amdgcn_raw_buffer_store_b32(SENT, rw, l < 4 ? (l >> 1) * 8192 + (l & 1) * 1024 : 0x7ffffff0,
                                              WS_WA + (w - 4) * 2048 + ((b >> 6) * 16 + (b & 15)) * 16 + ((b >> 4) & 3) * 4, SC1);
        FENCE();
        if (w == 6) {
            const int c = (l >> 3) & 3, f_ = 8 * b + (l & 7);
            const int e1 = ((c & 1) ? (2048 - f_) & 2047 : f_) * 514 + (c >> 1) * 512;
            sr_ = __builtin_amdgcn_raw_buffer_load_b16(rr, e1, 0, 0); si_ = __builtin_amdgcn_raw_buffer_load_b16(ri, e1, 0, 0);
        }
    }
    FENCE();
    const int zo = (64 * g + (g < 2 ? m : (m + 15) & 15)) * 4;
    const int rq = ((b >> 3) + b) & 3;
    const int row0 = (tid & 255) / 14, cch0 = (tid & 255) - row0 * 14;
    const char* wsrc = (const char*)w1 + nt1 * 224;
    u32 w1o[7];
    auto w1_burst = [&](int i0, int n) {
#pragma unroll
        for (int i = i0; i < i0 + n; ++i) {
            const int q = (i / 7 + rq) & 3, j = i % 7;
            glds16(wsrc + q * (128 * 3584), w1o[j], lds0 + (w - 4) * 1024 + q * 28672 + j * 4096);
        }
    };
    const int xoff = (256 * w + b) * 1024 + m * 32 + g * 8;
    u2 xl[2];
    float c16[4], s16[4];
    f2 twc[2], tws[2];
    u4 Gre3, Gim3, SGre, SGim, GA[2];
    auto consts0 = [&]() {
#pragma unroll
        for (int u = 0; u < 4; ++u) {
            float a = (4.0f * gf + u) * (mf * (1.0f / 16));
            c16[u] = cosr(a); s16[u] = sinr(a);
            twc[u >> 1][u & 1] = cosr(a * (1.0f / 32)); tws[u >> 1][u & 1] = sinr(a * (1.0f / 32));
        }
    };
    auto consts3 = [&]() {
#pragma unroll
        for (int u = 0; u < 4; ++u) {
            const float k = 4.0f * gf + u;
            const bool k0 = u == 0 && g == 0;
            float cc = c16[u], ss = s16[u];
            Gre3[u] = pk_b(cc, -ss); Gim3[u] = pk_b(ss, cc);
            float a2 = (2.0f * k + 1.0f) * (mf * (1.0f / 32));
            u32 sp = pk_b(g < 2 ? 2.f * cc : 2.f * cosr(a2), g < 2 ? -2.f * ss : -2.f * sinr(a2));
            SGre[u] = k0 ? pk_b(1.0f, sg) : (g < 2 ? sp : 0u);
            SGim[u] = g < 2 ? 0u : sp;
            float a4 = k * (mf * (1.0f / 32)), c4 = 2.f * cosr(a4), s4_ = -2.f * sinr(a4);
            GA[0][u] = k0 ? pk_b(1.0f, sg) : pk_b(c4, s4_);
            GA[1][u] = k0 ? pk_b(1.0f, sg) : ((u & 1) ? pk_b(-c4, -s4_) : pk_b(c4, s4_));
        }
        PIN(Gre3); PIN(Gim3); PIN(SGre); PIN(SGim); PIN(GA[0]); PIN(GA[1]); PIN(twc[0]); PIN(twc[1]); PIN(tws[0]); PIN(tws[1]);
    };
    const int pw = (w + 4 * th) & 7;
    GAS u32* fl = (GAS u32*)(ws + WS_FLAG);
    int ko[7];
    {
        const int r6 = (fb < 6 ? fb : fb - 5) + 3 * (grp & 1);
#pragma unroll
        for (int ks = 0; ks < 6; ++ks) { int k = ks + r6; k = k >= 6 ? k - 6 : k; ko[ks] = k >= 6 ? k - 6 : k; }
        ko[6] = 6;
    }
    u4 W[7][4];
    const u32 w2vo = (l >> 3) * 1024 + (((l & 7) ^ (((l >> 4) & 1) << 2)) * 16);
    auto w2_glds = [&](int p_, int ks, u32 dst) {
        const int q = ko[ks];
        const char* src = (const char*)w2 + (224 * p_ + (ks == 6 ? 96 : (q < 3 ? 32 * q : 16 + 32 * q))) * 1024 + fb * 128;
        glds16(src, w2vo, dst); glds16(src + 8192, w2vo, dst + 1024);
        glds16(src + (ks == 6 ? 112 : 16) * 1024, w2vo, dst + 2048); glds16(src + (ks == 6 ? 120 : 24) * 1024, w2vo, dst + 3072);
    };
    LAS char* w2rd = (LAS char*)smem + (4 * g + (m >> 2)) * 128 + (((m & 3) ^ (((m >> 3) & 1) << 2)) * 16);
    auto w2_read = [&](u4 (&f)[4], int off) {
#pragma unroll
        for (int t = 0; t < 4; ++t) {
            LAS char* p = (LAS char*)((u32)(uintptr_t)w2rd ^ ((t >> 1) * 64)) + off + (t & 1) * 8;
            u2 x0 = __builtin_bit_cast(u2, __builtin_amdgcn_ds_read_tr16_b64_v4i16((LAS s4*)p));
            u2 x1 = __builtin_bit_cast(u2, __builtin_amdgcn_ds_read_tr16_b64_v4i16((LAS s4*)(p + 2048)));
            f[t] = (u4){x0[0], x0[1], x1[0], x1[1]};
        }
    };
    auto bg = [&](int i0, int i1) { w1_burst(i0, i1 - i0); };
    u32 tag;
    if (__builtin_expect(aw, 1)) {
        if (tid < 8) hw[tid] = 0;
        FENCE();
        ST(2);
        {
            u4 FAre, FAim, Fre, Fim, Sre, Sim; u2 Mb; f2 pc[2], ps[2];
            consts0();
            float c0[8], s0[8];
#pragma unroll
            for (int j = 0; j < 8; ++j) {
                float a = (gf + 4.0f * j) * (mf * (1.0f / 32));
                c0[j] = cosr(a); s0[j] = m ? -sinr(a) : ((g & 1) ? -1.0f : 1.0f);
            }
            const float mo = (2.0f * mf + 1.0f) * (1.0f / 32);
#pragma unroll
            for (int u = 0; u < 4; ++u) {
                FAre[u] = pk_h(c0[2 * u], c0[2 * u + 1]); FAim[u] = pk_h(s0[2 * u], s0[2 * u + 1]);
                float cc = c16[u], ss = s16[u];
                Fre[u] = pk_h(cc, ss); Fim[u] = pk_h(-ss, cc);
                float a2 = (4.0f * gf + u) * mo;
                u32 re = pk_h(m < 8 ? cc : cosr(a2), 0.f), im = pk_h(m < 8 ? (m ? -ss : ((u & 1) ? -1.0f : 1.0f)) : -sinr(a2), 0.f);
                Sre[u] = m < 8 ? re : re << 16; Sim[u] = m < 8 ? im : im << 16;
                pc[u & 1][u >> 1] = twc[u >> 1][u & 1]; ps[u & 1][u >> 1] = tws[u >> 1][u & 1];
            }
#pragma unroll
            for (int e = 0; e < 2; ++e) {
                float a = (float)(((m >> 1) * (256 * (2 * g + e) + b)) & 2047) * (1.0f / 2048);
                float cc = cosr(a), ss = sinr(a);
                Mb[e] = (m & 1) ? pk_h(-ss, cc) : pk_h(cc, ss);
            }
            PIN(FAre); PIN(FAim); PIN(Fre); PIN(Fim); PIN(Sre); PIN(Sim); PIN(Mb); PIN(pc[0]); PIN(pc[1]); PIN(ps[0]); PIN(ps[1]);
            ST(3);
            f2 v[8];
#pragma unroll
            for (int j = 0; j < 8; ++j) v[j] = bf_2(xd[j]);
            f2 s2 = ((v[0] + v[1]) + (v[2] + v[3])) + ((v[4] + v[5]) + (v[6] + v[7]));
            const float mean = red_lanes<false>(s2[0] + s2[1]) * (1.0f / 512);
            f2 q0 = {0.f, 0.f}, q1 = {0.f, 0.f};
#pragma unroll
            for (int j = 0; j < 8; ++j) { v[j] -= mean; if (j & 1) q1 = fma2(v[j], v[j], q1); else q0 = fma2(v[j], v[j], q0); }
            q0 += q1;
            const float inv = ln_inv(red_lanes<false>(q0[0] + q0[1]) * (1.0f / 512));
            const float off = (mean - rbf(mean)) * inv;
#pragma unroll
            for (int j = 0; j < 8; ++j) v[j] = fma2(v[j], (f2){inv, inv}, (f2){off, off});
            u4 B[2];
#pragma unroll
            for (int p = 0; p < 2; ++p) {
                u4 A = {pk_h(v[0][p], v[1][p]), pk_h(v[2][p], v[3][p]), pk_h(v[4][p], v[5][p]), pk_h(v[6][p], v[7][p])};
                f4 Dre = mma_h(A, FAre, Z4), Dim = mma_h(A, FAim, Z4);
#pragma unroll
                for (int r = 0; r < 2; ++r) {
                    f2 re = r ? hi2(Dre) : lo2(Dre), im = r ? hi2(Dim) : lo2(Dim);
                    f2 tr = fma2(im, ps[p], re * pc[p]), ti = fma2(-re, ps[p], im * pc[p]);
                    B[r][p] = pk_h(tr[0], ti[0]); B[r][2 + p] = pk_h(tr[1], ti[1]);
                }
            }
#pragma unroll
            for (int r = 0; r < 2; ++r) {
                f4 Gre = mma_h(Fre, B[r], Z4), Gim = mma_h(Fim, B[r], Z4), Hre = mma_h(Sre, B[r], Z4), Him = mma_h(Sim, B[r], Z4);
#pragma unroll
                for (int i = 0; i < 4; ++i) *(u32*)(scr + (2 * w + r) * 1024 + zo + i * 64) = pk_h(m ? Gre[i] : Hre[i], m ? Gim[i] : Him[i]);
            }
            BARRIER();
            ST(18);
            u4 r0 = *(const u4*)(scr + (2 * g) * 1024 + w * 256 + m * 16);
            u4 r1 = *(const u4*)(scr + (2 * g + 1) * 1024 + w * 256 + m * 16);
            u4 o0, o1;
#pragma unroll
            for (int t = 0; t < 4; ++t) {
                f4 D = mma16_h(Mb, (u2){r0[t], r1[t]}, Z4);
                o0[t] = clean(pk_h(D[0], D[1])); o1[t] = clean(pk_h(D[2], D[3]));
            }
            bst4<SC1>(o0, rw, g * 2048 + m * 16, WS_U + b * 8192 + w * 256);
            bst4<SC1>(o1, rw, g * 2048 + m * 16 + 1024, WS_U + b * 8192 + w * 256);
        }
        ST(4);
        u4 Fre, Fim, Gre, Gim; u2 Nb; f2 tc[2], ts[2];
#pragma unroll
        for (int u = 0; u < 4; ++u) {
            float cc = c16[u], ss = s16[u];
            Fre[u] = pk_h(0.25f * cc, 0.25f * ss); Fim[u] = pk_h(-0.25f * ss, 0.25f * cc);
            Gre[u] = pk_b(cc * (1.0f / 256), -ss * (1.0f / 256)); Gim[u] = pk_b(ss * (1.0f / 256), cc * (1.0f / 256));
            float a3 = (4.0f * gf + u) * (mf * (1.0f / 256));
            tc[u >> 1][u & 1] = cosr(a3); ts[u >> 1][u & 1] = sinr(a3);
        }
        PIN(Fre); PIN(Fim); PIN(Gre); PIN(Gim); PIN(tc[0]); PIN(tc[1]); PIN(ts[0]); PIN(ts[1]);
        xl[0] = *(const u2*)((const char*)x + xoff); xl[1] = *(const u2*)((const char*)x + xoff + 512);
        FENCE();
        tag = __builtin_amdgcn_readfirstlane(epoch) + 1u;
        consts3();
#pragma unroll
        for (int e = 0; e < 2; ++e) {
            float a1 = (float)(((2 * g + e) * (256 * (m >> 1) + b)) & 2047) * (1.0f / 2048);
            float c1 = cosr(a1), s1 = sinr(a1);
            Nb[e] = (m & 1) ? pk_b(s1, c1) : pk_b(c1, -s1);
        }
        PIN(Nb);
        WAITV(0);
        ST(19);
        BARRIER();
        ST(5);
        u4 d[2], wa, wb;
        const int uo = (tid >> 1) * 8192 + (tid & 1) * 16, us = WS_U + f1 * 1024 + cg * 32;
        d[0] = bld4<SC1>(rw, uo, us); d[1] = bld4<SC1>(rw, uo, us + 128 * 8192);
        if (cg == 0) {
#pragma nounroll
            for (int sp = 0; sp < SPIN_MAX; ++sp) {
                FENCE();
                const u32 ux = __builtin_amdgcn_raw_buffer_load_b32(rw, tid * 8192, WS_U + ((8 - f1) & 7) * 1024, SC1);
                wa = bld4<SC1>(rw, l * 16, WS_WA + f1 * 1024); wb = bld4<SC1>(rw, l * 16, WS_WB + f1 * 1024);
                *(u32*)(scr + SCR_X + tid * 4) = ux;
                if (!any_sent(umax(ux, umax(max4(wa), max4(wb))))) break;
            }
        }
        poll2(d[0], d[1], rw, uo, us, us + 128 * 8192);
        char* sp = scr + SCR_S + (tid ^ ((tid >> 4) & 1)) * 16;
        *(u4*)sp = d[0]; *(u4*)(sp + 4096) = d[1];
        BARRIER();
        if (b == 0 && tid == 0) __hip_atomic_store((GAS u32*)(ws + WS_EP), tag, __ATOMIC_RELAXED, __HIP_MEMORY_SCOPE_AGENT);
        ST(6);
        u2 ld[4], wl[4];
        const int so = (64 * g + m) * 32 + (((w >> 1) ^ (m >> 3)) * 16) + (w & 1) * 8;
#pragma unroll
        for (int q = 0; q < 4; ++q) ld[q] = *(const u2*)(scr + SCR_S + so + 512 * q);
#pragma unroll
        for (int q = 0; q < 4; ++q) wl[q] = *(const u2*)(scr + SCR_S + so + 8192 + 512 * q);
        FENCE();
        auto fwd = [&](u4 A, f4& Xr, f4& Xi) {
            f4 Dre = mma_h(A, Fre, Z4), Dim = mma_h(A, Fim, Z4);
            f2 r0 = fma2(lo2(Dim), ts[0], lo2(Dre) * tc[0]), i0 = fma2(-lo2(Dre), ts[0], lo2(Dim) * tc[0]);
            f2 r1 = fma2(hi2(Dim), ts[1], hi2(Dre) * tc[1]), i1 = fma2(-hi2(Dre), ts[1], hi2(Dim) * tc[1]);
            u4 B = {pk_h(r0[0], i0[0]), pk_h(r0[1], i0[1]), pk_h(r1[0], i1[0]), pk_h(r1[1], i1[1])};
            Xr = mma_h(Fre, B, Z4); Xi = mma_h(Fim, B, Z4);
        };
        f4 Xre[2], Xim[2];
#pragma unroll
        for (int e = 0; e < 2; ++e) fwd((u4){ld[0][e], ld[1][e], ld[2][e], ld[3][e]}, Xre[e], Xim[e]);
        f2 yr[2][2], yi[2][2];
#pragma unroll
        for (int e = 0; e < 2; ++e)
#pragma unroll
            for (int hh = 0; hh < 2; ++hh) {
                f2 wre = {bf_lo(wl[2 * hh][e]), bf_lo(wl[2 * hh + 1][e])}, wim = {bf_hi(wl[2 * hh][e]), bf_hi(wl[2 * hh + 1][e])};
                f2 xr = hh ? hi2(Xre[e]) : lo2(Xre[e]), xi = hh ? hi2(Xim[e]) : lo2(Xim[e]);
                yr[e][hh] = fma2(-xi, wim, xr * wre); yi[e][hh] = fma2(xr, wim, xi * wre);
            }
        if (cg == 0 && w == 0) {
            const u32* up = (const u32*)(scr + SCR_X) + 64 * g + m;
            f4 Mr, Mi;
            fwd((u4){up[0], up[16], up[32], up[48]}, Mr, Mi);
            f2* sx = (f2*)(scr + SCR_X + 1024);
#pragma unroll
            for (int i = 0; i < 4; ++i) sx[m + 16 * (4 * g + i)] = (f2){Mr[i], Mi[i]};
#pragma unroll
            for (int i = 0; i < 4; ++i) {
                f2 xm = sx[((f1 ? 255 : 256) - (m + 16 * (4 * g + i))) & 255];
                float ar = bf_lo(wa[i]), ai = bf_hi(wa[i]), br = bf_lo(wb[i]), bi = bf_hi(wb[i]);
                yr[0][i >> 1][i & 1] = Xre[0][i] * ar - Xim[0][i] * ai + xm[0] * br + xm[1] * bi;
                yi[0][i >> 1][i & 1] = Xre[0][i] * ai + Xim[0][i] * ar + xm[0] * bi - xm[1] * br;
            }
        }
        char* pv = scr;
#pragma unroll
        for (int e = 0; e < 2; ++e) {
            u4 Y = {pk_b(yr[e][0][0], yi[e][0][0]), pk_b(yr[e][0][1], yi[e][0][1]), pk_b(yr[e][1][0], yi[e][1][0]), pk_b(yr[e][1][1], yi[e][1][1])};
            f4 Dre = mma_b(Y, Gre, Z4), Dim = mma_b(Y, Gim, Z4);
            f2 r0 = fma2(-lo2(Dim), ts[0], lo2(Dre) * tc[0]), i0 = fma2(lo2(Dre), ts[0], lo2(Dim) * tc[0]);
            f2 r1 = fma2(-hi2(Dim), ts[1], hi2(Dre) * tc[1]), i1 = fma2(hi2(Dre), ts[1], hi2(Dim) * tc[1]);
            u4 A = {pk_b(r0[0], i0[0]), pk_b(r0[1], i0[1]), pk_b(r1[0], i1[0]), pk_b(r1[1], i1[1])};
            f4 Ere = mma_b(A, Gre, Z4), Eim = mma_b(A, Gim, Z4);
            u4 o;
#pragma unroll
            for (int i = 0; i < 4; ++i) o[i] = clean(pk_b(Ere[i], Eim[i]));
            *(u4*)(pv + (2 * w + e) * 1024 + ((m * 4 + g) ^ ((w >> 1) * 4)) * 16) = o;
        }
        BARRIER();
        ST(7);
#pragma unroll
        for (int it = 0; it < 2; ++it) {
            const int ch = tid + 256 * it;
            const u32* q = (const u32*)(pv + (ch & 1) * 4096 + (((ch >> 1) * 4) ^ ((ch & 1) * 64)));
            bst4<SC1>((u4){q[0], q[256], q[512], q[768]}, rw, ch * 16, WS_V + b * 8192);
        }
        WAITV(0);
        BARRIER();
        ST(8);
        const int vo = (g * 64 + (m >> 1)) * 8192 + (m & 1) * 16, vs = WS_V + w * 8 * 8192 + b * 32;
        u4 v0 = bld4<SC1>(rw, vo, vs), v1 = bld4<SC1>(rw, vo, vs + 32 * 8192);
        poll2(v0, v1, rw, vo, vs, vs + 32 * 8192);
        ST(9);
        u4 z0, z1;
#pragma unroll
        for (int t = 0; t < 4; ++t) {
            f4 D = mma16_b(Nb, (u2){v0[t], v1[t]}, Z4);
            z0[t] = pk_b(D[0], D[1]); z1[t] = pk_b(D[2], D[3]);
        }
        *(u4*)(scr + SCR_X + g * 2048 + w * 256 + m * 16) = z0;
        *(u4*)(scr + SCR_X + g * 2048 + 1024 + w * 256 + m * 16) = z1;
    } else {
        auto announce = [&](int which) {
            if (l < 8) __hip_atomic_store((GAS u32*)(ws + WS_FA) + (which * 2048 + l * 256 + b) * FS, tag, __ATOMIC_RELAXED, __HIP_MEMORY_SCOPE_AGENT);
            FENCE();
        };
        auto all_announced = [&](int which) {
#pragma nounroll
            for (int sp = 0; sp < SPIN_MAX; ++sp) {
                FENCE();
                u4 f;
#pragma unroll
                for (int i = 0; i < 4; ++i) f[i] = __builtin_amdgcn_raw_buffer_load_b32(rw, (l + 64 * i) * 4 * FS, WS_FA + (which * 8192 + (b >> 5) * 1024) * FS, SC1);
                if (__builtin_amdgcn_ballot_w64(f[0] != tag || f[1] != tag || f[2] != tag || f[3] != tag) == 0) break;
                __builtin_amdgcn_s_sleep(4);
            }
        };
        FENCE();
        ST(2);
        tag = __builtin_amdgcn_readfirstlane(epoch) + 1u;
        if (w == 6) WAITV(2); else WAITV(0);
        if (l < 2) __hip_atomic_store((GAS u32*)(ws + WS_FA) + ((2 * (w - 4) + l) * 256 + b) * FS, tag, __ATOMIC_RELAXED, __HIP_MEMORY_SCOPE_AGENT);
        FENCE();
        ST(18);
#pragma unroll
        for (int jj = 0; jj < 7; ++jj) {
            const int cj_ = (4 * jj) % 14, rj = 18 * jj + (4 * jj) / 14;
            w1o[jj] = (row0 + rj) * 3584 + (cch0 + cj_) * 16 + (cch0 + cj_ >= 14 ? 3584 - 224 : 0);
            PIN(w1o[jj]);
        }
        BARRIER();
        if (w == 6) {
            float re = bf_lo(sr_), im = bf_lo(si_);
            re += dpp<0x128>(re); im -= dpp<0x128>(im);
            u32 a = __builtin_bit_cast(u32, re), a2 = a, c = __builtin_bit_cast(u32, im), c2 = c;
            asm("" : "+v"(a2), "+v"(c2));
            auto pr = __builtin_amdgcn_permlane16_swap(a, a2, false, false);
            auto pi = __builtin_amdgcn_permlane16_swap(c, c2, false, false);
            const float ar = __builtin_bit_cast(float, (u32)pr[0]), br = __builtin_bit_cast(float, (u32)pr[1]);
            const float ai = __builtin_bit_cast(float, (u32)pi[0]), bi = __builtin_bit_cast(float, (u32)pi[1]);
            const int wo = WS_WA + ((b >> 6) * 16 + (b & 15)) * 16 + ((b >> 4) & 3) * 4;
            if (l < 8) {
                __builtin_amdgcn_raw_buffer_store_b32(clean(pk_b(0.25f * (ar + br), 0.25f * (ai + bi))), rw, l * 1024, wo, SC1);
                __builtin_amdgcn_raw_buffer_store_b32(clean(pk_b(0.25f * (ar - br), 0.25f * (ai - bi))), rw, l * 1024, wo + 8192, SC1);
            }
        }
        const int f = f1 + 8 * (tid & 255), k0 = 16 * cg - 8 * (cg & 1);
        const int e = mir ? ((2048 - f) & 2047) * 514 + (504 - k0) * 2 : f * 514 + k0 * 2;
        consts0();
        __builtin_amdgcn_s_sleep(GA_SLEEP);
        fr = bld4<0>(rr, e, 0);
        FENCE();
        if (VEARLY) {
#pragma unroll
        for (int i = 0; i < 2; ++i) bst4<SC1>(SENT4, rw, l * 16 + i * 1024, WS_V + b * 8192 + (w - 4) * 2048);
        }
        __builtin_amdgcn_s_sleep(GB_SLEEP);
        if (w == 7) all_announced(0);
        fi = bld4<0>(ri, e, 0);
        xl[0] = *(const u2*)((const char*)x + xoff); xl[1] = *(const u2*)((const char*)x + xoff + 512);
        FENCE();
        ST(4);
        BARRIER();
        if (VEARLY) {
            WAITV(3);
            u32 prev = 0;
            if (l == 0) prev = __hip_atomic_fetch_add(hw + 6, 1u, __ATOMIC_RELAXED, __HIP_MEMORY_SCOPE_WORKGROUP);
            if (__builtin_amdgcn_readfirstlane(prev) == 3u) announce(1);
        }
        {
            u32 o[8];
#pragma unroll
            for (int c = 0; c < 8; ++c)
                o[c] = mir ? __builtin_amdgcn_perm(fi[3 - (c >> 1)], fr[3 - (c >> 1)], (c & 1) ? 0x05040100u : 0x07060302u) ^ 0x80000000u
                           : __builtin_amdgcn_perm(fi[c >> 1], fr[c >> 1], (c & 1) ? 0x07060302u : 0x05040100u);
            char* wp = scr + SCR_S + 8192 + (tid & 255) * 32;
            *(u4*)(wp + (m & 8) * 2) = (u4){o[0], o[1], o[2], o[3]};
            *(u4*)(wp + ((m & 8) * 2 ^ 16)) = (u4){o[4], o[5], o[6], o[7]};
        }
        if (!VEARLY) {
#pragma unroll
        for (int i = 0; i < 2; ++i) bst4<SC1>(SENT4, rw, l * 16 + i * 1024, WS_V + b * 8192 + (w - 4) * 2048);
        }
        FENCE();
        consts3();
        WAITV(0);
        ST(5);
        BARRIER();
        if (!VEARLY && w == 7) announce(1);
#pragma unroll
        for (int i = 0; i < 2; ++i) bst4<SC1>(SENT4, rw, (l >> 3) * 256 + (l & 7) * 16 + i * 2048, WS_LN2 + (b >> 1) * 16384 + (b & 1) * 128 + (w - 4) * 4096);
        bg(0, BG5);
        WAITV(BG5);
        ST(6);
        BARRIER();
        if (w == 7) all_announced(1);
        else __builtin_amdgcn_s_sleep(3);
        bg(BG5, BG5b);
        ST(7);
        BARRIER();
        __builtin_amdgcn_s_sleep(BG_DELAY);
        bg(BG5b, BG6);
    }
    BARRIER();
    {
        const u32* zp = (const u32*)(scr + SCR_X + w * 1024 + zo);
        u4 A = {zp[0], zp[16], zp[32], zp[48]};
        f4 Dre = mma_b(A, Gre3, Z4), Dim = mma_b(A, Gim3, Z4), Hre = mma_b(A, SGre, Z4), Him = mma_b(A, SGim, Z4);
        if (g == 0) { Dre[0] = Hre[0]; Dim[0] = Him[0]; }
        f2 r0 = fma2(-lo2(Dim), tws[0], lo2(Dre) * twc[0]), i0 = fma2(lo2(Dre), tws[0], lo2(Dim) * twc[0]);
        f2 r1 = fma2(-hi2(Dim), tws[1], hi2(Dre) * twc[1]), i1 = fma2(hi2(Dre), tws[1], hi2(Dim) * twc[1]);
        u4 A2 = {pk_b(r0[0], i0[0]), pk_b(r0[1], i0[1]), pk_b(r1[0], i1[0]), pk_b(r1[1], i1[1])};
        f2 h[4]; u2 hp[2];
#pragma unroll
        for (int t = 0; t < 2; ++t) {
            f4 Yv = mma_b(A2, GA[t], Z4);
            u32 h0 = pk_b2(bf_2(xl[t][0]) + bf_2(pk_b(Yv[0], Yv[1]))), h1 = pk_b2(bf_2(xl[t][1]) + bf_2(pk_b(Yv[2], Yv[3])));
            hp[t] = (u2){h0, h1};
            h[2 * t] = bf_2(h0); h[2 * t + 1] = bf_2(h1);
        }
        f2 s2 = (h[0] + h[1]) + (h[2] + h[3]);
        const float mean = red_lanes<true>(s2[0] + s2[1]) * (1.0f / 512);
        f2 q2 = {0.f, 0.f};
#pragma unroll
        for (int j = 0; j < 4; ++j) { f2 d = h[j] - mean; q2 = fma2(d, d, q2); }
        const float inv = ln_inv(red_lanes<true>(q2[0] + q2[1]) * (1.0f / 512));
        const float mb = rbf(mean);
#pragma unroll
        for (int t = 0; t < 2; ++t)
            *(u2*)(scr + ((8 * t + (m >> 1)) * 4 + g) * 128 + (w ^ (m >> 1)) * 16 + (m & 1) * 8) =
                (u2){clean(pk_b2(bf_2(pk_b2(h[2 * t] - mb)) * inv)), clean(pk_b2(bf_2(pk_b2(h[2 * t + 1] - mb)) * inv))};
        WAITV(0);
        BARRIER();
        u4 lo = *(const u4*)(scr + (tid ^ ((tid >> 5) & 7)) * 16);
        bst4<SC1>(lo, rw, (tid >> 3) * 256 + (tid & 7) * 16, WS_LN2 + (b >> 1) * 16384 + (b & 1) * 128);
        FENCE();
        if (aw) WAITV(0); else __builtin_amdgcn_s_sleep(8);
        bst2<SC1>(hp[0], ro, xoff, 0); bst2<SC1>(hp[1], ro, xoff + 512, 0);
        FENCE();
        ST(10);
    }
    auto stage_out = [&](int r) {
        w2_read(W[r], LDS_SCR + (w & 3) * 8192 + (r & 1) * 4096);
        asm volatile("s_waitcnt lgkmcnt(0)" ::: "memory");
        PIN(W[r][0]); PIN(W[r][1]); PIN(W[r][2]); PIN(W[r][3]);
        if (r + 2 < 7) w2_glds(pw, r + 2, lds0 + LDS_SCR + (w & 3) * 8192 + (r & 1) * 4096);
    };
    auto publish = [&]() {
        u32 prev = 0;
        if (l == 0) prev = __hip_atomic_fetch_add(hw + 4, 1u, __ATOMIC_RELAXED, __HIP_MEMORY_SCOPE_WORKGROUP);
        if (__builtin_amdgcn_readfirstlane(prev) == 7u && l < 2)
            __hip_atomic_store(fl + (l ? 4096 + (b >> 1) * 32 + (b & 1) : (mt1 * 8 + (nt1 >> 1)) * 32 + (nt1 & 1)), tag, __ATOMIC_RELAXED, __HIP_MEMORY_SCOPE_AGENT);
        FENCE();
    };
    {
        const int tg = w & 3;
        char* xch = smem + XCH + tg * 3072;
        const int hoff = WS_H1 + (mt1 * 8 + 2 * tg) * (56 * 1024) + (nt1 >> 1) * 7168;
        if (__builtin_expect(aw, 1)) {
            const int toff = WS_LN2 + (mt1 * 8 + 2 * tg) * 16384;
            LAS char* wb0 = (LAS char*)smem + (4 * g + (m >> 2)) * 224 + (m & 3) * 8;
            LAS char* wb[4];
            int tq[4];
#pragma unroll
            for (int q = 0; q < 4; ++q) { const int qq = (q + rq) & 3; wb[q] = wb0 + qq * 28672; tq[q] = toff + qq * 4096; }
            u4 T[2][16], A[2][7];
            auto ldt = [&](int kk) {
#pragma unroll
                for (int tt = 0; tt < 2; ++tt) T[tt][kk] = bld4<SC1>(rw, l * 16 + (kk & 3) * 1024, tq[kk >> 2] + tt * 16384);
            };
#pragma unroll
            for (int i = 0; i < 7; ++i) A[0][i] = ldw1(wb[0], 0, i);
            if (l == 0) __hip_atomic_fetch_add(hw + 5, 1u, __ATOMIC_RELAXED, __HIP_MEMORY_SCOPE_WORKGROUP);
            ST(11);
#pragma unroll
            for (int kk = 0; kk < G1_DEPTH; ++kk) { ldt(kk); SCHED(); }
            f4 acc[2][7];
#pragma unroll
            for (int tt = 0; tt < 2; ++tt)
#pragma unroll
                for (int i = 0; i < 7; ++i) acc[tt][i] = Z4;
#pragma unroll
            for (int kk = 0; kk < 16; ++kk) {
                if ((kk & 1) == 0) {
                    if (__builtin_expect(any_sent(umax(umax(max4(T[0][kk]), max4(T[1][kk])), umax(max4(T[0][kk + 1]), max4(T[1][kk + 1])))), 0)) {
                        wait_tok(T[0][kk], T[1][kk], rw, l * 16, tq[kk >> 2] + (kk & 3) * 1024);
                        wait_tok(T[0][kk + 1], T[1][kk + 1], rw, l * 16, tq[kk >> 2] + ((kk + 1) & 3) * 1024);
                    }
                    if (kk == 0) ST(12);
                    if (kk == (BG6 / 7) * 4 - 2) lds_wait(hw + 5, 8u);
                }
                if (kk + G1_DEPTH < 16) ldt(kk + G1_DEPTH);
                if (kk < 15) {
#pragma unroll
                    for (int i = 0; i < 7; ++i) A[(kk + 1) & 1][i] = ldw1(wb[(kk + 1) >> 2], (kk + 1) & 3, i);
                }
#pragma unroll
                for (int i = 0; i < 7; ++i)
#pragma unroll
                    for (int tt = 0; tt < 2; ++tt) acc[tt][i] = mma_b(A[kk & 1][i], T[tt][kk], acc[tt][i]);
#pragma unroll
                for (int i = 0; i < 14; ++i) {
                    __builtin_amdgcn_sched_group_barrier(0x8, 1, 0);
                    if (i == 1 || i == 7) __builtin_amdgcn_sched_group_barrier(0x20, 1, 0);
                    __builtin_amdgcn_sched_group_barrier(0x100, 1, 0);
                    if ((kk & 1) && i >= 3) __builtin_amdgcn_sched_group_barrier(0x2, 1, 0);
                }
                if (kk & 1) {} else SCHED();
            }
#pragma unroll
            for (int q = 0; q < 3; ++q) {
                u2 p0 = pk4(acc[1][2 * q]), p1 = pk4(acc[1][2 * q + 1]);
                *(u4*)(xch + q * 1024 + l * 16) = (u4){p0[0], p0[1], p1[0], p1[1]};
            }
            lds_set(hw + tg, 1u);
            ST(13);
#pragma unroll
            for (int q = 0; q < 3; ++q) {
                u2 p0 = pk4(acc[0][2 * q]), p1 = pk4(acc[0][2 * q + 1]);
                bst4<SC1>(silu_pk4((u4){p0[0], p0[1], p1[0], p1[1]}), rw, l * 16, hoff + ((nt1 & 1) * 3 + q) * 1024);
            }
#pragma unroll
            for (int tt = 0; tt < 2; ++tt) { u2 p = pk4(acc[tt][6]); bst2<SC1>(silu_pk2(p[0], p[1]), rw, l * 8, hoff + tt * (56 * 1024) + 6144 + (nt1 & 1) * 512); }
        } else {
            bg(BG6, 28);
            WAITV(0);
            if (l == 0) __hip_atomic_fetch_add(hw + 5, 1u, __ATOMIC_RELAXED, __HIP_MEMORY_SCOPE_WORKGROUP);
            __builtin_amdgcn_s_sleep(8);
            lds_wait(hw + 5, 8u);
            w2_glds(pw, 0, lds0 + LDS_SCR + tg * 8192);
            w2_glds(pw, 1, lds0 + LDS_SCR + tg * 8192 + 4096);
#pragma unroll
            for (int r = 0; r < B_R; ++r) { WAITV(4); stage_out(r); }
            ST(12);
            lds_wait(hw + tg, 1u);
            ST(13);
#pragma unroll
            for (int q = 0; q < 3; ++q)
                bst4<SC1>(silu_pk4(*(const u4*)(xch + q * 1024 + l * 16)), rw, l * 16, hoff + 56 * 1024 + ((nt1 & 1) * 3 + q) * 1024);
        }
        ST(14);
        WAITV(0);
        publish();
        ST(21);
        if (__builtin_expect(aw, 1)) {
            lds_wait(hw + 4, 8u);
#pragma unroll
            for (int ks = 0; ks < 7; ++ks) w2_glds(pw, ks, lds0 + tg * 28672 + ks * 4096);
        } else {
            stage_out(B_R);
#pragma unroll
            for (int r = B_R + 1; r < 7; ++r) { if (r + 1 < 7) WAITV(4); else WAITV(0); stage_out(r); }
        }
    }
    {
        ST(15);
        const int rot = w & 3;
        if (aw) {
            flag_wait(fl + ((l & 4) ? 4096 + (grp * 8 + th * 4 + rot) * 32 : (grp * 8 + (pw ^ ((l & 2) * 2))) * 32) + (l & 1), tag);
            lds_set(hw + 8 + rot, tag);
        } else {
#pragma nounroll
            for (int sp = 0; sp < SPIN_MAX; ++sp) {
                if (__builtin_amdgcn_readfirstlane(__hip_atomic_load(hw + 8 + rot, __ATOMIC_RELAXED, __HIP_MEMORY_SCOPE_WORKGROUP)) == tag) break;
                __builtin_amdgcn_s_sleep(1);
            }
            FENCE();
        }
        ST(16);
        const int toff = WS_H1 + (grp * 8 + th * 4) * (56 * 1024) + pw * 7168;
        int to[4];
#pragma unroll
        for (int tt = 0; tt < 4; ++tt) to[tt] = toff + ((tt + rot) & 3) * (56 * 1024);
        u4 T[7][4];
        auto ldtok = [&](int ks) {
#pragma unroll
            for (int tt = 0; tt < 4; ++tt) {
                if (ks < 6) T[ks][tt] = bld4<SC1>(rw, l * 16, to[tt] + ko[ks] * 1024);
                else {
                    u2 a = bld2<SC1>(rw, l * 8 + 2048, to[tt] + 4096), c = bld2<SC1>(rw, l * 8 + 2560, to[tt] + 4096);
                    T[ks][tt] = (u4){a[0], a[1], c[0], c[1]};
                }
            }
            SCHED();
        };
#pragma unroll
        for (int ks = 0; ks < G2_DEPTH; ++ks) ldtok(ks);
        if (aw) {
#pragma unroll
            for (int ks = 0; ks < 7; ++ks) w2_read(W[ks], rot * 28672 + ks * 4096);
            asm volatile("s_waitcnt lgkmcnt(0)" ::: "memory");
        }
        if (l == 0) __hip_atomic_fetch_add(hw + 7, 1u, __ATOMIC_RELAXED, __HIP_MEMORY_SCOPE_WORKGROUP);
        const int rho = grp * 128 + th * 64 + rot * 16 + m;
        const int ooff = (256 * (rho & 7) + (rho >> 3)) * 1024 + fb * 128 + (w >> 2) * 64 + g * 16;
        const u4 hres = bld4<SC1>(ro, ooff, 0);
        SCHED();
        f4 acc[4][4];
#pragma unroll
        for (int tt = 0; tt < 4; ++tt)
#pragma unroll
            for (int t = 0; t < 4; ++t) acc[tt][t] = Z4;
#pragma unroll
        for (int ks = 0; ks < 7; ++ks) {
            if (ks + G2_DEPTH < 7) { PIN(T[ks][0]); ldtok(ks + G2_DEPTH); }
#pragma unroll
            for (int tt = 0; tt < 4; ++tt)
#pragma unroll
                for (int t = 0; t < 4; ++t) {
                    acc[tt][t] = mma_b(W[ks][t], T[ks][tt], acc[tt][t]);
                }
            SCHED();
        }
        ST(17);
        lds_wait(hw + 7, 8u);
        char* dp = smem + w * 16384 + l * 16;
#pragma unroll
        for (int tt = 1; tt < 4; ++tt)
#pragma unroll
            for (int t = 0; t < 4; ++t) *(f4*)(dp + (((tt + rot) & 3) * 4 + t) * 1024) = acc[tt][t];
        f4 s0, s1;
        if (aw) { *(f4*)(dp + (rot * 4 + 2) * 1024) = acc[0][2]; *(f4*)(dp + (rot * 4 + 3) * 1024) = acc[0][3]; s0 = acc[0][0]; s1 = acc[0][1]; }
        else { *(f4*)(dp + (rot * 4) * 1024) = acc[0][0]; *(f4*)(dp + (rot * 4 + 1) * 1024) = acc[0][1]; s0 = acc[0][2]; s1 = acc[0][3]; }
        BARRIER();
        f4 r0[7], r1[7];
#pragma unroll
        for (int j = 1; j < 8; ++j) {
            const char* rp = smem + (((w + j) & 7) * 16 + rot * 4 + (w >> 2) * 2) * 1024 + l * 16;
            r0[j - 1] = *(const f4*)rp; r1[j - 1] = *(const f4*)(rp + 1024);
        }
        SCHED();
        s0 = ((s0 + r0[0]) + (r0[1] + r0[2])) + ((r0[3] + r0[4]) + (r0[5] + r0[6]));
        s1 = ((s1 + r1[0]) + (r1[1] + r1[2])) + ((r1[3] + r1[4]) + (r1[5] + r1[6]));
        u2 p0 = pk4(s0), p1 = pk4(s1);
        bst4<NT>((u4){pk_b2(bf_2(hres[0]) + bf_2(p0[0])), pk_b2(bf_2(hres[1]) + bf_2(p0[1])),
                           pk_b2(bf_2(hres[2]) + bf_2(p1[0])), pk_b2(bf_2(hres[3]) + bf_2(p1[1]))}, ro, ooff, 0);
    }
    ST_END(ws, 0);
}

extern "C" void kernel_launch(void* const* d_in, const int* in_sizes, int n_in, void* d_out, int out_size,
                              void* d_ws, size_t ws_size, hipStream_t stream) {
    (void)hipFuncSetAttribute((const void*)k_fused, hipFuncAttributeMaxDynamicSharedMemorySize, LDS_B);
    k_fused<<<256, 512, LDS_B, stream>>>((const uint16_t*)d_in[0], (const uint16_t*)d_in[1], (const uint16_t*)d_in[2],
                                         (const uint16_t*)d_in[4], (const uint16_t*)d_in[5], (uint16_t*)d_out, (char*)d_ws);
}
```

```cpp
#define BG_DELAY 16
#include <hip/hip_runtime.h>
#include <hip/hip_bf16.h>
#include <stdint.h>

typedef _Float16 h8 __attribute__((ext_vector_type(8)));
typedef _Float16 h4 __attribute__((ext_vector_type(4)));
typedef _Float16 h2 __attribute__((ext_vector_type(2)));
typedef __bf16 b8 __attribute__((ext_vector_type(8)));
typedef __bf16 b2 __attribute__((ext_vector_type(2)));
typedef short s4 __attribute__((ext_vector_type(4)));
typedef float f4 __attribute__((ext_vector_type(4)));
typedef float f2 __attribute__((ext_vector_type(2)));
typedef unsigned u32;
typedef u32 u4 __attribute__((ext_vector_type(4)));
typedef u32 u2 __attribute__((ext_vector_type(2)));
#define LAS __attribute__((address_space(3)))
#define GAS __attribute__((address_space(1)))
#define DI __device__ __forceinline__
#define FENCE() asm volatile("" ::: "memory")
#define PIN(x) asm volatile("" : "+v"(x))
#define SCHED() __builtin_amdgcn_sched_barrier(0)
#define WAITV(n) asm volatile("s_waitcnt vmcnt(%0)" :: "n"(n) : "memory")
#define BARRIER() do { asm volatile("s_waitcnt lgkmcnt(0)" ::: "memory"); __builtin_amdgcn_s_barrier(); FENCE(); } while (0)
constexpr int SC1 = 16, NT = 2;
constexpr u32 SENT = 0xffffffffu;
constexpr int SPIN_MAX = 1 << 20;

DI u32 pk_h(float a, float b) { f2 v = {a, b}; h2 r = __builtin_convertvector(v, h2); return __builtin_bit_cast(u32, r); }
DI u32 pk_b(float a, float b) { f2 v = {a, b}; b2 r = __builtin_convertvector(v, b2); return __builtin_bit_cast(u32, r); }
DI float bf_lo(u32 v) { return __builtin_bit_cast(float, v << 16); }
DI float bf_hi(u32 v) { return __builtin_bit_cast(float, v & 0xffff0000u); }
DI f2 bf_2(u32 v) { return (f2){bf_lo(v), bf_hi(v)}; }
DI u32 pk_b2(f2 v) { return pk_b(v[0], v[1]); }
DI float rbf(float a) { return bf_lo(pk_b(a, a)); }
DI float cosr(float rev) { return __builtin_amdgcn_cosf(rev); }
DI float sinr(float rev) { return __builtin_amdgcn_sinf(rev); }
DI f2 lo2(f4 v) { return __builtin_shufflevector(v, v, 0, 1); }
DI f2 hi2(f4 v) { return __builtin_shufflevector(v, v, 2, 3); }
DI f2 fma2(f2 a, f2 b, f2 c) { return __builtin_elementwise_fma(a, b, c); }
DI u32 umin(u32 a, u32 b) { return a < b ? a : b; }
DI u32 umax(u32 a, u32 b) { return a > b ? a : b; }
DI u32 clean(u32 v) { return umin(v, SENT - 1); }
DI u32 max4(u4 v) { return umax(umax(umax(v[0], v[1]), v[2]), v[3]); }
#define Z4 ((f4){0, 0, 0, 0})
DI f4 mma_h(u4 a, u4 b, f4 c) { return __builtin_amdgcn_mfma_f32_16x16x32_f16(__builtin_bit_cast(h8, a), __builtin_bit_cast(h8, b), c, 0, 0, 0); }
DI f4 mma_b(u4 a, u4 b, f4 c) { return __builtin_amdgcn_mfma_f32_16x16x32_bf16(__builtin_bit_cast(b8, a), __builtin_bit_cast(b8, b), c, 0, 0, 0); }
DI f4 mma16_b(u2 a, u2 b, f4 c) { return __builtin_amdgcn_mfma_f32_16x16x16bf16_1k(__builtin_bit_cast(s4, a), __builtin_bit_cast(s4, b), c, 0, 0, 0); }
DI f4 mma16_h(u2 a, u2 b, f4 c) { return __builtin_amdgcn_mfma_f32_16x16x16f16(__builtin_bit_cast(h4, a), __builtin_bit_cast(h4, b), c, 0, 0, 0); }
typedef __amdgpu_buffer_rsrc_t rsrc_t;
DI rsrc_t mkrs(const void* p, u32 bytes) { return __builtin_amdgcn_make_buffer_rsrc((void*)p, 0, bytes, 0x00020000); }
template <int AUX> DI u4 bld4(rsrc_t r, int v, int s) { return __builtin_amdgcn_raw_buffer_load_b128(r, v, s, AUX); }
template <int AUX> DI u2 bld2(rsrc_t r, int v, int s) { return __builtin_amdgcn_raw_buffer_load_b64(r, v, s, AUX); }
template <int AUX> DI void bst4(u4 d, rsrc_t r, int v, int s) {
    __builtin_amdgcn_raw_buffer_store_b128(d, r, v, s, AUX);
    asm volatile("s_nop 1" :: "v"(d));
}
template <int AUX> DI void bst2(u2 d, rsrc_t r, int v, int s) { __builtin_amdgcn_raw_buffer_store_b64(d, r, v, s, AUX); }

template <int CTRL> DI float dpp(float v) {
    return __builtin_bit_cast(float, __builtin_amdgcn_update_dpp(0, __builtin_bit_cast(int, v), CTRL, 0xf, 0xf, true));
}
template <bool ALL> DI float red_lanes(float v) {
    v += dpp<0xB1>(v);
    if (ALL) v += dpp<0x4E>(v);
    v += dpp<0x124>(v); v += dpp<0x128>(v);
    u32 x = __builtin_bit_cast(u32, v), y = x;
    asm("" : "+v"(y));
    auto r = __builtin_amdgcn_permlane16_swap(x, y, false, false);
    v = __builtin_bit_cast(float, (u32)r[0]) + __builtin_bit_cast(float, (u32)r[1]);
    x = __builtin_bit_cast(u32, v); y = x;
    asm("" : "+v"(y));
    auto q = __builtin_amdgcn_permlane32_swap(x, y, false, false);
    return __builtin_bit_cast(float, (u32)q[0]) + __builtin_bit_cast(float, (u32)q[1]);
}
DI float ln_inv(float var) { return __builtin_amdgcn_rcpf(rbf(__builtin_amdgcn_sqrtf(rbf(rbf(var) + 1e-5f)))); }

#define ST_DECL
#define ST(i)
#define ST_END(ws, kid)

#define FS 1
constexpr u32 WS_U = 0;
constexpr u32 WS_V = WS_U + (2u << 20);
constexpr u32 WS_LN2 = WS_V + (2u << 20);
constexpr u32 WS_WA = WS_LN2 + (2u << 20);
constexpr u32 WS_WB = WS_WA + 8192u;
constexpr u32 WS_FA = WS_WB + 8192u;
constexpr u32 WS_FLAG = WS_FA + 16384u * FS;
constexpr u32 WS_EP = WS_FLAG + 32768u;
constexpr u32 WS_H1 = WS_WA + (512u << 10);
constexpr u32 WS_END = WS_H1 + 7340032u;

#define BGA 9
#define BGB 15
#define BGC 21
#define G_SLEEP 16
#define GA_SLEEP 4
#define GB_SLEEP 8
#define A_SLEEP 0
#define PRO_SLEEP 4
constexpr int BG5 = BGA, BG5b = BGB, BG6 = BGC;
constexpr int G1_DEPTH = 10, G2_DEPTH = 3;
constexpr int B_R = 5;
constexpr int CODE_LINES = 290, CODE_PF = 10;
constexpr int LDS_SCR = 114688;
constexpr int SCR_X = 8448;
constexpr int SCR_S = SCR_X + 20480;
constexpr int SCR_F = 49024;
constexpr int XCH = 147456;
constexpr int LDS_B = 163840;

DI void glds16(const void* sbase, u32 voff, u32 lds_dst) {
    asm volatile("s_mov_b32 m0, %2\n\ts_nop 0\n\tglobal_load_lds_dwordx4 %0, %1" :: "v"(voff), "s"(sbase), "s"(lds_dst) : "memory");
}
DI bool any_sent(u32 v) { return __builtin_amdgcn_ballot_w64(v == SENT) != 0; }
DI void poll2(u4& a, u4& c, rsrc_t rw, int vo, int sa, int sc) {
#pragma nounroll
    for (int sp = 0; sp < SPIN_MAX; ++sp) {
        const bool ba = max4(a) == SENT, bc = max4(c) == SENT;
        if (__builtin_amdgcn_ballot_w64(ba || bc) == 0) break;
        __builtin_amdgcn_s_sleep(1);
        FENCE();
        if (ba) a = bld4<SC1>(rw, vo, sa);
        if (bc) c = bld4<SC1>(rw, vo, sc);
    }
}
DI void wait_tok(u4& t0, u4& t1, rsrc_t rw, int vo, int so) {
    if (any_sent(umax(max4(t0), max4(t1)))) {
#pragma nounroll
        for (int sp = 0; sp < SPIN_MAX; ++sp) {
            FENCE();
            t0 = bld4<SC1>(rw, vo, so); t1 = bld4<SC1>(rw, vo, so + 16384);
            if (!any_sent(umax(max4(t0), max4(t1)))) break;
        }
    }
}
DI u4 ldw1(LAS char* wb, int kk, int ft) {
    s4 a0 = __builtin_amdgcn_ds_read_tr16_b64_v4i16((LAS s4*)(wb + kk * 32 * 224 + ft * 32));
    s4 a1 = __builtin_amdgcn_ds_read_tr16_b64_v4i16((LAS s4*)(wb + (kk * 32 + 16) * 224 + ft * 32));
    u2 x0 = __builtin_bit_cast(u2, a0), x1 = __builtin_bit_cast(u2, a1);
    return (u4){x0[0], x0[1], x1[0], x1[1]};
}
DI u2 pk4(f4 a) { return (u2){pk_b(a[0], a[1]), pk_b(a[2], a[3])}; }
DI u2 silu_pk2(u32 p0, u32 p1) {
    u32 o0, o1; float a0, a1, a2, a3, t0, t1, t2, t3;
    asm("v_lshlrev_b32 %2, 16, %10\n\tv_and_b32 %3, 0xffff0000, %10\n\tv_lshlrev_b32 %4, 16, %11\n\tv_and_b32 %5, 0xffff0000, %11\n\t"
        "v_mul_f32 %6, %12, %2\n\tv_mul_f32 %7, %12, %3\n\tv_mul_f32 %8, %12, %4\n\tv_mul_f32 %9, %12, %5\n\t"
        "v_exp_f32 %6, %6\n\tv_exp_f32 %7, %7\n\tv_exp_f32 %8, %8\n\tv_exp_f32 %9, %9\n\t"
        "v_add_f32 %6, 1.0, %6\n\tv_add_f32 %7, 1.0, %7\n\tv_add_f32 %8, 1.0, %8\n\tv_add_f32 %9, 1.0, %9\n\t"
        "v_rcp_f32 %6, %6\n\tv_rcp_f32 %7, %7\n\tv_rcp_f32 %8, %8\n\tv_rcp_f32 %9, %9\n\t"
        "v_mul_f32 %2, %2, %6\n\tv_mul_f32 %3, %3, %7\n\tv_mul_f32 %4, %4, %8\n\tv_mul_f32 %5, %5, %9\n\t"
        "v_cvt_pk_bf16_f32 %0, %2, %3\n\tv_cvt_pk_bf16_f32 %1, %4, %5"
        : "=&v"(o0), "=&v"(o1), "=&v"(a0), "=&v"(a1), "=&v"(a2), "=&v"(a3), "=&v"(t0), "=&v"(t1), "=&v"(t2), "=&v"(t3)
        : "v"(p0), "v"(p1), "s"(-1.4426950408889634f));
    return (u2){o0, o1};
}
DI u4 silu_pk4(u4 p) { u2 a = silu_pk2(p[0], p[1]), b = silu_pk2(p[2], p[3]); return (u4){a[0], a[1], b[0], b[1]}; }
DI void lds_set(LAS u32* p, u32 v) {
    asm volatile("s_waitcnt lgkmcnt(0)" ::: "memory");
    __hip_atomic_store(p, v, __ATOMIC_RELAXED, __HIP_MEMORY_SCOPE_WORKGROUP);
}
DI void lds_wait(LAS u32* p, u32 v) {
#pragma nounroll
    for (int sp = 0; sp < SPIN_MAX; ++sp) {
        if (__builtin_amdgcn_readfirstlane(__hip_atomic_load(p, __ATOMIC_RELAXED, __HIP_MEMORY_SCOPE_WORKGROUP)) >= v) break;
        __builtin_amdgcn_s_sleep(1);
    }
    FENCE();
}
DI void flag_wait(GAS u32* p, u32 tag) {
#pragma nounroll
    for (int sp = 0; sp < SPIN_MAX; ++sp) {
        if (__builtin_amdgcn_ballot_w64(__hip_atomic_load(p, __ATOMIC_RELAXED, __HIP_MEMORY_SCOPE_AGENT) != tag) == 0) break;
        __builtin_amdgcn_s_sleep(2);
    }
    FENCE();
}
#define SENT4 ((u4){SENT, SENT, SENT, SENT})

__global__ __launch_bounds__(512, 2) void k_fused(const uint16_t* x, const uint16_t* wr, const uint16_t* wi, const uint16_t* w1,
                                                  const uint16_t* w2, uint16_t* out, char* ws) {
    extern __shared__ __attribute__((aligned(16))) char smem[];
    const int tid = threadIdx.x, l = tid & 63, g = l >> 4, m = l & 15;
    const int w = __builtin_amdgcn_readfirstlane(tid >> 6);
    const int b = (blockIdx.x & 7) * 32 + (blockIdx.x >> 3);
    const int mt1 = (b >> 6) * 4 + ((b >> 3) & 3), nt1 = ((b >> 5) & 1) * 8 + (b & 7);
    const int grp = b >> 4, th = (b >> 3) & 1, fb = b & 7;
    const rsrc_t rw = mkrs(ws, WS_END), ro = mkrs(out, 2097152);
    char* scr = smem + LDS_SCR;
    LAS u32* hw = (LAS u32*)((LAS char*)smem + LDS_SCR + SCR_F);
    const u32 lds0 = (u32)(uintptr_t)(LAS char*)smem;
    const float mf = (float)m, gf = (float)g, sg = (m & 1) ? -1.0f : 1.0f;
    const bool aw = w < 4;
    ST_DECL;
    unsigned long long pc0;
    asm volatile("s_getpc_b64 %0" : "=s"(pc0));
    if (tid >= 448 && l < CODE_PF)
        asm volatile("s_mov_b32 m0, %2\n\ts_nop 0\n\tglobal_load_lds_dword %0, %1"
                     :: "v"(umin((blockIdx.x >> 3) * CODE_PF + l, CODE_LINES - 1) * 128), "s"(pc0), "s"(lds0 + LDS_SCR + SCR_F + 64) : "memory");
    const u32 epoch = __hip_atomic_load((GAS u32*)(ws + WS_EP), __ATOMIC_RELAXED, __HIP_MEMORY_SCOPE_AGENT);
    FENCE();
    u32 xd[8];
    const int f1 = b >> 5, cg = b & 31;
    const bool mir = cg >= 16;
    const rsrc_t rr = mkrs(wr, 1052672), ri = mkrs(wi, 1052672);
    u4 fr, fi; u32 sr_, si_;
    if (aw) {
        __builtin_amdgcn_s_sleep(A_SLEEP);
        const u32* xr = (const u32*)(x + (256 * (2 * w + ((m >> 1) & 1)) + b) * 512) + 8 * g + 2 * (m >> 2) + (m & 1);
#pragma unroll
        for (int j = 0; j < 8; ++j) xd[j] = xr[32 * j];
    } else {
        bst4<SC1>(SENT4, rw, l * 16, WS_U + b * 8192 + (w - 4) * 2048); bst4<SC1>(SENT4, rw, l * 16 + 1024, WS_U + b * 8192 + (w - 4) * 2048);
        __builtin_amdgcn_raw_buffer_store_b32(SENT, rw, l < 4 ? (l >> 1) * 8192 + (l & 1) * 1024 : 0x7ffffff0,
                                              WS_WA + (w - 4) * 2048 + ((b >> 6) * 16 + (b & 15)) * 16 + ((b >> 4) & 3) * 4, SC1);
        FENCE();
        if (w == 6) {
            const int c = (l >> 3) & 3, f_ = 8 * b + (l & 7);
            const int e1 = ((c & 1) ? (2048 - f_) & 2047 : f_) * 514 + (c >> 1) * 512;
            sr_ = __builtin_amdgcn_raw_buffer_load_b16(rr, e1, 0, 0); si_ = __builtin_amdgcn_raw_buffer_load_b16(ri, e1, 0, 0);
        }
    }
    FENCE();
    const int zo = (64 * g + (g < 2 ? m : (m + 15) & 15)) * 4;
    const int rq = ((b >> 3) + b) & 3;
    const int row0 = (tid & 255) / 14, cch0 = (tid & 255) - row0 * 14;
    const char* wsrc = (const char*)w1 + nt1 * 224;
    u32 w1o[7];
    auto w1_burst = [&](int i0, int n) {
#pragma unroll
        for (int i = i0; i < i0 + n; ++i) {
            const int q = (i / 7 + rq) & 3, j = i % 7;
            glds16(wsrc + q * (128 * 3584), w1o[j], lds0 + (w - 4) * 1024 + q * 28672 + j * 4096);
        }
    };
    const int xoff = (256 * w + b) * 1024 + m * 32 + g * 8;
    u2 xl[2];
    float c16[4], s16[4];
    f2 twc[2], tws[2];
    u4 Gre3, Gim3, SGre, SGim, GA[2];
    auto consts0 = [&]() {
#pragma unroll
        for (int u = 0; u < 4; ++u) {
            float a = (4.0f * gf + u) * (mf * (1.0f / 16));
            c16[u] = cosr(a); s16[u] = sinr(a);
            twc[u >> 1][u & 1] = cosr(a * (1.0f / 32)); tws[u >> 1][u & 1] = sinr(a * (1.0f / 32));
        }
    };
    auto consts3 = [&]() {
#pragma unroll
        for (int u = 0; u < 4; ++u) {
            const float k = 4.0f * gf + u;
            const bool k0 = u == 0 && g == 0;
            float cc = c16[u], ss = s16[u];
            Gre3[u] = pk_b(cc, -ss); Gim3[u] = pk_b(ss, cc);
            float a2 = (2.0f * k + 1.0f) * (mf * (1.0f / 32));
            u32 sp = pk_b(g < 2 ? 2.f * cc : 2.f * cosr(a2), g < 2 ? -2.f * ss : -2.f * sinr(a2));
            SGre[u] = k0 ? pk_b(1.0f, sg) : (g < 2 ? sp : 0u);
            SGim[u] = g < 2 ? 0u : sp;
            float a4 = k * (mf * (1.0f / 32)), c4 = 2.f * cosr(a4), s4_ = -2.f * sinr(a4);
            GA[0][u] = k0 ? pk_b(1.0f, sg) : pk_b(c4, s4_);
            GA[1][u] = k0 ? pk_b(1.0f, sg) : ((u & 1) ? pk_b(-c4, -s4_) : pk_b(c4, s4_));
        }
        PIN(Gre3); PIN(Gim3); PIN(SGre); PIN(SGim); PIN(GA[0]); PIN(GA[1]); PIN(twc[0]); PIN(twc[1]); PIN(tws[0]); PIN(tws[1]);
    };
    const int pw = (w + 4 * th) & 7;
    GAS u32* fl = (GAS u32*)(ws + WS_FLAG);
    int ko[7];
    {
        const int r6 = (fb < 6 ? fb : fb - 5) + 3 * (grp & 1);
#pragma unroll
        for (int ks = 0; ks < 6; ++ks) { int k = ks + r6; k = k >= 6 ? k - 6 : k; ko[ks] = k >= 6 ? k - 6 : k; }
        ko[6] = 6;
    }
    u4 W[7][4];
    const u32 w2vo = (l >> 3) * 1024 + (((l & 7) ^ (((l >> 4) & 1) << 2)) * 16);
    auto w2_glds = [&](int p_, int ks, u32 dst) {
        const int q = ko[ks];
        const char* src = (const char*)w2 + (224 * p_ + (ks == 6 ? 96 : (q < 3 ? 32 * q : 16 + 32 * q))) * 1024 + fb * 128;
        glds16(src, w2vo, dst); glds16(src + 8192, w2vo, dst + 1024);
        glds16(src + (ks == 6 ? 112 : 16) * 1024, w2vo, dst + 2048); glds16(src + (ks == 6 ? 120 : 24) * 1024, w2vo, dst + 3072);
    };
    LAS char* w2rd = (LAS char*)smem + (4 * g + (m >> 2)) * 128 + (((m & 3) ^ (((m >> 3) & 1) << 2)) * 16);
    auto w2_read = [&](u4 (&f)[4], int off) {
#pragma unroll
        for (int t = 0; t < 4; ++t) {
            LAS char* p = (LAS char*)((u32)(uintptr_t)w2rd ^ ((t >> 1) * 64)) + off + (t & 1) * 8;
            u2 x0 = __builtin_bit_cast(u2, __builtin_amdgcn_ds_read_tr16_b64_v4i16((LAS s4*)p));
            u2 x1 = __builtin_bit_cast(u2, __builtin_amdgcn_ds_read_tr16_b64_v4i16((LAS s4*)(p + 2048)));
            f[t] = (u4){x0[0], x0[1], x1[0], x1[1]};
        }
    };
    auto bg = [&](int i0, int i1) { w1_burst(i0, i1 - i0); };
    u32 tag;
    if (__builtin_expect(aw, 1)) {
        if (tid < 8) hw[tid] = 0;
        FENCE();
        ST(2);
        {
            u4 FAre, FAim, Fre, Fim, Sre, Sim; u2 Mb; f2 pc[2], ps[2];
            consts0();
            float c0[8], s0[8];
#pragma unroll
            for (int j = 0; j < 8; ++j) {
                float a = (gf + 4.0f * j) * (mf * (1.0f / 32));
                c0[j] = cosr(a); s0[j] = m ? -sinr(a) : ((g & 1) ? -1.0f : 1.0f);
            }
            const float mo = (2.0f * mf + 1.0f) * (1.0f / 32);
#pragma unroll
            for (int u = 0; u < 4; ++u) {
                FAre[u] = pk_h(c0[2 * u], c0[2 * u + 1]); FAim[u] = pk_h(s0[2 * u], s0[2 * u + 1]);
                float cc = c16[u], ss = s16[u];
                Fre[u] = pk_h(cc, ss); Fim[u] = pk_h(-ss, cc);
                float a2 = (4.0f * gf + u) * mo;
                u32 re = pk_h(m < 8 ? cc : cosr(a2), 0.f), im = pk_h(m < 8 ? (m ? -ss : ((u & 1) ? -1.0f : 1.0f)) : -sinr(a2), 0.f);
                Sre[u] = m < 8 ? re : re << 16; Sim[u] = m < 8 ? im : im << 16;
                pc[u & 1][u >> 1] = twc[u >> 1][u & 1]; ps[u & 1][u >> 1] = tws[u >> 1][u & 1];
            }
#pragma unroll
            for (int e = 0; e < 2; ++e) {
                float a = (float)(((m >> 1) * (256 * (2 * g + e) + b)) & 2047) * (1.0f / 2048);
                float cc = cosr(a), ss = sinr(a);
                Mb[e] = (m & 1) ? pk_h(-ss, cc) : pk_h(cc, ss);
            }
            PIN(FAre); PIN(FAim); PIN(Fre); PIN(Fim); PIN(Sre); PIN(Sim); PIN(Mb); PIN(pc[0]); PIN(pc[1]); PIN(ps[0]); PIN(ps[1]);
            ST(3);
            f2 v[8];
#pragma unroll
            for (int j = 0; j < 8; ++j) v[j] = bf_2(xd[j]);
            f2 s2 = ((v[0] + v[1]) + (v[2] + v[3])) + ((v[4] + v[5]) + (v[6] + v[7]));
            const float mean = red_lanes<false>(s2[0] + s2[1]) * (1.0f / 512);
            f2 q0 = {0.f, 0.f}, q1 = {0.f, 0.f};
#pragma unroll
            for (int j = 0; j < 8; ++j) { v[j] -= mean; if (j & 1) q1 = fma2(v[j], v[j], q1); else q0 = fma2(v[j], v[j], q0); }
            q0 += q1;
            const float inv = ln_inv(red_lanes<false>(q0[0] + q0[1]) * (1.0f / 512));
            const float off = (mean - rbf(mean)) * inv;
#pragma unroll
            for (int j = 0; j < 8; ++j) v[j] = fma2(v[j], (f2){inv, inv}, (f2){off, off});
            u4 B[2];
#pragma unroll
            for (int p = 0; p < 2; ++p) {
                u4 A = {pk_h(v[0][p], v[1][p]), pk_h(v[2][p], v[3][p]), pk_h(v[4][p], v[5][p]), pk_h(v[6][p], v[7][p])};
                f4 Dre = mma_h(A, FAre, Z4), Dim = mma_h(A, FAim, Z4);
#pragma unroll
                for (int r = 0; r < 2; ++r) {
                    f2 re = r ? hi2(Dre) : lo2(Dre), im = r ? hi2(Dim) : lo2(Dim);
                    f2 tr = fma2(im, ps[p], re * pc[p]), ti = fma2(-re, ps[p], im * pc[p]);
                    B[r][p] = pk_h(tr[0], ti[0]); B[r][2 + p] = pk_h(tr[1], ti[1]);
                }
            }
#pragma unroll
            for (int r = 0; r < 2; ++r) {
                f4 Gre = mma_h(Fre, B[r], Z4), Gim = mma_h(Fim, B[r], Z4), Hre = mma_h(Sre, B[r], Z4), Him = mma_h(Sim, B[r], Z4);
#pragma unroll
                for (int i = 0; i < 4; ++i) *(u32*)(scr + (2 * w + r) * 1024 + zo + i * 64) = pk_h(m ? Gre[i] : Hre[i], m ? Gim[i] : Him[i]);
            }
            BARRIER();
            ST(18);
            u4 r0 = *(const u4*)(scr + (2 * g) * 1024 + w * 256 + m * 16);
            u4 r1 = *(const u4*)(scr + (2 * g + 1) * 1024 + w * 256 + m * 16);
            u4 o0, o1;
#pragma unroll
            for (int t = 0; t < 4; ++t) {
                f4 D = mma16_h(Mb, (u2){r0[t], r1[t]}, Z4);
                o0[t] = clean(pk_h(D[0], D[1])); o1[t] = clean(pk_h(D[2], D[3]));
            }
            bst4<SC1>(o0, rw, g * 2048 + m * 16, WS_U + b * 8192 + w * 256);
            bst4<SC1>(o1, rw, g * 2048 + m * 16 + 1024, WS_U + b * 8192 + w * 256);
        }
        ST(4);
        u4 Fre, Fim, Gre, Gim; u2 Nb; f2 tc[2], ts[2];
#pragma unroll
        for (int u = 0; u < 4; ++u) {
            float cc = c16[u], ss = s16[u];
            Fre[u] = pk_h(0.25f * cc, 0.25f * ss); Fim[u] = pk_h(-0.25f * ss, 0.25f * cc);
            Gre[u] = pk_b(cc * (1.0f / 256), -ss * (1.0f / 256)); Gim[u] = pk_b(ss * (1.0f / 256), cc * (1.0f / 256));
            float a3 = (4.0f * gf + u) * (mf * (1.0f / 256));
            tc[u >> 1][u & 1] = cosr(a3); ts[u >> 1][u & 1] = sinr(a3);
        }
        PIN(Fre); PIN(Fim); PIN(Gre); PIN(Gim); PIN(tc[0]); PIN(tc[1]); PIN(ts[0]); PIN(ts[1]);
        xl[0] = *(const u2*)((const char*)x + xoff); xl[1] = *(const u2*)((const char*)x + xoff + 512);
        FENCE();
        tag = __builtin_amdgcn_readfirstlane(epoch) + 1u;
        consts3();
#pragma unroll
        for (int e = 0; e < 2; ++e) {
            float a1 = (float)(((2 * g + e) * (256 * (m >> 1) + b)) & 2047) * (1.0f / 2048);
            float c1 = cosr(a1), s1 = sinr(a1);
            Nb[e] = (m & 1) ? pk_b(s1, c1) : pk_b(c1, -s1);
        }
        PIN(Nb);
        WAITV(0);
        ST(19);
        BARRIER();
        ST(5);
        u4 d[2], wa, wb;
        const int uo = (tid >> 1) * 8192 + (tid & 1) * 16, us = WS_U + f1 * 1024 + cg * 32;
        d[0] = bld4<SC1>(rw, uo, us); d[1] = bld4<SC1>(rw, uo, us + 128 * 8192);
        if (cg == 0) {
#pragma nounroll
            for (int sp = 0; sp < SPIN_MAX; ++sp) {
                FENCE();
                const u32 ux = __builtin_amdgcn_raw_buffer_load_b32(rw, tid * 8192, WS_U + ((8 - f1) & 7) * 1024, SC1);
                wa = bld4<SC1>(rw, l * 16, WS_WA + f1 * 1024); wb = bld4<SC1>(rw, l * 16, WS_WB + f1 * 1024);
                *(u32*)(scr + SCR_X + tid * 4) = ux;
                if (!any_sent(umax(ux, umax(max4(wa), max4(wb))))) break;
            }
        }
        poll2(d[0], d[1], rw, uo, us, us + 128 * 8192);
        char* sp = scr + SCR_S + (tid ^ ((tid >> 4) & 1)) * 16;
        *(u4*)sp = d[0]; *(u4*)(sp + 4096) = d[1];
        BARRIER();
        if (b == 0 && tid == 0) __hip_atomic_store((GAS u32*)(ws + WS_EP), tag, __ATOMIC_RELAXED, __HIP_MEMORY_SCOPE_AGENT);
        ST(6);
        u2 ld[4], wl[4];
        const int so = (64 * g + m) * 32 + (((w >> 1) ^ (m >> 3)) * 16) + (w & 1) * 8;
#pragma unroll
        for (int q = 0; q < 4; ++q) ld[q] = *(const u2*)(scr + SCR_S + so + 512 * q);
#pragma unroll
        for (int q = 0; q < 4; ++q) wl[q] = *(const u2*)(scr + SCR_S + so + 8192 + 512 * q);
        FENCE();
        auto fwd = [&](u4 A, f4& Xr, f4& Xi) {
            f4 Dre = mma_h(A, Fre, Z4), Dim = mma_h(A, Fim, Z4);
            f2 r0 = fma2(lo2(Dim), ts[0], lo2(Dre) * tc[0]), i0 = fma2(-lo2(Dre), ts[0], lo2(Dim) * tc[0]);
            f2 r1 = fma2(hi2(Dim), ts[1], hi2(Dre) * tc[1]), i1 = fma2(-hi2(Dre), ts[1], hi2(Dim) * tc[1]);
            u4 B = {pk_h(r0[0], i0[0]), pk_h(r0[1], i0[1]), pk_h(r1[0], i1[0]), pk_h(r1[1], i1[1])};
            Xr = mma_h(Fre, B, Z4); Xi = mma_h(Fim, B, Z4);
        };
        f4 Xre[2], Xim[2];
#pragma unroll
        for (int e = 0; e < 2; ++e) fwd((u4){ld[0][e], ld[1][e], ld[2][e], ld[3][e]}, Xre[e], Xim[e]);
        f2 yr[2][2], yi[2][2];
#pragma unroll
        for (int e = 0; e < 2; ++e)
#pragma unroll
            for (int hh = 0; hh < 2; ++hh) {
                f2 wre = {bf_lo(wl[2 * hh][e]), bf_lo(wl[2 * hh + 1][e])}, wim = {bf_hi(wl[2 * hh][e]), bf_hi(wl[2 * hh + 1][e])};
                f2 xr = hh ? hi2(Xre[e]) : lo2(Xre[e]), xi = hh ? hi2(Xim[e]) : lo2(Xim[e]);
                yr[e][hh] = fma2(-xi, wim, xr * wre); yi[e][hh] = fma2(xr, wim, xi * wre);
            }
        if (cg == 0 && w == 0) {
            const u32* up = (const u32*)(scr + SCR_X) + 64 * g + m;
            f4 Mr, Mi;
            fwd((u4){up[0], up[16], up[32], up[48]}, Mr, Mi);
            f2* sx = (f2*)(scr + SCR_X + 1024);
#pragma unroll
            for (int i = 0; i < 4; ++i) sx[m + 16 * (4 * g + i)] = (f2){Mr[i], Mi[i]};
#pragma unroll
            for (int i = 0; i < 4; ++i) {
                f2 xm = sx[((f1 ? 255 : 256) - (m + 16 * (4 * g + i))) & 255];
                float ar = bf_lo(wa[i]), ai = bf_hi(wa[i]), br = bf_lo(wb[i]), bi = bf_hi(wb[i]);
                yr[0][i >> 1][i & 1] = Xre[0][i] * ar - Xim[0][i] * ai + xm[0] * br + xm[1] * bi;
                yi[0][i >> 1][i & 1] = Xre[0][i] * ai + Xim[0][i] * ar + xm[0] * bi - xm[1] * br;
            }
        }
        char* pv = scr;
#pragma unroll
        for (int e = 0; e < 2; ++e) {
            u4 Y = {pk_b(yr[e][0][0], yi[e][0][0]), pk_b(yr[e][0][1], yi[e][0][1]), pk_b(yr[e][1][0], yi[e][1][0]), pk_b(yr[e][1][1], yi[e][1][1])};
            f4 Dre = mma_b(Y, Gre, Z4), Dim = mma_b(Y, Gim, Z4);
            f2 r0 = fma2(-lo2(Dim), ts[0], lo2(Dre) * tc[0]), i0 = fma2(lo2(Dre), ts[0], lo2(Dim) * tc[0]);
            f2 r1 = fma2(-hi2(Dim), ts[1], hi2(Dre) * tc[1]), i1 = fma2(hi2(Dre), ts[1], hi2(Dim) * tc[1]);
            u4 A = {pk_b(r0[0], i0[0]), pk_b(r0[1], i0[1]), pk_b(r1[0], i1[0]), pk_b(r1[1], i1[1])};
            f4 Ere = mma_b(A, Gre, Z4), Eim = mma_b(A, Gim, Z4);
            u4 o;
#pragma unroll
            for (int i = 0; i < 4; ++i) o[i] = clean(pk_b(Ere[i], Eim[i]));
            *(u4*)(pv + (2 * w + e) * 1024 + ((m * 4 + g) ^ ((w >> 1) * 4)) * 16) = o;
        }
        BARRIER();
        ST(7);
#pragma unroll
        for (int it = 0; it < 2; ++it) {
            const int ch = tid + 256 * it;
            const u32* q = (const u32*)(pv + (ch & 1) * 4096 + (((ch >> 1) * 4) ^ ((ch & 1) * 64)));
            bst4<SC1>((u4){q[0], q[256], q[512], q[768]}, rw, ch * 16, WS_V + b * 8192);
        }
        WAITV(0);
        BARRIER();
        ST(8);
        const int vo = (g * 64 + (m >> 1)) * 8192 + (m & 1) * 16, vs = WS_V + w * 8 * 8192 + b * 32;
        u4 v0 = bld4<SC1>(rw, vo, vs), v1 = bld4<SC1>(rw, vo, vs + 32 * 8192);
        poll2(v0, v1, rw, vo, vs, vs + 32 * 8192);
        ST(9);
        u4 z0, z1;
#pragma unroll
        for (int t = 0; t < 4; ++t) {
            f4 D = mma16_b(Nb, (u2){v0[t], v1[t]}, Z4);
            z0[t] = pk_b(D[0], D[1]); z1[t] = pk_b(D[2], D[3]);
        }
        *(u4*)(scr + SCR_X + g * 2048 + w * 256 + m * 16) = z0;
        *(u4*)(scr + SCR_X + g * 2048 + 1024 + w * 256 + m * 16) = z1;
    } else {
        auto announce = [&](int which) {
            if (l < 8) __hip_atomic_store((GAS u32*)(ws + WS_FA) + (which * 2048 + l * 256 + b) * FS, tag, __ATOMIC_RELAXED, __HIP_MEMORY_SCOPE_AGENT);
            FENCE();
        };
        auto all_announced = [&](int which) {
#pragma nounroll
            for (int sp = 0; sp < SPIN_MAX; ++sp) {
                FENCE();
                u4 f;
#pragma unroll
                for (int i = 0; i < 4; ++i) f[i] = __builtin_amdgcn_raw_buffer_load_b32(rw, (l + 64 * i) * 4 * FS, WS_FA + (which * 8192 + (b >> 5) * 1024) * FS, SC1);
                if (__builtin_amdgcn_ballot_w64(f[0] != tag || f[1] != tag || f[2] != tag || f[3] != tag) == 0) break;
                __builtin_amdgcn_s_sleep(4);
            }
        };
        FENCE();
        ST(2);
        tag = __builtin_amdgcn_readfirstlane(epoch) + 1u;
        if (w == 6) WAITV(2); else WAITV(0);
        if (l < 2) __hip_atomic_store((GAS u32*)(ws + WS_FA) + ((2 * (w - 4) + l) * 256 + b) * FS, tag, __ATOMIC_RELAXED, __HIP_MEMORY_SCOPE_AGENT);
        FENCE();
        ST(18);
#pragma unroll
        for (int jj = 0; jj < 7; ++jj) {
            const int cj_ = (4 * jj) % 14, rj = 18 * jj + (4 * jj) / 14;
            w1o[jj] = (row0 + rj) * 3584 + (cch0 + cj_) * 16 + (cch0 + cj_ >= 14 ? 3584 - 224 : 0);
            PIN(w1o[jj]);
        }
        BARRIER();
        if (w == 6) {
            float re = bf_lo(sr_), im = bf_lo(si_);
            re += dpp<0x128>(re); im -= dpp<0x128>(im);
            u32 a = __builtin_bit_cast(u32, re), a2 = a, c = __builtin_bit_cast(u32, im), c2 = c;
            asm("" : "+v"(a2), "+v"(c2));
            auto pr = __builtin_amdgcn_permlane16_swap(a, a2, false, false);
            auto pi = __builtin_amdgcn_permlane16_swap(c, c2, false, false);
            const float ar = __builtin_bit_cast(float, (u32)pr[0]), br = __builtin_bit_cast(float, (u32)pr[1]);
            const float ai = __builtin_bit_cast(float, (u32)pi[0]), bi = __builtin_bit_cast(float, (u32)pi[1]);
            const int wo = WS_WA + ((b >> 6) * 16 + (b & 15)) * 16 + ((b >> 4) & 3) * 4;
            if (l < 8) {
                __builtin_amdgcn_raw_buffer_store_b32(clean(pk_b(0.25f * (ar + br), 0.25f * (ai + bi))), rw, l * 1024, wo, SC1);
                __builtin_amdgcn_raw_buffer_store_b32(clean(pk_b(0.25f * (ar - br), 0.25f * (ai - bi))), rw, l * 1024, wo + 8192, SC1);
            }
        }
        const int f = f1 + 8 * (tid & 255), k0 = 16 * cg - 8 * (cg & 1);
        const int e = mir ? ((2048 - f) & 2047) * 514 + (504 - k0) * 2 : f * 514 + k0 * 2;
        consts0();
        __builtin_amdgcn_s_sleep(GA_SLEEP);
        fr = bld4<0>(rr, e, 0);
        FENCE();
        __builtin_amdgcn_s_sleep(GB_SLEEP);
        if (w == 7) all_announced(0);
        fi = bld4<0>(ri, e, 0);
        xl[0] = *(const u2*)((const char*)x + xoff); xl[1] = *(const u2*)((const char*)x + xoff + 512);
        FENCE();
        ST(4);
        BARRIER();
        {
            u32 o[8];
#pragma unroll
            for (int c = 0; c < 8; ++c)
                o[c] = mir ? __builtin_amdgcn_perm(fi[3 - (c >> 1)], fr[3 - (c >> 1)], (c & 1) ? 0x05040100u : 0x07060302u) ^ 0x80000000u
                           : __builtin_amdgcn_perm(fi[c >> 1], fr[c >> 1], (c & 1) ? 0x07060302u : 0x05040100u);
            char* wp = scr + SCR_S + 8192 + (tid & 255) * 32;
            *(u4*)(wp + (m & 8) * 2) = (u4){o[0], o[1], o[2], o[3]};
            *(u4*)(wp + ((m & 8) * 2 ^ 16)) = (u4){o[4], o[5], o[6], o[7]};
        }
#pragma unroll
        for (int i = 0; i < 2; ++i) bst4<SC1>(SENT4, rw, l * 16 + i * 1024, WS_V + b * 8192 + (w - 4) * 2048);
        FENCE();
        consts3();
        WAITV(0);
        ST(5);
        BARRIER();
        if (w == 7) announce(1);
#pragma unroll
        for (int i = 0; i < 2; ++i) bst4<SC1>(SENT4, rw, (l >> 3) * 256 + (l & 7) * 16 + i * 2048, WS_LN2 + (b >> 1) * 16384 + (b & 1) * 128 + (w - 4) * 4096);
        bg(0, BG5);
        WAITV(BG5);
        ST(6);
        BARRIER();
        if (w == 7) all_announced(1);
        else __builtin_amdgcn_s_sleep(3);
        bg(BG5, BG5b);
        ST(7);
        BARRIER();
        __builtin_amdgcn_s_sleep(BG_DELAY);
        bg(BG5b, BG6);
    }
    BARRIER();
    {
        const u32* zp = (const u32*)(scr + SCR_X + w * 1024 + zo);
        u4 A = {zp[0], zp[16], zp[32], zp[48]};
        f4 Dre = mma_b(A, Gre3, Z4), Dim = mma_b(A, Gim3, Z4), Hre = mma_b(A, SGre, Z4), Him = mma_b(A, SGim, Z4);
        if (g == 0) { Dre[0] = Hre[0]; Dim[0] = Him[0]; }
        f2 r0 = fma2(-lo2(Dim), tws[0], lo2(Dre) * twc[0]), i0 = fma2(lo2(Dre), tws[0], lo2(Dim) * twc[0]);
        f2 r1 = fma2(-hi2(Dim), tws[1], hi2(Dre) * twc[1]), i1 = fma2(hi2(Dre), tws[1], hi2(Dim) * twc[1]);
        u4 A2 = {pk_b(r0[0], i0[0]), pk_b(r0[1], i0[1]), pk_b(r1[0], i1[0]), pk_b(r1[1], i1[1])};
        f2 h[4]; u2 hp[2];
#pragma unroll
        for (int t = 0; t < 2; ++t) {
            f4 Yv = mma_b(A2, GA[t], Z4);
            u32 h0 = pk_b2(bf_2(xl[t][0]) + bf_2(pk_b(Yv[0], Yv[1]))), h1 = pk_b2(bf_2(xl[t][1]) + bf_2(pk_b(Yv[2], Yv[3])));
            hp[t] = (u2){h0, h1};
            h[2 * t] = bf_2(h0); h[2 * t + 1] = bf_2(h1);
        }
        f2 s2 = (h[0] + h[1]) + (h[2] + h[3]);
        const float mean = red_lanes<true>(s2[0] + s2[1]) * (1.0f / 512);
        f2 q2 = {0.f, 0.f};
#pragma unroll
        for (int j = 0; j < 4; ++j) { f2 d = h[j] - mean; q2 = fma2(d, d, q2); }
        const float inv = ln_inv(red_lanes<true>(q2[0] + q2[1]) * (1.0f / 512));
        const float mb = rbf(mean);
#pragma unroll
        for (int t = 0; t < 2; ++t)
            *(u2*)(scr + ((8 * t + (m >> 1)) * 4 + g) * 128 + (w ^ (m >> 1)) * 16 + (m & 1) * 8) =
                (u2){clean(pk_b2(bf_2(pk_b2(h[2 * t] - mb)) * inv)), clean(pk_b2(bf_2(pk_b2(h[2 * t + 1] - mb)) * inv))};
        WAITV(0);
        BARRIER();
        u4 lo = *(const u4*)(scr + (tid ^ ((tid >> 5) & 7)) * 16);
        bst4<SC1>(lo, rw, (tid >> 3) * 256 + (tid & 7) * 16, WS_LN2 + (b >> 1) * 16384 + (b & 1) * 128);
        FENCE();
        if (aw) WAITV(0); else __builtin_amdgcn_s_sleep(8);
        bst2<SC1>(hp[0], ro, xoff, 0); bst2<SC1>(hp[1], ro, xoff + 512, 0);
        FENCE();
        ST(10);
    }
    auto stage_out = [&](int r) {
        w2_read(W[r], LDS_SCR + (w & 3) * 8192 + (r & 1) * 4096);
        asm volatile("s_waitcnt lgkmcnt(0)" ::: "memory");
        PIN(W[r][0]); PIN(W[r][1]); PIN(W[r][2]); PIN(W[r][3]);
        if (r + 2 < 7) w2_glds(pw, r + 2, lds0 + LDS_SCR + (w & 3) * 8192 + (r & 1) * 4096);
    };
    auto publish = [&]() {
        u32 prev = 0;
        if (l == 0) prev = __hip_atomic_fetch_add(hw + 4, 1u, __ATOMIC_RELAXED, __HIP_MEMORY_SCOPE_WORKGROUP);
        if (__builtin_amdgcn_readfirstlane(prev) == 7u && l < 2)
            __hip_atomic_store(fl + (l ? 4096 + (b >> 1) * 32 + (b & 1) : (mt1 * 8 + (nt1 >> 1)) * 32 + (nt1 & 1)), tag, __ATOMIC_RELAXED, __HIP_MEMORY_SCOPE_AGENT);
        FENCE();
    };
    {
        const int tg = w & 3;
        char* xch = smem + XCH + tg * 3072;
        const int hoff = WS_H1 + (mt1 * 8 + 2 * tg) * (56 * 1024) + (nt1 >> 1) * 7168;
        if (__builtin_expect(aw, 1)) {
            const int toff = WS_LN2 + (mt1 * 8 + 2 * tg) * 16384;
            LAS char* wb0 = (LAS char*)smem + (4 * g + (m >> 2)) * 224 + (m & 3) * 8;
            LAS char* wb[4];
            int tq[4];
#pragma unroll
            for (int q = 0; q < 4; ++q) { const int qq = (q + rq) & 3; wb[q] = wb0 + qq * 28672; tq[q] = toff + qq * 4096; }
            u4 T[2][16], A[2][7];
            auto ldt = [&](int kk) {
#pragma unroll
                for (int tt = 0; tt < 2; ++tt) T[tt][kk] = bld4<SC1>(rw, l * 16 + (kk & 3) * 1024, tq[kk >> 2] + tt * 16384);
            };
#pragma unroll
            for (int i = 0; i < 7; ++i) A[0][i] = ldw1(wb[0], 0, i);
            if (l == 0) __hip_atomic_fetch_add(hw + 5, 1u, __ATOMIC_RELAXED, __HIP_MEMORY_SCOPE_WORKGROUP);
            ST(11);
#pragma unroll
            for (int kk = 0; kk < G1_DEPTH; ++kk) { ldt(kk); SCHED(); }
            f4 acc[2][7];
#pragma unroll
            for (int tt = 0; tt < 2; ++tt)
#pragma unroll
                for (int i = 0; i < 7; ++i) acc[tt][i] = Z4;
#pragma unroll
            for (int kk = 0; kk < 16; ++kk) {
                if ((kk & 1) == 0) {
                    if (__builtin_expect(any_sent(umax(umax(max4(T[0][kk]), max4(T[1][kk])), umax(max4(T[0][kk + 1]), max4(T[1][kk + 1])))), 0)) {
                        wait_tok(T[0][kk], T[1][kk], rw, l * 16, tq[kk >> 2] + (kk & 3) * 1024);
                        wait_tok(T[0][kk + 1], T[1][kk + 1], rw, l * 16, tq[kk >> 2] + ((kk + 1) & 3) * 1024);
                    }
                    if (kk == 0) ST(12);
                    if (kk == (BG6 / 7) * 4 - 2) lds_wait(hw + 5, 8u);
                }
                if (kk + G1_DEPTH < 16) ldt(kk + G1_DEPTH);
                if (kk < 15) {
#pragma unroll
                    for (int i = 0; i < 7; ++i) A[(kk + 1) & 1][i] = ldw1(wb[(kk + 1) >> 2], (kk + 1) & 3, i);
                }
#pragma unroll
                for (int i = 0; i < 7; ++i)
#pragma unroll
                    for (int tt = 0; tt < 2; ++tt) acc[tt][i] = mma_b(A[kk & 1][i], T[tt][kk], acc[tt][i]);
#pragma unroll
                for (int i = 0; i < 14; ++i) {
                    __builtin_amdgcn_sched_group_barrier(0x8, 1, 0);
                    if (i == 1 || i == 7) __builtin_amdgcn_sched_group_barrier(0x20, 1, 0);
                    __builtin_amdgcn_sched_group_barrier(0x100, 1, 0);
                    if ((kk & 1) && i >= 3) __builtin_amdgcn_sched_group_barrier(0x2, 1, 0);
                }
                if (kk & 1) {} else SCHED();
            }
#pragma unroll
            for (int q = 0; q < 3; ++q) {
                u2 p0 = pk4(acc[1][2 * q]), p1 = pk4(acc[1][2 * q + 1]);
                *(u4*)(xch + q * 1024 + l * 16) = (u4){p0[0], p0[1], p1[0], p1[1]};
            }
            lds_set(hw + tg, 1u);
            ST(13);
#pragma unroll
            for (int q = 0; q < 3; ++q) {
                u2 p0 = pk4(acc[0][2 * q]), p1 = pk4(acc[0][2 * q + 1]);
                bst4<SC1>(silu_pk4((u4){p0[0], p0[1], p1[0], p1[1]}), rw, l * 16, hoff + ((nt1 & 1) * 3 + q) * 1024);
            }
#pragma unroll
            for (int tt = 0; tt < 2; ++tt) { u2 p = pk4(acc[tt][6]); bst2<SC1>(silu_pk2(p[0], p[1]), rw, l * 8, hoff + tt * (56 * 1024) + 6144 + (nt1 & 1) * 512); }
        } else {
            bg(BG6, 28);
            WAITV(0);
            if (l == 0) __hip_atomic_fetch_add(hw + 5, 1u, __ATOMIC_RELAXED, __HIP_MEMORY_SCOPE_WORKGROUP);
            __builtin_amdgcn_s_sleep(8);
            lds_wait(hw + 5, 8u);
            w2_glds(pw, 0, lds0 + LDS_SCR + tg * 8192);
            w2_glds(pw, 1, lds0 + LDS_SCR + tg * 8192 + 4096);
#pragma unroll
            for (int r = 0; r < B_R; ++r) { WAITV(4); stage_out(r); }
            ST(12);
            lds_wait(hw + tg, 1u);
            ST(13);
#pragma unroll
            for (int q = 0; q < 3; ++q)
                bst4<SC1>(silu_pk4(*(const u4*)(xch + q * 1024 + l * 16)), rw, l * 16, hoff + 56 * 1024 + ((nt1 & 1) * 3 + q) * 1024);
        }
        ST(14);
        WAITV(0);
        publish();
        ST(21);
        if (__builtin_expect(aw, 1)) {
            lds_wait(hw + 4, 8u);
#pragma unroll
            for (int ks = 0; ks < 7; ++ks) w2_glds(pw, ks, lds0 + tg * 28672 + ks * 4096);
        } else {
            stage_out(B_R);
#pragma unroll
            for (int r = B_R + 1; r < 7; ++r) { if (r + 1 < 7) WAITV(4); else WAITV(0); stage_out(r); }
        }
    }
    {
        ST(15);
        const int rot = w & 3;
        if (aw) {
            flag_wait(fl + ((l & 4) ? 4096 + (grp * 8 + th * 4 + rot) * 32 : (grp * 8 + (pw ^ ((l & 2) * 2))) * 32) + (l & 1), tag);
            lds_set(hw + 8 + rot, tag);
        } else {
#pragma nounroll
            for (int sp = 0; sp < SPIN_MAX; ++sp) {
                if (__builtin_amdgcn_readfirstlane(__hip_atomic_load(hw + 8 + rot, __ATOMIC_RELAXED, __HIP_MEMORY_SCOPE_WORKGROUP)) == tag) break;
                __builtin_amdgcn_s_sleep(1);
            }
            FENCE();
        }
        ST(16);
        const int toff = WS_H1 + (grp * 8 + th * 4) * (56 * 1024) + pw * 7168;
        int to[4];
#pragma unroll
        for (int tt = 0; tt < 4; ++tt) to[tt] = toff + ((tt + rot) & 3) * (56 * 1024);
        u4 T[7][4];
        auto ldtok = [&](int ks) {
#pragma unroll
            for (int tt = 0; tt < 4; ++tt) {
                if (ks < 6) T[ks][tt] = bld4<SC1>(rw, l * 16, to[tt] + ko[ks] * 1024);
                else {
                    u2 a = bld2<SC1>(rw, l * 8 + 2048, to[tt] + 4096), c = bld2<SC1>(rw, l * 8 + 2560, to[tt] + 4096);
                    T[ks][tt] = (u4){a[0], a[1], c[0], c[1]};
                }
            }
            SCHED();
        };
#pragma unroll
        for (int ks = 0; ks < G2_DEPTH; ++ks) ldtok(ks);
        if (aw) {
#pragma unroll
            for (int ks = 0; ks < 7; ++ks) w2_read(W[ks], rot * 28672 + ks * 4096);
            asm volatile("s_waitcnt lgkmcnt(0)" ::: "memory");
        }
        if (l == 0) __hip_atomic_fetch_add(hw + 7, 1u, __ATOMIC_RELAXED, __HIP_MEMORY_SCOPE_WORKGROUP);
        const int rho = grp * 128 + th * 64 + rot * 16 + m;
        const int ooff = (256 * (rho & 7) + (rho >> 3)) * 1024 + fb * 128 + (w >> 2) * 64 + g * 16;
        const u4 hres = bld4<SC1>(ro, ooff, 0);
        SCHED();
        f4 acc[4][4];
#pragma unroll
        for (int tt = 0; tt < 4; ++tt)
#pragma unroll
            for (int t = 0; t < 4; ++t) acc[tt][t] = Z4;
#pragma unroll
        for (int ks = 0; ks < 7; ++ks) {
            if (ks + G2_DEPTH < 7) { PIN(T[ks][0]); ldtok(ks + G2_DEPTH); }
#pragma unroll
            for (int tt = 0; tt < 4; ++tt)
#pragma unroll
                for (int t = 0; t < 4; ++t) {
                    acc[tt][t] = mma_b(W[ks][t], T[ks][tt], acc[tt][t]);
                }
            SCHED();
        }
        ST(17);
        lds_wait(hw + 7, 8u);
        char* dp = smem + w * 16384 + l * 16;
#pragma unroll
        for (int tt = 1; tt < 4; ++tt)
#pragma unroll
            for (int t = 0; t < 4; ++t) *(f4*)(dp + (((tt + rot) & 3) * 4 + t) * 1024) = acc[tt][t];
        f4 s0, s1;
        if (aw) { *(f4*)(dp + (rot * 4 + 2) * 1024) = acc[0][2]; *(f4*)(dp + (rot * 4 + 3) * 1024) = acc[0][3]; s0 = acc[0][0]; s1 = acc[0][1]; }
        else { *(f4*)(dp + (rot * 4) * 1024) = acc[0][0]; *(f4*)(dp + (rot * 4 + 1) * 1024) = acc[0][1]; s0 = acc[0][2]; s1 = acc[0][3]; }
        BARRIER();
        f4 r0[7], r1[7];
#pragma unroll
        for (int j = 1; j < 8; ++j) {
            const char* rp = smem + (((w + j) & 7) * 16 + rot * 4 + (w >> 2) * 2) * 1024 + l * 16;
            r0[j - 1] = *(const f4*)rp; r1[j - 1] = *(const f4*)(rp + 1024);
        }
        SCHED();
        s0 = ((s0 + r0[0]) + (r0[1] + r0[2])) + ((r0[3] + r0[4]) + (r0[5] + r0[6]));
        s1 = ((s1 + r1[0]) + (r1[1] + r1[2])) + ((r1[3] + r1[4]) + (r1[5] + r1[6]));
        u2 p0 = pk4(s0), p1 = pk4(s1);
        bst4<NT>((u4){pk_b2(bf_2(hres[0]) + bf_2(p0[0])), pk_b2(bf_2(hres[1]) + bf_2(p0[1])),
                           pk_b2(bf_2(hres[2]) + bf_2(p1[0])), pk_b2(bf_2(hres[3]) + bf_2(p1[1]))}, ro, ooff, 0);
    }
    ST_END(ws, 0);
}

extern "C" void kernel_launch(void* const* d_in, const int* in_sizes, int n_in, void* d_out, int out_size,
                              void* d_ws, size_t ws_size, hipStream_t stream) {
    (void)hipFuncSetAttribute((const void*)k_fused, hipFuncAttributeMaxDynamicSharedMemorySize, LDS_B);
    k_fused<<<256, 512, LDS_B, stream>>>((const uint16_t*)d_in[0], (const uint16_t*)d_in[1], (const uint16_t*)d_in[2],
                                         (const uint16_t*)d_in[4], (const uint16_t*)d_in[5], (uint16_t*)d_out, (char*)d_ws);
}
```

```cpp
#define LPOS 1
#define BGA 10
#define BGB 10
#define BGC 21
#define BG_DELAY 14
#include <hip/hip_runtime.h>
#include <hip/hip_bf16.h>
#include <stdint.h>

typedef _Float16 h8 __attribute__((ext_vector_type(8)));
typedef _Float16 h4 __attribute__((ext_vector_type(4)));
typedef _Float16 h2 __attribute__((ext_vector_type(2)));
typedef __bf16 b8 __attribute__((ext_vector_type(8)));
typedef __bf16 b2 __attribute__((ext_vector_type(2)));
typedef short s4 __attribute__((ext_vector_type(4)));
typedef float f4 __attribute__((ext_vector_type(4)));
typedef float f2 __attribute__((ext_vector_type(2)));
typedef unsigned u32;
typedef u32 u4 __attribute__((ext_vector_type(4)));
typedef u32 u2 __attribute__((ext_vector_type(2)));
#define LAS __attribute__((address_space(3)))
#define GAS __attribute__((address_space(1)))
#define DI __device__ __forceinline__
#define FENCE() asm volatile("" ::: "memory")
#define PIN(x) asm volatile("" : "+v"(x))
#define SCHED() __builtin_amdgcn_sched_barrier(0)
#define WAITV(n) asm volatile("s_waitcnt vmcnt(%0)" :: "n"(n) : "memory")
#define BARRIER() do { asm volatile("s_waitcnt lgkmcnt(0)" ::: "memory"); __builtin_amdgcn_s_barrier(); FENCE(); } while (0)
constexpr int SC1 = 16, NT = 2;
constexpr u32 SENT = 0xffffffffu;
constexpr int SPIN_MAX = 1 << 20;

DI u32 pk_h(float a, float b) { f2 v = {a, b}; h2 r = __builtin_convertvector(v, h2); return __builtin_bit_cast(u32, r); }
DI u32 pk_b(float a, float b) { f2 v = {a, b}; b2 r = __builtin_convertvector(v, b2); return __builtin_bit_cast(u32, r); }
DI float bf_lo(u32 v) { return __builtin_bit_cast(float, v << 16); }
DI float bf_hi(u32 v) { return __builtin_bit_cast(float, v & 0xffff0000u); }
DI f2 bf_2(u32 v) { return (f2){bf_lo(v), bf_hi(v)}; }
DI u32 pk_b2(f2 v) { return pk_b(v[0], v[1]); }
DI float rbf(float a) { return bf_lo(pk_b(a, a)); }
DI float cosr(float rev) { return __builtin_amdgcn_cosf(rev); }
DI float sinr(float rev) { return __builtin_amdgcn_sinf(rev); }
DI f2 lo2(f4 v) { return __builtin_shufflevector(v, v, 0, 1); }
DI f2 hi2(f4 v) { return __builtin_shufflevector(v, v, 2, 3); }
DI f2 fma2(f2 a, f2 b, f2 c) { return __builtin_elementwise_fma(a, b, c); }
DI u32 umin(u32 a, u32 b) { return a < b ? a : b; }
DI u32 umax(u32 a, u32 b) { return a > b ? a : b; }
DI u32 clean(u32 v) { return umin(v, SENT - 1); }
DI u32 max4(u4 v) { return umax(umax(umax(v[0], v[1]), v[2]), v[3]); }
#define Z4 ((f4){0, 0, 0, 0})
DI f4 mma_h(u4 a, u4 b, f4 c) { return __builtin_amdgcn_mfma_f32_16x16x32_f16(__builtin_bit_cast(h8, a), __builtin_bit_cast(h8, b), c, 0, 0, 0); }
DI f4 mma_b(u4 a, u4 b, f4 c) { return __builtin_amdgcn_mfma_f32_16x16x32_bf16(__builtin_bit_cast(b8, a), __builtin_bit_cast(b8, b), c, 0, 0, 0); }
DI f4 mma16_b(u2 a, u2 b, f4 c) { return __builtin_amdgcn_mfma_f32_16x16x16bf16_1k(__builtin_bit_cast(s4, a), __builtin_bit_cast(s4, b), c, 0, 0, 0); }
DI f4 mma16_h(u2 a, u2 b, f4 c) { return __builtin_amdgcn_mfma_f32_16x16x16f16(__builtin_bit_cast(h4, a), __builtin_bit_cast(h4, b), c, 0, 0, 0); }
typedef __amdgpu_buffer_rsrc_t rsrc_t;
DI rsrc_t mkrs(const void* p, u32 bytes) { return __builtin_amdgcn_make_buffer_rsrc((void*)p, 0, bytes, 0x00020000); }
template <int AUX> DI u4 bld4(rsrc_t r, int v, int s) { return __builtin_amdgcn_raw_buffer_load_b128(r, v, s, AUX); }
template <int AUX> DI u2 bld2(rsrc_t r, int v, int s) { return __builtin_amdgcn_raw_buffer_load_b64(r, v, s, AUX); }
template <int AUX> DI void bst4(u4 d, rsrc_t r, int v, int s) {
    __builtin_amdgcn_raw_buffer_store_b128(d, r, v, s, AUX);
    asm volatile("s_nop 1" :: "v"(d));
}
template <int AUX> DI void bst2(u2 d, rsrc_t r, int v, int s) { __builtin_amdgcn_raw_buffer_store_b64(d, r, v, s, AUX); }

template <int CTRL> DI float dpp(float v) {
    return __builtin_bit_cast(float, __builtin_amdgcn_update_dpp(0, __builtin_bit_cast(int, v), CTRL, 0xf, 0xf, true));
}
template <bool ALL> DI float red_lanes(float v) {
    v += dpp<0xB1>(v);
    if (ALL) v += dpp<0x4E>(v);
    v += dpp<0x124>(v); v += dpp<0x128>(v);
    u32 x = __builtin_bit_cast(u32, v), y = x;
    asm("" : "+v"(y));
    auto r = __builtin_amdgcn_permlane16_swap(x, y, false, false);
    v = __builtin_bit_cast(float, (u32)r[0]) + __builtin_bit_cast(float, (u32)r[1]);
    x = __builtin_bit_cast(u32, v); y = x;
    asm("" : "+v"(y));
    auto q = __builtin_amdgcn_permlane32_swap(x, y, false, false);
    return __builtin_bit_cast(float, (u32)q[0]) + __builtin_bit_cast(float, (u32)q[1]);
}
DI float ln_inv(float var) { return __builtin_amdgcn_rcpf(rbf(__builtin_amdgcn_sqrtf(rbf(rbf(var) + 1e-5f)))); }

#define ST_DECL
#define ST(i)
#define ST_END(ws, kid)

#define FS 1
constexpr u32 WS_U = 0;
constexpr u32 WS_V = WS_U + (2u << 20);
constexpr u32 WS_LN2 = WS_V + (2u << 20);
constexpr u32 WS_WA = WS_LN2 + (2u << 20);
constexpr u32 WS_WB = WS_WA + 8192u;
constexpr u32 WS_UX = WS_WB + 8192u;
constexpr u32 WS_FA = WS_UX + 8192u;
constexpr u32 WS_FLAG = WS_FA + 16384u * FS;
constexpr u32 WS_EP = WS_FLAG + 32768u;
constexpr u32 WS_H1 = WS_WA + (512u << 10);
constexpr u32 WS_END = WS_H1 + 7340032u;

#define G_SLEEP 16
#define GA_SLEEP 4
#define GB_SLEEP 8
#define A_SLEEP 0
#define VPOS 0
#define PRO_SLEEP 4
constexpr int BG5 = BGA, BG5b = BGB, BG6 = BGC;
constexpr int G1_DEPTH = 10, G2_DEPTH = 3;
constexpr int B_R = 5;
constexpr int CODE_LINES = 290, CODE_PF = 10;
constexpr int LDS_SCR = 114688;
constexpr int SCR_X = 8448;
constexpr int SCR_S = SCR_X + 20480;
constexpr int SCR_F = 49024;
constexpr int XCH = 147456;
constexpr int LDS_B = 163840;

DI void glds16(const void* sbase, u32 voff, u32 lds_dst) {
    asm volatile("s_mov_b32 m0, %2\n\ts_nop 0\n\tglobal_load_lds_dwordx4 %0, %1" :: "v"(voff), "s"(sbase), "s"(lds_dst) : "memory");
}
DI bool any_sent(u32 v) { return __builtin_amdgcn_ballot_w64(v == SENT) != 0; }
DI void poll2(u4& a, u4& c, rsrc_t rw, int vo, int sa, int sc) {
#pragma nounroll
    for (int sp = 0; sp < SPIN_MAX; ++sp) {
        const bool ba = max4(a) == SENT, bc = max4(c) == SENT;
        if (__builtin_amdgcn_ballot_w64(ba || bc) == 0) break;
        __builtin_amdgcn_s_sleep(1);
        FENCE();
        if (ba) a = bld4<SC1>(rw, vo, sa);
        if (bc) c = bld4<SC1>(rw, vo, sc);
    }
}
DI void wait_tok(u4& t0, u4& t1, rsrc_t rw, int vo, int so) {
    if (any_sent(umax(max4(t0), max4(t1)))) {
#pragma nounroll
        for (int sp = 0; sp < SPIN_MAX; ++sp) {
            FENCE();
            t0 = bld4<SC1>(rw, vo, so); t1 = bld4<SC1>(rw, vo, so + 16384);
            if (!any_sent(umax(max4(t0), max4(t1)))) break;
        }
    }
}
DI u4 ldw1(LAS char* wb, int kk, int ft) {
    s4 a0 = __builtin_amdgcn_ds_read_tr16_b64_v4i16((LAS s4*)(wb + kk * 32 * 224 + ft * 32));
    s4 a1 = __builtin_amdgcn_ds_read_tr16_b64_v4i16((LAS s4*)(wb + (kk * 32 + 16) * 224 + ft * 32));
    u2 x0 = __builtin_bit_cast(u2, a0), x1 = __builtin_bit_cast(u2, a1);
    return (u4){x0[0], x0[1], x1[0], x1[1]};
}
DI u2 pk4(f4 a) { return (u2){pk_b(a[0], a[1]), pk_b(a[2], a[3])}; }
DI u2 silu_pk2(u32 p0, u32 p1) {
    u32 o0, o1; float a0, a1, a2, a3, t0, t1, t2, t3;
    asm("v_lshlrev_b32 %2, 16, %10\n\tv_and_b32 %3, 0xffff0000, %10\n\tv_lshlrev_b32 %4, 16, %11\n\tv_and_b32 %5, 0xffff0000, %11\n\t"
        "v_mul_f32 %6, %12, %2\n\tv_mul_f32 %7, %12, %3\n\tv_mul_f32 %8, %12, %4\n\tv_mul_f32 %9, %12, %5\n\t"
        "v_exp_f32 %6, %6\n\tv_exp_f32 %7, %7\n\tv_exp_f32 %8, %8\n\tv_exp_f32 %9, %9\n\t"
        "v_add_f32 %6, 1.0, %6\n\tv_add_f32 %7, 1.0, %7\n\tv_add_f32 %8, 1.0, %8\n\tv_add_f32 %9, 1.0, %9\n\t"
        "v_rcp_f32 %6, %6\n\tv_rcp_f32 %7, %7\n\tv_rcp_f32 %8, %8\n\tv_rcp_f32 %9, %9\n\t"
        "v_mul_f32 %2, %2, %6\n\tv_mul_f32 %3, %3, %7\n\tv_mul_f32 %4, %4, %8\n\tv_mul_f32 %5, %5, %9\n\t"
        "v_cvt_pk_bf16_f32 %0, %2, %3\n\tv_cvt_pk_bf16_f32 %1, %4, %5"
        : "=&v"(o0), "=&v"(o1), "=&v"(a0), "=&v"(a1), "=&v"(a2), "=&v"(a3), "=&v"(t0), "=&v"(t1), "=&v"(t2), "=&v"(t3)
        : "v"(p0), "v"(p1), "s"(-1.4426950408889634f));
    return (u2){o0, o1};
}
DI u4 silu_pk4(u4 p) { u2 a = silu_pk2(p[0], p[1]), b = silu_pk2(p[2], p[3]); return (u4){a[0], a[1], b[0], b[1]}; }
DI void lds_set(LAS u32* p, u32 v) {
    asm volatile("s_waitcnt lgkmcnt(0)" ::: "memory");
    __hip_atomic_store(p, v, __ATOMIC_RELAXED, __HIP_MEMORY_SCOPE_WORKGROUP);
}
DI void lds_wait(LAS u32* p, u32 v) {
#pragma nounroll
    for (int sp = 0; sp < SPIN_MAX; ++sp) {
        if (__builtin_amdgcn_readfirstlane(__hip_atomic_load(p, __ATOMIC_RELAXED, __HIP_MEMORY_SCOPE_WORKGROUP)) >= v) break;
        __builtin_amdgcn_s_sleep(1);
    }
    FENCE();
}
DI void flag_wait(GAS u32* p, u32 tag) {
#pragma nounroll
    for (int sp = 0; sp < SPIN_MAX; ++sp) {
        if (__builtin_amdgcn_ballot_w64(__hip_atomic_load(p, __ATOMIC_RELAXED, __HIP_MEMORY_SCOPE_AGENT) != tag) == 0) break;
        __builtin_amdgcn_s_sleep(2);
    }
    FENCE();
}
#define SENT4 ((u4){SENT, SENT, SENT, SENT})

__global__ __launch_bounds__(512, 2) void k_fused(const uint16_t* x, const uint16_t* wr, const uint16_t* wi, const uint16_t* w1,
                                                  const uint16_t* w2, uint16_t* out, char* ws) {
    extern __shared__ __attribute__((aligned(16))) char smem[];
    const int tid = threadIdx.x, l = tid & 63, g = l >> 4, m = l & 15;
    const int w = __builtin_amdgcn_readfirstlane(tid >> 6);
    const int b = (blockIdx.x & 7) * 32 + (blockIdx.x >> 3);
    const int mt1 = (b >> 6) * 4 + ((b >> 3) & 3), nt1 = ((b >> 5) & 1) * 8 + (b & 7);
    const int grp = b >> 4, th = (b >> 3) & 1, fb = b & 7;
    const rsrc_t rw = mkrs(ws, WS_END), ro = mkrs(out, 2097152);
    char* scr = smem + LDS_SCR;
    LAS u32* hw = (LAS u32*)((LAS char*)smem + LDS_SCR + SCR_F);
    const u32 lds0 = (u32)(uintptr_t)(LAS char*)smem;
    const float mf = (float)m, gf = (float)g, sg = (m & 1) ? -1.0f : 1.0f;
    const bool aw = w < 4;
    ST_DECL;
    unsigned long long pc0;
    asm volatile("s_getpc_b64 %0" : "=s"(pc0));
    if (tid >= 448 && l < CODE_PF)
        asm volatile("s_mov_b32 m0, %2\n\ts_nop 0\n\tglobal_load_lds_dword %0, %1"
                     :: "v"(umin((blockIdx.x >> 3) * CODE_PF + l, CODE_LINES - 1) * 128), "s"(pc0), "s"(lds0 + LDS_SCR + SCR_F + 64) : "memory");
    const u32 epoch = __hip_atomic_load((GAS u32*)(ws + WS_EP), __ATOMIC_RELAXED, __HIP_MEMORY_SCOPE_AGENT);
    FENCE();
    u32 xd[8];
    const int f1 = b >> 5, cg = b & 31;
    const bool mir = cg >= 16;
    const rsrc_t rr = mkrs(wr, 1052672), ri = mkrs(wi, 1052672);
    u32 sr_, si_;
    if (aw) {
        __builtin_amdgcn_s_sleep(A_SLEEP);
        const u32* xr = (const u32*)(x + (256 * (2 * w + ((m >> 1) & 1)) + b) * 512) + 8 * g + 2 * (m >> 2) + (m & 1);
#pragma unroll
        for (int j = 0; j < 8; ++j) xd[j] = xr[32 * j];
    } else {
        bst4<SC1>(SENT4, rw, l * 16, WS_U + b * 8192 + (w - 4) * 2048); bst4<SC1>(SENT4, rw, l * 16 + 1024, WS_U + b * 8192 + (w - 4) * 2048);
        __builtin_amdgcn_raw_buffer_store_b32(SENT, rw, l < 6 ? (l >> 1) * 8192 + (l & 1) * 1024 : 0x7ffffff0,
                                              WS_WA + (w - 4) * 2048 + ((b >> 6) * 16 + (b & 15)) * 16 + ((b >> 4) & 3) * 4, SC1);
        FENCE();
        if (w == 7 && VPOS == 0) {
#pragma unroll
            for (int i = 0; i < 8; ++i) bst4<SC1>(SENT4, rw, l * 16 + (i & 3) * 1024, WS_V + b * 8192 + (i >> 2) * 4096);
        }
        if (w == 6) {
            const int c = (l >> 3) & 3, f_ = 8 * b + (l & 7);
            const int e1 = ((c & 1) ? (2048 - f_) & 2047 : f_) * 514 + (c >> 1) * 512;
            sr_ = __builtin_amdgcn_raw_buffer_load_b16(rr, e1, 0, 0); si_ = __builtin_amdgcn_raw_buffer_load_b16(ri, e1, 0, 0);
        }
    }
    FENCE();
    const int zo = (64 * g + (g < 2 ? m : (m + 15) & 15)) * 4;
    const int rq = ((b >> 3) + b) & 3;
    const int row0 = (tid & 255) / 14, cch0 = (tid & 255) - row0 * 14;
    const char* wsrc = (const char*)w1 + nt1 * 224;
    u32 w1o[7];
    auto w1_burst = [&](int i0, int n) {
#pragma unroll
        for (int i = i0; i < i0 + n; ++i) {
            const int q = (i / 7 + rq) & 3, j = i % 7;
            glds16(wsrc + q * (128 * 3584), w1o[j], lds0 + (w - 4) * 1024 + q * 28672 + j * 4096);
        }
    };
    const int xoff = (256 * w + b) * 1024 + m * 32 + g * 8;
    u2 xl[2];
    float c16[4], s16[4];
    f2 twc[2], tws[2];
    u4 Gre3, Gim3, SGre, SGim, GA[2];
    auto consts0 = [&]() {
#pragma unroll
        for (int u = 0; u < 4; ++u) {
            float a = (4.0f * gf + u) * (mf * (1.0f / 16));
            c16[u] = cosr(a); s16[u] = sinr(a);
            twc[u >> 1][u & 1] = cosr(a * (1.0f / 32)); tws[u >> 1][u & 1] = sinr(a * (1.0f / 32));
        }
    };
    auto consts3 = [&]() {
#pragma unroll
        for (int u = 0; u < 4; ++u) {
            const float k = 4.0f * gf + u;
            const bool k0 = u == 0 && g == 0;
            float cc = c16[u], ss = s16[u];
            Gre3[u] = pk_b(cc, -ss); Gim3[u] = pk_b(ss, cc);
            float a2 = (2.0f * k + 1.0f) * (mf * (1.0f / 32));
            u32 sp = pk_b(g < 2 ? 2.f * cc : 2.f * cosr(a2), g < 2 ? -2.f * ss : -2.f * sinr(a2));
            SGre[u] = k0 ? pk_b(1.0f, sg) : (g < 2 ? sp : 0u);
            SGim[u] = g < 2 ? 0u : sp;
            float a4 = k * (mf * (1.0f / 32)), c4 = 2.f * cosr(a4), s4_ = -2.f * sinr(a4);
            GA[0][u] = k0 ? pk_b(1.0f, sg) : pk_b(c4, s4_);
            GA[1][u] = k0 ? pk_b(1.0f, sg) : ((u & 1) ? pk_b(-c4, -s4_) : pk_b(c4, s4_));
        }
        PIN(Gre3); PIN(Gim3); PIN(SGre); PIN(SGim); PIN(GA[0]); PIN(GA[1]); PIN(twc[0]); PIN(twc[1]); PIN(tws[0]); PIN(tws[1]);
    };
    const int pw = (w + 4 * th) & 7;
    GAS u32* fl = (GAS u32*)(ws + WS_FLAG);
    int ko[7];
    {
        const int r6 = (fb < 6 ? fb : fb - 5) + 3 * (grp & 1);
#pragma unroll
        for (int ks = 0; ks < 6; ++ks) { int k = ks + r6; k = k >= 6 ? k - 6 : k; ko[ks] = k >= 6 ? k - 6 : k; }
        ko[6] = 6;
    }
    u4 W[7][4];
    const u32 w2vo = (l >> 3) * 1024 + (((l & 7) ^ (((l >> 4) & 1) << 2)) * 16);
    auto w2_glds = [&](int p_, int ks, u32 dst) {
        const int q = ko[ks];
        const char* src = (const char*)w2 + (224 * p_ + (ks == 6 ? 96 : (q < 3 ? 32 * q : 16 + 32 * q))) * 1024 + fb * 128;
        glds16(src, w2vo, dst); glds16(src + 8192, w2vo, dst + 1024);
        glds16(src + (ks == 6 ? 112 : 16) * 1024, w2vo, dst + 2048); glds16(src + (ks == 6 ? 120 : 24) * 1024, w2vo, dst + 3072);
    };
    LAS char* w2rd = (LAS char*)smem + (4 * g + (m >> 2)) * 128 + (((m & 3) ^ (((m >> 3) & 1) << 2)) * 16);
    auto w2_read = [&](u4 (&f)[4], int off) {
#pragma unroll
        for (int t = 0; t < 4; ++t) {
            LAS char* p = (LAS char*)((u32)(uintptr_t)w2rd ^ ((t >> 1) * 64)) + off + (t & 1) * 8;
            u2 x0 = __builtin_bit_cast(u2, __builtin_amdgcn_ds_read_tr16_b64_v4i16((LAS s4*)p));
            u2 x1 = __builtin_bit_cast(u2, __builtin_amdgcn_ds_read_tr16_b64_v4i16((LAS s4*)(p + 2048)));
            f[t] = (u4){x0[0], x0[1], x1[0], x1[1]};
        }
    };
    auto bg = [&](int i0, int i1) { w1_burst(i0, i1 - i0); };
    u32 tag;
    if (__builtin_expect(aw, 1)) {
        if (tid < 8) hw[tid] = 0;
        FENCE();
        ST(2);
        {
            u4 FAre, FAim, Fre, Fim, Sre, Sim; u2 Mb; f2 pc[2], ps[2];
            consts0();
            float c0[8], s0[8];
#pragma unroll
            for (int j = 0; j < 8; ++j) {
                float a = (gf + 4.0f * j) * (mf * (1.0f / 32));
                c0[j] = cosr(a); s0[j] = m ? -sinr(a) : ((g & 1) ? -1.0f : 1.0f);
            }
            const float mo = (2.0f * mf + 1.0f) * (1.0f / 32);
#pragma unroll
            for (int u = 0; u < 4; ++u) {
                FAre[u] = pk_h(c0[2 * u], c0[2 * u + 1]); FAim[u] = pk_h(s0[2 * u], s0[2 * u + 1]);
                float cc = c16[u], ss = s16[u];
                Fre[u] = pk_h(cc, ss); Fim[u] = pk_h(-ss, cc);
                float a2 = (4.0f * gf + u) * mo;
                u32 re = pk_h(m < 8 ? cc : cosr(a2), 0.f), im = pk_h(m < 8 ? (m ? -ss : ((u & 1) ? -1.0f : 1.0f)) : -sinr(a2), 0.f);
                Sre[u] = m < 8 ? re : re << 16; Sim[u] = m < 8 ? im : im << 16;
                pc[u & 1][u >> 1] = twc[u >> 1][u & 1]; ps[u & 1][u >> 1] = tws[u >> 1][u & 1];
            }
#pragma unroll
            for (int e = 0; e < 2; ++e) {
                float a = (float)(((m >> 1) * (256 * (2 * g + e) + b)) & 2047) * (1.0f / 2048);
                float cc = cosr(a), ss = sinr(a);
                Mb[e] = (m & 1) ? pk_h(-ss, cc) : pk_h(cc, ss);
            }
            PIN(FAre); PIN(FAim); PIN(Fre); PIN(Fim); PIN(Sre); PIN(Sim); PIN(Mb); PIN(pc[0]); PIN(pc[1]); PIN(ps[0]); PIN(ps[1]);
            ST(3);
            f2 v[8];
#pragma unroll
            for (int j = 0; j < 8; ++j) v[j] = bf_2(xd[j]);
            f2 s2 = ((v[0] + v[1]) + (v[2] + v[3])) + ((v[4] + v[5]) + (v[6] + v[7]));
            const float mean = red_lanes<false>(s2[0] + s2[1]) * (1.0f / 512);
            f2 q0 = {0.f, 0.f}, q1 = {0.f, 0.f};
#pragma unroll
            for (int j = 0; j < 8; ++j) { v[j] -= mean; if (j & 1) q1 = fma2(v[j], v[j], q1); else q0 = fma2(v[j], v[j], q0); }
            q0 += q1;
            const float inv = ln_inv(red_lanes<false>(q0[0] + q0[1]) * (1.0f / 512));
            const float off = (mean - rbf(mean)) * inv;
#pragma unroll
            for (int j = 0; j < 8; ++j) v[j] = fma2(v[j], (f2){inv, inv}, (f2){off, off});
            u4 B[2];
#pragma unroll
            for (int p = 0; p < 2; ++p) {
                u4 A = {pk_h(v[0][p], v[1][p]), pk_h(v[2][p], v[3][p]), pk_h(v[4][p], v[5][p]), pk_h(v[6][p], v[7][p])};
                f4 Dre = mma_h(A, FAre, Z4), Dim = mma_h(A, FAim, Z4);
#pragma unroll
                for (int r = 0; r < 2; ++r) {
                    f2 re = r ? hi2(Dre) : lo2(Dre), im = r ? hi2(Dim) : lo2(Dim);
                    f2 tr = fma2(im, ps[p], re * pc[p]), ti = fma2(-re, ps[p], im * pc[p]);
                    B[r][p] = pk_h(tr[0], ti[0]); B[r][2 + p] = pk_h(tr[1], ti[1]);
                }
            }
#pragma unroll
            for (int r = 0; r < 2; ++r) {
                f4 Gre = mma_h(Fre, B[r], Z4), Gim = mma_h(Fim, B[r], Z4), Hre = mma_h(Sre, B[r], Z4), Him = mma_h(Sim, B[r], Z4);
#pragma unroll
                for (int i = 0; i < 4; ++i) *(u32*)(scr + (2 * w + r) * 1024 + zo + i * 64) = pk_h(m ? Gre[i] : Hre[i], m ? Gim[i] : Him[i]);
            }
            BARRIER();
            ST(18);
            u4 r0 = *(const u4*)(scr + (2 * g) * 1024 + w * 256 + m * 16);
            u4 r1 = *(const u4*)(scr + (2 * g + 1) * 1024 + w * 256 + m * 16);
            u4 o0, o1;
#pragma unroll
            for (int t = 0; t < 4; ++t) {
                f4 D = mma16_h(Mb, (u2){r0[t], r1[t]}, Z4);
                o0[t] = clean(pk_h(D[0], D[1])); o1[t] = clean(pk_h(D[2], D[3]));
            }
            bst4<SC1>(o0, rw, g * 2048 + m * 16, WS_U + b * 8192 + w * 256);
            bst4<SC1>(o1, rw, g * 2048 + m * 16 + 1024, WS_U + b * 8192 + w * 256);
            if (w == 0 && m == 0) {
                const int xo = WS_UX + ((b >> 6) * 16 + (b & 15)) * 16 + ((b >> 4) & 3) * 4;
                __builtin_amdgcn_raw_buffer_store_b32(o0[0], rw, g * 2048, xo, SC1); __builtin_amdgcn_raw_buffer_store_b32(o1[0], rw, g * 2048 + 1024, xo, SC1);
            }
        }
        ST(4);
        u4 Fre, Fim, Gre, Gim; u2 Nb; f2 tc[2], ts[2];
#pragma unroll
        for (int u = 0; u < 4; ++u) {
            float cc = c16[u], ss = s16[u];
            Fre[u] = pk_h(0.25f * cc, 0.25f * ss); Fim[u] = pk_h(-0.25f * ss, 0.25f * cc);
            Gre[u] = pk_b(cc * (1.0f / 256), -ss * (1.0f / 256)); Gim[u] = pk_b(ss * (1.0f / 256), cc * (1.0f / 256));
            float a3 = (4.0f * gf + u) * (mf * (1.0f / 256));
            tc[u >> 1][u & 1] = cosr(a3); ts[u >> 1][u & 1] = sinr(a3);
        }
        PIN(Fre); PIN(Fim); PIN(Gre); PIN(Gim); PIN(tc[0]); PIN(tc[1]); PIN(ts[0]); PIN(ts[1]);
        xl[0] = *(const u2*)((const char*)x + xoff); xl[1] = *(const u2*)((const char*)x + xoff + 512);
        FENCE();
        tag = __builtin_amdgcn_readfirstlane(epoch) + 1u;
        consts3();
#pragma unroll
        for (int e = 0; e < 2; ++e) {
            float a1 = (float)(((2 * g + e) * (256 * (m >> 1) + b)) & 2047) * (1.0f / 2048);
            float c1 = cosr(a1), s1 = sinr(a1);
            Nb[e] = (m & 1) ? pk_b(s1, c1) : pk_b(c1, -s1);
        }
        PIN(Nb);
        WAITV(0);
        ST(19);
        BARRIER();
        ST(5);
        u4 d[2], wa, wb;
        const int uo = (tid >> 1) * 8192 + (tid & 1) * 16, us = WS_U + f1 * 1024 + cg * 32;
        d[0] = bld4<SC1>(rw, uo, us); d[1] = bld4<SC1>(rw, uo, us + 128 * 8192);
        u4 ux;
        if (cg == 0 && w == 0) {
            ux = bld4<SC1>(rw, l * 16, WS_UX + ((8 - f1) & 7) * 1024);
            wa = bld4<SC1>(rw, l * 16, WS_WA + f1 * 1024); wb = bld4<SC1>(rw, l * 16, WS_WB + f1 * 1024);
        }
        poll2(d[0], d[1], rw, uo, us, us + 128 * 8192);
        char* sp = scr + SCR_S + (tid ^ ((tid >> 4) & 1)) * 16;
        *(u4*)sp = d[0]; *(u4*)(sp + 4096) = d[1];
        BARRIER();
        if (b == 0 && tid == 0) __hip_atomic_store((GAS u32*)(ws + WS_EP), tag, __ATOMIC_RELAXED, __HIP_MEMORY_SCOPE_AGENT);
        ST(6);
        u2 ld[4], wl[4];
        const int so = (64 * g + m) * 32 + (((w >> 1) ^ (m >> 3)) * 16) + (w & 1) * 8;
#pragma unroll
        for (int q = 0; q < 4; ++q) ld[q] = *(const u2*)(scr + SCR_S + so + 512 * q);
#pragma unroll
        for (int q = 0; q < 4; ++q) wl[q] = *(const u2*)(scr + SCR_S + so + 8192 + 512 * q);
        FENCE();
        auto fwd = [&](u4 A, f4& Xr, f4& Xi) {
            f4 Dre = mma_h(A, Fre, Z4), Dim = mma_h(A, Fim, Z4);
            f2 r0 = fma2(lo2(Dim), ts[0], lo2(Dre) * tc[0]), i0 = fma2(-lo2(Dre), ts[0], lo2(Dim) * tc[0]);
            f2 r1 = fma2(hi2(Dim), ts[1], hi2(Dre) * tc[1]), i1 = fma2(-hi2(Dre), ts[1], hi2(Dim) * tc[1]);
            u4 B = {pk_h(r0[0], i0[0]), pk_h(r0[1], i0[1]), pk_h(r1[0], i1[0]), pk_h(r1[1], i1[1])};
            Xr = mma_h(Fre, B, Z4); Xi = mma_h(Fim, B, Z4);
        };
        f4 Xre[2], Xim[2];
#pragma unroll
        for (int e = 0; e < 2; ++e) fwd((u4){ld[0][e], ld[1][e], ld[2][e], ld[3][e]}, Xre[e], Xim[e]);
        f2 yr[2][2], yi[2][2];
#pragma unroll
        for (int e = 0; e < 2; ++e)
#pragma unroll
            for (int hh = 0; hh < 2; ++hh) {
                f2 wre = {bf_lo(wl[2 * hh][e]), bf_lo(wl[2 * hh + 1][e])}, wim = {bf_hi(wl[2 * hh][e]), bf_hi(wl[2 * hh + 1][e])};
                f2 xr = hh ? hi2(Xre[e]) : lo2(Xre[e]), xi = hh ? hi2(Xim[e]) : lo2(Xim[e]);
                yr[e][hh] = fma2(-xi, wim, xr * wre); yi[e][hh] = fma2(xr, wim, xi * wre);
            }
        if (cg == 0 && w == 0) {
            poll2(wa, wb, rw, l * 16, WS_WA + f1 * 1024, WS_WB + f1 * 1024);
            poll2(ux, wb, rw, l * 16, WS_UX + ((8 - f1) & 7) * 1024, WS_WB + f1 * 1024);
            f4 Mr, Mi;
            fwd(ux, Mr, Mi);
            f2* sx = (f2*)(scr + SCR_X);
#pragma unroll
            for (int i = 0; i < 4; ++i) sx[m + 16 * (4 * g + i)] = (f2){Mr[i], Mi[i]};
#pragma unroll
            for (int i = 0; i < 4; ++i) {
                f2 xm = sx[((f1 ? 255 : 256) - (m + 16 * (4 * g + i))) & 255];
                float ar = bf_lo(wa[i]), ai = bf_hi(wa[i]), br = bf_lo(wb[i]), bi = bf_hi(wb[i]);
                yr[0][i >> 1][i & 1] = Xre[0][i] * ar - Xim[0][i] * ai + xm[0] * br + xm[1] * bi;
                yi[0][i >> 1][i & 1] = Xre[0][i] * ai + Xim[0][i] * ar + xm[0] * bi - xm[1] * br;
            }
        }
        char* pv = scr;
#pragma unroll
        for (int e = 0; e < 2; ++e) {
            u4 Y = {pk_b(yr[e][0][0], yi[e][0][0]), pk_b(yr[e][0][1], yi[e][0][1]), pk_b(yr[e][1][0], yi[e][1][0]), pk_b(yr[e][1][1], yi[e][1][1])};
            f4 Dre = mma_b(Y, Gre, Z4), Dim = mma_b(Y, Gim, Z4);
            f2 r0 = fma2(-lo2(Dim), ts[0], lo2(Dre) * tc[0]), i0 = fma2(lo2(Dre), ts[0], lo2(Dim) * tc[0]);
            f2 r1 = fma2(-hi2(Dim), ts[1], hi2(Dre) * tc[1]), i1 = fma2(hi2(Dre), ts[1], hi2(Dim) * tc[1]);
            u4 A = {pk_b(r0[0], i0[0]), pk_b(r0[1], i0[1]), pk_b(r1[0], i1[0]), pk_b(r1[1], i1[1])};
            f4 Ere = mma_b(A, Gre, Z4), Eim = mma_b(A, Gim, Z4);
            u4 o;
#pragma unroll
            for (int i = 0; i < 4; ++i) o[i] = clean(pk_b(Ere[i], Eim[i]));
            *(u4*)(pv + (2 * w + e) * 1024 + ((m * 4 + g) ^ ((w >> 1) * 4)) * 16) = o;
        }
        BARRIER();
        ST(7);
#pragma unroll
        for (int it = 0; it < 2; ++it) {
            const int ch = tid + 256 * it;
            const u32* q = (const u32*)(pv + (ch & 1) * 4096 + (((ch >> 1) * 4) ^ ((ch & 1) * 64)));
            bst4<SC1>((u4){q[0], q[256], q[512], q[768]}, rw, ch * 16, WS_V + b * 8192);
        }
        WAITV(0);
        ST(8);
        const int vo = (g * 64 + (m >> 1)) * 8192 + (m & 1) * 16, vs = WS_V + w * 8 * 8192 + b * 32;
        u4 v0 = bld4<SC1>(rw, vo, vs), v1 = bld4<SC1>(rw, vo, vs + 32 * 8192);
        poll2(v0, v1, rw, vo, vs, vs + 32 * 8192);
        ST(9);
        u4 z0, z1;
#pragma unroll
        for (int t = 0; t < 4; ++t) {
            f4 D = mma16_b(Nb, (u2){v0[t], v1[t]}, Z4);
            z0[t] = pk_b(D[0], D[1]); z1[t] = pk_b(D[2], D[3]);
        }
        *(u4*)(scr + SCR_X + g * 2048 + w * 256 + m * 16) = z0;
        *(u4*)(scr + SCR_X + g * 2048 + 1024 + w * 256 + m * 16) = z1;
    } else {
        auto announce = [&](int which) {
            if (l < 8) __hip_atomic_store((GAS u32*)(ws + WS_FA) + (which * 2048 + l * 256 + b) * FS, tag, __ATOMIC_RELAXED, __HIP_MEMORY_SCOPE_AGENT);
            FENCE();
        };
        auto all_announced = [&](int which) {
#pragma nounroll
            for (int sp = 0; sp < SPIN_MAX; ++sp) {
                FENCE();
                u4 f;
#pragma unroll
                for (int i = 0; i < 4; ++i) f[i] = __builtin_amdgcn_raw_buffer_load_b32(rw, (l + 64 * i) * 4 * FS, WS_FA + (which * 8192 + (b >> 5) * 1024) * FS, SC1);
                if (__builtin_amdgcn_ballot_w64(f[0] != tag || f[1] != tag || f[2] != tag || f[3] != tag) == 0) break;
                __builtin_amdgcn_s_sleep(4);
            }
        };
        FENCE();
        ST(2);
        tag = __builtin_amdgcn_readfirstlane(epoch) + 1u;
        if (w == 6) WAITV(2); else if (w == 7 && VPOS == 0) WAITV(8); else WAITV(0);
        if (l < 2) __hip_atomic_store((GAS u32*)(ws + WS_FA) + ((2 * (w - 4) + l) * 256 + b) * FS, tag, __ATOMIC_RELAXED, __HIP_MEMORY_SCOPE_AGENT);
        FENCE();
        ST(18);
#pragma unroll
        for (int jj = 0; jj < 7; ++jj) {
            const int cj_ = (4 * jj) % 14, rj = 18 * jj + (4 * jj) / 14;
            w1o[jj] = (row0 + rj) * 3584 + (cch0 + cj_) * 16 + (cch0 + cj_ >= 14 ? 3584 - 224 : 0);
            PIN(w1o[jj]);
        }
        BARRIER();
        if (w == 6) {
            float re = bf_lo(sr_), im = bf_lo(si_);
            re += dpp<0x128>(re); im -= dpp<0x128>(im);
            u32 a = __builtin_bit_cast(u32, re), a2 = a, c = __builtin_bit_cast(u32, im), c2 = c;
            asm("" : "+v"(a2), "+v"(c2));
            auto pr = __builtin_amdgcn_permlane16_swap(a, a2, false, false);
            auto pi = __builtin_amdgcn_permlane16_swap(c, c2, false, false);
            const float ar = __builtin_bit_cast(float, (u32)pr[0]), br = __builtin_bit_cast(float, (u32)pr[1]);
            const float ai = __builtin_bit_cast(float, (u32)pi[0]), bi = __builtin_bit_cast(float, (u32)pi[1]);
            const int wo = WS_WA + ((b >> 6) * 16 + (b & 15)) * 16 + ((b >> 4) & 3) * 4;
            if (l < 8) {
                __builtin_amdgcn_raw_buffer_store_b32(clean(pk_b(0.25f * (ar + br), 0.25f * (ai + bi))), rw, l * 1024, wo, SC1);
                __builtin_amdgcn_raw_buffer_store_b32(clean(pk_b(0.25f * (ar - br), 0.25f * (ai - bi))), rw, l * 1024, wo + 8192, SC1);
            }
        }
        const int f = f1 + 8 * (tid & 255), k0 = 16 * cg - 8 * (cg & 1);
        const int e = mir ? ((2048 - f) & 2047) * 514 + (504 - k0) * 2 : f * 514 + k0 * 2;
        consts0();
        if (w == 7 && VPOS == 0) { WAITV(0); announce(1); }
        __builtin_amdgcn_s_sleep(GA_SLEEP);
        const u4 fa = bld4<0>(mkrs((cg & 1) ? wi : wr, 1052672), e, 0);
        FENCE();
        __builtin_amdgcn_s_sleep(GB_SLEEP);
        if (w == 4) all_announced(0);
        const u4 fb = bld4<0>(mkrs((cg & 1) ? wr : wi, 1052672), e, 0);
        xl[0] = *(const u2*)((const char*)x + xoff); xl[1] = *(const u2*)((const char*)x + xoff + 512);
        FENCE();
        ST(4);
        BARRIER();
        {
            u32 o[8];
            const u4 fr = (cg & 1) ? fb : fa, fi = (cg & 1) ? fa : fb;
#pragma unroll
            for (int c = 0; c < 8; ++c)
                o[c] = mir ? __builtin_amdgcn_perm(fi[3 - (c >> 1)], fr[3 - (c >> 1)], (c & 1) ? 0x05040100u : 0x07060302u) ^ 0x80000000u
                           : __builtin_amdgcn_perm(fi[c >> 1], fr[c >> 1], (c & 1) ? 0x07060302u : 0x05040100u);
            char* wp = scr + SCR_S + 8192 + (tid & 255) * 32;
            *(u4*)(wp + (m & 8) * 2) = (u4){o[0], o[1], o[2], o[3]};
            *(u4*)(wp + ((m & 8) * 2 ^ 16)) = (u4){o[4], o[5], o[6], o[7]};
        }
        FENCE();
        if (w == 7 && VPOS == 1) {
#pragma unroll
            for (int i = 0; i < 8; ++i) bst4<SC1>(SENT4, rw, l * 16 + (i & 3) * 1024, WS_V + b * 8192 + (i >> 2) * 4096);
            WAITV(0); announce(1);
        }
        if (LPOS == 1) {
#pragma unroll
        for (int i = 0; i < 2; ++i) bst4<SC1>(SENT4, rw, (l >> 3) * 256 + (l & 7) * 16 + i * 2048, WS_LN2 + (b >> 1) * 16384 + (b & 1) * 128 + (w - 4) * 4096);
        }
        consts3();
        ST(5);
        BARRIER();
        if (LPOS == 0) {
#pragma unroll
        for (int i = 0; i < 2; ++i) bst4<SC1>(SENT4, rw, (l >> 3) * 256 + (l & 7) * 16 + i * 2048, WS_LN2 + (b >> 1) * 16384 + (b & 1) * 128 + (w - 4) * 4096);
        }
        if (w == 5) all_announced(1);
        bg(0, BG5);
        WAITV(BG5);
        ST(6);
        BARRIER();
        __builtin_amdgcn_s_sleep(3);
        bg(BG5, BG5b);
        ST(7);
        __builtin_amdgcn_s_sleep(BG_DELAY);
        bg(BG5b, BG6);
    }
    BARRIER();
    {
        const u32* zp = (const u32*)(scr + SCR_X + w * 1024 + zo);
        u4 A = {zp[0], zp[16], zp[32], zp[48]};
        f4 Dre = mma_b(A, Gre3, Z4), Dim = mma_b(A, Gim3, Z4), Hre = mma_b(A, SGre, Z4), Him = mma_b(A, SGim, Z4);
        if (g == 0) { Dre[0] = Hre[0]; Dim[0] = Him[0]; }
        f2 r0 = fma2(-lo2(Dim), tws[0], lo2(Dre) * twc[0]), i0 = fma2(lo2(Dre), tws[0], lo2(Dim) * twc[0]);
        f2 r1 = fma2(-hi2(Dim), tws[1], hi2(Dre) * twc[1]), i1 = fma2(hi2(Dre), tws[1], hi2(Dim) * twc[1]);
        u4 A2 = {pk_b(r0[0], i0[0]), pk_b(r0[1], i0[1]), pk_b(r1[0], i1[0]), pk_b(r1[1], i1[1])};
        f2 h[4]; u2 hp[2];
#pragma unroll
        for (int t = 0; t < 2; ++t) {
            f4 Yv = mma_b(A2, GA[t], Z4);
            u32 h0 = pk_b2(bf_2(xl[t][0]) + bf_2(pk_b(Yv[0], Yv[1]))), h1 = pk_b2(bf_2(xl[t][1]) + bf_2(pk_b(Yv[2], Yv[3])));
            hp[t] = (u2){h0, h1};
            h[2 * t] = bf_2(h0); h[2 * t + 1] = bf_2(h1);
        }
        f2 s2 = (h[0] + h[1]) + (h[2] + h[3]);
        const float mean = red_lanes<true>(s2[0] + s2[1]) * (1.0f / 512);
        f2 q2 = {0.f, 0.f};
#pragma unroll
        for (int j = 0; j < 4; ++j) { f2 d = h[j] - mean; q2 = fma2(d, d, q2); }
        const float inv = ln_inv(red_lanes<true>(q2[0] + q2[1]) * (1.0f / 512));
        const float mb = rbf(mean);
#pragma unroll
        for (int t = 0; t < 2; ++t)
            *(u2*)(scr + ((8 * t + (m >> 1)) * 4 + g) * 128 + (w ^ (m >> 1)) * 16 + (m & 1) * 8) =
                (u2){clean(pk_b2(bf_2(pk_b2(h[2 * t] - mb)) * inv)), clean(pk_b2(bf_2(pk_b2(h[2 * t + 1] - mb)) * inv))};
        WAITV(0);
        BARRIER();
        u4 lo = *(const u4*)(scr + (tid ^ ((tid >> 5) & 7)) * 16);
        bst4<SC1>(lo, rw, (tid >> 3) * 256 + (tid & 7) * 16, WS_LN2 + (b >> 1) * 16384 + (b & 1) * 128);
        FENCE();
        if (aw) WAITV(0); else __builtin_amdgcn_s_sleep(8);
        bst2<SC1>(hp[0], ro, xoff, 0); bst2<SC1>(hp[1], ro, xoff + 512, 0);
        FENCE();
        ST(10);
    }
    auto stage_out = [&](int r) {
        w2_read(W[r], LDS_SCR + (w & 3) * 8192 + (r & 1) * 4096);
        asm volatile("s_waitcnt lgkmcnt(0)" ::: "memory");
        PIN(W[r][0]); PIN(W[r][1]); PIN(W[r][2]); PIN(W[r][3]);
        if (r + 2 < 7) w2_glds(pw, r + 2, lds0 + LDS_SCR + (w & 3) * 8192 + (r & 1) * 4096);
    };
    auto publish = [&]() {
        u32 prev = 0;
        if (l == 0) prev = __hip_atomic_fetch_add(hw + 4, 1u, __ATOMIC_RELAXED, __HIP_MEMORY_SCOPE_WORKGROUP);
        if (__builtin_amdgcn_readfirstlane(prev) == 7u && l < 2)
            __hip_atomic_store(fl + (l ? 4096 + (b >> 1) * 32 + (b & 1) : (mt1 * 8 + (nt1 >> 1)) * 32 + (nt1 & 1)), tag, __ATOMIC_RELAXED, __HIP_MEMORY_SCOPE_AGENT);
        FENCE();
    };
    {
        const int tg = w & 3;
        char* xch = smem + XCH + tg * 3072;
        const int hoff = WS_H1 + (mt1 * 8 + 2 * tg) * (56 * 1024) + (nt1 >> 1) * 7168;
        if (__builtin_expect(aw, 1)) {
            const int toff = WS_LN2 + (mt1 * 8 + 2 * tg) * 16384;
            LAS char* wb0 = (LAS char*)smem + (4 * g + (m >> 2)) * 224 + (m & 3) * 8;
            LAS char* wb[4];
            int tq[4];
#pragma unroll
            for (int q = 0; q < 4; ++q) { const int qq = (q + rq) & 3; wb[q] = wb0 + qq * 28672; tq[q] = toff + qq * 4096; }
            u4 T[2][16], A[2][7];
            auto ldt = [&](int kk) {
#pragma unroll
                for (int tt = 0; tt < 2; ++tt) T[tt][kk] = bld4<SC1>(rw, l * 16 + (kk & 3) * 1024, tq[kk >> 2] + tt * 16384);
            };
#pragma unroll
            for (int i = 0; i < 7; ++i) A[0][i] = ldw1(wb[0], 0, i);
            if (l == 0) __hip_atomic_fetch_add(hw + 5, 1u, __ATOMIC_RELAXED, __HIP_MEMORY_SCOPE_WORKGROUP);
            ST(11);
#pragma unroll
            for (int kk = 0; kk < G1_DEPTH; ++kk) { ldt(kk); SCHED(); }
            f4 acc[2][7];
#pragma unroll
            for (int tt = 0; tt < 2; ++tt)
#pragma unroll
                for (int i = 0; i < 7; ++i) acc[tt][i] = Z4;
#pragma unroll
            for (int kk = 0; kk < 16; ++kk) {
                if ((kk & 1) == 0) {
                    if (__builtin_expect(any_sent(umax(umax(max4(T[0][kk]), max4(T[1][kk])), umax(max4(T[0][kk + 1]), max4(T[1][kk + 1])))), 0)) {
                        wait_tok(T[0][kk], T[1][kk], rw, l * 16, tq[kk >> 2] + (kk & 3) * 1024);
                        wait_tok(T[0][kk + 1], T[1][kk + 1], rw, l * 16, tq[kk >> 2] + ((kk + 1) & 3) * 1024);
                    }
                    if (kk == 0) ST(12);
                    if (kk == (BG6 / 7) * 4 - 2) lds_wait(hw + 5, 8u);
                }
                if (kk + G1_DEPTH < 16) ldt(kk + G1_DEPTH);
                if (kk < 15) {
#pragma unroll
                    for (int i = 0; i < 7; ++i) A[(kk + 1) & 1][i] = ldw1(wb[(kk + 1) >> 2], (kk + 1) & 3, i);
                }
#pragma unroll
                for (int i = 0; i < 7; ++i)
#pragma unroll
                    for (int tt = 0; tt < 2; ++tt) acc[tt][i] = mma_b(A[kk & 1][i], T[tt][kk], acc[tt][i]);
#pragma unroll
                for (int i = 0; i < 14; ++i) {
                    __builtin_amdgcn_sched_group_barrier(0x8, 1, 0);
                    if (i == 1 || i == 7) __builtin_amdgcn_sched_group_barrier(0x20, 1, 0);
                    __builtin_amdgcn_sched_group_barrier(0x100, 1, 0);
                    if ((kk & 1) && i >= 3) __builtin_amdgcn_sched_group_barrier(0x2, 1, 0);
                }
                if (kk & 1) {} else SCHED();
            }
#pragma unroll
            for (int q = 0; q < 3; ++q) {
                u2 p0 = pk4(acc[1][2 * q]), p1 = pk4(acc[1][2 * q + 1]);
                *(u4*)(xch + q * 1024 + l * 16) = (u4){p0[0], p0[1], p1[0], p1[1]};
            }
            lds_set(hw + tg, 1u);
            ST(13);
#pragma unroll
            for (int q = 0; q < 3; ++q) {
                u2 p0 = pk4(acc[0][2 * q]), p1 = pk4(acc[0][2 * q + 1]);
                bst4<SC1>(silu_pk4((u4){p0[0], p0[1], p1[0], p1[1]}), rw, l * 16, hoff + ((nt1 & 1) * 3 + q) * 1024);
            }
#pragma unroll
            for (int tt = 0; tt < 2; ++tt) { u2 p = pk4(acc[tt][6]); bst2<SC1>(silu_pk2(p[0], p[1]), rw, l * 8, hoff + tt * (56 * 1024) + 6144 + (nt1 & 1) * 512); }
        } else {
            bg(BG6, 28);
            WAITV(0);
            if (l == 0) __hip_atomic_fetch_add(hw + 5, 1u, __ATOMIC_RELAXED, __HIP_MEMORY_SCOPE_WORKGROUP);
            __builtin_amdgcn_s_sleep(8);
            lds_wait(hw + 5, 8u);
            w2_glds(pw, 0, lds0 + LDS_SCR + tg * 8192);
            w2_glds(pw, 1, lds0 + LDS_SCR + tg * 8192 + 4096);
#pragma unroll
            for (int r = 0; r < B_R; ++r) { WAITV(4); stage_out(r); }
            ST(12);
            lds_wait(hw + tg, 1u);
            ST(13);
#pragma unroll
            for (int q = 0; q < 3; ++q)
                bst4<SC1>(silu_pk4(*(const u4*)(xch + q * 1024 + l * 16)), rw, l * 16, hoff + 56 * 1024 + ((nt1 & 1) * 3 + q) * 1024);
        }
        ST(14);
        WAITV(0);
        publish();
        ST(21);
        if (__builtin_expect(aw, 1)) {
            lds_wait(hw + 4, 8u);
#pragma unroll
            for (int ks = 0; ks < 7; ++ks) w2_glds(pw, ks, lds0 + tg * 28672 + ks * 4096);
        } else {
            stage_out(B_R);
#pragma unroll
            for (int r = B_R + 1; r < 7; ++r) { if (r + 1 < 7) WAITV(4); else WAITV(0); stage_out(r); }
        }
    }
    {
        ST(15);
        const int rot = w & 3;
        if (aw) {
            flag_wait(fl + ((l & 4) ? 4096 + (grp * 8 + th * 4 + rot) * 32 : (grp * 8 + (pw ^ ((l & 2) * 2))) * 32) + (l & 1), tag);
            lds_set(hw + 8 + rot, tag);
        } else {
#pragma nounroll
            for (int sp = 0; sp < SPIN_MAX; ++sp) {
                if (__builtin_amdgcn_readfirstlane(__hip_atomic_load(hw + 8 + rot, __ATOMIC_RELAXED, __HIP_MEMORY_SCOPE_WORKGROUP)) == tag) break;
                __builtin_amdgcn_s_sleep(1);
            }
            FENCE();
        }
        ST(16);
        const int toff = WS_H1 + (grp * 8 + th * 4) * (56 * 1024) + pw * 7168;
        int to[4];
#pragma unroll
        for (int tt = 0; tt < 4; ++tt) to[tt] = toff + ((tt + rot) & 3) * (56 * 1024);
        u4 T[7][4];
        auto ldtok = [&](int ks) {
#pragma unroll
            for (int tt = 0; tt < 4; ++tt) {
                if (ks < 6) T[ks][tt] = bld4<SC1>(rw, l * 16, to[tt] + ko[ks] * 1024);
                else {
                    u2 a = bld2<SC1>(rw, l * 8 + 2048, to[tt] + 4096), c = bld2<SC1>(rw, l * 8 + 2560, to[tt] + 4096);
                    T[ks][tt] = (u4){a[0], a[1], c[0], c[1]};
                }
            }
            SCHED();
        };
#pragma unroll
        for (int ks = 0; ks < G2_DEPTH; ++ks) ldtok(ks);
        if (aw) {
#pragma unroll
            for (int ks = 0; ks < 7; ++ks) w2_read(W[ks], rot * 28672 + ks * 4096);
            asm volatile("s_waitcnt lgkmcnt(0)" ::: "memory");
        }
        if (l == 0) __hip_atomic_fetch_add(hw + 7, 1u, __ATOMIC_RELAXED, __HIP_MEMORY_SCOPE_WORKGROUP);
        const int rho = grp * 128 + th * 64 + rot * 16 + m;
        const int ooff = (256 * (rho & 7) + (rho >> 3)) * 1024 + fb * 128 + (w >> 2) * 64 + g * 16;
        const u4 hres = bld4<SC1>(ro, ooff, 0);
        SCHED();
        f4 acc[4][4];
#pragma unroll
        for (int tt = 0; tt < 4; ++tt)
#pragma unroll
            for (int t = 0; t < 4; ++t) acc[tt][t] = Z4;
#pragma unroll
        for (int ks = 0; ks < 7; ++ks) {
            if (ks + G2_DEPTH < 7) { PIN(T[ks][0]); ldtok(ks + G2_DEPTH); }
#pragma unroll
            for (int tt = 0; tt < 4; ++tt)
#pragma unroll
                for (int t = 0; t < 4; ++t) {
                    acc[tt][t] = mma_b(W[ks][t], T[ks][tt], acc[tt][t]);
                }
            SCHED();
        }
        ST(17);
        lds_wait(hw + 7, 8u);
        char* dp = smem + w * 16384 + l * 16;
#pragma unroll
        for (int tt = 1; tt < 4; ++tt)
#pragma unroll
            for (int t = 0; t < 4; ++t) *(f4*)(dp + (((tt + rot) & 3) * 4 + t) * 1024) = acc[tt][t];
        f4 s0, s1;
        if (aw) { *(f4*)(dp + (rot * 4 + 2) * 1024) = acc[0][2]; *(f4*)(dp + (rot * 4 + 3) * 1024) = acc[0][3]; s0 = acc[0][0]; s1 = acc[0][1]; }
        else { *(f4*)(dp + (rot * 4) * 1024) = acc[0][0]; *(f4*)(dp + (rot * 4 + 1) * 1024) = acc[0][1]; s0 = acc[0][2]; s1 = acc[0][3]; }
        BARRIER();
        f4 r0[7], r1[7];
#pragma unroll
        for (int j = 1; j < 8; ++j) {
            const char* rp = smem + (((w + j) & 7) * 16 + rot * 4 + (w >> 2) * 2) * 1024 + l * 16;
            r0[j - 1] = *(const f4*)rp; r1[j - 1] = *(const f4*)(rp + 1024);
        }
        SCHED();
        s0 = ((s0 + r0[0]) + (r0[1] + r0[2])) + ((r0[3] + r0[4]) + (r0[5] + r0[6]));
        s1 = ((s1 + r1[0]) + (r1[1] + r1[2])) + ((r1[3] + r1[4]) + (r1[5] + r1[6]));
        u2 p0 = pk4(s0), p1 = pk4(s1);
        bst4<NT>((u4){pk_b2(bf_2(hres[0]) + bf_2(p0[0])), pk_b2(bf_2(hres[1]) + bf_2(p0[1])),
                           pk_b2(bf_2(hres[2]) + bf_2(p1[0])), pk_b2(bf_2(hres[3]) + bf_2(p1[1]))}, ro, ooff, 0);
    }
    ST_END(ws, 0);
}

extern "C" void kernel_launch(void* const* d_in, const int* in_sizes, int n_in, void* d_out, int out_size,
                              void* d_ws, size_t ws_size, hipStream_t stream) {
    (void)hipFuncSetAttribute((const void*)k_fused, hipFuncAttributeMaxDynamicSharedMemorySize, LDS_B);
    k_fused<<<256, 512, LDS_B, stream>>>((const uint16_t*)d_in[0], (const uint16_t*)d_in[1], (const uint16_t*)d_in[2],
                                         (const uint16_t*)d_in[4], (const uint16_t*)d_in[5], (uint16_t*)d_out, (char*)d_ws);
}
```

```cpp
#include <hip/hip_runtime.h>
#include <hip/hip_bf16.h>
#include <stdint.h>

typedef _Float16 h8 __attribute__((ext_vector_type(8)));
typedef _Float16 h4 __attribute__((ext_vector_type(4)));
typedef _Float16 h2 __attribute__((ext_vector_type(2)));
typedef __bf16 b8 __attribute__((ext_vector_type(8)));
typedef __bf16 b2 __attribute__((ext_vector_type(2)));
typedef short s4 __attribute__((ext_vector_type(4)));
typedef float f4 __attribute__((ext_vector_type(4)));
typedef float f2 __attribute__((ext_vector_type(2)));
typedef unsigned u32;
typedef u32 u4 __attribute__((ext_vector_type(4)));
typedef u32 u2 __attribute__((ext_vector_type(2)));
#define LAS __attribute__((address_space(3)))
#define GAS __attribute__((address_space(1)))
#define DI __device__ __forceinline__
#define FENCE() asm volatile("" ::: "memory")
#define PIN(x) asm volatile("" : "+v"(x))
#define SCHED() __builtin_amdgcn_sched_barrier(0)
#define WAITV(n) asm volatile("s_waitcnt vmcnt(%0)" :: "n"(n) : "memory")
#define BARRIER() do { asm volatile("s_waitcnt lgkmcnt(0)" ::: "memory"); __builtin_amdgcn_s_barrier(); FENCE(); } while (0)
constexpr int SC1 = 16, NT = 2;
constexpr u32 SENT = 0xffffffffu;
constexpr int SPIN_MAX = 1 << 20;

DI u32 pk_h(float a, float b) { f2 v = {a, b}; h2 r = __builtin_convertvector(v, h2); return __builtin_bit_cast(u32, r); }
DI u32 pk_b(float a, float b) { f2 v = {a, b}; b2 r = __builtin_convertvector(v, b2); return __builtin_bit_cast(u32, r); }
DI float bf_lo(u32 v) { return __builtin_bit_cast(float, v << 16); }
DI float bf_hi(u32 v) { return __builtin_bit_cast(float, v & 0xffff0000u); }
DI f2 bf_2(u32 v) { return (f2){bf_lo(v), bf_hi(v)}; }
DI u32 pk_b2(f2 v) { return pk_b(v[0], v[1]); }
DI float rbf(float a) { return bf_lo(pk_b(a, a)); }
DI float cosr(float rev) { return __builtin_amdgcn_cosf(rev); }
DI float sinr(float rev) { return __builtin_amdgcn_sinf(rev); }
DI f2 lo2(f4 v) { return __builtin_shufflevector(v, v, 0, 1); }
DI f2 hi2(f4 v) { return __builtin_shufflevector(v, v, 2, 3); }
DI f2 fma2(f2 a, f2 b, f2 c) { return __builtin_elementwise_fma(a, b, c); }
DI u32 umin(u32 a, u32 b) { return a < b ? a : b; }
DI u32 umax(u32 a, u32 b) { return a > b ? a : b; }
DI u32 clean(u32 v) { return umin(v, SENT - 1); }
DI u32 max4(u4 v) { return umax(umax(umax(v[0], v[1]), v[2]), v[3]); }
#define Z4 ((f4){0, 0, 0, 0})
DI f4 mma_h(u4 a, u4 b, f4 c) { return __builtin_amdgcn_mfma_f32_16x16x32_f16(__builtin_bit_cast(h8, a), __builtin_bit_cast(h8, b), c, 0, 0, 0); }
DI f4 mma_b(u4 a, u4 b, f4 c) { return __builtin_amdgcn_mfma_f32_16x16x32_bf16(__builtin_bit_cast(b8, a), __builtin_bit_cast(b8, b), c, 0, 0, 0); }
DI f4 mma16_b(u2 a, u2 b, f4 c) { return __builtin_amdgcn_mfma_f32_16x16x16bf16_1k(__builtin_bit_cast(s4, a), __builtin_bit_cast(s4, b), c, 0, 0, 0); }
DI f4 mma16_h(u2 a, u2 b, f4 c) { return __builtin_amdgcn_mfma_f32_16x16x16f16(__builtin_bit_cast(h4, a), __builtin_bit_cast(h4, b), c, 0, 0, 0); }
typedef __amdgpu_buffer_rsrc_t rsrc_t;
DI rsrc_t mkrs(const void* p, u32 bytes) { return __builtin_amdgcn_make_buffer_rsrc((void*)p, 0, bytes, 0x00020000); }
template <int AUX> DI u4 bld4(rsrc_t r, int v, int s) { return __builtin_amdgcn_raw_buffer_load_b128(r, v, s, AUX); }
template <int AUX> DI u2 bld2(rsrc_t r, int v, int s) { return __builtin_amdgcn_raw_buffer_load_b64(r, v, s, AUX); }
template <int AUX> DI void bst4(u4 d, rsrc_t r, int v, int s) {
    __builtin_amdgcn_raw_buffer_store_b128(d, r, v, s, AUX);
    asm volatile("s_nop 1" :: "v"(d));
}
template <int AUX> DI void bst2(u2 d, rsrc_t r, int v, int s) { __builtin_amdgcn_raw_buffer_store_b64(d, r, v, s, AUX); }

template <int CTRL> DI float dpp(float v) {
    return __builtin_bit_cast(float, __builtin_amdgcn_update_dpp(0, __builtin_bit_cast(int, v), CTRL, 0xf, 0xf, true));
}
template <bool ALL> DI float red_lanes(float v) {
    v += dpp<0xB1>(v);
    if (ALL) v += dpp<0x4E>(v);
    v += dpp<0x124>(v); v += dpp<0x128>(v);
    u32 x = __builtin_bit_cast(u32, v), y = x;
    asm("" : "+v"(y));
    auto r = __builtin_amdgcn_permlane16_swap(x, y, false, false);
    v = __builtin_bit_cast(float, (u32)r[0]) + __builtin_bit_cast(float, (u32)r[1]);
    x = __builtin_bit_cast(u32, v); y = x;
    asm("" : "+v"(y));
    auto q = __builtin_amdgcn_permlane32_swap(x, y, false, false);
    return __builtin_bit_cast(float, (u32)q[0]) + __builtin_bit_cast(float, (u32)q[1]);
}
DI float ln_inv(float var) { return __builtin_amdgcn_rcpf(rbf(__builtin_amdgcn_sqrtf(rbf(rbf(var) + 1e-5f)))); }


constexpr u32 WS_U = 0;
constexpr u32 WS_V = WS_U + (2u << 20);
constexpr u32 WS_LN2 = WS_V + (2u << 20);
constexpr u32 WS_WA = WS_LN2 + (2u << 20);
constexpr u32 WS_WB = WS_WA + 8192u;
constexpr u32 WS_UX = WS_WB + 8192u;
constexpr u32 WS_FA = WS_UX + 8192u;
constexpr u32 WS_FLAG = WS_FA + 16384u;
constexpr u32 WS_EP = WS_FLAG + 32768u;
constexpr u32 WS_H1 = WS_WA + (512u << 10);
constexpr u32 WS_END = WS_H1 + 7340032u;

constexpr int BG5 = 10, BG6 = 21;
constexpr int G1_DEPTH = 10, G2_DEPTH = 3;
constexpr int B_R = 5;
constexpr int CODE_LINES = 290, CODE_PF = 10;
constexpr int LDS_SCR = 114688;
constexpr int SCR_X = 8448;
constexpr int SCR_S = SCR_X + 20480;
constexpr int SCR_F = 49024;
constexpr int XCH = 147456;
constexpr int LDS_B = 163840;

DI void glds16(const void* sbase, u32 voff, u32 lds_dst) {
    asm volatile("s_mov_b32 m0, %2\n\ts_nop 0\n\tglobal_load_lds_dwordx4 %0, %1" :: "v"(voff), "s"(sbase), "s"(lds_dst) : "memory");
}
DI bool any_sent(u32 v) { return __builtin_amdgcn_ballot_w64(v == SENT) != 0; }
DI void poll2(u4& a, u4& c, rsrc_t rw, int vo, int sa, int sc) {
#pragma nounroll
    for (int sp = 0; sp < SPIN_MAX; ++sp) {
        const bool ba = max4(a) == SENT, bc = max4(c) == SENT;
        if (__builtin_amdgcn_ballot_w64(ba || bc) == 0) break;
        __builtin_amdgcn_s_sleep(1);
        FENCE();
        if (ba) a = bld4<SC1>(rw, vo, sa);
        if (bc) c = bld4<SC1>(rw, vo, sc);
    }
}
DI void wait_tok(u4& t0, u4& t1, rsrc_t rw, int vo, int so) {
    if (any_sent(umax(max4(t0), max4(t1)))) {
#pragma nounroll
        for (int sp = 0; sp < SPIN_MAX; ++sp) {
            FENCE();
            t0 = bld4<SC1>(rw, vo, so); t1 = bld4<SC1>(rw, vo, so + 16384);
            if (!any_sent(umax(max4(t0), max4(t1)))) break;
        }
    }
}
DI u4 ldw1(LAS char* wb, int kk, int ft) {
    s4 a0 = __builtin_amdgcn_ds_read_tr16_b64_v4i16((LAS s4*)(wb + kk * 32 * 224 + ft * 32));
    s4 a1 = __builtin_amdgcn_ds_read_tr16_b64_v4i16((LAS s4*)(wb + (kk * 32 + 16) * 224 + ft * 32));
    u2 x0 = __builtin_bit_cast(u2, a0), x1 = __builtin_bit_cast(u2, a1);
    return (u4){x0[0], x0[1], x1[0], x1[1]};
}
DI u2 pk4(f4 a) { return (u2){pk_b(a[0], a[1]), pk_b(a[2], a[3])}; }
DI u2 silu_pk2(u32 p0, u32 p1) {
    u32 o0, o1; float a0, a1, a2, a3, t0, t1, t2, t3;
    asm("v_lshlrev_b32 %2, 16, %10\n\tv_and_b32 %3, 0xffff0000, %10\n\tv_lshlrev_b32 %4, 16, %11\n\tv_and_b32 %5, 0xffff0000, %11\n\t"
        "v_mul_f32 %6, %12, %2\n\tv_mul_f32 %7, %12, %3\n\tv_mul_f32 %8, %12, %4\n\tv_mul_f32 %9, %12, %5\n\t"
        "v_exp_f32 %6, %6\n\tv_exp_f32 %7, %7\n\tv_exp_f32 %8, %8\n\tv_exp_f32 %9, %9\n\t"
        "v_add_f32 %6, 1.0, %6\n\tv_add_f32 %7, 1.0, %7\n\tv_add_f32 %8, 1.0, %8\n\tv_add_f32 %9, 1.0, %9\n\t"
        "v_rcp_f32 %6, %6\n\tv_rcp_f32 %7, %7\n\tv_rcp_f32 %8, %8\n\tv_rcp_f32 %9, %9\n\t"
        "v_mul_f32 %2, %2, %6\n\tv_mul_f32 %3, %3, %7\n\tv_mul_f32 %4, %4, %8\n\tv_mul_f32 %5, %5, %9\n\t"
        "v_cvt_pk_bf16_f32 %0, %2, %3\n\tv_cvt_pk_bf16_f32 %1, %4, %5"
        : "=&v"(o0), "=&v"(o1), "=&v"(a0), "=&v"(a1), "=&v"(a2), "=&v"(a3), "=&v"(t0), "=&v"(t1), "=&v"(t2), "=&v"(t3)
        : "v"(p0), "v"(p1), "s"(-1.4426950408889634f));
    return (u2){o0, o1};
}
DI u4 silu_pk4(u4 p) { u2 a = silu_pk2(p[0], p[1]), b = silu_pk2(p[2], p[3]); return (u4){a[0], a[1], b[0], b[1]}; }
DI void lds_set(LAS u32* p, u32 v) {
    asm volatile("s_waitcnt lgkmcnt(0)" ::: "memory");
    __hip_atomic_store(p, v, __ATOMIC_RELAXED, __HIP_MEMORY_SCOPE_WORKGROUP);
}
DI void lds_wait(LAS u32* p, u32 v) {
#pragma nounroll
    for (int sp = 0; sp < SPIN_MAX; ++sp) {
        if (__builtin_amdgcn_readfirstlane(__hip_atomic_load(p, __ATOMIC_RELAXED, __HIP_MEMORY_SCOPE_WORKGROUP)) >= v) break;
        __builtin_amdgcn_s_sleep(1);
    }
    FENCE();
}
DI void flag_wait(GAS u32* p, u32 tag) {
#pragma nounroll
    for (int sp = 0; sp < SPIN_MAX; ++sp) {
        if (__builtin_amdgcn_ballot_w64(__hip_atomic_load(p, __ATOMIC_RELAXED, __HIP_MEMORY_SCOPE_AGENT) != tag) == 0) break;
        __builtin_amdgcn_s_sleep(2);
    }
    FENCE();
}
#define SENT4 ((u4){SENT, SENT, SENT, SENT})

__global__ __launch_bounds__(512, 2) void k_fused(const uint16_t* x, const uint16_t* wr, const uint16_t* wi, const uint16_t* w1,
                                                  const uint16_t* w2, uint16_t* out, char* ws) {
    extern __shared__ __attribute__((aligned(16))) char smem[];
    const int tid = threadIdx.x, l = tid & 63, g = l >> 4, m = l & 15;
    const int w = __builtin_amdgcn_readfirstlane(tid >> 6);
    const int b = (blockIdx.x & 7) * 32 + (blockIdx.x >> 3);
    const int mt1 = (b >> 6) * 4 + ((b >> 3) & 3), nt1 = ((b >> 5) & 1) * 8 + (b & 7);
    const int grp = b >> 4, th = (b >> 3) & 1, fb = b & 7;
    const rsrc_t rw = mkrs(ws, WS_END), ro = mkrs(out, 2097152);
    char* scr = smem + LDS_SCR;
    LAS u32* hw = (LAS u32*)((LAS char*)smem + LDS_SCR + SCR_F);
    const u32 lds0 = (u32)(uintptr_t)(LAS char*)smem;
    const float mf = (float)m, gf = (float)g, sg = (m & 1) ? -1.0f : 1.0f;
    const bool aw = w < 4;
    unsigned long long pc0;
    asm volatile("s_getpc_b64 %0" : "=s"(pc0));
    if (tid >= 448 && l < CODE_PF)
        asm volatile("s_mov_b32 m0, %2\n\ts_nop 0\n\tglobal_load_lds_dword %0, %1"
                     :: "v"(umin((blockIdx.x >> 3) * CODE_PF + l, CODE_LINES - 1) * 128), "s"(pc0), "s"(lds0 + LDS_SCR + SCR_F + 64) : "memory");
    const u32 epoch = __hip_atomic_load((GAS u32*)(ws + WS_EP), __ATOMIC_RELAXED, __HIP_MEMORY_SCOPE_AGENT);
    FENCE();
    u32 xd[8];
    const int f1 = b >> 5, cg = b & 31;
    const bool mir = cg >= 16;
    const rsrc_t rr = mkrs(wr, 1052672), ri = mkrs(wi, 1052672);
    u32 sr_, si_;
    if (aw) {
        const u32* xr = (const u32*)(x + (256 * (2 * w + ((m >> 1) & 1)) + b) * 512) + 8 * g + 2 * (m >> 2) + (m & 1);
#pragma unroll
        for (int j = 0; j < 8; ++j) xd[j] = xr[32 * j];
    } else {
        bst4<SC1>(SENT4, rw, l * 16, WS_U + b * 8192 + (w - 4) * 2048); bst4<SC1>(SENT4, rw, l * 16 + 1024, WS_U + b * 8192 + (w - 4) * 2048);
        __builtin_amdgcn_raw_buffer_store_b32(SENT, rw, l < 6 ? (l >> 1) * 8192 + (l & 1) * 1024 : 0x7ffffff0,
                                              WS_WA + (w - 4) * 2048 + ((b >> 6) * 16 + (b & 15)) * 16 + ((b >> 4) & 3) * 4, SC1);
        FENCE();
        if (w == 7) {
#pragma unroll
            for (int i = 0; i < 8; ++i) bst4<SC1>(SENT4, rw, l * 16 + (i & 3) * 1024, WS_V + b * 8192 + (i >> 2) * 4096);
        }
        if (w == 6) {
            const int c = (l >> 3) & 3, f_ = 8 * b + (l & 7);
            const int e1 = ((c & 1) ? (2048 - f_) & 2047 : f_) * 514 + (c >> 1) * 512;
            sr_ = __builtin_amdgcn_raw_buffer_load_b16(rr, e1, 0, 0); si_ = __builtin_amdgcn_raw_buffer_load_b16(ri, e1, 0, 0);
        }
    }
    FENCE();
    const int zo = (64 * g + (g < 2 ? m : (m + 15) & 15)) * 4;
    const int rq = ((b >> 3) + b) & 3;
    const int row0 = (tid & 255) / 14, cch0 = (tid & 255) - row0 * 14;
    const char* wsrc = (const char*)w1 + nt1 * 224;
    u32 w1o[7];
    auto w1_burst = [&](int i0, int n) {
#pragma unroll
        for (int i = i0; i < i0 + n; ++i) {
            const int q = (i / 7 + rq) & 3, j = i % 7;
            glds16(wsrc + q * (128 * 3584), w1o[j], lds0 + (w - 4) * 1024 + q * 28672 + j * 4096);
        }
    };
    const int xoff = (256 * w + b) * 1024 + m * 32 + g * 8;
    u2 xl[2];
    float c16[4], s16[4];
    f2 twc[2], tws[2];
    u4 Gre3, Gim3, SGre, SGim, GA[2];
    auto consts0 = [&]() {
#pragma unroll
        for (int u = 0; u < 4; ++u) {
            float a = (4.0f * gf + u) * (mf * (1.0f / 16));
            c16[u] = cosr(a); s16[u] = sinr(a);
            twc[u >> 1][u & 1] = cosr(a * (1.0f / 32)); tws[u >> 1][u & 1] = sinr(a * (1.0f / 32));
        }
    };
    auto consts3 = [&]() {
#pragma unroll
        for (int u = 0; u < 4; ++u) {
            const float k = 4.0f * gf + u;
            const bool k0 = u == 0 && g == 0;
            float cc = c16[u], ss = s16[u];
            Gre3[u] = pk_b(cc, -ss); Gim3[u] = pk_b(ss, cc);
            float a2 = (2.0f * k + 1.0f) * (mf * (1.0f / 32));
            u32 sp = pk_b(g < 2 ? 2.f * cc : 2.f * cosr(a2), g < 2 ? -2.f * ss : -2.f * sinr(a2));
            SGre[u] = k0 ? pk_b(1.0f, sg) : (g < 2 ? sp : 0u);
            SGim[u] = g < 2 ? 0u : sp;
            float a4 = k * (mf * (1.0f / 32)), c4 = 2.f * cosr(a4), s4_ = -2.f * sinr(a4);
            GA[0][u] = k0 ? pk_b(1.0f, sg) : pk_b(c4, s4_);
            GA[1][u] = k0 ? pk_b(1.0f, sg) : ((u & 1) ? pk_b(-c4, -s4_) : pk_b(c4, s4_));
        }
        PIN(Gre3); PIN(Gim3); PIN(SGre); PIN(SGim); PIN(GA[0]); PIN(GA[1]); PIN(twc[0]); PIN(twc[1]); PIN(tws[0]); PIN(tws[1]);
    };
    const int pw = (w + 4 * th) & 7;
    GAS u32* fl = (GAS u32*)(ws + WS_FLAG);
    int ko[7];
    {
        const int r6 = (fb < 6 ? fb : fb - 5) + 3 * (grp & 1);
#pragma unroll
        for (int ks = 0; ks < 6; ++ks) { int k = ks + r6; k = k >= 6 ? k - 6 : k; ko[ks] = k >= 6 ? k - 6 : k; }
        ko[6] = 6;
    }
    u4 W[7][4];
    const u32 w2vo = (l >> 3) * 1024 + (((l & 7) ^ (((l >> 4) & 1) << 2)) * 16);
    auto w2_glds = [&](int p_, int ks, u32 dst) {
        const int q = ko[ks];
        const char* src = (const char*)w2 + (224 * p_ + (ks == 6 ? 96 : (q < 3 ? 32 * q : 16 + 32 * q))) * 1024 + fb * 128;
        glds16(src, w2vo, dst); glds16(src + 8192, w2vo, dst + 1024);
        glds16(src + (ks == 6 ? 112 : 16) * 1024, w2vo, dst + 2048); glds16(src + (ks == 6 ? 120 : 24) * 1024, w2vo, dst + 3072);
    };
    LAS char* w2rd = (LAS char*)smem + (4 * g + (m >> 2)) * 128 + (((m & 3) ^ (((m >> 3) & 1) << 2)) * 16);
    auto w2_read = [&](u4 (&f)[4], int off) {
#pragma unroll
        for (int t = 0; t < 4; ++t) {
            LAS char* p = (LAS char*)(uintptr_t)((u32)(uintptr_t)w2rd ^ ((t >> 1) * 64)) + off + (t & 1) * 8;
            u2 x0 = __builtin_bit_cast(u2, __builtin_amdgcn_ds_read_tr16_b64_v4i16((LAS s4*)p));
            u2 x1 = __builtin_bit_cast(u2, __builtin_amdgcn_ds_read_tr16_b64_v4i16((LAS s4*)(p + 2048)));
            f[t] = (u4){x0[0], x0[1], x1[0], x1[1]};
        }
    };
    auto bg = [&](int i0, int i1) { w1_burst(i0, i1 - i0); };
    u32 tag;
    if (__builtin_expect(aw, 1)) {
        if (tid < 8) hw[tid] = 0;
        FENCE();
        {
            u4 FAre, FAim, Fre, Fim, Sre, Sim; u2 Mb; f2 pc[2], ps[2];
            consts0();
            float c0[8], s0[8];
#pragma unroll
            for (int j = 0; j < 8; ++j) {
                float a = (gf + 4.0f * j) * (mf * (1.0f / 32));
                c0[j] = cosr(a); s0[j] = m ? -sinr(a) : ((g & 1) ? -1.0f : 1.0f);
            }
            const float mo = (2.0f * mf + 1.0f) * (1.0f / 32);
#pragma unroll
            for (int u = 0; u < 4; ++u) {
                FAre[u] = pk_h(c0[2 * u], c0[2 * u + 1]); FAim[u] = pk_h(s0[2 * u], s0[2 * u + 1]);
                float cc = c16[u], ss = s16[u];
                Fre[u] = pk_h(cc, ss); Fim[u] = pk_h(-ss, cc);
                float a2 = (4.0f * gf + u) * mo;
                u32 re = pk_h(m < 8 ? cc : cosr(a2), 0.f), im = pk_h(m < 8 ? (m ? -ss : ((u & 1) ? -1.0f : 1.0f)) : -sinr(a2), 0.f);
                Sre[u] = m < 8 ? re : re << 16; Sim[u] = m < 8 ? im : im << 16;
                pc[u & 1][u >> 1] = twc[u >> 1][u & 1]; ps[u & 1][u >> 1] = tws[u >> 1][u & 1];
            }
#pragma unroll
            for (int e = 0; e < 2; ++e) {
                float a = (float)(((m >> 1) * (256 * (2 * g + e) + b)) & 2047) * (1.0f / 2048);
                float cc = cosr(a), ss = sinr(a);
                Mb[e] = (m & 1) ? pk_h(-ss, cc) : pk_h(cc, ss);
            }
            PIN(FAre); PIN(FAim); PIN(Fre); PIN(Fim); PIN(Sre); PIN(Sim); PIN(Mb); PIN(pc[0]); PIN(pc[1]); PIN(ps[0]); PIN(ps[1]);
            f2 v[8];
#pragma unroll
            for (int j = 0; j < 8; ++j) v[j] = bf_2(xd[j]);
            f2 s2 = ((v[0] + v[1]) + (v[2] + v[3])) + ((v[4] + v[5]) + (v[6] + v[7]));
            const float mean = red_lanes<false>(s2[0] + s2[1]) * (1.0f / 512);
            f2 q0 = {0.f, 0.f}, q1 = {0.f, 0.f};
#pragma unroll
            for (int j = 0; j < 8; ++j) { v[j] -= mean; if (j & 1) q1 = fma2(v[j], v[j], q1); else q0 = fma2(v[j], v[j], q0); }
            q0 += q1;
            const float inv = ln_inv(red_lanes<false>(q0[0] + q0[1]) * (1.0f / 512));
            const float off = (mean - rbf(mean)) * inv;
#pragma unroll
            for (int j = 0; j < 8; ++j) v[j] = fma2(v[j], (f2){inv, inv}, (f2){off, off});
            u4 B[2];
#pragma unroll
            for (int p = 0; p < 2; ++p) {
                u4 A = {pk_h(v[0][p], v[1][p]), pk_h(v[2][p], v[3][p]), pk_h(v[4][p], v[5][p]), pk_h(v[6][p], v[7][p])};
                f4 Dre = mma_h(A, FAre, Z4), Dim = mma_h(A, FAim, Z4);
#pragma unroll
                for (int r = 0; r < 2; ++r) {
                    f2 re = r ? hi2(Dre) : lo2(Dre), im = r ? hi2(Dim) : lo2(Dim);
                    f2 tr = fma2(im, ps[p], re * pc[p]), ti = fma2(-re, ps[p], im * pc[p]);
                    B[r][p] = pk_h(tr[0], ti[0]); B[r][2 + p] = pk_h(tr[1], ti[1]);
                }
            }
#pragma unroll
            for (int r = 0; r < 2; ++r) {
                f4 Gre = mma_h(Fre, B[r], Z4), Gim = mma_h(Fim, B[r], Z4), Hre = mma_h(Sre, B[r], Z4), Him = mma_h(Sim, B[r], Z4);
#pragma unroll
                for (int i = 0; i < 4; ++i) *(u32*)(scr + (2 * w + r) * 1024 + zo + i * 64) = pk_h(m ? Gre[i] : Hre[i], m ? Gim[i] : Him[i]);
            }
            BARRIER();
            u4 r0 = *(const u4*)(scr + (2 * g) * 1024 + w * 256 + m * 16);
            u4 r1 = *(const u4*)(scr + (2 * g + 1) * 1024 + w * 256 + m * 16);
            u4 o0, o1;
#pragma unroll
            for (int t = 0; t < 4; ++t) {
                f4 D = mma16_h(Mb, (u2){r0[t], r1[t]}, Z4);
                o0[t] = clean(pk_h(D[0], D[1])); o1[t] = clean(pk_h(D[2], D[3]));
            }
            bst4<SC1>(o0, rw, g * 2048 + m * 16, WS_U + b * 8192 + w * 256);
            bst4<SC1>(o1, rw, g * 2048 + m * 16 + 1024, WS_U + b * 8192 + w * 256);
            if (w == 0 && m == 0) {
                const int xo = WS_UX + ((b >> 6) * 16 + (b & 15)) * 16 + ((b >> 4) & 3) * 4;
                __builtin_amdgcn_raw_buffer_store_b32(o0[0], rw, ((8 - 2 * g) & 7) * 1024, xo, SC1); __builtin_amdgcn_raw_buffer_store_b32(o1[0], rw, (7 - 2 * g) * 1024, xo, SC1);
            }
        }
        u4 Fre, Fim, Gre, Gim; u2 Nb; f2 tc[2], ts[2];
#pragma unroll
        for (int u = 0; u < 4; ++u) {
            float cc = c16[u], ss = s16[u];
            Fre[u] = pk_h(0.25f * cc, 0.25f * ss); Fim[u] = pk_h(-0.25f * ss, 0.25f * cc);
            Gre[u] = pk_b(cc * (1.0f / 256), -ss * (1.0f / 256)); Gim[u] = pk_b(ss * (1.0f / 256), cc * (1.0f / 256));
            float a3 = (4.0f * gf + u) * (mf * (1.0f / 256));
            tc[u >> 1][u & 1] = cosr(a3); ts[u >> 1][u & 1] = sinr(a3);
        }
        PIN(Fre); PIN(Fim); PIN(Gre); PIN(Gim); PIN(tc[0]); PIN(tc[1]); PIN(ts[0]); PIN(ts[1]);
        xl[0] = *(const u2*)((const char*)x + xoff); xl[1] = *(const u2*)((const char*)x + xoff + 512);
        FENCE();
        tag = __builtin_amdgcn_readfirstlane(epoch) + 1u;
        consts3();
#pragma unroll
        for (int e = 0; e < 2; ++e) {
            float a1 = (float)(((2 * g + e) * (256 * (m >> 1) + b)) & 2047) * (1.0f / 2048);
            float c1 = cosr(a1), s1 = sinr(a1);
            Nb[e] = (m & 1) ? pk_b(s1, c1) : pk_b(c1, -s1);
        }
        PIN(Nb);
        WAITV(0);
        BARRIER();
        u4 d[2], wa, wb;
        const int uo = (tid >> 1) * 8192 + (tid & 1) * 16, us = WS_U + f1 * 1024 + cg * 32;
        d[0] = bld4<SC1>(rw, uo, us); d[1] = bld4<SC1>(rw, uo, us + 128 * 8192);
        u4 ux;
        if (cg == 0 && w == 0) {
            ux = bld4<SC1>(rw, l * 16, WS_UX + f1 * 1024);
            wa = bld4<SC1>(rw, l * 16, WS_WA + f1 * 1024); wb = bld4<SC1>(rw, l * 16, WS_WB + f1 * 1024);
        }
        poll2(d[0], d[1], rw, uo, us, us + 128 * 8192);
        char* sp = scr + SCR_S + (tid ^ ((tid >> 4) & 1)) * 16;
        *(u4*)sp = d[0]; *(u4*)(sp + 4096) = d[1];
        BARRIER();
        if (b == 0 && tid == 0) __hip_atomic_store((GAS u32*)(ws + WS_EP), tag, __ATOMIC_RELAXED, __HIP_MEMORY_SCOPE_AGENT);
        u2 ld[4], wl[4];
        const int so = (64 * g + m) * 32 + (((w >> 1) ^ (m >> 3)) * 16) + (w & 1) * 8;
#pragma unroll
        for (int q = 0; q < 4; ++q) ld[q] = *(const u2*)(scr + SCR_S + so + 512 * q);
#pragma unroll
        for (int q = 0; q < 4; ++q) wl[q] = *(const u2*)(scr + SCR_S + so + 8192 + 512 * q);
        FENCE();
        auto fwd = [&](u4 A, f4& Xr, f4& Xi) {
            f4 Dre = mma_h(A, Fre, Z4), Dim = mma_h(A, Fim, Z4);
            f2 r0 = fma2(lo2(Dim), ts[0], lo2(Dre) * tc[0]), i0 = fma2(-lo2(Dre), ts[0], lo2(Dim) * tc[0]);
            f2 r1 = fma2(hi2(Dim), ts[1], hi2(Dre) * tc[1]), i1 = fma2(-hi2(Dre), ts[1], hi2(Dim) * tc[1]);
            u4 B = {pk_h(r0[0], i0[0]), pk_h(r0[1], i0[1]), pk_h(r1[0], i1[0]), pk_h(r1[1], i1[1])};
            Xr = mma_h(Fre, B, Z4); Xi = mma_h(Fim, B, Z4);
        };
        f4 Xre[2], Xim[2];
#pragma unroll
        for (int e = 0; e < 2; ++e) fwd((u4){ld[0][e], ld[1][e], ld[2][e], ld[3][e]}, Xre[e], Xim[e]);
        f2 yr[2][2], yi[2][2];
#pragma unroll
        for (int e = 0; e < 2; ++e)
#pragma unroll
            for (int hh = 0; hh < 2; ++hh) {
                f2 wre = {bf_lo(wl[2 * hh][e]), bf_lo(wl[2 * hh + 1][e])}, wim = {bf_hi(wl[2 * hh][e]), bf_hi(wl[2 * hh + 1][e])};
                f2 xr = hh ? hi2(Xre[e]) : lo2(Xre[e]), xi = hh ? hi2(Xim[e]) : lo2(Xim[e]);
                yr[e][hh] = fma2(-xi, wim, xr * wre); yi[e][hh] = fma2(xr, wim, xi * wre);
            }
        if (cg == 0 && w == 0) {
            poll2(wa, wb, rw, l * 16, WS_WA + f1 * 1024, WS_WB + f1 * 1024);
            poll2(ux, wb, rw, l * 16, WS_UX + f1 * 1024, WS_WB + f1 * 1024);
            f4 Mr, Mi;
            fwd(ux, Mr, Mi);
            f2* sx = (f2*)(scr + SCR_X);
#pragma unroll
            for (int i = 0; i < 4; ++i) sx[m + 16 * (4 * g + i)] = (f2){Mr[i], Mi[i]};
#pragma unroll
            for (int i = 0; i < 4; ++i) {
                f2 xm = sx[((f1 ? 255 : 256) - (m + 16 * (4 * g + i))) & 255];
                float ar = bf_lo(wa[i]), ai = bf_hi(wa[i]), br = bf_lo(wb[i]), bi = bf_hi(wb[i]);
                yr[0][i >> 1][i & 1] = Xre[0][i] * ar - Xim[0][i] * ai + xm[0] * br + xm[1] * bi;
                yi[0][i >> 1][i & 1] = Xre[0][i] * ai + Xim[0][i] * ar + xm[0] * bi - xm[1] * br;
            }
        }
        char* pv = scr;
#pragma unroll
        for (int e = 0; e < 2; ++e) {
            u4 Y = {pk_b(yr[e][0][0], yi[e][0][0]), pk_b(yr[e][0][1], yi[e][0][1]), pk_b(yr[e][1][0], yi[e][1][0]), pk_b(yr[e][1][1], yi[e][1][1])};
            f4 Dre = mma_b(Y, Gre, Z4), Dim = mma_b(Y, Gim, Z4);
            f2 r0 = fma2(-lo2(Dim), ts[0], lo2(Dre) * tc[0]), i0 = fma2(lo2(Dre), ts[0], lo2(Dim) * tc[0]);
            f2 r1 = fma2(-hi2(Dim), ts[1], hi2(Dre) * tc[1]), i1 = fma2(hi2(Dre), ts[1], hi2(Dim) * tc[1]);
            u4 A = {pk_b(r0[0], i0[0]), pk_b(r0[1], i0[1]), pk_b(r1[0], i1[0]), pk_b(r1[1], i1[1])};
            f4 Ere = mma_b(A, Gre, Z4), Eim = mma_b(A, Gim, Z4);
            u4 o;
#pragma unroll
            for (int i = 0; i < 4; ++i) o[i] = clean(pk_b(Ere[i], Eim[i]));
            *(u4*)(pv + (2 * w + e) * 1024 + ((m * 4 + g) ^ ((w >> 1) * 4)) * 16) = o;
        }
        BARRIER();
#pragma unroll
        for (int it = 0; it < 2; ++it) {
            const int ch = tid + 256 * it;
            const u32* q = (const u32*)(pv + (ch & 1) * 4096 + (((ch >> 1) * 4) ^ ((ch & 1) * 64)));
            bst4<SC1>((u4){q[0], q[256], q[512], q[768]}, rw, ch * 16, WS_V + b * 8192);
        }
        WAITV(0);
        const int vo = (g * 64 + (m >> 1)) * 8192 + (m & 1) * 16, vs = WS_V + w * 8 * 8192 + b * 32;
        u4 v0 = bld4<SC1>(rw, vo, vs), v1 = bld4<SC1>(rw, vo, vs + 32 * 8192);
        poll2(v0, v1, rw, vo, vs, vs + 32 * 8192);
        u4 z0, z1;
#pragma unroll
        for (int t = 0; t < 4; ++t) {
            f4 D = mma16_b(Nb, (u2){v0[t], v1[t]}, Z4);
            z0[t] = pk_b(D[0], D[1]); z1[t] = pk_b(D[2], D[3]);
        }
        *(u4*)(scr + SCR_X + g * 2048 + w * 256 + m * 16) = z0;
        *(u4*)(scr + SCR_X + g * 2048 + 1024 + w * 256 + m * 16) = z1;
    } else {
        auto announce = [&](int which) {
            if (l < 8) __hip_atomic_store((GAS u32*)(ws + WS_FA) + (which * 2048 + l * 256 + b), tag, __ATOMIC_RELAXED, __HIP_MEMORY_SCOPE_AGENT);
            FENCE();
        };
        auto all_announced = [&](int which) {
#pragma nounroll
            for (int sp = 0; sp < SPIN_MAX; ++sp) {
                FENCE();
                u4 f;
#pragma unroll
                for (int i = 0; i < 4; ++i) f[i] = __builtin_amdgcn_raw_buffer_load_b32(rw, (l + 64 * i) * 4, WS_FA + (which * 8192 + (b >> 5) * 1024), SC1);
                if (__builtin_amdgcn_ballot_w64(f[0] != tag || f[1] != tag || f[2] != tag || f[3] != tag) == 0) break;
                __builtin_amdgcn_s_sleep(4);
            }
        };
        FENCE();
        tag = __builtin_amdgcn_readfirstlane(epoch) + 1u;
        if (w == 6) WAITV(2); else if (w == 7) WAITV(8); else WAITV(0);
        if (l < 2) __hip_atomic_store((GAS u32*)(ws + WS_FA) + ((2 * (w - 4) + l) * 256 + b), tag, __ATOMIC_RELAXED, __HIP_MEMORY_SCOPE_AGENT);
        FENCE();
#pragma unroll
        for (int jj = 0; jj < 7; ++jj) {
            const int cj_ = (4 * jj) % 14, rj = 18 * jj + (4 * jj) / 14;
            w1o[jj] = (row0 + rj) * 3584 + (cch0 + cj_) * 16 + (cch0 + cj_ >= 14 ? 3584 - 224 : 0);
            PIN(w1o[jj]);
        }
        BARRIER();
        if (w == 6) {
            float re = bf_lo(sr_), im = bf_lo(si_);
            re += dpp<0x128>(re); im -= dpp<0x128>(im);
            u32 a = __builtin_bit_cast(u32, re), a2 = a, c = __builtin_bit_cast(u32, im), c2 = c;
            asm("" : "+v"(a2), "+v"(c2));
            auto pr = __builtin_amdgcn_permlane16_swap(a, a2, false, false);
            auto pi = __builtin_amdgcn_permlane16_swap(c, c2, false, false);
            const float ar = __builtin_bit_cast(float, (u32)pr[0]), br = __builtin_bit_cast(float, (u32)pr[1]);
            const float ai = __builtin_bit_cast(float, (u32)pi[0]), bi = __builtin_bit_cast(float, (u32)pi[1]);
            const int wo = WS_WA + ((b >> 6) * 16 + (b & 15)) * 16 + ((b >> 4) & 3) * 4;
            if (l < 8) {
                __builtin_amdgcn_raw_buffer_store_b32(clean(pk_b(0.25f * (ar + br), 0.25f * (ai + bi))), rw, l * 1024, wo, SC1);
                __builtin_amdgcn_raw_buffer_store_b32(clean(pk_b(0.25f * (ar - br), 0.25f * (ai - bi))), rw, l * 1024, wo + 8192, SC1);
            }
        }
        const int f = f1 + 8 * (tid & 255), k0 = 16 * cg - 8 * (cg & 1);
        const int e = mir ? ((2048 - f) & 2047) * 514 + (504 - k0) * 2 : f * 514 + k0 * 2;
        consts0();
        if (w == 7) { WAITV(0); announce(1); }
        __builtin_amdgcn_s_sleep(4);
        const u4 fa = bld4<0>(mkrs((cg & 1) ? wi : wr, 1052672), e, 0);
        FENCE();
        __builtin_amdgcn_s_sleep(8);
        if (w == 4) all_announced(0);
        const u4 fb = bld4<0>(mkrs((cg & 1) ? wr : wi, 1052672), e, 0);
        xl[0] = *(const u2*)((const char*)x + xoff); xl[1] = *(const u2*)((const char*)x + xoff + 512);
        FENCE();
        BARRIER();
        {
            u32 o[8];
            const u4 fr = (cg & 1) ? fb : fa, fi = (cg & 1) ? fa : fb;
#pragma unroll
            for (int c = 0; c < 8; ++c)
                o[c] = mir ? __builtin_amdgcn_perm(fi[3 - (c >> 1)], fr[3 - (c >> 1)], (c & 1) ? 0x05040100u : 0x07060302u) ^ 0x80000000u
                           : __builtin_amdgcn_perm(fi[c >> 1], fr[c >> 1], (c & 1) ? 0x07060302u : 0x05040100u);
            char* wp = scr + SCR_S + 8192 + (tid & 255) * 32;
            *(u4*)(wp + (m & 8) * 2) = (u4){o[0], o[1], o[2], o[3]};
            *(u4*)(wp + ((m & 8) * 2 ^ 16)) = (u4){o[4], o[5], o[6], o[7]};
        }
        FENCE();
#pragma unroll
        for (int i = 0; i < 2; ++i) bst4<SC1>(SENT4, rw, (l >> 3) * 256 + (l & 7) * 16 + i * 2048, WS_LN2 + (b >> 1) * 16384 + (b & 1) * 128 + (w - 4) * 4096);
        consts3();
        BARRIER();
        if (w == 5) all_announced(1);
        bg(0, BG5);
        WAITV(BG5);
        BARRIER();
        __builtin_amdgcn_s_sleep(17);
        bg(BG5, BG6);
    }
    BARRIER();
    {
        const u32* zp = (const u32*)(scr + SCR_X + w * 1024 + zo);
        u4 A = {zp[0], zp[16], zp[32], zp[48]};
        f4 Dre = mma_b(A, Gre3, Z4), Dim = mma_b(A, Gim3, Z4), Hre = mma_b(A, SGre, Z4), Him = mma_b(A, SGim, Z4);
        if (g == 0) { Dre[0] = Hre[0]; Dim[0] = Him[0]; }
        f2 r0 = fma2(-lo2(Dim), tws[0], lo2(Dre) * twc[0]), i0 = fma2(lo2(Dre), tws[0], lo2(Dim) * twc[0]);
        f2 r1 = fma2(-hi2(Dim), tws[1], hi2(Dre) * twc[1]), i1 = fma2(hi2(Dre), tws[1], hi2(Dim) * twc[1]);
        u4 A2 = {pk_b(r0[0], i0[0]), pk_b(r0[1], i0[1]), pk_b(r1[0], i1[0]), pk_b(r1[1], i1[1])};
        f2 h[4]; u2 hp[2];
#pragma unroll
        for (int t = 0; t < 2; ++t) {
            f4 Yv = mma_b(A2, GA[t], Z4);
            u32 h0 = pk_b2(bf_2(xl[t][0]) + bf_2(pk_b(Yv[0], Yv[1]))), h1 = pk_b2(bf_2(xl[t][1]) + bf_2(pk_b(Yv[2], Yv[3])));
            hp[t] = (u2){h0, h1};
            h[2 * t] = bf_2(h0); h[2 * t + 1] = bf_2(h1);
        }
        f2 s2 = (h[0] + h[1]) + (h[2] + h[3]);
        const float mean = red_lanes<true>(s2[0] + s2[1]) * (1.0f / 512);
        f2 q2 = {0.f, 0.f};
#pragma unroll
        for (int j = 0; j < 4; ++j) { f2 d = h[j] - mean; q2 = fma2(d, d, q2); }
        const float inv = ln_inv(red_lanes<true>(q2[0] + q2[1]) * (1.0f / 512));
        const float mb = rbf(mean);
#pragma unroll
        for (int t = 0; t < 2; ++t)
            *(u2*)(scr + ((8 * t + (m >> 1)) * 4 + g) * 128 + (w ^ (m >> 1)) * 16 + (m & 1) * 8) =
                (u2){clean(pk_b2(bf_2(pk_b2(h[2 * t] - mb)) * inv)), clean(pk_b2(bf_2(pk_b2(h[2 * t + 1] - mb)) * inv))};
        WAITV(0);
        BARRIER();
        u4 lo = *(const u4*)(scr + (tid ^ ((tid >> 5) & 7)) * 16);
        bst4<SC1>(lo, rw, (tid >> 3) * 256 + (tid & 7) * 16, WS_LN2 + (b >> 1) * 16384 + (b & 1) * 128);
        FENCE();
        if (aw) WAITV(0); else __builtin_amdgcn_s_sleep(8);
        bst2<SC1>(hp[0], ro, xoff, 0); bst2<SC1>(hp[1], ro, xoff + 512, 0);
        FENCE();
    }
    auto stage_out = [&](int r) {
        w2_read(W[r], LDS_SCR + (w & 3) * 8192 + (r & 1) * 4096);
        asm volatile("s_waitcnt lgkmcnt(0)" ::: "memory");
        PIN(W[r][0]); PIN(W[r][1]); PIN(W[r][2]); PIN(W[r][3]);
        if (r + 2 < 7) w2_glds(pw, r + 2, lds0 + LDS_SCR + (w & 3) * 8192 + (r & 1) * 4096);
    };
    auto publish = [&]() {
        u32 prev = 0;
        if (l == 0) prev = __hip_atomic_fetch_add(hw + 4, 1u, __ATOMIC_RELAXED, __HIP_MEMORY_SCOPE_WORKGROUP);
        if (__builtin_amdgcn_readfirstlane(prev) == 7u && l < 2)
            __hip_atomic_store(fl + (l ? 4096 + (b >> 1) * 32 + (b & 1) : (mt1 * 8 + (nt1 >> 1)) * 32 + (nt1 & 1)), tag, __ATOMIC_RELAXED, __HIP_MEMORY_SCOPE_AGENT);
        FENCE();
    };
    {
        const int tg = w & 3;
        char* xch = smem + XCH + tg * 3072;
        const int hoff = WS_H1 + (mt1 * 8 + 2 * tg) * (56 * 1024) + (nt1 >> 1) * 7168;
        if (__builtin_expect(aw, 1)) {
            const int toff = WS_LN2 + (mt1 * 8 + 2 * tg) * 16384;
            LAS char* wb0 = (LAS char*)smem + (4 * g + (m >> 2)) * 224 + (m & 3) * 8;
            LAS char* wb[4];
            int tq[4];
#pragma unroll
            for (int q = 0; q < 4; ++q) { const int qq = (q + rq) & 3; wb[q] = wb0 + qq * 28672; tq[q] = toff + qq * 4096; }
            u4 T[2][16], A[2][7];
            auto ldt = [&](int kk) {
#pragma unroll
                for (int tt = 0; tt < 2; ++tt) T[tt][kk] = bld4<SC1>(rw, l * 16 + (kk & 3) * 1024, tq[kk >> 2] + tt * 16384);
            };
#pragma unroll
            for (int i = 0; i < 7; ++i) A[0][i] = ldw1(wb[0], 0, i);
            if (l == 0) __hip_atomic_fetch_add(hw + 5, 1u, __ATOMIC_RELAXED, __HIP_MEMORY_SCOPE_WORKGROUP);
#pragma unroll
            for (int kk = 0; kk < G1_DEPTH; ++kk) { ldt(kk); SCHED(); }
            f4 acc[2][7];
#pragma unroll
            for (int tt = 0; tt < 2; ++tt)
#pragma unroll
                for (int i = 0; i < 7; ++i) acc[tt][i] = Z4;
#pragma unroll
            for (int kk = 0; kk < 16; ++kk) {
                if ((kk & 1) == 0) {
                    if (__builtin_expect(any_sent(umax(umax(max4(T[0][kk]), max4(T[1][kk])), umax(max4(T[0][kk + 1]), max4(T[1][kk + 1])))), 0)) {
                        wait_tok(T[0][kk], T[1][kk], rw, l * 16, tq[kk >> 2] + (kk & 3) * 1024);
                        wait_tok(T[0][kk + 1], T[1][kk + 1], rw, l * 16, tq[kk >> 2] + ((kk + 1) & 3) * 1024);
                    }
                    if (kk == (BG6 / 7) * 4 - 2) lds_wait(hw + 5, 8u);
                }
                if (kk + G1_DEPTH < 16) ldt(kk + G1_DEPTH);
                if (kk < 15) {
#pragma unroll
                    for (int i = 0; i < 7; ++i) A[(kk + 1) & 1][i] = ldw1(wb[(kk + 1) >> 2], (kk + 1) & 3, i);
                }
#pragma unroll
                for (int i = 0; i < 7; ++i)
#pragma unroll
                    for (int tt = 0; tt < 2; ++tt) acc[tt][i] = mma_b(A[kk & 1][i], T[tt][kk], acc[tt][i]);
#pragma unroll
                for (int i = 0; i < 14; ++i) {
                    __builtin_amdgcn_sched_group_barrier(0x8, 1, 0);
                    if (i == 1 || i == 7) __builtin_amdgcn_sched_group_barrier(0x20, 1, 0);
                    __builtin_amdgcn_sched_group_barrier(0x100, 1, 0);
                    if ((kk & 1) && i >= 3) __builtin_amdgcn_sched_group_barrier(0x2, 1, 0);
                }
                if (kk & 1) {} else SCHED();
            }
#pragma unroll
            for (int q = 0; q < 3; ++q) {
                u2 p0 = pk4(acc[1][2 * q]), p1 = pk4(acc[1][2 * q + 1]);
                *(u4*)(xch + q * 1024 + l * 16) = (u4){p0[0], p0[1], p1[0], p1[1]};
            }
            lds_set(hw + tg, 1u);
#pragma unroll
            for (int q = 0; q < 3; ++q) {
                u2 p0 = pk4(acc[0][2 * q]), p1 = pk4(acc[0][2 * q + 1]);
                bst4<SC1>(silu_pk4((u4){p0[0], p0[1], p1[0], p1[1]}), rw, l * 16, hoff + ((nt1 & 1) * 3 + q) * 1024);
            }
#pragma unroll
            for (int tt = 0; tt < 2; ++tt) { u2 p = pk4(acc[tt][6]); bst2<SC1>(silu_pk2(p[0], p[1]), rw, l * 8, hoff + tt * (56 * 1024) + 6144 + (nt1 & 1) * 512); }
        } else {
            bg(BG6, 28);
            WAITV(0);
            if (l == 0) __hip_atomic_fetch_add(hw + 5, 1u, __ATOMIC_RELAXED, __HIP_MEMORY_SCOPE_WORKGROUP);
            __builtin_amdgcn_s_sleep(8);
            lds_wait(hw + 5, 8u);
            w2_glds(pw, 0, lds0 + LDS_SCR + tg * 8192);
            w2_glds(pw, 1, lds0 + LDS_SCR + tg * 8192 + 4096);
#pragma unroll
            for (int r = 0; r < B_R; ++r) { WAITV(4); stage_out(r); }
            lds_wait(hw + tg, 1u);
#pragma unroll
            for (int q = 0; q < 3; ++q)
                bst4<SC1>(silu_pk4(*(const u4*)(xch + q * 1024 + l * 16)), rw, l * 16, hoff + 56 * 1024 + ((nt1 & 1) * 3 + q) * 1024);
        }
        WAITV(0);
        publish();
        if (__builtin_expect(aw, 1)) {
            lds_wait(hw + 4, 8u);
#pragma unroll
            for (int ks = 0; ks < 7; ++ks) w2_glds(pw, ks, lds0 + tg * 28672 + ks * 4096);
        } else {
            stage_out(B_R);
#pragma unroll
            for (int r = B_R + 1; r < 7; ++r) { if (r + 1 < 7) WAITV(4); else WAITV(0); stage_out(r); }
        }
    }
    {
        const int rot = w & 3;
        if (aw) {
            flag_wait(fl + ((l & 4) ? 4096 + (grp * 8 + th * 4 + rot) * 32 : (grp * 8 + (pw ^ ((l & 2) * 2))) * 32) + (l & 1), tag);
            lds_set(hw + 8 + rot, tag);
        } else {
#pragma nounroll
            for (int sp = 0; sp < SPIN_MAX; ++sp) {
                if (__builtin_amdgcn_readfirstlane(__hip_atomic_load(hw + 8 + rot, __ATOMIC_RELAXED, __HIP_MEMORY_SCOPE_WORKGROUP)) == tag) break;
                __builtin_amdgcn_s_sleep(1);
            }
            FENCE();
        }
        const int toff = WS_H1 + (grp * 8 + th * 4) * (56 * 1024) + pw * 7168;
        int to[4];
#pragma unroll
        for (int tt = 0; tt < 4; ++tt) to[tt] = toff + ((tt + rot) & 3) * (56 * 1024);
        u4 T[7][4];
        auto ldtok = [&](int ks) {
#pragma unroll
            for (int tt = 0; tt < 4; ++tt) {
                if (ks < 6) T[ks][tt] = bld4<SC1>(rw, l * 16, to[tt] + ko[ks] * 1024);
                else {
                    u2 a = bld2<SC1>(rw, l * 8 + 2048, to[tt] + 4096), c = bld2<SC1>(rw, l * 8 + 2560, to[tt] + 4096);
                    T[ks][tt] = (u4){a[0], a[1], c[0], c[1]};
                }
            }
            SCHED();
        };
#pragma unroll
        for (int ks = 0; ks < G2_DEPTH; ++ks) ldtok(ks);
        if (aw) {
#pragma unroll
            for (int ks = 0; ks < 7; ++ks) w2_read(W[ks], rot * 28672 + ks * 4096);
            asm volatile("s_waitcnt lgkmcnt(0)" ::: "memory");
        }
        if (l == 0) __hip_atomic_fetch_add(hw + 7, 1u, __ATOMIC_RELAXED, __HIP_MEMORY_SCOPE_WORKGROUP);
        const int rho = grp * 128 + th * 64 + rot * 16 + m;
        const int ooff = (256 * (rho & 7) + (rho >> 3)) * 1024 + fb * 128 + (w >> 2) * 64 + g * 16;
        const u4 hres = bld4<SC1>(ro, ooff, 0);
        SCHED();
        f4 acc[4][4];
#pragma unroll
        for (int tt = 0; tt < 4; ++tt)
#pragma unroll
            for (int t = 0; t < 4; ++t) acc[tt][t] = Z4;
#pragma unroll
        for (int ks = 0; ks < 7; ++ks) {
            if (ks + G2_DEPTH < 7) { PIN(T[ks][0]); ldtok(ks + G2_DEPTH); }
#pragma unroll
            for (int tt = 0; tt < 4; ++tt)
#pragma unroll
                for (int t = 0; t < 4; ++t) {
                    acc[tt][t] = mma_b(W[ks][t], T[ks][tt], acc[tt][t]);
                }
            SCHED();
        }
        lds_wait(hw + 7, 8u);
        char* dp = smem + w * 16384 + l * 16;
#pragma unroll
        for (int tt = 1; tt < 4; ++tt)
#pragma unroll
            for (int t = 0; t < 4; ++t) *(f4*)(dp + (((tt + rot) & 3) * 4 + t) * 1024) = acc[tt][t];
        f4 s0, s1;
        if (aw) { *(f4*)(dp + (rot * 4 + 2) * 1024) = acc[0][2]; *(f4*)(dp + (rot * 4 + 3) * 1024) = acc[0][3]; s0 = acc[0][0]; s1 = acc[0][1]; }
        else { *(f4*)(dp + (rot * 4) * 1024) = acc[0][0]; *(f4*)(dp + (rot * 4 + 1) * 1024) = acc[0][1]; s0 = acc[0][2]; s1 = acc[0][3]; }
        BARRIER();
        f4 r0[7], r1[7];
#pragma unroll
        for (int j = 1; j < 8; ++j) {
            const char* rp = smem + (((w + j) & 7) * 16 + rot * 4 + (w >> 2) * 2) * 1024 + l * 16;
            r0[j - 1] = *(const f4*)rp; r1[j - 1] = *(const f4*)(rp + 1024);
        }
        SCHED();
        s0 = ((s0 + r0[0]) + (r0[1] + r0[2])) + ((r0[3] + r0[4]) + (r0[5] + r0[6]));
        s1 = ((s1 + r1[0]) + (r1[1] + r1[2])) + ((r1[3] + r1[4]) + (r1[5] + r1[6]));
        u2 p0 = pk4(s0), p1 = pk4(s1);
        bst4<NT>((u4){pk_b2(bf_2(hres[0]) + bf_2(p0[0])), pk_b2(bf_2(hres[1]) + bf_2(p0[1])),
                           pk_b2(bf_2(hres[2]) + bf_2(p1[0])), pk_b2(bf_2(hres[3]) + bf_2(p1[1]))}, ro, ooff, 0);
    }
}

extern "C" void kernel_launch(void* const* d_in, const int* in_sizes, int n_in, void* d_out, int out_size,
                              void* d_ws, size_t ws_size, hipStream_t stream) {
    (void)hipFuncSetAttribute((const void*)k_fused, hipFuncAttributeMaxDynamicSharedMemorySize, LDS_B);
    k_fused<<<256, 512, LDS_B, stream>>>((const uint16_t*)d_in[0], (const uint16_t*)d_in[1], (const uint16_t*)d_in[2],
                                         (const uint16_t*)d_in[4], (const uint16_t*)d_in[5], (uint16_t*)d_out, (char*)d_ws);
}
```

```cpp
#define SB_MASK 8
#include <hip/hip_runtime.h>
#include <hip/hip_bf16.h>
#include <stdint.h>

typedef _Float16 h8 __attribute__((ext_vector_type(8)));
typedef _Float16 h4 __attribute__((ext_vector_type(4)));
typedef _Float16 h2 __attribute__((ext_vector_type(2)));
typedef __bf16 b8 __attribute__((ext_vector_type(8)));
typedef __bf16 b2 __attribute__((ext_vector_type(2)));
typedef short s4 __attribute__((ext_vector_type(4)));
typedef float f4 __attribute__((ext_vector_type(4)));
typedef float f2 __attribute__((ext_vector_type(2)));
typedef unsigned u32;
typedef u32 u4 __attribute__((ext_vector_type(4)));
typedef u32 u2 __attribute__((ext_vector_type(2)));
#define LAS __attribute__((address_space(3)))
#define GAS __attribute__((address_space(1)))
#define DI __device__ __forceinline__
#define FENCE() asm volatile("" ::: "memory")
#define PIN(x) asm volatile("" : "+v"(x))
#define SCHED() __builtin_amdgcn_sched_barrier(0)
#define WAITV(n) asm volatile("s_waitcnt vmcnt(%0)" :: "n"(n) : "memory")
#define BARRIER() do { asm volatile("s_waitcnt lgkmcnt(0)" ::: "memory"); __builtin_amdgcn_s_barrier(); FENCE(); } while (0)
constexpr int SC1 = 16, NT = 2;
constexpr u32 SENT = 0xffffffffu;
constexpr int SPIN_MAX = 1 << 20;

DI u32 pk_h(float a, float b) { f2 v = {a, b}; h2 r = __builtin_convertvector(v, h2); return __builtin_bit_cast(u32, r); }
DI u32 pk_b(float a, float b) { f2 v = {a, b}; b2 r = __builtin_convertvector(v, b2); return __builtin_bit_cast(u32, r); }
DI float bf_lo(u32 v) { return __builtin_bit_cast(float, v << 16); }
DI float bf_hi(u32 v) { return __builtin_bit_cast(float, v & 0xffff0000u); }
DI f2 bf_2(u32 v) { return (f2){bf_lo(v), bf_hi(v)}; }
DI u32 pk_b2(f2 v) { return pk_b(v[0], v[1]); }
DI float rbf(float a) { return bf_lo(pk_b(a, a)); }
DI float cosr(float rev) { return __builtin_amdgcn_cosf(rev); }
DI float sinr(float rev) { return __builtin_amdgcn_sinf(rev); }
DI f2 lo2(f4 v) { return __builtin_shufflevector(v, v, 0, 1); }
DI f2 hi2(f4 v) { return __builtin_shufflevector(v, v, 2, 3); }
DI f2 fma2(f2 a, f2 b, f2 c) { return __builtin_elementwise_fma(a, b, c); }
DI u32 umin(u32 a, u32 b) { return a < b ? a : b; }
DI u32 umax(u32 a, u32 b) { return a > b ? a : b; }
DI u32 clean(u32 v) { return umin(v, SENT - 1); }
DI u32 max4(u4 v) { return umax(umax(umax(v[0], v[1]), v[2]), v[3]); }
#define Z4 ((f4){0, 0, 0, 0})
DI f4 mma_h(u4 a, u4 b, f4 c) { return __builtin_amdgcn_mfma_f32_16x16x32_f16(__builtin_bit_cast(h8, a), __builtin_bit_cast(h8, b), c, 0, 0, 0); }
DI f4 mma_b(u4 a, u4 b, f4 c) { return __builtin_amdgcn_mfma_f32_16x16x32_bf16(__builtin_bit_cast(b8, a), __builtin_bit_cast(b8, b), c, 0, 0, 0); }
DI f4 mma16_b(u2 a, u2 b, f4 c) { return __builtin_amdgcn_mfma_f32_16x16x16bf16_1k(__builtin_bit_cast(s4, a), __builtin_bit_cast(s4, b), c, 0, 0, 0); }
DI f4 mma16_h(u2 a, u2 b, f4 c) { return __builtin_amdgcn_mfma_f32_16x16x16f16(__builtin_bit_cast(h4, a), __builtin_bit_cast(h4, b), c, 0, 0, 0); }
typedef __amdgpu_buffer_rsrc_t rsrc_t;
DI rsrc_t mkrs(const void* p, u32 bytes) { return __builtin_amdgcn_make_buffer_rsrc((void*)p, 0, bytes, 0x00020000); }
template <int AUX> DI u4 bld4(rsrc_t r, int v, int s) { return __builtin_amdgcn_raw_buffer_load_b128(r, v, s, AUX); }
template <int AUX> DI u2 bld2(rsrc_t r, int v, int s) { return __builtin_amdgcn_raw_buffer_load_b64(r, v, s, AUX); }
template <int AUX> DI void bst4(u4 d, rsrc_t r, int v, int s) {
    __builtin_amdgcn_raw_buffer_store_b128(d, r, v, s, AUX);
    asm volatile("s_nop 1" :: "v"(d));
}
template <int AUX> DI void bst2(u2 d, rsrc_t r, int v, int s) { __builtin_amdgcn_raw_buffer_store_b64(d, r, v, s, AUX); }

template <int CTRL> DI float dpp(float v) {
    return __builtin_bit_cast(float, __builtin_amdgcn_update_dpp(0, __builtin_bit_cast(int, v), CTRL, 0xf, 0xf, true));
}
DI float swap_add16(float v) {
    u32 x = __builtin_bit_cast(u32, v), y = x;
    asm("" : "+v"(y));
    auto r = __builtin_amdgcn_permlane16_swap(x, y, false, false);
    return __builtin_bit_cast(float, (u32)r[0]) + __builtin_bit_cast(float, (u32)r[1]);
}
DI float swap_add32(float v) {
    u32 x = __builtin_bit_cast(u32, v), y = x;
    asm("" : "+v"(y));
    auto r = __builtin_amdgcn_permlane32_swap(x, y, false, false);
    return __builtin_bit_cast(float, (u32)r[0]) + __builtin_bit_cast(float, (u32)r[1]);
}
template <bool ALL> DI void red_lanes(float& a, float& b) {
    a += dpp<0xB1>(a); b += dpp<0xB1>(b);
    if (ALL) { a += dpp<0x4E>(a); b += dpp<0x4E>(b); }
    a += dpp<0x124>(a); b += dpp<0x124>(b);
    a += dpp<0x128>(a); b += dpp<0x128>(b);
    a = swap_add16(a); b = swap_add16(b);
    a = swap_add32(a); b = swap_add32(b);
}
DI float ln_inv(float var) { return __builtin_amdgcn_rcpf(rbf(__builtin_amdgcn_sqrtf(rbf(rbf(var) + 1e-5f)))); }

#define ST_DECL
#define ST(i) do { if (SB_MASK >> (i) & 1) SCHED(); } while (0)
#define ST_END(ws, kid)

#define FS 1
constexpr u32 WS_U = 0;
constexpr u32 WS_V = WS_U + (2u << 20);
constexpr u32 WS_LN2 = WS_V + (2u << 20);
constexpr u32 WS_WA = WS_LN2 + (2u << 20);
constexpr u32 WS_WB = WS_WA + 8192u;
constexpr u32 WS_UX = WS_WB + 8192u;
constexpr u32 WS_FA = WS_UX + 8192u;
constexpr u32 WS_FLAG = WS_FA + 16384u * FS;
constexpr u32 WS_EP = WS_FLAG + 32768u;
constexpr u32 WS_H1 = WS_WA + (512u << 10);
constexpr u32 WS_END = WS_H1 + 7340032u;

#define BGA 10
#define BGB 10
#define BGC 21
#define G_SLEEP 16
#define GA_SLEEP 4
#define GB_SLEEP 8
#define A_SLEEP 0
#define VPOS 0
#define LPOS 1
#define BG_DELAY 14
#define PRO_SLEEP 4
constexpr int BG5 = BGA, BG5b = BGB, BG6 = BGC;
constexpr int G1_DEPTH = 10, G2_DEPTH = 3;
constexpr int B_R = 5;
constexpr int CODE_LINES = 290, CODE_PF = 10;
constexpr int LDS_SCR = 114688;
constexpr int SCR_X = 8448;
constexpr int SCR_S = SCR_X + 20480;
constexpr int SCR_F = 49024;
constexpr int XCH = 147456;
constexpr int LDS_B = 163840;

DI void glds16(const void* sbase, u32 voff, u32 lds_dst) {
    asm volatile("s_mov_b32 m0, %2\n\ts_nop 0\n\tglobal_load_lds_dwordx4 %0, %1" :: "v"(voff), "s"(sbase), "s"(lds_dst) : "memory");
}
DI bool any_sent(u32 v) { return __builtin_amdgcn_ballot_w64(v == SENT) != 0; }
DI void poll2(u4& a, u4& c, rsrc_t rw, int vo, int sa, int sc) {
#pragma nounroll
    for (int sp = 0; sp < SPIN_MAX; ++sp) {
        const bool ba = max4(a) == SENT, bc = max4(c) == SENT;
        if (__builtin_amdgcn_ballot_w64(ba || bc) == 0) break;
        __builtin_amdgcn_s_sleep(1);
        FENCE();
        if (ba) a = bld4<SC1>(rw, vo, sa);
        if (bc) c = bld4<SC1>(rw, vo, sc);
    }
}
DI void wait_tok(u4& t0, u4& t1, rsrc_t rw, int vo, int so) {
    if (any_sent(umax(max4(t0), max4(t1)))) {
#pragma nounroll
        for (int sp = 0; sp < SPIN_MAX; ++sp) {
            FENCE();
            t0 = bld4<SC1>(rw, vo, so); t1 = bld4<SC1>(rw, vo, so + 16384);
            if (!any_sent(umax(max4(t0), max4(t1)))) break;
        }
    }
}
DI u4 ldw1(LAS char* wb, int kk, int ft) {
    s4 a0 = __builtin_amdgcn_ds_read_tr16_b64_v4i16((LAS s4*)(wb + kk * 32 * 224 + ft * 32));
    s4 a1 = __builtin_amdgcn_ds_read_tr16_b64_v4i16((LAS s4*)(wb + (kk * 32 + 16) * 224 + ft * 32));
    u2 x0 = __builtin_bit_cast(u2, a0), x1 = __builtin_bit_cast(u2, a1);
    return (u4){x0[0], x0[1], x1[0], x1[1]};
}
DI u2 pk4(f4 a) { return (u2){pk_b(a[0], a[1]), pk_b(a[2], a[3])}; }
DI u2 silu_pk2(u32 p0, u32 p1) {
    u32 o0, o1; float a0, a1, a2, a3, t0, t1, t2, t3;
    asm("v_lshlrev_b32 %2, 16, %10\n\tv_and_b32 %3, 0xffff0000, %10\n\tv_lshlrev_b32 %4, 16, %11\n\tv_and_b32 %5, 0xffff0000, %11\n\t"
        "v_mul_f32 %6, %12, %2\n\tv_mul_f32 %7, %12, %3\n\tv_mul_f32 %8, %12, %4\n\tv_mul_f32 %9, %12, %5\n\t"
        "v_exp_f32 %6, %6\n\tv_exp_f32 %7, %7\n\tv_exp_f32 %8, %8\n\tv_exp_f32 %9, %9\n\t"
        "v_add_f32 %6, 1.0, %6\n\tv_add_f32 %7, 1.0, %7\n\tv_add_f32 %8, 1.0, %8\n\tv_add_f32 %9, 1.0, %9\n\t"
        "v_rcp_f32 %6, %6\n\tv_rcp_f32 %7, %7\n\tv_rcp_f32 %8, %8\n\tv_rcp_f32 %9, %9\n\t"
        "v_mul_f32 %2, %2, %6\n\tv_mul_f32 %3, %3, %7\n\tv_mul_f32 %4, %4, %8\n\tv_mul_f32 %5, %5, %9\n\t"
        "v_cvt_pk_bf16_f32 %0, %2, %3\n\tv_cvt_pk_bf16_f32 %1, %4, %5"
        : "=&v"(o0), "=&v"(o1), "=&v"(a0), "=&v"(a1), "=&v"(a2), "=&v"(a3), "=&v"(t0), "=&v"(t1), "=&v"(t2), "=&v"(t3)
        : "v"(p0), "v"(p1), "s"(-1.4426950408889634f));
    return (u2){o0, o1};
}
DI u4 silu_pk4(u4 p) { u2 a = silu_pk2(p[0], p[1]), b = silu_pk2(p[2], p[3]); return (u4){a[0], a[1], b[0], b[1]}; }
DI void lds_set(LAS u32* p, u32 v) {
    asm volatile("s_waitcnt lgkmcnt(0)" ::: "memory");
    __hip_atomic_store(p, v, __ATOMIC_RELAXED, __HIP_MEMORY_SCOPE_WORKGROUP);
}
DI void lds_wait(LAS u32* p, u32 v) {
#pragma nounroll
    for (int sp = 0; sp < SPIN_MAX; ++sp) {
        if (__builtin_amdgcn_readfirstlane(__hip_atomic_load(p, __ATOMIC_RELAXED, __HIP_MEMORY_SCOPE_WORKGROUP)) >= v) break;
        __builtin_amdgcn_s_sleep(1);
    }
    FENCE();
}
DI void flag_wait(GAS u32* p, u32 tag) {
#pragma nounroll
    for (int sp = 0; sp < SPIN_MAX; ++sp) {
        if (__builtin_amdgcn_ballot_w64(__hip_atomic_load(p, __ATOMIC_RELAXED, __HIP_MEMORY_SCOPE_AGENT) != tag) == 0) break;
        __builtin_amdgcn_s_sleep(2);
    }
    FENCE();
}
#define SENT4 ((u4){SENT, SENT, SENT, SENT})

__global__ __launch_bounds__(512, 2) void k_fused(const uint16_t* x, const uint16_t* wr, const uint16_t* wi, const uint16_t* w1,
                                                  const uint16_t* w2, uint16_t* out, char* ws) {
    extern __shared__ __attribute__((aligned(16))) char smem[];
    const int tid = threadIdx.x, l = tid & 63, g = l >> 4, m = l & 15;
    const int w = __builtin_amdgcn_readfirstlane(tid >> 6);
    const int b = (blockIdx.x & 7) * 32 + (blockIdx.x >> 3);
    const int mt1 = (b >> 6) * 4 + ((b >> 3) & 3), nt1 = ((b >> 5) & 1) * 8 + (b & 7);
    const int grp = b >> 4, th = (b >> 3) & 1, fb = b & 7;
    const rsrc_t rw = mkrs(ws, WS_END), ro = mkrs(out, 2097152);
    char* scr = smem + LDS_SCR;
    LAS u32* hw = (LAS u32*)((LAS char*)smem + LDS_SCR + SCR_F);
    const u32 lds0 = (u32)(uintptr_t)(LAS char*)smem;
    const float mf = (float)m, gf = (float)g, sg = (m & 1) ? -1.0f : 1.0f;
    const bool aw = w < 4;
    ST_DECL;
    unsigned long long pc0;
    asm volatile("s_getpc_b64 %0" : "=s"(pc0));
    if (tid >= 448 && l < CODE_PF)
        asm volatile("s_mov_b32 m0, %2\n\ts_nop 0\n\tglobal_load_lds_dword %0, %1"
                     :: "v"(umin((blockIdx.x >> 3) * CODE_PF + l, CODE_LINES - 1) * 128), "s"(pc0), "s"(lds0 + LDS_SCR + SCR_F + 64) : "memory");
    const u32 epoch = __hip_atomic_load((GAS u32*)(ws + WS_EP), __ATOMIC_RELAXED, __HIP_MEMORY_SCOPE_AGENT);
    FENCE();
    u32 xd[8];
    const int f1 = b >> 5, cg = b & 31;
    const bool mir = cg >= 16;
    const rsrc_t rr = mkrs(wr, 1052672), ri = mkrs(wi, 1052672);
    u32 sr_, si_;
    if (aw) {
        __builtin_amdgcn_s_sleep(A_SLEEP);
        const u32* xr = (const u32*)(x + (256 * (2 * w + ((m >> 1) & 1)) + b) * 512) + 8 * g + 2 * (m >> 2) + (m & 1);
#pragma unroll
        for (int j = 0; j < 8; ++j) xd[j] = xr[32 * j];
    } else {
        bst4<SC1>(SENT4, rw, l * 16, WS_U + b * 8192 + (w - 4) * 2048); bst4<SC1>(SENT4, rw, l * 16 + 1024, WS_U + b * 8192 + (w - 4) * 2048);
        __builtin_amdgcn_raw_buffer_store_b32(SENT, rw, l < 6 ? (l >> 1) * 8192 + (l & 1) * 1024 : 0x7ffffff0,
                                              WS_WA + (w - 4) * 2048 + ((b >> 6) * 16 + (b & 15)) * 16 + ((b >> 4) & 3) * 4, SC1);
        FENCE();
        if (w == 7 && VPOS == 0) {
#pragma unroll
            for (int i = 0; i < 8; ++i) bst4<SC1>(SENT4, rw, l * 16 + (i & 3) * 1024, WS_V + b * 8192 + (i >> 2) * 4096);
        }
        if (w == 6) {
            const int c = (l >> 3) & 3, f_ = 8 * b + (l & 7);
            const int e1 = ((c & 1) ? (2048 - f_) & 2047 : f_) * 514 + (c >> 1) * 512;
            sr_ = __builtin_amdgcn_raw_buffer_load_b16(rr, e1, 0, 0); si_ = __builtin_amdgcn_raw_buffer_load_b16(ri, e1, 0, 0);
        }
    }
    FENCE();
    const int zo = (64 * g + (g < 2 ? m : (m + 15) & 15)) * 4;
    const int rq = ((b >> 3) + b) & 3;
    const int row0 = (tid & 255) / 14, cch0 = (tid & 255) - row0 * 14;
    const char* wsrc = (const char*)w1 + nt1 * 224;
    u32 w1o[7];
    auto w1_burst = [&](int i0, int n) {
#pragma unroll
        for (int i = i0; i < i0 + n; ++i) {
            const int q = (i / 7 + rq) & 3, j = i % 7;
            glds16(wsrc + q * (128 * 3584), w1o[j], lds0 + (w - 4) * 1024 + q * 28672 + j * 4096);
        }
    };
    const int xoff = (256 * w + b) * 1024 + m * 32 + g * 8;
    u2 xl[2];
    float c16[4], s16[4];
    f2 twc[2], tws[2];
    u4 Gre3, Gim3, SGre, SGim, GA[2];
    auto consts0 = [&]() {
#pragma unroll
        for (int u = 0; u < 4; ++u) {
            float a = (4.0f * gf + u) * (mf * (1.0f / 16));
            c16[u] = cosr(a); s16[u] = sinr(a);
            twc[u >> 1][u & 1] = cosr(a * (1.0f / 32)); tws[u >> 1][u & 1] = sinr(a * (1.0f / 32));
        }
    };
    auto consts3 = [&]() {
#pragma unroll
        for (int u = 0; u < 4; ++u) {
            const float k = 4.0f * gf + u;
            const bool k0 = u == 0 && g == 0;
            float cc = c16[u], ss = s16[u];
            Gre3[u] = pk_b(cc, -ss); Gim3[u] = pk_b(ss, cc);
            float a2 = (2.0f * k + 1.0f) * (mf * (1.0f / 32));
            u32 sp = pk_b(g < 2 ? 2.f * cc : 2.f * cosr(a2), g < 2 ? -2.f * ss : -2.f * sinr(a2));
            SGre[u] = k0 ? pk_b(1.0f, sg) : (g < 2 ? sp : 0u);
            SGim[u] = g < 2 ? 0u : sp;
            float a4 = k * (mf * (1.0f / 32)), c4 = 2.f * cosr(a4), s4_ = -2.f * sinr(a4);
            GA[0][u] = k0 ? pk_b(1.0f, sg) : pk_b(c4, s4_);
            GA[1][u] = k0 ? pk_b(1.0f, sg) : ((u & 1) ? pk_b(-c4, -s4_) : pk_b(c4, s4_));
        }
        PIN(Gre3); PIN(Gim3); PIN(SGre); PIN(SGim); PIN(GA[0]); PIN(GA[1]); PIN(twc[0]); PIN(twc[1]); PIN(tws[0]); PIN(tws[1]);
    };
    const int pw = (w + 4 * th) & 7;
    GAS u32* fl = (GAS u32*)(ws + WS_FLAG);
    int ko[7];
    {
        const int r6 = (fb < 6 ? fb : fb - 5) + 3 * (grp & 1);
#pragma unroll
        for (int ks = 0; ks < 6; ++ks) { int k = ks + r6; k = k >= 6 ? k - 6 : k; ko[ks] = k >= 6 ? k - 6 : k; }
        ko[6] = 6;
    }
    u4 W[7][4];
    const u32 w2vo = (l >> 3) * 1024 + (((l & 7) ^ (((l >> 4) & 1) << 2)) * 16);
    auto w2_glds = [&](int p_, int ks, u32 dst) {
        const int q = ko[ks];
        const char* src = (const char*)w2 + (224 * p_ + (ks == 6 ? 96 : (q < 3 ? 32 * q : 16 + 32 * q))) * 1024 + fb * 128;
        glds16(src, w2vo, dst); glds16(src + 8192, w2vo, dst + 1024);
        glds16(src + (ks == 6 ? 112 : 16) * 1024, w2vo, dst + 2048); glds16(src + (ks == 6 ? 120 : 24) * 1024, w2vo, dst + 3072);
    };
    LAS char* w2rd = (LAS char*)smem + (4 * g + (m >> 2)) * 128 + (((m & 3) ^ (((m >> 3) & 1) << 2)) * 16);
    auto w2_read = [&](u4 (&f)[4], int off) {
#pragma unroll
        for (int t = 0; t < 4; ++t) {
            LAS char* p = (LAS char*)((u32)(uintptr_t)w2rd ^ ((t >> 1) * 64)) + off + (t & 1) * 8;
            u2 x0 = __builtin_bit_cast(u2, __builtin_amdgcn_ds_read_tr16_b64_v4i16((LAS s4*)p));
            u2 x1 = __builtin_bit_cast(u2, __builtin_amdgcn_ds_read_tr16_b64_v4i16((LAS s4*)(p + 2048)));
            f[t] = (u4){x0[0], x0[1], x1[0], x1[1]};
        }
    };
    auto bg = [&](int i0, int i1) { w1_burst(i0, i1 - i0); };
    u32 tag;
    if (__builtin_expect(aw, 1)) {
        if (tid < 8) hw[tid] = 0;
        FENCE();
        ST(2);
        {
            u4 FAre, FAim, Fre, Fim, Sre, Sim; u2 Mb; f2 pc[2], ps[2];
            consts0();
            float c0[8], s0[8];
#pragma unroll
            for (int j = 0; j < 8; ++j) {
                float a = (gf + 4.0f * j) * (mf * (1.0f / 32));
                c0[j] = cosr(a); s0[j] = m ? -sinr(a) : ((g & 1) ? -1.0f : 1.0f);
            }
            const float mo = (2.0f * mf + 1.0f) * (1.0f / 32);
#pragma unroll
            for (int u = 0; u < 4; ++u) {
                FAre[u] = pk_h(c0[2 * u], c0[2 * u + 1]); FAim[u] = pk_h(s0[2 * u], s0[2 * u + 1]);
                float cc = c16[u], ss = s16[u];
                Fre[u] = pk_h(cc, ss); Fim[u] = pk_h(-ss, cc);
                float a2 = (4.0f * gf + u) * mo;
                u32 re = pk_h(m < 8 ? cc : cosr(a2), 0.f), im = pk_h(m < 8 ? (m ? -ss : ((u & 1) ? -1.0f : 1.0f)) : -sinr(a2), 0.f);
                Sre[u] = m < 8 ? re : re << 16; Sim[u] = m < 8 ? im : im << 16;
                pc[u & 1][u >> 1] = twc[u >> 1][u & 1]; ps[u & 1][u >> 1] = tws[u >> 1][u & 1];
            }
#pragma unroll
            for (int e = 0; e < 2; ++e) {
                float a = (float)(((m >> 1) * (256 * (2 * g + e) + b)) & 2047) * (1.0f / 2048);
                float cc = cosr(a), ss = sinr(a);
                Mb[e] = (m & 1) ? pk_h(-ss, cc) : pk_h(cc, ss);
            }
            PIN(FAre); PIN(FAim); PIN(Fre); PIN(Fim); PIN(Sre); PIN(Sim); PIN(Mb); PIN(pc[0]); PIN(pc[1]); PIN(ps[0]); PIN(ps[1]);
            ST(3);
            f2 v[8];
#pragma unroll
            for (int j = 0; j < 8; ++j) v[j] = bf_2(xd[j]);
            const u32 x0 = __builtin_amdgcn_readlane(xd[0], 0), x2 = __builtin_amdgcn_readlane(xd[0], 2);
            const float c = bf_lo((m & 2) ? x2 : x0);
            f2 q0 = {0.f, 0.f}, q1 = {0.f, 0.f};
#pragma unroll
            for (int j = 0; j < 8; ++j) { v[j] -= c; if (j & 1) q1 = fma2(v[j], v[j], q1); else q0 = fma2(v[j], v[j], q0); }
            f2 s2 = ((v[0] + v[1]) + (v[2] + v[3])) + ((v[4] + v[5]) + (v[6] + v[7]));
            q0 += q1;
            float s1 = s2[0] + s2[1], sq = q0[0] + q0[1];
            red_lanes<false>(s1, sq);
            s1 *= 1.0f / 512;
            const float inv = ln_inv(__builtin_fmaxf(sq * (1.0f / 512) - s1 * s1, 0.f));
            const float off = (c - rbf(c + s1)) * inv;
#pragma unroll
            for (int j = 0; j < 8; ++j) v[j] = fma2(v[j], (f2){inv, inv}, (f2){off, off});
            u4 B[2];
#pragma unroll
            for (int p = 0; p < 2; ++p) {
                u4 A = {pk_h(v[0][p], v[1][p]), pk_h(v[2][p], v[3][p]), pk_h(v[4][p], v[5][p]), pk_h(v[6][p], v[7][p])};
                f4 Dre = mma_h(A, FAre, Z4), Dim = mma_h(A, FAim, Z4);
#pragma unroll
                for (int r = 0; r < 2; ++r) {
                    f2 re = r ? hi2(Dre) : lo2(Dre), im = r ? hi2(Dim) : lo2(Dim);
                    f2 tr = fma2(im, ps[p], re * pc[p]), ti = fma2(-re, ps[p], im * pc[p]);
                    B[r][p] = pk_h(tr[0], ti[0]); B[r][2 + p] = pk_h(tr[1], ti[1]);
                }
            }
#pragma unroll
            for (int r = 0; r < 2; ++r) {
                f4 Gre = mma_h(Fre, B[r], Z4), Gim = mma_h(Fim, B[r], Z4), Hre = mma_h(Sre, B[r], Z4), Him = mma_h(Sim, B[r], Z4);
#pragma unroll
                for (int i = 0; i < 4; ++i) *(u32*)(scr + (2 * w + r) * 1024 + zo + i * 64) = pk_h(m ? Gre[i] : Hre[i], m ? Gim[i] : Him[i]);
            }
            BARRIER();
            ST(18);
            u4 r0 = *(const u4*)(scr + (2 * g) * 1024 + w * 256 + m * 16);
            u4 r1 = *(const u4*)(scr + (2 * g + 1) * 1024 + w * 256 + m * 16);
            u4 o0, o1;
#pragma unroll
            for (int t = 0; t < 4; ++t) {
                f4 D = mma16_h(Mb, (u2){r0[t], r1[t]}, Z4);
                o0[t] = clean(pk_h(D[0], D[1])); o1[t] = clean(pk_h(D[2], D[3]));
            }
            bst4<SC1>(o0, rw, g * 2048 + m * 16, WS_U + b * 8192 + w * 256);
            bst4<SC1>(o1, rw, g * 2048 + m * 16 + 1024, WS_U + b * 8192 + w * 256);
            if (w == 0 && m == 0) {
                const int xo = WS_UX + ((b >> 6) * 16 + (b & 15)) * 16 + ((b >> 4) & 3) * 4;
                __builtin_amdgcn_raw_buffer_store_b32(o0[0], rw, ((8 - 2 * g) & 7) * 1024, xo, SC1); __builtin_amdgcn_raw_buffer_store_b32(o1[0], rw, (7 - 2 * g) * 1024, xo, SC1);
            }
        }
        ST(4);
        u4 Fre, Fim, Gre, Gim; u2 Nb; f2 tc[2], ts[2];
#pragma unroll
        for (int u = 0; u < 4; ++u) {
            float cc = c16[u], ss = s16[u];
            Fre[u] = pk_h(0.25f * cc, 0.25f * ss); Fim[u] = pk_h(-0.25f * ss, 0.25f * cc);
            Gre[u] = pk_b(cc * (1.0f / 256), -ss * (1.0f / 256)); Gim[u] = pk_b(ss * (1.0f / 256), cc * (1.0f / 256));
            float a3 = (4.0f * gf + u) * (mf * (1.0f / 256));
            tc[u >> 1][u & 1] = cosr(a3); ts[u >> 1][u & 1] = sinr(a3);
        }
        PIN(Fre); PIN(Fim); PIN(Gre); PIN(Gim); PIN(tc[0]); PIN(tc[1]); PIN(ts[0]); PIN(ts[1]);
        xl[0] = *(const u2*)((const char*)x + xoff); xl[1] = *(const u2*)((const char*)x + xoff + 512);
        FENCE();
        tag = __builtin_amdgcn_readfirstlane(epoch) + 1u;
        consts3();
#pragma unroll
        for (int e = 0; e < 2; ++e) {
            float a1 = (float)(((2 * g + e) * (256 * (m >> 1) + b)) & 2047) * (1.0f / 2048);
            float c1 = cosr(a1), s1 = sinr(a1);
            Nb[e] = (m & 1) ? pk_b(s1, c1) : pk_b(c1, -s1);
        }
        PIN(Nb);
        WAITV(0);
        PIN(xl[0]); PIN(xl[1]);
        ST(19);
        BARRIER();
        ST(5);
        u4 d[2], wa, wb;
        const int uo = (tid >> 1) * 8192 + (tid & 1) * 16, us = WS_U + f1 * 1024 + cg * 32;
        d[0] = bld4<SC1>(rw, uo, us); d[1] = bld4<SC1>(rw, uo, us + 128 * 8192);
        u4 ux;
        if (cg == 0 && w == 0) {
            ux = bld4<SC1>(rw, l * 16, WS_UX + f1 * 1024);
            wa = bld4<SC1>(rw, l * 16, WS_WA + f1 * 1024); wb = bld4<SC1>(rw, l * 16, WS_WB + f1 * 1024);
        }
        poll2(d[0], d[1], rw, uo, us, us + 128 * 8192);
        char* sp = scr + SCR_S + (tid ^ ((tid >> 4) & 1)) * 16;
        *(u4*)sp = d[0]; *(u4*)(sp + 4096) = d[1];
        BARRIER();
        if (b == 0 && tid == 0) __hip_atomic_store((GAS u32*)(ws + WS_EP), tag, __ATOMIC_RELAXED, __HIP_MEMORY_SCOPE_AGENT);
        ST(6);
        u2 ld[4], wl[4];
        const int so = (64 * g + m) * 32 + (((w >> 1) ^ (m >> 3)) * 16) + (w & 1) * 8;
#pragma unroll
        for (int q = 0; q < 4; ++q) ld[q] = *(const u2*)(scr + SCR_S + so + 512 * q);
#pragma unroll
        for (int q = 0; q < 4; ++q) wl[q] = *(const u2*)(scr + SCR_S + so + 8192 + 512 * q);
        FENCE();
        auto fwd = [&](u4 A, f4& Xr, f4& Xi) {
            f4 Dre = mma_h(A, Fre, Z4), Dim = mma_h(A, Fim, Z4);
            f2 r0 = fma2(lo2(Dim), ts[0], lo2(Dre) * tc[0]), i0 = fma2(-lo2(Dre), ts[0], lo2(Dim) * tc[0]);
            f2 r1 = fma2(hi2(Dim), ts[1], hi2(Dre) * tc[1]), i1 = fma2(-hi2(Dre), ts[1], hi2(Dim) * tc[1]);
            u4 B = {pk_h(r0[0], i0[0]), pk_h(r0[1], i0[1]), pk_h(r1[0], i1[0]), pk_h(r1[1], i1[1])};
            Xr = mma_h(Fre, B, Z4); Xi = mma_h(Fim, B, Z4);
        };
        f4 Xre[2], Xim[2];
#pragma unroll
        for (int e = 0; e < 2; ++e) fwd((u4){ld[0][e], ld[1][e], ld[2][e], ld[3][e]}, Xre[e], Xim[e]);
        f2 yr[2][2], yi[2][2];
#pragma unroll
        for (int e = 0; e < 2; ++e)
#pragma unroll
            for (int hh = 0; hh < 2; ++hh) {
                f2 wre = {bf_lo(wl[2 * hh][e]), bf_lo(wl[2 * hh + 1][e])}, wim = {bf_hi(wl[2 * hh][e]), bf_hi(wl[2 * hh + 1][e])};
                f2 xr = hh ? hi2(Xre[e]) : lo2(Xre[e]), xi = hh ? hi2(Xim[e]) : lo2(Xim[e]);
                yr[e][hh] = fma2(-xi, wim, xr * wre); yi[e][hh] = fma2(xr, wim, xi * wre);
            }
        if (cg == 0 && w == 0) {
            poll2(wa, wb, rw, l * 16, WS_WA + f1 * 1024, WS_WB + f1 * 1024);
            poll2(ux, wb, rw, l * 16, WS_UX + f1 * 1024, WS_WB + f1 * 1024);
            f4 Mr, Mi;
            fwd(ux, Mr, Mi);
            f2* sx = (f2*)(scr + SCR_X);
#pragma unroll
            for (int i = 0; i < 4; ++i) sx[m + 16 * (4 * g + i)] = (f2){Mr[i], Mi[i]};
#pragma unroll
            for (int i = 0; i < 4; ++i) {
                f2 xm = sx[((f1 ? 255 : 256) - (m + 16 * (4 * g + i))) & 255];
                float ar = bf_lo(wa[i]), ai = bf_hi(wa[i]), br = bf_lo(wb[i]), bi = bf_hi(wb[i]);
                yr[0][i >> 1][i & 1] = Xre[0][i] * ar - Xim[0][i] * ai + xm[0] * br + xm[1] * bi;
                yi[0][i >> 1][i & 1] = Xre[0][i] * ai + Xim[0][i] * ar + xm[0] * bi - xm[1] * br;
            }
        }
        char* pv = scr;
#pragma unroll
        for (int e = 0; e < 2; ++e) {
            u4 Y = {pk_b(yr[e][0][0], yi[e][0][0]), pk_b(yr[e][0][1], yi[e][0][1]), pk_b(yr[e][1][0], yi[e][1][0]), pk_b(yr[e][1][1], yi[e][1][1])};
            f4 Dre = mma_b(Y, Gre, Z4), Dim = mma_b(Y, Gim, Z4);
            f2 r0 = fma2(-lo2(Dim), ts[0], lo2(Dre) * tc[0]), i0 = fma2(lo2(Dre), ts[0], lo2(Dim) * tc[0]);
            f2 r1 = fma2(-hi2(Dim), ts[1], hi2(Dre) * tc[1]), i1 = fma2(hi2(Dre), ts[1], hi2(Dim) * tc[1]);
            u4 A = {pk_b(r0[0], i0[0]), pk_b(r0[1], i0[1]), pk_b(r1[0], i1[0]), pk_b(r1[1], i1[1])};
            f4 Ere = mma_b(A, Gre, Z4), Eim = mma_b(A, Gim, Z4);
            u4 o;
#pragma unroll
            for (int i = 0; i < 4; ++i) o[i] = clean(pk_b(Ere[i], Eim[i]));
            *(u4*)(pv + (2 * w + e) * 1024 + ((m * 4 + g) ^ ((w >> 1) * 4)) * 16) = o;
        }
        BARRIER();
        ST(7);
        {
            const u32* q = (const u32*)(pv + (tid & 1) * 4096 + (((tid >> 1) * 4) ^ ((tid & 1) * 64)));
            const u4 o0 = {q[0], q[256], q[512], q[768]}, o1 = {q[128], q[384], q[640], q[896]};
            SCHED();
            bst4<SC1>(o0, rw, tid * 16, WS_V + b * 8192); bst4<SC1>(o1, rw, tid * 16, WS_V + b * 8192 + 4096);
        }
        WAITV(0);
        ST(8);
        const int vo = (g * 64 + (m >> 1)) * 8192 + (m & 1) * 16, vs = WS_V + w * 8 * 8192 + b * 32;
        u4 v0 = bld4<SC1>(rw, vo, vs), v1 = bld4<SC1>(rw, vo, vs + 32 * 8192);
        poll2(v0, v1, rw, vo, vs, vs + 32 * 8192);
        ST(9);
        u4 z0, z1;
#pragma unroll
        for (int t = 0; t < 4; ++t) {
            f4 D = mma16_b(Nb, (u2){v0[t], v1[t]}, Z4);
            z0[t] = pk_b(D[0], D[1]); z1[t] = pk_b(D[2], D[3]);
        }
        *(u4*)(scr + SCR_X + g * 2048 + w * 256 + m * 16) = z0;
        *(u4*)(scr + SCR_X + g * 2048 + 1024 + w * 256 + m * 16) = z1;
    } else {
        auto announce = [&](int which) {
            if (l < 8) __hip_atomic_store((GAS u32*)(ws + WS_FA) + (which * 2048 + l * 256 + b) * FS, tag, __ATOMIC_RELAXED, __HIP_MEMORY_SCOPE_AGENT);
            FENCE();
        };
        auto all_announced = [&](int which) {
#pragma nounroll
            for (int sp = 0; sp < SPIN_MAX; ++sp) {
                FENCE();
                u4 f;
#pragma unroll
                for (int i = 0; i < 4; ++i) f[i] = __builtin_amdgcn_raw_buffer_load_b32(rw, (l + 64 * i) * 4 * FS, WS_FA + (which * 8192 + (b >> 5) * 1024) * FS, SC1);
                if (__builtin_amdgcn_ballot_w64(f[0] != tag || f[1] != tag || f[2] != tag || f[3] != tag) == 0) break;
                __builtin_amdgcn_s_sleep(4);
            }
        };
        FENCE();
        ST(2);
        tag = __builtin_amdgcn_readfirstlane(epoch) + 1u;
        if (w == 6) WAITV(2); else if (w == 7 && VPOS == 0) WAITV(8); else WAITV(0);
        if (l < 2) __hip_atomic_store((GAS u32*)(ws + WS_FA) + ((2 * (w - 4) + l) * 256 + b) * FS, tag, __ATOMIC_RELAXED, __HIP_MEMORY_SCOPE_AGENT);
        FENCE();
        ST(18);
#pragma unroll
        for (int jj = 0; jj < 7; ++jj) {
            const int cj_ = (4 * jj) % 14, rj = 18 * jj + (4 * jj) / 14;
            w1o[jj] = (row0 + rj) * 3584 + (cch0 + cj_) * 16 + (cch0 + cj_ >= 14 ? 3584 - 224 : 0);
            PIN(w1o[jj]);
        }
        BARRIER();
        if (w == 6) {
            float re = bf_lo(sr_), im = bf_lo(si_);
            re += dpp<0x128>(re); im -= dpp<0x128>(im);
            u32 a = __builtin_bit_cast(u32, re), a2 = a, c = __builtin_bit_cast(u32, im), c2 = c;
            asm("" : "+v"(a2), "+v"(c2));
            auto pr = __builtin_amdgcn_permlane16_swap(a, a2, false, false);
            auto pi = __builtin_amdgcn_permlane16_swap(c, c2, false, false);
            const float ar = __builtin_bit_cast(float, (u32)pr[0]), br = __builtin_bit_cast(float, (u32)pr[1]);
            const float ai = __builtin_bit_cast(float, (u32)pi[0]), bi = __builtin_bit_cast(float, (u32)pi[1]);
            const int wo = WS_WA + ((b >> 6) * 16 + (b & 15)) * 16 + ((b >> 4) & 3) * 4;
            if (l < 8) {
                __builtin_amdgcn_raw_buffer_store_b32(clean(pk_b(0.25f * (ar + br), 0.25f * (ai + bi))), rw, l * 1024, wo, SC1);
                __builtin_amdgcn_raw_buffer_store_b32(clean(pk_b(0.25f * (ar - br), 0.25f * (ai - bi))), rw, l * 1024, wo + 8192, SC1);
            }
        }
        const int f = f1 + 8 * (tid & 255), k0 = 16 * cg - 8 * (cg & 1);
        const int e = mir ? ((2048 - f) & 2047) * 514 + (504 - k0) * 2 : f * 514 + k0 * 2;
        consts0();
        if (w == 7 && VPOS == 0) { WAITV(0); announce(1); }
        __builtin_amdgcn_s_sleep(GA_SLEEP);
        const u4 fa = bld4<0>(mkrs((cg & 1) ? wi : wr, 1052672), e, 0);
        FENCE();
        __builtin_amdgcn_s_sleep(GB_SLEEP);
        if (w == 4) all_announced(0);
        const u4 fb = bld4<0>(mkrs((cg & 1) ? wr : wi, 1052672), e, 0);
        xl[0] = *(const u2*)((const char*)x + xoff); xl[1] = *(const u2*)((const char*)x + xoff + 512);
        FENCE();
        ST(4);
        BARRIER();
        {
            u32 o[8];
            const u4 fr = (cg & 1) ? fb : fa, fi = (cg & 1) ? fa : fb;
#pragma unroll
            for (int c = 0; c < 8; ++c)
                o[c] = mir ? __builtin_amdgcn_perm(fi[3 - (c >> 1)], fr[3 - (c >> 1)], (c & 1) ? 0x05040100u : 0x07060302u) ^ 0x80000000u
                           : __builtin_amdgcn_perm(fi[c >> 1], fr[c >> 1], (c & 1) ? 0x07060302u : 0x05040100u);
            char* wp = scr + SCR_S + 8192 + (tid & 255) * 32;
            *(u4*)(wp + (m & 8) * 2) = (u4){o[0], o[1], o[2], o[3]};
            *(u4*)(wp + ((m & 8) * 2 ^ 16)) = (u4){o[4], o[5], o[6], o[7]};
        }
        FENCE();
        if (w == 7 && VPOS == 1) {
#pragma unroll
            for (int i = 0; i < 8; ++i) bst4<SC1>(SENT4, rw, l * 16 + (i & 3) * 1024, WS_V + b * 8192 + (i >> 2) * 4096);
            WAITV(0); announce(1);
        }
        if (LPOS == 1) {
#pragma unroll
        for (int i = 0; i < 2; ++i) bst4<SC1>(SENT4, rw, (l >> 3) * 256 + (l & 7) * 16 + i * 2048, WS_LN2 + (b >> 1) * 16384 + (b & 1) * 128 + (w - 4) * 4096);
        }
        PIN(xl[0]); PIN(xl[1]);
        consts3();
        ST(5);
        BARRIER();
        if (LPOS == 0) {
#pragma unroll
        for (int i = 0; i < 2; ++i) bst4<SC1>(SENT4, rw, (l >> 3) * 256 + (l & 7) * 16 + i * 2048, WS_LN2 + (b >> 1) * 16384 + (b & 1) * 128 + (w - 4) * 4096);
        }
        if (w == 5) all_announced(1);
        bg(0, BG5);
        WAITV(BG5);
        ST(6);
        BARRIER();
        __builtin_amdgcn_s_sleep(3);
        bg(BG5, BG5b);
        ST(7);
        __builtin_amdgcn_s_sleep(BG_DELAY);
        bg(BG5b, BG6);
    }
    BARRIER();
    {
        const u32* zp = (const u32*)(scr + SCR_X + w * 1024 + zo);
        u4 A = {zp[0], zp[16], zp[32], zp[48]};
        f4 Dre = mma_b(A, Gre3, Z4), Dim = mma_b(A, Gim3, Z4), Hre = mma_b(A, SGre, Z4), Him = mma_b(A, SGim, Z4);
        if (g == 0) { Dre[0] = Hre[0]; Dim[0] = Him[0]; }
        f2 r0 = fma2(-lo2(Dim), tws[0], lo2(Dre) * twc[0]), i0 = fma2(lo2(Dre), tws[0], lo2(Dim) * twc[0]);
        f2 r1 = fma2(-hi2(Dim), tws[1], hi2(Dre) * twc[1]), i1 = fma2(hi2(Dre), tws[1], hi2(Dim) * twc[1]);
        u4 A2 = {pk_b(r0[0], i0[0]), pk_b(r0[1], i0[1]), pk_b(r1[0], i1[0]), pk_b(r1[1], i1[1])};
        f2 h[4]; u2 hp[2];
#pragma unroll
        for (int t = 0; t < 2; ++t) {
            f4 Yv = mma_b(A2, GA[t], Z4);
            u32 h0 = pk_b2(bf_2(xl[t][0]) + bf_2(pk_b(Yv[0], Yv[1]))), h1 = pk_b2(bf_2(xl[t][1]) + bf_2(pk_b(Yv[2], Yv[3])));
            hp[t] = (u2){h0, h1};
            h[2 * t] = bf_2(h0); h[2 * t + 1] = bf_2(h1);
        }
        const float c = bf_lo(__builtin_amdgcn_readfirstlane(hp[0][0]));
        f2 q2 = {0.f, 0.f}, s2 = {0.f, 0.f};
#pragma unroll
        for (int j = 0; j < 4; ++j) { f2 d = h[j] - c; q2 = fma2(d, d, q2); s2 += d; }
        float s1 = s2[0] + s2[1], sq = q2[0] + q2[1];
        red_lanes<true>(s1, sq);
        s1 *= 1.0f / 512;
        const float inv = ln_inv(__builtin_fmaxf(sq * (1.0f / 512) - s1 * s1, 0.f));
        const float mb = rbf(c + s1);
#pragma unroll
        for (int t = 0; t < 2; ++t)
            *(u2*)(scr + ((8 * t + (m >> 1)) * 4 + g) * 128 + (w ^ (m >> 1)) * 16 + (m & 1) * 8) =
                (u2){clean(pk_b2(bf_2(pk_b2(h[2 * t] - mb)) * inv)), clean(pk_b2(bf_2(pk_b2(h[2 * t + 1] - mb)) * inv))};
        WAITV(0);
        BARRIER();
        u4 lo = *(const u4*)(scr + (tid ^ ((tid >> 5) & 7)) * 16);
        bst4<SC1>(lo, rw, (tid >> 3) * 256 + (tid & 7) * 16, WS_LN2 + (b >> 1) * 16384 + (b & 1) * 128);
        FENCE();
        if (aw) WAITV(0); else __builtin_amdgcn_s_sleep(8);
        bst2<SC1>(hp[0], ro, xoff, 0); bst2<SC1>(hp[1], ro, xoff + 512, 0);
        FENCE();
        ST(10);
    }
    auto stage_out = [&](int r) {
        w2_read(W[r], LDS_SCR + (w & 3) * 8192 + (r & 1) * 4096);
        asm volatile("s_waitcnt lgkmcnt(0)" ::: "memory");
        PIN(W[r][0]); PIN(W[r][1]); PIN(W[r][2]); PIN(W[r][3]);
        if (r + 2 < 7) w2_glds(pw, r + 2, lds0 + LDS_SCR + (w & 3) * 8192 + (r & 1) * 4096);
    };
    auto publish = [&]() {
        u32 prev = 0;
        if (l == 0) prev = __hip_atomic_fetch_add(hw + 4, 1u, __ATOMIC_RELAXED, __HIP_MEMORY_SCOPE_WORKGROUP);
        if (__builtin_amdgcn_readfirstlane(prev) == 7u && l < 2)
            __hip_atomic_store(fl + (l ? 4096 + (b >> 1) * 32 + (b & 1) : (mt1 * 8 + (nt1 >> 1)) * 32 + (nt1 & 1)), tag, __ATOMIC_RELAXED, __HIP_MEMORY_SCOPE_AGENT);
        FENCE();
    };
    {
        const int tg = w & 3;
        char* xch = smem + XCH + tg * 3072;
        const int hoff = WS_H1 + (mt1 * 8 + 2 * tg) * (56 * 1024) + (nt1 >> 1) * 7168;
        if (__builtin_expect(aw, 1)) {
            const int toff = WS_LN2 + (mt1 * 8 + 2 * tg) * 16384;
            LAS char* wb0 = (LAS char*)smem + (4 * g + (m >> 2)) * 224 + (m & 3) * 8;
            LAS char* wb[4];
            int tq[4];
#pragma unroll
            for (int q = 0; q < 4; ++q) { const int qq = (q + rq) & 3; wb[q] = wb0 + qq * 28672; tq[q] = toff + qq * 4096; }
            u4 T[2][16], A[2][7];
            auto ldt = [&](int kk) {
#pragma unroll
                for (int tt = 0; tt < 2; ++tt) T[tt][kk] = bld4<SC1>(rw, l * 16 + (kk & 3) * 1024, tq[kk >> 2] + tt * 16384);
            };
#pragma unroll
            for (int i = 0; i < 7; ++i) A[0][i] = ldw1(wb[0], 0, i);
            if (l == 0) __hip_atomic_fetch_add(hw + 5, 1u, __ATOMIC_RELAXED, __HIP_MEMORY_SCOPE_WORKGROUP);
            ST(11);
#pragma unroll
            for (int kk = 0; kk < G1_DEPTH; ++kk) { ldt(kk); SCHED(); }
            f4 acc[2][7];
#pragma unroll
            for (int tt = 0; tt < 2; ++tt)
#pragma unroll
                for (int i = 0; i < 7; ++i) acc[tt][i] = Z4;
#pragma unroll
            for (int kk = 0; kk < 16; ++kk) {
                if ((kk & 1) == 0) {
                    if (__builtin_expect(any_sent(umax(umax(max4(T[0][kk]), max4(T[1][kk])), umax(max4(T[0][kk + 1]), max4(T[1][kk + 1])))), 0)) {
                        wait_tok(T[0][kk], T[1][kk], rw, l * 16, tq[kk >> 2] + (kk & 3) * 1024);
                        wait_tok(T[0][kk + 1], T[1][kk + 1], rw, l * 16, tq[kk >> 2] + ((kk + 1) & 3) * 1024);
                    }
                    if (kk == 0) ST(12);
                    if (kk == (BG6 / 7) * 4 - 2) lds_wait(hw + 5, 8u);
                }
                if (kk + G1_DEPTH < 16) ldt(kk + G1_DEPTH);
                if (kk < 15) {
#pragma unroll
                    for (int i = 0; i < 7; ++i) A[(kk + 1) & 1][i] = ldw1(wb[(kk + 1) >> 2], (kk + 1) & 3, i);
                }
#pragma unroll
                for (int i = 0; i < 7; ++i)
#pragma unroll
                    for (int tt = 0; tt < 2; ++tt) acc[tt][i] = mma_b(A[kk & 1][i], T[tt][kk], acc[tt][i]);
#pragma unroll
                for (int i = 0; i < 14; ++i) {
                    __builtin_amdgcn_sched_group_barrier(0x8, 1, 0);
                    if (i == 1 || i == 7) __builtin_amdgcn_sched_group_barrier(0x20, 1, 0);
                    __builtin_amdgcn_sched_group_barrier(0x100, 1, 0);
                    if ((kk & 1) && i >= 3) __builtin_amdgcn_sched_group_barrier(0x2, 1, 0);
                }
                if (kk & 1) {} else SCHED();
            }
#pragma unroll
            for (int q = 0; q < 3; ++q) {
                u2 p0 = pk4(acc[1][2 * q]), p1 = pk4(acc[1][2 * q + 1]);
                *(u4*)(xch + q * 1024 + l * 16) = (u4){p0[0], p0[1], p1[0], p1[1]};
            }
            lds_set(hw + tg, 1u);
            ST(13);
#pragma unroll
            for (int q = 0; q < 3; ++q) {
                u2 p0 = pk4(acc[0][2 * q]), p1 = pk4(acc[0][2 * q + 1]);
                bst4<SC1>(silu_pk4((u4){p0[0], p0[1], p1[0], p1[1]}), rw, l * 16, hoff + ((nt1 & 1) * 3 + q) * 1024);
            }
#pragma unroll
            for (int tt = 0; tt < 2; ++tt) { u2 p = pk4(acc[tt][6]); bst2<SC1>(silu_pk2(p[0], p[1]), rw, l * 8, hoff + tt * (56 * 1024) + 6144 + (nt1 & 1) * 512); }
        } else {
            bg(BG6, 28);
            WAITV(0);
            if (l == 0) __hip_atomic_fetch_add(hw + 5, 1u, __ATOMIC_RELAXED, __HIP_MEMORY_SCOPE_WORKGROUP);
            __builtin_amdgcn_s_sleep(8);
            lds_wait(hw + 5, 8u);
            w2_glds(pw, 0, lds0 + LDS_SCR + tg * 8192);
            w2_glds(pw, 1, lds0 + LDS_SCR + tg * 8192 + 4096);
#pragma unroll
            for (int r = 0; r < B_R; ++r) { WAITV(4); stage_out(r); }
            ST(12);
            lds_wait(hw + tg, 1u);
            ST(13);
#pragma unroll
            for (int q = 0; q < 3; ++q)
                bst4<SC1>(silu_pk4(*(const u4*)(xch + q * 1024 + l * 16)), rw, l * 16, hoff + 56 * 1024 + ((nt1 & 1) * 3 + q) * 1024);
        }
        ST(14);
        WAITV(0);
        publish();
        ST(21);
        if (__builtin_expect(aw, 1)) {
            lds_wait(hw + 4, 8u);
#pragma unroll
            for (int ks = 0; ks < 7; ++ks) w2_glds(pw, ks, lds0 + tg * 28672 + ks * 4096);
        } else {
            stage_out(B_R);
#pragma unroll
            for (int r = B_R + 1; r < 7; ++r) { if (r + 1 < 7) WAITV(4); else WAITV(0); stage_out(r); }
        }
    }
    {
        ST(15);
        const int rot = w & 3;
        if (aw) {
            flag_wait(fl + ((l & 4) ? 4096 + (grp * 8 + th * 4 + rot) * 32 : (grp * 8 + (pw ^ ((l & 2) * 2))) * 32) + (l & 1), tag);
            lds_set(hw + 8 + rot, tag);
        } else {
#pragma nounroll
            for (int sp = 0; sp < SPIN_MAX; ++sp) {
                if (__builtin_amdgcn_readfirstlane(__hip_atomic_load(hw + 8 + rot, __ATOMIC_RELAXED, __HIP_MEMORY_SCOPE_WORKGROUP)) == tag) break;
                __builtin_amdgcn_s_sleep(1);
            }
            FENCE();
        }
        ST(16);
        const int toff = WS_H1 + (grp * 8 + th * 4) * (56 * 1024) + pw * 7168;
        int to[4];
#pragma unroll
        for (int tt = 0; tt < 4; ++tt) to[tt] = toff + ((tt + rot) & 3) * (56 * 1024);
        u4 T[7][4];
        auto ldtok = [&](int ks) {
#pragma unroll
            for (int tt = 0; tt < 4; ++tt) {
                if (ks < 6) T[ks][tt] = bld4<SC1>(rw, l * 16, to[tt] + ko[ks] * 1024);
                else {
                    u2 a = bld2<SC1>(rw, l * 8 + 2048, to[tt] + 4096), c = bld2<SC1>(rw, l * 8 + 2560, to[tt] + 4096);
                    T[ks][tt] = (u4){a[0], a[1], c[0], c[1]};
                }
            }
            SCHED();
        };
#pragma unroll
        for (int ks = 0; ks < G2_DEPTH; ++ks) ldtok(ks);
        if (aw) {
#pragma unroll
            for (int ks = 0; ks < 7; ++ks) w2_read(W[ks], rot * 28672 + ks * 4096);
            asm volatile("s_waitcnt lgkmcnt(0)" ::: "memory");
        }
        if (l == 0) __hip_atomic_fetch_add(hw + 7, 1u, __ATOMIC_RELAXED, __HIP_MEMORY_SCOPE_WORKGROUP);
        const int rho = grp * 128 + th * 64 + rot * 16 + m;
        const int ooff = (256 * (rho & 7) + (rho >> 3)) * 1024 + fb * 128 + (w >> 2) * 64 + g * 16;
        const u4 hres = bld4<SC1>(ro, ooff, 0);
        SCHED();
        f4 acc[4][4];
#pragma unroll
        for (int tt = 0; tt < 4; ++tt)
#pragma unroll
            for (int t = 0; t < 4; ++t) acc[tt][t] = Z4;
#pragma unroll
        for (int ks = 0; ks < 7; ++ks) {
            if (ks + G2_DEPTH < 7) { PIN(T[ks][0]); ldtok(ks + G2_DEPTH); }
#pragma unroll
            for (int tt = 0; tt < 4; ++tt)
#pragma unroll
                for (int t = 0; t < 4; ++t) {
                    acc[tt][t] = mma_b(W[ks][t], T[ks][tt], acc[tt][t]);
                }
            SCHED();
        }
        ST(17);
        lds_wait(hw + 7, 8u);
        char* dp = smem + w * 16384 + l * 16;
#pragma unroll
        for (int tt = 1; tt < 4; ++tt)
#pragma unroll
            for (int t = 0; t < 4; ++t) *(f4*)(dp + (((tt + rot) & 3) * 4 + t) * 1024) = acc[tt][t];
        f4 s0, s1;
        if (aw) { *(f4*)(dp + (rot * 4 + 2) * 1024) = acc[0][2]; *(f4*)(dp + (rot * 4 + 3) * 1024) = acc[0][3]; s0 = acc[0][0]; s1 = acc[0][1]; }
        else { *(f4*)(dp + (rot * 4) * 1024) = acc[0][0]; *(f4*)(dp + (rot * 4 + 1) * 1024) = acc[0][1]; s0 = acc[0][2]; s1 = acc[0][3]; }
        BARRIER();
        f4 r0[7], r1[7];
#pragma unroll
        for (int j = 1; j < 8; ++j) {
            const char* rp = smem + (((w + j) & 7) * 16 + rot * 4 + (w >> 2) * 2) * 1024 + l * 16;
            r0[j - 1] = *(const f4*)rp; r1[j - 1] = *(const f4*)(rp + 1024);
        }
        SCHED();
        s0 = ((s0 + r0[0]) + (r0[1] + r0[2])) + ((r0[3] + r0[4]) + (r0[5] + r0[6]));
        s1 = ((s1 + r1[0]) + (r1[1] + r1[2])) + ((r1[3] + r1[4]) + (r1[5] + r1[6]));
        u2 p0 = pk4(s0), p1 = pk4(s1);
        bst4<NT>((u4){pk_b2(bf_2(hres[0]) + bf_2(p0[0])), pk_b2(bf_2(hres[1]) + bf_2(p0[1])),
                           pk_b2(bf_2(hres[2]) + bf_2(p1[0])), pk_b2(bf_2(hres[3]) + bf_2(p1[1]))}, ro, ooff, 0);
    }
    ST_END(ws, 0);
}

extern "C" void kernel_launch(void* const* d_in, const int* in_sizes, int n_in, void* d_out, int out_size,
                              void* d_ws, size_t ws_size, hipStream_t stream) {
    (void)hipFuncSetAttribute((const void*)k_fused, hipFuncAttributeMaxDynamicSharedMemorySize, LDS_B);
    k_fused<<<256, 512, LDS_B, stream>>>((const uint16_t*)d_in[0], (const uint16_t*)d_in[1], (const uint16_t*)d_in[2],
                                         (const uint16_t*)d_in[4], (const uint16_t*)d_in[5], (uint16_t*)d_out, (char*)d_ws);
}
```

```cpp
#include <hip/hip_runtime.h>
#include <hip/hip_bf16.h>
#include <stdint.h>

typedef _Float16 h8 __attribute__((ext_vector_type(8)));
typedef _Float16 h4 __attribute__((ext_vector_type(4)));
typedef _Float16 h2 __attribute__((ext_vector_type(2)));
typedef __bf16 b8 __attribute__((ext_vector_type(8)));
typedef __bf16 b2 __attribute__((ext_vector_type(2)));
typedef short s4 __attribute__((ext_vector_type(4)));
typedef float f4 __attribute__((ext_vector_type(4)));
typedef float f2 __attribute__((ext_vector_type(2)));
typedef unsigned u32;
typedef u32 u4 __attribute__((ext_vector_type(4)));
typedef u32 u2 __attribute__((ext_vector_type(2)));
#define LAS __attribute__((address_space(3)))
#define GAS __attribute__((address_space(1)))
#define DI __device__ __forceinline__
#define FENCE() asm volatile("" ::: "memory")
#define PIN(x) asm volatile("" : "+v"(x))
#define SCHED() __builtin_amdgcn_sched_barrier(0)
#define WAITV(n) asm volatile("s_waitcnt vmcnt(%0)" :: "n"(n) : "memory")
#define BARRIER() do { asm volatile("s_waitcnt lgkmcnt(0)" ::: "memory"); __builtin_amdgcn_s_barrier(); FENCE(); } while (0)
constexpr int SC1 = 16, NT = 2;
constexpr u32 SENT = 0xffffffffu;
constexpr int SPIN_MAX = 1 << 20;

DI u32 pk_h(float a, float b) { f2 v = {a, b}; h2 r = __builtin_convertvector(v, h2); return __builtin_bit_cast(u32, r); }
DI u32 pk_b(float a, float b) { f2 v = {a, b}; b2 r = __builtin_convertvector(v, b2); return __builtin_bit_cast(u32, r); }
DI float bf_lo(u32 v) { return __builtin_bit_cast(float, v << 16); }
DI float bf_hi(u32 v) { return __builtin_bit_cast(float, v & 0xffff0000u); }
DI f2 bf_2(u32 v) { return (f2){bf_lo(v), bf_hi(v)}; }
DI u32 pk_b2(f2 v) { return pk_b(v[0], v[1]); }
DI float rbf(float a) { return bf_lo(pk_b(a, a)); }
DI float cosr(float rev) { return __builtin_amdgcn_cosf(rev); }
DI float sinr(float rev) { return __builtin_amdgcn_sinf(rev); }
DI f2 lo2(f4 v) { return __builtin_shufflevector(v, v, 0, 1); }
DI f2 hi2(f4 v) { return __builtin_shufflevector(v, v, 2, 3); }
DI f2 fma2(f2 a, f2 b, f2 c) { return __builtin_elementwise_fma(a, b, c); }
DI u32 umin(u32 a, u32 b) { return a < b ? a : b; }
DI u32 umax(u32 a, u32 b) { return a > b ? a : b; }
DI u32 clean(u32 v) { return umin(v, SENT - 1); }
DI u32 max4(u4 v) { return umax(umax(umax(v[0], v[1]), v[2]), v[3]); }
#define Z4 ((f4){0, 0, 0, 0})
DI f4 mma_h(u4 a, u4 b, f4 c) { return __builtin_amdgcn_mfma_f32_16x16x32_f16(__builtin_bit_cast(h8, a), __builtin_bit_cast(h8, b), c, 0, 0, 0); }
DI f4 mma_b(u4 a, u4 b, f4 c) { return __builtin_amdgcn_mfma_f32_16x16x32_bf16(__builtin_bit_cast(b8, a), __builtin_bit_cast(b8, b), c, 0, 0, 0); }
DI f4 mma16_b(u2 a, u2 b, f4 c) { return __builtin_amdgcn_mfma_f32_16x16x16bf16_1k(__builtin_bit_cast(s4, a), __builtin_bit_cast(s4, b), c, 0, 0, 0); }
DI f4 mma16_h(u2 a, u2 b, f4 c) { return __builtin_amdgcn_mfma_f32_16x16x16f16(__builtin_bit_cast(h4, a), __builtin_bit_cast(h4, b), c, 0, 0, 0); }
typedef __amdgpu_buffer_rsrc_t rsrc_t;
DI rsrc_t mkrs(const void* p, u32 bytes) { return __builtin_amdgcn_make_buffer_rsrc((void*)p, 0, bytes, 0x00020000); }
template <int AUX> DI u4 bld4(rsrc_t r, int v, int s) { return __builtin_amdgcn_raw_buffer_load_b128(r, v, s, AUX); }
template <int AUX> DI u2 bld2(rsrc_t r, int v, int s) { return __builtin_amdgcn_raw_buffer_load_b64(r, v, s, AUX); }
template <int AUX> DI void bst4(u4 d, rsrc_t r, int v, int s) {
    __builtin_amdgcn_raw_buffer_store_b128(d, r, v, s, AUX);
    asm volatile("s_nop 1" :: "v"(d));
}
template <int AUX> DI void bst2(u2 d, rsrc_t r, int v, int s) { __builtin_amdgcn_raw_buffer_store_b64(d, r, v, s, AUX); }

template <int CTRL> DI float dpp(float v) {
    return __builtin_bit_cast(float, __builtin_amdgcn_update_dpp(0, __builtin_bit_cast(int, v), CTRL, 0xf, 0xf, true));
}
DI float swap_add16(float v) {
    u32 x = __builtin_bit_cast(u32, v), y = x;
    asm("" : "+v"(y));
    auto r = __builtin_amdgcn_permlane16_swap(x, y, false, false);
    return __builtin_bit_cast(float, (u32)r[0]) + __builtin_bit_cast(float, (u32)r[1]);
}
DI float swap_add32(float v) {
    u32 x = __builtin_bit_cast(u32, v), y = x;
    asm("" : "+v"(y));
    auto r = __builtin_amdgcn_permlane32_swap(x, y, false, false);
    return __builtin_bit_cast(float, (u32)r[0]) + __builtin_bit_cast(float, (u32)r[1]);
}
template <bool ALL> DI void red_lanes(float& a, float& b) {
    a += dpp<0xB1>(a); b += dpp<0xB1>(b);
    if (ALL) { a += dpp<0x4E>(a); b += dpp<0x4E>(b); }
    a += dpp<0x124>(a); b += dpp<0x124>(b);
    a += dpp<0x128>(a); b += dpp<0x128>(b);
    a = swap_add16(a); b = swap_add16(b);
    a = swap_add32(a); b = swap_add32(b);
}
DI float ln_inv(float var) { return __builtin_amdgcn_rcpf(rbf(__builtin_amdgcn_sqrtf(rbf(rbf(var) + 1e-5f)))); }

#define ST_DECL
#define ST(i)
#define ST_END(ws, kid)

#define FS 1
constexpr u32 WS_U = 0;
constexpr u32 WS_V = WS_U + (2u << 20);
constexpr u32 WS_LN2 = WS_V + (2u << 20);
constexpr u32 WS_WA = WS_LN2 + (2u << 20);
constexpr u32 WS_FA = WS_WA + 32768u;
constexpr u32 WS_FLAG = WS_FA + 16384u * FS;
constexpr u32 WS_EP = WS_FLAG + 32768u;
constexpr u32 WS_H1 = WS_WA + (512u << 10);
constexpr u32 WS_END = WS_H1 + 7340032u;

#define BGA 10
#define BGB 10
#define BGC 21
#define G_SLEEP 16
#define GA_SLEEP 4
#define GB_SLEEP 8
#define A_SLEEP 0
#define VPOS 0
#define LPOS 1
#define SP_SLEEP 6
#define SP_BURST 1
#define EARLY_STAGE 1
#define STG_SLEEP 8
#define H_SLEEP 2
#define BRR 5
#define BG_DELAY 14
#define PRO_SLEEP 4
constexpr int BG5 = BGA, BG5b = BGB, BG6 = BGC;
constexpr int G1_DEPTH = 10, G2_DEPTH = 3;
constexpr int B_R = BRR;
constexpr int CODE_LINES = 290, CODE_PF = 10;
constexpr int LDS_SCR = 114688;
constexpr int SCR_X = 8448;
constexpr int SCR_S = SCR_X + 20480;
constexpr int SCR_F = 49024;
constexpr int XCH = 147456;
constexpr int LDS_B = 163840;

DI void glds16(const void* sbase, u32 voff, u32 lds_dst) {
    asm volatile("s_mov_b32 m0, %2\n\ts_nop 0\n\tglobal_load_lds_dwordx4 %0, %1" :: "v"(voff), "s"(sbase), "s"(lds_dst) : "memory");
}
DI bool any_sent(u32 v) { return __builtin_amdgcn_ballot_w64(v == SENT) != 0; }
DI void poll2(u4& a, u4& c, rsrc_t rw, int vo, int sa, int sc) {
#pragma nounroll
    for (int sp = 0; sp < SPIN_MAX; ++sp) {
        const bool ba = max4(a) == SENT, bc = max4(c) == SENT;
        if (__builtin_amdgcn_ballot_w64(ba || bc) == 0) break;
        __builtin_amdgcn_s_sleep(1);
        FENCE();
        if (ba) a = bld4<SC1>(rw, vo, sa);
        if (bc) c = bld4<SC1>(rw, vo, sc);
    }
}
DI void wait_tok(u4& t0, u4& t1, rsrc_t rw, int vo, int so) {
    if (any_sent(umax(max4(t0), max4(t1)))) {
#pragma nounroll
        for (int sp = 0; sp < SPIN_MAX; ++sp) {
            FENCE();
            t0 = bld4<SC1>(rw, vo, so); t1 = bld4<SC1>(rw, vo, so + 16384);
            if (!any_sent(umax(max4(t0), max4(t1)))) break;
        }
    }
}
DI u4 ldw1(LAS char* wb, int kk, int ft) {
    s4 a0 = __builtin_amdgcn_ds_read_tr16_b64_v4i16((LAS s4*)(wb + kk * 32 * 224 + ft * 32));
    s4 a1 = __builtin_amdgcn_ds_read_tr16_b64_v4i16((LAS s4*)(wb + (kk * 32 + 16) * 224 + ft * 32));
    u2 x0 = __builtin_bit_cast(u2, a0), x1 = __builtin_bit_cast(u2, a1);
    return (u4){x0[0], x0[1], x1[0], x1[1]};
}
DI u2 pk4(f4 a) { return (u2){pk_b(a[0], a[1]), pk_b(a[2], a[3])}; }
DI u2 silu_pk2(u32 p0, u32 p1) {
    u32 o0, o1; float a0, a1, a2, a3, t0, t1, t2, t3;
    asm("v_lshlrev_b32 %2, 16, %10\n\tv_and_b32 %3, 0xffff0000, %10\n\tv_lshlrev_b32 %4, 16, %11\n\tv_and_b32 %5, 0xffff0000, %11\n\t"
        "v_mul_f32 %6, %12, %2\n\tv_mul_f32 %7, %12, %3\n\tv_mul_f32 %8, %12, %4\n\tv_mul_f32 %9, %12, %5\n\t"
        "v_exp_f32 %6, %6\n\tv_exp_f32 %7, %7\n\tv_exp_f32 %8, %8\n\tv_exp_f32 %9, %9\n\t"
        "v_add_f32 %6, 1.0, %6\n\tv_add_f32 %7, 1.0, %7\n\tv_add_f32 %8, 1.0, %8\n\tv_add_f32 %9, 1.0, %9\n\t"
        "v_rcp_f32 %6, %6\n\tv_rcp_f32 %7, %7\n\tv_rcp_f32 %8, %8\n\tv_rcp_f32 %9, %9\n\t"
        "v_mul_f32 %2, %2, %6\n\tv_mul_f32 %3, %3, %7\n\tv_mul_f32 %4, %4, %8\n\tv_mul_f32 %5, %5, %9\n\t"
        "v_cvt_pk_bf16_f32 %0, %2, %3\n\tv_cvt_pk_bf16_f32 %1, %4, %5"
        : "=&v"(o0), "=&v"(o1), "=&v"(a0), "=&v"(a1), "=&v"(a2), "=&v"(a3), "=&v"(t0), "=&v"(t1), "=&v"(t2), "=&v"(t3)
        : "v"(p0), "v"(p1), "s"(-1.4426950408889634f));
    return (u2){o0, o1};
}
DI u4 silu_pk4(u4 p) { u2 a = silu_pk2(p[0], p[1]), b = silu_pk2(p[2], p[3]); return (u4){a[0], a[1], b[0], b[1]}; }
DI void lds_set(LAS u32* p, u32 v) {
    asm volatile("s_waitcnt lgkmcnt(0)" ::: "memory");
    __hip_atomic_store(p, v, __ATOMIC_RELAXED, __HIP_MEMORY_SCOPE_WORKGROUP);
}
DI void lds_wait(LAS u32* p, u32 v) {
#pragma nounroll
    for (int sp = 0; sp < SPIN_MAX; ++sp) {
        if (__builtin_amdgcn_readfirstlane(__hip_atomic_load(p, __ATOMIC_RELAXED, __HIP_MEMORY_SCOPE_WORKGROUP)) >= v) break;
        __builtin_amdgcn_s_sleep(1);
    }
    FENCE();
}
DI void flag_wait(GAS u32* p, u32 tag) {
#pragma nounroll
    for (int sp = 0; sp < SPIN_MAX; ++sp) {
        if (__builtin_amdgcn_ballot_w64(__hip_atomic_load(p, __ATOMIC_RELAXED, __HIP_MEMORY_SCOPE_AGENT) != tag) == 0) break;
        __builtin_amdgcn_s_sleep(2);
    }
    FENCE();
}
#define SENT4 ((u4){SENT, SENT, SENT, SENT})

__global__ __launch_bounds__(512, 2) void k_fused(const uint16_t* x, const uint16_t* wr, const uint16_t* wi, const uint16_t* w1,
                                                  const uint16_t* w2, uint16_t* out, char* ws) {
    extern __shared__ __attribute__((aligned(16))) char smem[];
    const int tid = threadIdx.x, l = tid & 63, g = l >> 4, m = l & 15;
    const int w = __builtin_amdgcn_readfirstlane(tid >> 6);
    const int b = (blockIdx.x & 7) * 32 + (blockIdx.x >> 3);
    const int mt1 = (b >> 6) * 4 + ((b >> 3) & 3), nt1 = ((b >> 5) & 1) * 8 + (b & 7);
    const int grp = b >> 4, th = (b >> 3) & 1, fb = b & 7;
    const rsrc_t rw = mkrs(ws, WS_END), ro = mkrs(out, 2097152);
    char* scr = smem + LDS_SCR;
    LAS u32* hw = (LAS u32*)((LAS char*)smem + LDS_SCR + SCR_F);
    const u32 lds0 = (u32)(uintptr_t)(LAS char*)smem;
    const float mf = (float)m, gf = (float)g, sg = (m & 1) ? -1.0f : 1.0f;
    const bool aw = w < 4;
    ST_DECL;
    unsigned long long pc0;
    asm volatile("s_getpc_b64 %0" : "=s"(pc0));
    if (tid >= 448 && l < CODE_PF)
        asm volatile("s_mov_b32 m0, %2\n\ts_nop 0\n\tglobal_load_lds_dword %0, %1"
                     :: "v"(umin((blockIdx.x >> 3) * CODE_PF + l, CODE_LINES - 1) * 128), "s"(pc0), "s"(lds0 + LDS_SCR + SCR_F + 64) : "memory");
    const u32 epoch = __hip_atomic_load((GAS u32*)(ws + WS_EP), __ATOMIC_RELAXED, __HIP_MEMORY_SCOPE_AGENT);
    FENCE();
    u32 xd[8];
    const int f1 = b >> 5, cg = b & 31;
    const bool mir = cg >= 16;
    const rsrc_t rr = mkrs(wr, 1052672), ri = mkrs(wi, 1052672);
    u32 sr_, si_;
    if (aw) {
        __builtin_amdgcn_s_sleep(A_SLEEP);
        const u32* xr = (const u32*)(x + (256 * (2 * w + ((m >> 1) & 1)) + b) * 512) + 8 * g + 2 * (m >> 2) + (m & 1);
#pragma unroll
        for (int j = 0; j < 8; ++j) xd[j] = xr[32 * j];
    } else {
        bst4<SC1>(SENT4, rw, l * 16, WS_U + b * 8192 + (w - 4) * 2048); bst4<SC1>(SENT4, rw, l * 16 + 1024, WS_U + b * 8192 + (w - 4) * 2048);
        bst4<SC1>(SENT4, rw, l < 2 ? l * 4096 : 0x7ffffff0, WS_WA + (w - 4) * 8192 + b * 16);
        FENCE();
        if (w == 7 && VPOS == 0) {
#pragma unroll
            for (int i = 0; i < 8; ++i) bst4<SC1>(SENT4, rw, l * 16 + (i & 3) * 1024, WS_V + b * 8192 + (i >> 2) * 4096);
        }
        if (w == 6) {
            const int c = (l >> 3) & 3, f_ = 8 * b + (l & 7);
            const int e1 = ((c & 1) ? (2048 - f_) & 2047 : f_) * 514 + (c >> 1) * 512;
            sr_ = __builtin_amdgcn_raw_buffer_load_b16(rr, e1, 0, 0); si_ = __builtin_amdgcn_raw_buffer_load_b16(ri, e1, 0, 0);
        }
    }
    FENCE();
    const int zo = (64 * g + (g < 2 ? m : (m + 15) & 15)) * 4;
    const int rq = ((b >> 3) + b) & 3;
    const int row0 = (tid & 255) / 14, cch0 = (tid & 255) - row0 * 14;
    const char* wsrc = (const char*)w1 + nt1 * 224;
    u32 w1o[7];
    auto w1_burst = [&](int i0, int n) {
#pragma unroll
        for (int i = i0; i < i0 + n; ++i) {
            const int q = (i / 7 + rq) & 3, j = i % 7;
            glds16(wsrc + q * (128 * 3584), w1o[j], lds0 + (w - 4) * 1024 + q * 28672 + j * 4096);
        }
    };
    const int xoff = (256 * w + b) * 1024 + m * 32 + g * 8;
    u2 xl[2];
    float c16[4], s16[4];
    f2 twc[2], tws[2];
    u4 Gre3, Gim3, SGre, SGim, GA[2];
    auto consts0 = [&]() {
#pragma unroll
        for (int u = 0; u < 4; ++u) {
            float a = (4.0f * gf + u) * (mf * (1.0f / 16));
            c16[u] = cosr(a); s16[u] = sinr(a);
            twc[u >> 1][u & 1] = cosr(a * (1.0f / 32)); tws[u >> 1][u & 1] = sinr(a * (1.0f / 32));
        }
    };
    auto consts3 = [&]() {
#pragma unroll
        for (int u = 0; u < 4; ++u) {
            const float k = 4.0f * gf + u;
            const bool k0 = u == 0 && g == 0;
            float cc = c16[u], ss = s16[u];
            Gre3[u] = pk_b(cc, -ss); Gim3[u] = pk_b(ss, cc);
            float a2 = (2.0f * k + 1.0f) * (mf * (1.0f / 32));
            u32 sp = pk_b(g < 2 ? 2.f * cc : 2.f * cosr(a2), g < 2 ? -2.f * ss : -2.f * sinr(a2));
            SGre[u] = k0 ? pk_b(1.0f, sg) : (g < 2 ? sp : 0u);
            SGim[u] = g < 2 ? 0u : sp;
            float a4 = k * (mf * (1.0f / 32)), c4 = 2.f * cosr(a4), s4_ = -2.f * sinr(a4);
            GA[0][u] = k0 ? pk_b(1.0f, sg) : pk_b(c4, s4_);
            GA[1][u] = k0 ? pk_b(1.0f, sg) : ((u & 1) ? pk_b(-c4, -s4_) : pk_b(c4, s4_));
        }
        PIN(Gre3); PIN(Gim3); PIN(SGre); PIN(SGim); PIN(GA[0]); PIN(GA[1]); PIN(twc[0]); PIN(twc[1]); PIN(tws[0]); PIN(tws[1]);
    };
    const int pw = (w + 4 * th) & 7;
    GAS u32* fl = (GAS u32*)(ws + WS_FLAG);
    int ko[7];
    {
        const int r6 = (fb < 6 ? fb : fb - 5) + 3 * (grp & 1);
#pragma unroll
        for (int ks = 0; ks < 6; ++ks) { int k = ks + r6; k = k >= 6 ? k - 6 : k; ko[ks] = k >= 6 ? k - 6 : k; }
        ko[6] = 6;
    }
    u4 W[7][4];
    const u32 w2vo = (l >> 3) * 1024 + (((l & 7) ^ (((l >> 4) & 1) << 2)) * 16);
    auto w2_glds = [&](int p_, int ks, u32 dst) {
        const int q = ko[ks];
        const char* src = (const char*)w2 + (224 * p_ + (ks == 6 ? 96 : (q < 3 ? 32 * q : 16 + 32 * q))) * 1024 + fb * 128;
        glds16(src, w2vo, dst); glds16(src + 8192, w2vo, dst + 1024);
        glds16(src + (ks == 6 ? 112 : 16) * 1024, w2vo, dst + 2048); glds16(src + (ks == 6 ? 120 : 24) * 1024, w2vo, dst + 3072);
    };
    LAS char* w2rd = (LAS char*)smem + (4 * g + (m >> 2)) * 128 + (((m & 3) ^ (((m >> 3) & 1) << 2)) * 16);
    auto w2_read = [&](u4 (&f)[4], int off) {
#pragma unroll
        for (int t = 0; t < 4; ++t) {
            LAS char* p = (LAS char*)((u32)(uintptr_t)w2rd ^ ((t >> 1) * 64)) + off + (t & 1) * 8;
            u2 x0 = __builtin_bit_cast(u2, __builtin_amdgcn_ds_read_tr16_b64_v4i16((LAS s4*)p));
            u2 x1 = __builtin_bit_cast(u2, __builtin_amdgcn_ds_read_tr16_b64_v4i16((LAS s4*)(p + 2048)));
            f[t] = (u4){x0[0], x0[1], x1[0], x1[1]};
        }
    };
    auto bg = [&](int i0, int i1) { w1_burst(i0, i1 - i0); };
    u32 tag;
    if (__builtin_expect(aw, 1)) {
        if (tid < 8) hw[tid] = 0;
        FENCE();
        ST(2);
        {
            u4 FAre, FAim, Fre, Fim, Sre, Sim; u2 Mb; f2 pc[2], ps[2];
            consts0();
            float c0[8], s0[8];
#pragma unroll
            for (int j = 0; j < 8; ++j) {
                float a = (gf + 4.0f * j) * (mf * (1.0f / 32));
                c0[j] = cosr(a); s0[j] = m ? -sinr(a) : ((g & 1) ? -1.0f : 1.0f);
            }
            const float mo = (2.0f * mf + 1.0f) * (1.0f / 32);
#pragma unroll
            for (int u = 0; u < 4; ++u) {
                FAre[u] = pk_h(c0[2 * u], c0[2 * u + 1]); FAim[u] = pk_h(s0[2 * u], s0[2 * u + 1]);
                float cc = c16[u], ss = s16[u];
                Fre[u] = pk_h(cc, ss); Fim[u] = pk_h(-ss, cc);
                float a2 = (4.0f * gf + u) * mo;
                u32 re = pk_h(m < 8 ? cc : cosr(a2), 0.f), im = pk_h(m < 8 ? (m ? -ss : ((u & 1) ? -1.0f : 1.0f)) : -sinr(a2), 0.f);
                Sre[u] = m < 8 ? re : re << 16; Sim[u] = m < 8 ? im : im << 16;
                pc[u & 1][u >> 1] = twc[u >> 1][u & 1]; ps[u & 1][u >> 1] = tws[u >> 1][u & 1];
            }
#pragma unroll
            for (int e = 0; e < 2; ++e) {
                float a = (float)(((m >> 1) * (256 * (2 * g + e) + b)) & 2047) * (1.0f / 2048);
                float cc = cosr(a), ss = sinr(a);
                Mb[e] = (m & 1) ? pk_h(-ss, cc) : pk_h(cc, ss);
            }
            PIN(FAre); PIN(FAim); PIN(Fre); PIN(Fim); PIN(Sre); PIN(Sim); PIN(Mb); PIN(pc[0]); PIN(pc[1]); PIN(ps[0]); PIN(ps[1]);
            ST(3);
            f2 v[8];
#pragma unroll
            for (int j = 0; j < 8; ++j) v[j] = bf_2(xd[j]);
            const u32 x0 = __builtin_amdgcn_readlane(xd[0], 0), x2 = __builtin_amdgcn_readlane(xd[0], 2);
            const float c = bf_lo((m & 2) ? x2 : x0);
            f2 q0 = {0.f, 0.f}, q1 = {0.f, 0.f};
#pragma unroll
            for (int j = 0; j < 8; ++j) { v[j] -= c; if (j & 1) q1 = fma2(v[j], v[j], q1); else q0 = fma2(v[j], v[j], q0); }
            f2 s2 = ((v[0] + v[1]) + (v[2] + v[3])) + ((v[4] + v[5]) + (v[6] + v[7]));
            q0 += q1;
            float s1 = s2[0] + s2[1], sq = q0[0] + q0[1];
            red_lanes<false>(s1, sq);
            s1 *= 1.0f / 512;
            const float inv = ln_inv(__builtin_fmaxf(sq * (1.0f / 512) - s1 * s1, 0.f));
            const float off = (c - rbf(c + s1)) * inv;
#pragma unroll
            for (int j = 0; j < 8; ++j) v[j] = fma2(v[j], (f2){inv, inv}, (f2){off, off});
            u4 B[2];
#pragma unroll
            for (int p = 0; p < 2; ++p) {
                u4 A = {pk_h(v[0][p], v[1][p]), pk_h(v[2][p], v[3][p]), pk_h(v[4][p], v[5][p]), pk_h(v[6][p], v[7][p])};
                f4 Dre = mma_h(A, FAre, Z4), Dim = mma_h(A, FAim, Z4);
#pragma unroll
                for (int r = 0; r < 2; ++r) {
                    f2 re = r ? hi2(Dre) : lo2(Dre), im = r ? hi2(Dim) : lo2(Dim);
                    f2 tr = fma2(im, ps[p], re * pc[p]), ti = fma2(-re, ps[p], im * pc[p]);
                    B[r][p] = pk_h(tr[0], ti[0]); B[r][2 + p] = pk_h(tr[1], ti[1]);
                }
            }
#pragma unroll
            for (int r = 0; r < 2; ++r) {
                f4 Gre = mma_h(Fre, B[r], Z4), Gim = mma_h(Fim, B[r], Z4), Hre = mma_h(Sre, B[r], Z4), Him = mma_h(Sim, B[r], Z4);
#pragma unroll
                for (int i = 0; i < 4; ++i) *(u32*)(scr + (2 * w + r) * 1024 + zo + i * 64) = pk_h(m ? Gre[i] : Hre[i], m ? Gim[i] : Him[i]);
            }
            BARRIER();
            ST(18);
            u4 r0 = *(const u4*)(scr + (2 * g) * 1024 + w * 256 + m * 16);
            u4 r1 = *(const u4*)(scr + (2 * g + 1) * 1024 + w * 256 + m * 16);
            u4 o0, o1;
#pragma unroll
            for (int t = 0; t < 4; ++t) {
                f4 D = mma16_h(Mb, (u2){r0[t], r1[t]}, Z4);
                o0[t] = clean(pk_h(D[0], D[1])); o1[t] = clean(pk_h(D[2], D[3]));
            }
            bst4<SC1>(o0, rw, g * 2048 + m * 16, WS_U + b * 8192 + w * 256);
            bst4<SC1>(o1, rw, g * 2048 + m * 16 + 1024, WS_U + b * 8192 + w * 256);
            if (w == 0 && m == 0) {
                __builtin_amdgcn_raw_buffer_store_b32(o0[0], rw, ((8 - 2 * g) & 7) * 4096, WS_WA + b * 16, SC1);
                __builtin_amdgcn_raw_buffer_store_b32(o1[0], rw, (7 - 2 * g) * 4096, WS_WA + b * 16, SC1);
            }
        }
        ST(4);
        u4 Fre, Fim, Gre, Gim; u2 Nb; f2 tc[2], ts[2];
#pragma unroll
        for (int u = 0; u < 4; ++u) {
            float cc = c16[u], ss = s16[u];
            Fre[u] = pk_h(0.25f * cc, 0.25f * ss); Fim[u] = pk_h(-0.25f * ss, 0.25f * cc);
            Gre[u] = pk_b(cc * (1.0f / 256), -ss * (1.0f / 256)); Gim[u] = pk_b(ss * (1.0f / 256), cc * (1.0f / 256));
            float a3 = (4.0f * gf + u) * (mf * (1.0f / 256));
            tc[u >> 1][u & 1] = cosr(a3); ts[u >> 1][u & 1] = sinr(a3);
        }
        PIN(Fre); PIN(Fim); PIN(Gre); PIN(Gim); PIN(tc[0]); PIN(tc[1]); PIN(ts[0]); PIN(ts[1]);
        xl[0] = *(const u2*)((const char*)x + xoff); xl[1] = *(const u2*)((const char*)x + xoff + 512);
        FENCE();
        tag = __builtin_amdgcn_readfirstlane(epoch) + 1u;
        consts3();
#pragma unroll
        for (int e = 0; e < 2; ++e) {
            float a1 = (float)(((2 * g + e) * (256 * (m >> 1) + b)) & 2047) * (1.0f / 2048);
            float c1 = cosr(a1), s1 = sinr(a1);
            Nb[e] = (m & 1) ? pk_b(s1, c1) : pk_b(c1, -s1);
        }
        PIN(Nb);
        WAITV(0);
        PIN(xl[0]); PIN(xl[1]);
        ST(19);
        BARRIER();
        ST(5);
        u4 d[2], rec[4];
        const int uo = (tid >> 1) * 8192 + (tid & 1) * 16, us = WS_U + f1 * 1024 + cg * 32;
        d[0] = bld4<SC1>(rw, uo, us); d[1] = bld4<SC1>(rw, uo, us + 128 * 8192);
        poll2(d[0], d[1], rw, uo, us, us + 128 * 8192);
        char* sp = scr + SCR_S + (tid ^ ((tid >> 4) & 1)) * 16;
        *(u4*)sp = d[0]; *(u4*)(sp + 4096) = d[1];
        BARRIER();
        if (b == 0 && tid == 0) __hip_atomic_store((GAS u32*)(ws + WS_EP), tag, __ATOMIC_RELAXED, __HIP_MEMORY_SCOPE_AGENT);
        const int ro_ = (64 * g + m) * 16, rs_ = WS_WA + f1 * 4096;
        if (cg == 0 && w == 0) {
#pragma unroll
            for (int i = 0; i < 4; ++i) rec[i] = bld4<SC1>(rw, ro_ + i * 256, rs_);
        }
        ST(6);
        u2 ld[4], wl[4];
        const int so = (64 * g + m) * 32 + (((w >> 1) ^ (m >> 3)) * 16) + (w & 1) * 8;
#pragma unroll
        for (int q = 0; q < 4; ++q) ld[q] = *(const u2*)(scr + SCR_S + so + 512 * q);
#pragma unroll
        for (int q = 0; q < 4; ++q) wl[q] = *(const u2*)(scr + SCR_S + so + 8192 + 512 * q);
        FENCE();
        auto fwd = [&](u4 A, f4& Xr, f4& Xi) {
            f4 Dre = mma_h(A, Fre, Z4), Dim = mma_h(A, Fim, Z4);
            f2 r0 = fma2(lo2(Dim), ts[0], lo2(Dre) * tc[0]), i0 = fma2(-lo2(Dre), ts[0], lo2(Dim) * tc[0]);
            f2 r1 = fma2(hi2(Dim), ts[1], hi2(Dre) * tc[1]), i1 = fma2(-hi2(Dre), ts[1], hi2(Dim) * tc[1]);
            u4 B = {pk_h(r0[0], i0[0]), pk_h(r0[1], i0[1]), pk_h(r1[0], i1[0]), pk_h(r1[1], i1[1])};
            Xr = mma_h(Fre, B, Z4); Xi = mma_h(Fim, B, Z4);
        };
        f4 Xre[2], Xim[2];
#pragma unroll
        for (int e = 0; e < 2; ++e) fwd((u4){ld[0][e], ld[1][e], ld[2][e], ld[3][e]}, Xre[e], Xim[e]);
        f2 yr[2][2], yi[2][2];
#pragma unroll
        for (int e = 0; e < 2; ++e)
#pragma unroll
            for (int hh = 0; hh < 2; ++hh) {
                f2 wre = {bf_lo(wl[2 * hh][e]), bf_lo(wl[2 * hh + 1][e])}, wim = {bf_hi(wl[2 * hh][e]), bf_hi(wl[2 * hh + 1][e])};
                f2 xr = hh ? hi2(Xre[e]) : lo2(Xre[e]), xi = hh ? hi2(Xim[e]) : lo2(Xim[e]);
                yr[e][hh] = fma2(-xi, wim, xr * wre); yi[e][hh] = fma2(xr, wim, xi * wre);
            }
        if (cg == 0 && w == 0) {
#pragma nounroll
            for (int sp = 0; sp < SPIN_MAX; ++sp) {
                u32 mx = 0;
#pragma unroll
                for (int i = 0; i < 4; ++i) mx = umax(mx, umax(umax(rec[i][0], rec[i][1]), rec[i][2]));
                if (!any_sent(mx)) break;
                FENCE();
#pragma unroll
                for (int i = 0; i < 4; ++i) rec[i] = bld4<SC1>(rw, ro_ + i * 256, rs_);
            }
            f4 Mr, Mi;
            fwd((u4){rec[0][0], rec[1][0], rec[2][0], rec[3][0]}, Mr, Mi);
            f2* sx = (f2*)(scr + SCR_X);
#pragma unroll
            for (int i = 0; i < 4; ++i) sx[m + 16 * (4 * g + i)] = (f2){Mr[i], Mi[i]};
#pragma unroll
            for (int i = 0; i < 4; ++i) {
                f2 xm = sx[((f1 ? 255 : 256) - (m + 16 * (4 * g + i))) & 255];
                float ar = bf_lo(rec[i][1]), ai = bf_hi(rec[i][1]), br = bf_lo(rec[i][2]), bi = bf_hi(rec[i][2]);
                yr[0][i >> 1][i & 1] = Xre[0][i] * ar - Xim[0][i] * ai + xm[0] * br + xm[1] * bi;
                yi[0][i >> 1][i & 1] = Xre[0][i] * ai + Xim[0][i] * ar + xm[0] * bi - xm[1] * br;
            }
        }
        char* pv = scr;
#pragma unroll
        for (int e = 0; e < 2; ++e) {
            u4 Y = {pk_b(yr[e][0][0], yi[e][0][0]), pk_b(yr[e][0][1], yi[e][0][1]), pk_b(yr[e][1][0], yi[e][1][0]), pk_b(yr[e][1][1], yi[e][1][1])};
            f4 Dre = mma_b(Y, Gre, Z4), Dim = mma_b(Y, Gim, Z4);
            f2 r0 = fma2(-lo2(Dim), ts[0], lo2(Dre) * tc[0]), i0 = fma2(lo2(Dre), ts[0], lo2(Dim) * tc[0]);
            f2 r1 = fma2(-hi2(Dim), ts[1], hi2(Dre) * tc[1]), i1 = fma2(hi2(Dre), ts[1], hi2(Dim) * tc[1]);
            u4 A = {pk_b(r0[0], i0[0]), pk_b(r0[1], i0[1]), pk_b(r1[0], i1[0]), pk_b(r1[1], i1[1])};
            f4 Ere = mma_b(A, Gre, Z4), Eim = mma_b(A, Gim, Z4);
            u4 o;
#pragma unroll
            for (int i = 0; i < 4; ++i) o[i] = clean(pk_b(Ere[i], Eim[i]));
            *(u4*)(pv + (2 * w + e) * 1024 + ((m * 4 + g) ^ ((w >> 1) * 4)) * 16) = o;
        }
        BARRIER();
        ST(7);
        {
            const u32* q = (const u32*)(pv + (tid & 1) * 4096 + (((tid >> 1) * 4) ^ ((tid & 1) * 64)));
            const u4 o0 = {q[0], q[256], q[512], q[768]}, o1 = {q[128], q[384], q[640], q[896]};
            SCHED();
            bst4<SC1>(o0, rw, tid * 16, WS_V + b * 8192); bst4<SC1>(o1, rw, tid * 16, WS_V + b * 8192 + 4096);
        }
        WAITV(0);
        ST(8);
        const int vo = (g * 64 + (m >> 1)) * 8192 + (m & 1) * 16, vs = WS_V + w * 8 * 8192 + b * 32;
        u4 v0 = bld4<SC1>(rw, vo, vs), v1 = bld4<SC1>(rw, vo, vs + 32 * 8192);
        poll2(v0, v1, rw, vo, vs, vs + 32 * 8192);
        ST(9);
        u4 z0, z1;
#pragma unroll
        for (int t = 0; t < 4; ++t) {
            f4 D = mma16_b(Nb, (u2){v0[t], v1[t]}, Z4);
            z0[t] = pk_b(D[0], D[1]); z1[t] = pk_b(D[2], D[3]);
        }
        *(u4*)(scr + SCR_X + g * 2048 + w * 256 + m * 16) = z0;
        *(u4*)(scr + SCR_X + g * 2048 + 1024 + w * 256 + m * 16) = z1;
    } else {
        auto announce = [&](int which) {
            if (l < 8) __hip_atomic_store((GAS u32*)(ws + WS_FA) + (which * 2048 + l * 256 + b) * FS, tag, __ATOMIC_RELAXED, __HIP_MEMORY_SCOPE_AGENT);
            FENCE();
        };
        auto all_announced = [&](int which) {
#pragma nounroll
            for (int sp = 0; sp < SPIN_MAX; ++sp) {
                FENCE();
                u4 f;
#pragma unroll
                for (int i = 0; i < 4; ++i) f[i] = __builtin_amdgcn_raw_buffer_load_b32(rw, (l + 64 * i) * 4 * FS, WS_FA + (which * 8192 + (b >> 5) * 1024) * FS, SC1);
                if (__builtin_amdgcn_ballot_w64(f[0] != tag || f[1] != tag || f[2] != tag || f[3] != tag) == 0) break;
                __builtin_amdgcn_s_sleep(4);
            }
        };
        FENCE();
        ST(2);
        tag = __builtin_amdgcn_readfirstlane(epoch) + 1u;
        if (w == 6) WAITV(2); else if (w == 7 && VPOS == 0) WAITV(8); else WAITV(0);
        if (l < 2) __hip_atomic_store((GAS u32*)(ws + WS_FA) + ((2 * (w - 4) + l) * 256 + b) * FS, tag, __ATOMIC_RELAXED, __HIP_MEMORY_SCOPE_AGENT);
        FENCE();
        ST(18);
#pragma unroll
        for (int jj = 0; jj < 7; ++jj) {
            const int cj_ = (4 * jj) % 14, rj = 18 * jj + (4 * jj) / 14;
            w1o[jj] = (row0 + rj) * 3584 + (cch0 + cj_) * 16 + (cch0 + cj_ >= 14 ? 3584 - 224 : 0);
            PIN(w1o[jj]);
        }
        BARRIER();
        if (w == 6) {
            float re = bf_lo(sr_), im = bf_lo(si_);
            re += dpp<0x128>(re); im -= dpp<0x128>(im);
            u32 a = __builtin_bit_cast(u32, re), a2 = a, c = __builtin_bit_cast(u32, im), c2 = c;
            asm("" : "+v"(a2), "+v"(c2));
            auto pr = __builtin_amdgcn_permlane16_swap(a, a2, false, false);
            auto pi = __builtin_amdgcn_permlane16_swap(c, c2, false, false);
            const float ar = __builtin_bit_cast(float, (u32)pr[0]), br = __builtin_bit_cast(float, (u32)pr[1]);
            const float ai = __builtin_bit_cast(float, (u32)pi[0]), bi = __builtin_bit_cast(float, (u32)pi[1]);
            if (l < 8) {
                __builtin_amdgcn_raw_buffer_store_b32(clean(pk_b(0.25f * (ar + br), 0.25f * (ai + bi))), rw, l * 4096 + 4, WS_WA + b * 16, SC1);
                __builtin_amdgcn_raw_buffer_store_b32(clean(pk_b(0.25f * (ar - br), 0.25f * (ai - bi))), rw, l * 4096 + 8, WS_WA + b * 16, SC1);
            }
        }
        const int f = f1 + 8 * (tid & 255), k0 = 16 * cg - 8 * (cg & 1);
        const int e = mir ? ((2048 - f) & 2047) * 514 + (504 - k0) * 2 : f * 514 + k0 * 2;
        consts0();
        if (w == 7 && VPOS == 0) { WAITV(0); announce(1); }
        __builtin_amdgcn_s_sleep(GA_SLEEP);
        const u4 fa = bld4<0>(mkrs((cg & 1) ? wi : wr, 1052672), e, 0);
        FENCE();
        __builtin_amdgcn_s_sleep(GB_SLEEP);
        if (w == 4) all_announced(0);
        const u4 fb = bld4<0>(mkrs((cg & 1) ? wr : wi, 1052672), e, 0);
        xl[0] = *(const u2*)((const char*)x + xoff); xl[1] = *(const u2*)((const char*)x + xoff + 512);
        FENCE();
        ST(4);
        BARRIER();
        {
            u32 o[8];
            const u4 fr = (cg & 1) ? fb : fa, fi = (cg & 1) ? fa : fb;
#pragma unroll
            for (int c = 0; c < 8; ++c)
                o[c] = mir ? __builtin_amdgcn_perm(fi[3 - (c >> 1)], fr[3 - (c >> 1)], (c & 1) ? 0x05040100u : 0x07060302u) ^ 0x80000000u
                           : __builtin_amdgcn_perm(fi[c >> 1], fr[c >> 1], (c & 1) ? 0x07060302u : 0x05040100u);
            char* wp = scr + SCR_S + 8192 + (tid & 255) * 32;
            *(u4*)(wp + (m & 8) * 2) = (u4){o[0], o[1], o[2], o[3]};
            *(u4*)(wp + ((m & 8) * 2 ^ 16)) = (u4){o[4], o[5], o[6], o[7]};
        }
        FENCE();
        if (w == 7 && VPOS == 1) {
#pragma unroll
            for (int i = 0; i < 8; ++i) bst4<SC1>(SENT4, rw, l * 16 + (i & 3) * 1024, WS_V + b * 8192 + (i >> 2) * 4096);
            WAITV(0); announce(1);
        }
        if (LPOS == 1) {
#pragma unroll
        for (int i = 0; i < 2; ++i) bst4<SC1>(SENT4, rw, (l >> 3) * 256 + (l & 7) * 16 + i * 2048, WS_LN2 + (b >> 1) * 16384 + (b & 1) * 128 + (w - 4) * 4096);
        }
        PIN(xl[0]); PIN(xl[1]);
        consts3();
        ST(5);
        BARRIER();
        if (LPOS == 0) {
#pragma unroll
        for (int i = 0; i < 2; ++i) bst4<SC1>(SENT4, rw, (l >> 3) * 256 + (l & 7) * 16 + i * 2048, WS_LN2 + (b >> 1) * 16384 + (b & 1) * 128 + (w - 4) * 4096);
        }
        if (w == 5) all_announced(1);
        if (cg == 0) __builtin_amdgcn_s_sleep(SP_SLEEP);
        if (cg != 0 || SP_BURST) bg(0, BG5);
        if (cg != 0 || SP_BURST) WAITV(BG5); else WAITV(0);
        ST(6);
        BARRIER();
        __builtin_amdgcn_s_sleep(3);
        bg(BG5, BG5b);
        ST(7);
        __builtin_amdgcn_s_sleep(BG_DELAY);
        if (cg == 0 && !SP_BURST) bg(0, BG5);
        bg(BG5b, BG6);
    }
    BARRIER();
    {
        const u32* zp = (const u32*)(scr + SCR_X + w * 1024 + zo);
        u4 A = {zp[0], zp[16], zp[32], zp[48]};
        f4 Dre = mma_b(A, Gre3, Z4), Dim = mma_b(A, Gim3, Z4), Hre = mma_b(A, SGre, Z4), Him = mma_b(A, SGim, Z4);
        if (g == 0) { Dre[0] = Hre[0]; Dim[0] = Him[0]; }
        f2 r0 = fma2(-lo2(Dim), tws[0], lo2(Dre) * twc[0]), i0 = fma2(lo2(Dre), tws[0], lo2(Dim) * twc[0]);
        f2 r1 = fma2(-hi2(Dim), tws[1], hi2(Dre) * twc[1]), i1 = fma2(hi2(Dre), tws[1], hi2(Dim) * twc[1]);
        u4 A2 = {pk_b(r0[0], i0[0]), pk_b(r0[1], i0[1]), pk_b(r1[0], i1[0]), pk_b(r1[1], i1[1])};
        f2 h[4]; u2 hp[2];
#pragma unroll
        for (int t = 0; t < 2; ++t) {
            f4 Yv = mma_b(A2, GA[t], Z4);
            u32 h0 = pk_b2(bf_2(xl[t][0]) + bf_2(pk_b(Yv[0], Yv[1]))), h1 = pk_b2(bf_2(xl[t][1]) + bf_2(pk_b(Yv[2], Yv[3])));
            hp[t] = (u2){h0, h1};
            h[2 * t] = bf_2(h0); h[2 * t + 1] = bf_2(h1);
        }
        const float c = bf_lo(__builtin_amdgcn_readfirstlane(hp[0][0]));
        f2 q2 = {0.f, 0.f}, s2 = {0.f, 0.f};
#pragma unroll
        for (int j = 0; j < 4; ++j) { f2 d = h[j] - c; q2 = fma2(d, d, q2); s2 += d; }
        float s1 = s2[0] + s2[1], sq = q2[0] + q2[1];
        red_lanes<true>(s1, sq);
        s1 *= 1.0f / 512;
        const float inv = ln_inv(__builtin_fmaxf(sq * (1.0f / 512) - s1 * s1, 0.f));
        const float mb = rbf(c + s1);
#pragma unroll
        for (int t = 0; t < 2; ++t)
            *(u2*)(scr + ((8 * t + (m >> 1)) * 4 + g) * 128 + (w ^ (m >> 1)) * 16 + (m & 1) * 8) =
                (u2){clean(pk_b2(bf_2(pk_b2(h[2 * t] - mb)) * inv)), clean(pk_b2(bf_2(pk_b2(h[2 * t + 1] - mb)) * inv))};
        WAITV(0);
        BARRIER();
        u4 lo = *(const u4*)(scr + (tid ^ ((tid >> 5) & 7)) * 16);
        bst4<SC1>(lo, rw, (tid >> 3) * 256 + (tid & 7) * 16, WS_LN2 + (b >> 1) * 16384 + (b & 1) * 128);
        FENCE();
        if (aw && l == 0) __hip_atomic_fetch_add(hw + 6, 1u, __ATOMIC_RELAXED, __HIP_MEMORY_SCOPE_WORKGROUP);
        if (aw) WAITV(0); else __builtin_amdgcn_s_sleep(H_SLEEP);
        bst2<SC1>(hp[0], ro, xoff, 0); bst2<SC1>(hp[1], ro, xoff + 512, 0);
        FENCE();
        ST(10);
    }
    auto stage_out = [&](int r) {
        w2_read(W[r], LDS_SCR + (w & 3) * 8192 + (r & 1) * 4096);
        asm volatile("s_waitcnt lgkmcnt(0)" ::: "memory");
        PIN(W[r][0]); PIN(W[r][1]); PIN(W[r][2]); PIN(W[r][3]);
        if (r + 2 < 7) w2_glds(pw, r + 2, lds0 + LDS_SCR + (w & 3) * 8192 + (r & 1) * 4096);
    };
    auto publish = [&]() {
        u32 prev = 0;
        if (l == 0) prev = __hip_atomic_fetch_add(hw + 4, 1u, __ATOMIC_RELAXED, __HIP_MEMORY_SCOPE_WORKGROUP);
        if (__builtin_amdgcn_readfirstlane(prev) == 7u && l < 2)
            __hip_atomic_store(fl + (l ? 4096 + (b >> 1) * 32 + (b & 1) : (mt1 * 8 + (nt1 >> 1)) * 32 + (nt1 & 1)), tag, __ATOMIC_RELAXED, __HIP_MEMORY_SCOPE_AGENT);
        FENCE();
    };
    {
        const int tg = w & 3;
        char* xch = smem + XCH + tg * 3072;
        const int hoff = WS_H1 + (mt1 * 8 + 2 * tg) * (56 * 1024) + (nt1 >> 1) * 7168;
        if (__builtin_expect(aw, 1)) {
            const int toff = WS_LN2 + (mt1 * 8 + 2 * tg) * 16384;
            LAS char* wb0 = (LAS char*)smem + (4 * g + (m >> 2)) * 224 + (m & 3) * 8;
            LAS char* wb[4];
            int tq[4];
#pragma unroll
            for (int q = 0; q < 4; ++q) { const int qq = (q + rq) & 3; wb[q] = wb0 + qq * 28672; tq[q] = toff + qq * 4096; }
            u4 T[2][16], A[2][7];
            auto ldt = [&](int kk) {
#pragma unroll
                for (int tt = 0; tt < 2; ++tt) T[tt][kk] = bld4<SC1>(rw, l * 16 + (kk & 3) * 1024, tq[kk >> 2] + tt * 16384);
            };
#pragma unroll
            for (int i = 0; i < 7; ++i) A[0][i] = ldw1(wb[0], 0, i);
            if (l == 0) __hip_atomic_fetch_add(hw + 5, 1u, __ATOMIC_RELAXED, __HIP_MEMORY_SCOPE_WORKGROUP);
            ST(11);
#pragma unroll
            for (int kk = 0; kk < G1_DEPTH; ++kk) { ldt(kk); SCHED(); }
            f4 acc[2][7];
#pragma unroll
            for (int tt = 0; tt < 2; ++tt)
#pragma unroll
                for (int i = 0; i < 7; ++i) acc[tt][i] = Z4;
#pragma unroll
            for (int kk = 0; kk < 16; ++kk) {
                if ((kk & 1) == 0) {
                    if (__builtin_expect(any_sent(umax(umax(max4(T[0][kk]), max4(T[1][kk])), umax(max4(T[0][kk + 1]), max4(T[1][kk + 1])))), 0)) {
                        wait_tok(T[0][kk], T[1][kk], rw, l * 16, tq[kk >> 2] + (kk & 3) * 1024);
                        wait_tok(T[0][kk + 1], T[1][kk + 1], rw, l * 16, tq[kk >> 2] + ((kk + 1) & 3) * 1024);
                    }
                    if (kk == 0) ST(12);
                    if (kk == (BG6 / 7) * 4 - 2) lds_wait(hw + 5, 8u);
                }
                if (kk + G1_DEPTH < 16) ldt(kk + G1_DEPTH);
                if (kk < 15) {
#pragma unroll
                    for (int i = 0; i < 7; ++i) A[(kk + 1) & 1][i] = ldw1(wb[(kk + 1) >> 2], (kk + 1) & 3, i);
                }
#pragma unroll
                for (int i = 0; i < 7; ++i)
#pragma unroll
                    for (int tt = 0; tt < 2; ++tt) acc[tt][i] = mma_b(A[kk & 1][i], T[tt][kk], acc[tt][i]);
#pragma unroll
                for (int i = 0; i < 14; ++i) {
                    __builtin_amdgcn_sched_group_barrier(0x8, 1, 0);
                    if (i == 1 || i == 7) __builtin_amdgcn_sched_group_barrier(0x20, 1, 0);
                    __builtin_amdgcn_sched_group_barrier(0x100, 1, 0);
                    if ((kk & 1) && i >= 3) __builtin_amdgcn_sched_group_barrier(0x2, 1, 0);
                }
                if (kk & 1) {} else SCHED();
            }
#pragma unroll
            for (int q = 0; q < 3; ++q) {
                u2 p0 = pk4(acc[1][2 * q]), p1 = pk4(acc[1][2 * q + 1]);
                *(u4*)(xch + q * 1024 + l * 16) = (u4){p0[0], p0[1], p1[0], p1[1]};
            }
            lds_set(hw + tg, 1u);
            ST(13);
#pragma unroll
            for (int q = 0; q < 3; ++q) {
                u2 p0 = pk4(acc[0][2 * q]), p1 = pk4(acc[0][2 * q + 1]);
                bst4<SC1>(silu_pk4((u4){p0[0], p0[1], p1[0], p1[1]}), rw, l * 16, hoff + ((nt1 & 1) * 3 + q) * 1024);
            }
#pragma unroll
            for (int tt = 0; tt < 2; ++tt) { u2 p = pk4(acc[tt][6]); bst2<SC1>(silu_pk2(p[0], p[1]), rw, l * 8, hoff + tt * (56 * 1024) + 6144 + (nt1 & 1) * 512); }
        } else {
            bg(BG6, 28);
            if (l == 0) __hip_atomic_fetch_add(hw + 6, 1u, __ATOMIC_RELAXED, __HIP_MEMORY_SCOPE_WORKGROUP);
            lds_wait(hw + 6, 8u);
            w2_glds(pw, 0, lds0 + LDS_SCR + tg * 8192);
            w2_glds(pw, 1, lds0 + LDS_SCR + tg * 8192 + 4096);
            WAITV(8);
            if (l == 0) __hip_atomic_fetch_add(hw + 5, 1u, __ATOMIC_RELAXED, __HIP_MEMORY_SCOPE_WORKGROUP);
#pragma unroll
            for (int r = 0; r < B_R; ++r) { if (r < 6) WAITV(4); else WAITV(0); stage_out(r); }
            ST(12);
            lds_wait(hw + tg, 1u);
            ST(13);
#pragma unroll
            for (int q = 0; q < 3; ++q)
                bst4<SC1>(silu_pk4(*(const u4*)(xch + q * 1024 + l * 16)), rw, l * 16, hoff + 56 * 1024 + ((nt1 & 1) * 3 + q) * 1024);
        }
        ST(14);
        WAITV(0);
        publish();
        ST(21);
        if (__builtin_expect(aw, 1)) {
            lds_wait(hw + 4, 8u);
#pragma unroll
            for (int ks = 0; ks < 7; ++ks) w2_glds(pw, ks, lds0 + tg * 28672 + ks * 4096);
        } else {
            if (B_R < 7) stage_out(B_R);
#pragma unroll
            for (int r = B_R + 1; r < 7; ++r) { if (r + 1 < 7) WAITV(4); else WAITV(0); stage_out(r); }
        }
    }
    {
        ST(15);
        const int rot = w & 3;
        if (aw) {
            flag_wait(fl + ((l & 4) ? 4096 + (grp * 8 + th * 4 + rot) * 32 : (grp * 8 + (pw ^ ((l & 2) * 2))) * 32) + (l & 1), tag);
            lds_set(hw + 8 + rot, tag);
        } else {
#pragma nounroll
            for (int sp = 0; sp < SPIN_MAX; ++sp) {
                if (__builtin_amdgcn_readfirstlane(__hip_atomic_load(hw + 8 + rot, __ATOMIC_RELAXED, __HIP_MEMORY_SCOPE_WORKGROUP)) == tag) break;
                __builtin_amdgcn_s_sleep(1);
            }
            FENCE();
        }
        ST(16);
        const int toff = WS_H1 + (grp * 8 + th * 4) * (56 * 1024) + pw * 7168;
        int to[4];
#pragma unroll
        for (int tt = 0; tt < 4; ++tt) to[tt] = toff + ((tt + rot) & 3) * (56 * 1024);
        u4 T[7][4];
        auto ldtok = [&](int ks) {
#pragma unroll
            for (int tt = 0; tt < 4; ++tt) {
                if (ks < 6) T[ks][tt] = bld4<SC1>(rw, l * 16, to[tt] + ko[ks] * 1024);
                else {
                    u2 a = bld2<SC1>(rw, l * 8 + 2048, to[tt] + 4096), c = bld2<SC1>(rw, l * 8 + 2560, to[tt] + 4096);
                    T[ks][tt] = (u4){a[0], a[1], c[0], c[1]};
                }
            }
            SCHED();
        };
#pragma unroll
        for (int ks = 0; ks < G2_DEPTH; ++ks) ldtok(ks);
        if (aw) {
#pragma unroll
            for (int ks = 0; ks < 7; ++ks) w2_read(W[ks], rot * 28672 + ks * 4096);
            asm volatile("s_waitcnt lgkmcnt(0)" ::: "memory");
        }
        if (l == 0) __hip_atomic_fetch_add(hw + 7, 1u, __ATOMIC_RELAXED, __HIP_MEMORY_SCOPE_WORKGROUP);
        const int rho = grp * 128 + th * 64 + rot * 16 + m;
        const int ooff = (256 * (rho & 7) + (rho >> 3)) * 1024 + fb * 128 + (w >> 2) * 64 + g * 16;
        const u4 hres = bld4<SC1>(ro, ooff, 0);
        SCHED();
        f4 acc[4][4];
#pragma unroll
        for (int tt = 0; tt < 4; ++tt)
#pragma unroll
            for (int t = 0; t < 4; ++t) acc[tt][t] = Z4;
#pragma unroll
        for (int ks = 0; ks < 7; ++ks) {
            if (ks + G2_DEPTH < 7) { PIN(T[ks][0]); ldtok(ks + G2_DEPTH); }
#pragma unroll
            for (int tt = 0; tt < 4; ++tt)
#pragma unroll
                for (int t = 0; t < 4; ++t) {
                    acc[tt][t] = mma_b(W[ks][t], T[ks][tt], acc[tt][t]);
                }
            SCHED();
        }
        ST(17);
        lds_wait(hw + 7, 8u);
        char* dp = smem + w * 16384 + l * 16;
#pragma unroll
        for (int tt = 1; tt < 4; ++tt)
#pragma unroll
            for (int t = 0; t < 4; ++t) *(f4*)(dp + (((tt + rot) & 3) * 4 + t) * 1024) = acc[tt][t];
        f4 s0, s1;
        if (aw) { *(f4*)(dp + (rot * 4 + 2) * 1024) = acc[0][2]; *(f4*)(dp + (rot * 4 + 3) * 1024) = acc[0][3]; s0 = acc[0][0]; s1 = acc[0][1]; }
        else { *(f4*)(dp + (rot * 4) * 1024) = acc[0][0]; *(f4*)(dp + (rot * 4 + 1) * 1024) = acc[0][1]; s0 = acc[0][2]; s1 = acc[0][3]; }
        BARRIER();
        f4 r0[7], r1[7];
#pragma unroll
        for (int j = 1; j < 8; ++j) {
            const char* rp = smem + (((w + j) & 7) * 16 + rot * 4 + (w >> 2) * 2) * 1024 + l * 16;
            r0[j - 1] = *(const f4*)rp; r1[j - 1] = *(const f4*)(rp + 1024);
        }
        SCHED();
        s0 = ((s0 + r0[0]) + (r0[1] + r0[2])) + ((r0[3] + r0[4]) + (r0[5] + r0[6]));
        s1 = ((s1 + r1[0]) + (r1[1] + r1[2])) + ((r1[3] + r1[4]) + (r1[5] + r1[6]));
        u2 p0 = pk4(s0), p1 = pk4(s1);
        bst4<NT>((u4){pk_b2(bf_2(hres[0]) + bf_2(p0[0])), pk_b2(bf_2(hres[1]) + bf_2(p0[1])),
                           pk_b2(bf_2(hres[2]) + bf_2(p1[0])), pk_b2(bf_2(hres[3]) + bf_2(p1[1]))}, ro, ooff, 0);
    }
    ST_END(ws, 0);
}

extern "C" void kernel_launch(void* const* d_in, const int* in_sizes, int n_in, void* d_out, int out_size,
                              void* d_ws, size_t ws_size, hipStream_t stream) {
    (void)hipFuncSetAttribute((const void*)k_fused, hipFuncAttributeMaxDynamicSharedMemorySize, LDS_B);
    k_fused<<<256, 512, LDS_B, stream>>>((const uint16_t*)d_in[0], (const uint16_t*)d_in[1], (const uint16_t*)d_in[2],
                                         (const uint16_t*)d_in[4], (const uint16_t*)d_in[5], (uint16_t*)d_out, (char*)d_ws);
}
```

```cpp
#define TLBPOS 1
#include <hip/hip_runtime.h>
#include <hip/hip_bf16.h>
#include <stdint.h>

typedef _Float16 h8 __attribute__((ext_vector_type(8)));
typedef _Float16 h4 __attribute__((ext_vector_type(4)));
typedef _Float16 h2 __attribute__((ext_vector_type(2)));
typedef __bf16 b8 __attribute__((ext_vector_type(8)));
typedef __bf16 b2 __attribute__((ext_vector_type(2)));
typedef short s4 __attribute__((ext_vector_type(4)));
typedef float f4 __attribute__((ext_vector_type(4)));
typedef float f2 __attribute__((ext_vector_type(2)));
typedef unsigned u32;
typedef u32 u4 __attribute__((ext_vector_type(4)));
typedef u32 u2 __attribute__((ext_vector_type(2)));
#define LAS __attribute__((address_space(3)))
#define GAS __attribute__((address_space(1)))
#define DI __device__ __forceinline__
#define FENCE() asm volatile("" ::: "memory")
#define PIN(x) asm volatile("" : "+v"(x))
#define SCHED() __builtin_amdgcn_sched_barrier(0)
#define WAITV(n) asm volatile("s_waitcnt vmcnt(%0)" :: "n"(n) : "memory")
#define BARRIER() do { asm volatile("s_waitcnt lgkmcnt(0)" ::: "memory"); __builtin_amdgcn_s_barrier(); FENCE(); } while (0)
constexpr int SC1 = 16, NT = 2;
constexpr u32 SENT = 0xffffffffu;
constexpr int SPIN_MAX = 1 << 20;

DI u32 pk_h(float a, float b) { f2 v = {a, b}; h2 r = __builtin_convertvector(v, h2); return __builtin_bit_cast(u32, r); }
DI u32 pk_b(float a, float b) { f2 v = {a, b}; b2 r = __builtin_convertvector(v, b2); return __builtin_bit_cast(u32, r); }
DI float bf_lo(u32 v) { return __builtin_bit_cast(float, v << 16); }
DI float bf_hi(u32 v) { return __builtin_bit_cast(float, v & 0xffff0000u); }
DI f2 bf_2(u32 v) { return (f2){bf_lo(v), bf_hi(v)}; }
DI u32 pk_b2(f2 v) { return pk_b(v[0], v[1]); }
DI float rbf(float a) { return bf_lo(pk_b(a, a)); }
DI float cosr(float rev) { return __builtin_amdgcn_cosf(rev); }
DI float sinr(float rev) { return __builtin_amdgcn_sinf(rev); }
DI f2 lo2(f4 v) { return __builtin_shufflevector(v, v, 0, 1); }
DI f2 hi2(f4 v) { return __builtin_shufflevector(v, v, 2, 3); }
DI f2 fma2(f2 a, f2 b, f2 c) { return __builtin_elementwise_fma(a, b, c); }
DI u32 umin(u32 a, u32 b) { return a < b ? a : b; }
DI u32 umax(u32 a, u32 b) { return a > b ? a : b; }
DI u32 clean(u32 v) { return umin(v, SENT - 1); }
DI u32 max4(u4 v) { return umax(umax(umax(v[0], v[1]), v[2]), v[3]); }
#define Z4 ((f4){0, 0, 0, 0})
DI f4 mma_h(u4 a, u4 b, f4 c) { return __builtin_amdgcn_mfma_f32_16x16x32_f16(__builtin_bit_cast(h8, a), __builtin_bit_cast(h8, b), c, 0, 0, 0); }
DI f4 mma_b(u4 a, u4 b, f4 c) { return __builtin_amdgcn_mfma_f32_16x16x32_bf16(__builtin_bit_cast(b8, a), __builtin_bit_cast(b8, b), c, 0, 0, 0); }
DI f4 mma16_b(u2 a, u2 b, f4 c) { return __builtin_amdgcn_mfma_f32_16x16x16bf16_1k(__builtin_bit_cast(s4, a), __builtin_bit_cast(s4, b), c, 0, 0, 0); }
DI f4 mma16_h(u2 a, u2 b, f4 c) { return __builtin_amdgcn_mfma_f32_16x16x16f16(__builtin_bit_cast(h4, a), __builtin_bit_cast(h4, b), c, 0, 0, 0); }
typedef __amdgpu_buffer_rsrc_t rsrc_t;
DI rsrc_t mkrs(const void* p, u32 bytes) { return __builtin_amdgcn_make_buffer_rsrc((void*)p, 0, bytes, 0x00020000); }
template <int AUX> DI u4 bld4(rsrc_t r, int v, int s) { return __builtin_amdgcn_raw_buffer_load_b128(r, v, s, AUX); }
template <int AUX> DI u2 bld2(rsrc_t r, int v, int s) { return __builtin_amdgcn_raw_buffer_load_b64(r, v, s, AUX); }
template <int AUX> DI void bst4(u4 d, rsrc_t r, int v, int s) {
    __builtin_amdgcn_raw_buffer_store_b128(d, r, v, s, AUX);
    asm volatile("s_nop 1" :: "v"(d));
}
template <int AUX> DI void bst2(u2 d, rsrc_t r, int v, int s) { __builtin_amdgcn_raw_buffer_store_b64(d, r, v, s, AUX); }

template <int CTRL> DI float dpp(float v) {
    return __builtin_bit_cast(float, __builtin_amdgcn_update_dpp(0, __builtin_bit_cast(int, v), CTRL, 0xf, 0xf, true));
}
DI float swap_add16(float v) {
    u32 x = __builtin_bit_cast(u32, v), y = x;
    asm("" : "+v"(y));
    auto r = __builtin_amdgcn_permlane16_swap(x, y, false, false);
    return __builtin_bit_cast(float, (u32)r[0]) + __builtin_bit_cast(float, (u32)r[1]);
}
DI float swap_add32(float v) {
    u32 x = __builtin_bit_cast(u32, v), y = x;
    asm("" : "+v"(y));
    auto r = __builtin_amdgcn_permlane32_swap(x, y, false, false);
    return __builtin_bit_cast(float, (u32)r[0]) + __builtin_bit_cast(float, (u32)r[1]);
}
template <bool ALL> DI void red_lanes(float& a, float& b) {
    a += dpp<0xB1>(a); b += dpp<0xB1>(b);
    if (ALL) { a += dpp<0x4E>(a); b += dpp<0x4E>(b); }
    a += dpp<0x124>(a); b += dpp<0x124>(b);
    a += dpp<0x128>(a); b += dpp<0x128>(b);
    a = swap_add16(a); b = swap_add16(b);
    a = swap_add32(a); b = swap_add32(b);
}
DI float ln_inv(float var) { return __builtin_amdgcn_rcpf(rbf(__builtin_amdgcn_sqrtf(rbf(rbf(var) + 1e-5f)))); }

#define ST_DECL
#define ST(i)
#define ST_END(ws, kid)

#define FS 1
constexpr u32 WS_U = 0;
constexpr u32 WS_V = WS_U + (2u << 20);
constexpr u32 WS_LN2 = WS_V + (2u << 20);
constexpr u32 WS_WA = WS_LN2 + (2u << 20);
constexpr u32 WS_FA = WS_WA + 32768u;
constexpr u32 WS_FLAG = WS_FA + 16384u * FS;
constexpr u32 WS_EP = WS_FLAG + 32768u;
constexpr u32 WS_H1 = WS_WA + (512u << 10);
constexpr u32 WS_END = WS_H1 + 7340032u;

#define BGA 10
#define BGB 10
#define BGC 21
#define G_SLEEP 16
#define GA_SLEEP 4
#define GB_SLEEP 8
#define A_SLEEP 0
#define VPOS 0
#define LPOS 1
#define SP_SLEEP 6
#define SP_BURST 1
#define EARLY_STAGE 1
#define STG_SLEEP 8
#define H_SLEEP 2
#define BRR 5
#define TLBWAVE 6
#define BG_DELAY 14
#define PRO_SLEEP 4
constexpr int BG5 = BGA, BG5b = BGB, BG6 = BGC;
constexpr int G1_DEPTH = 10, G2_DEPTH = 3;
constexpr int B_R = BRR;
constexpr int CODE_LINES = 290, CODE_PF = 10;
constexpr int LDS_SCR = 114688;
constexpr int SCR_X = 8448;
constexpr int SCR_S = SCR_X + 20480;
constexpr int SCR_F = 49024;
constexpr int XCH = 147456;
constexpr int LDS_B = 163840;

DI void glds16(const void* sbase, u32 voff, u32 lds_dst) {
    asm volatile("s_mov_b32 m0, %2\n\ts_nop 0\n\tglobal_load_lds_dwordx4 %0, %1" :: "v"(voff), "s"(sbase), "s"(lds_dst) : "memory");
}
DI bool any_sent(u32 v) { return __builtin_amdgcn_ballot_w64(v == SENT) != 0; }
DI void poll2(u4& a, u4& c, rsrc_t rw, int vo, int sa, int sc) {
#pragma nounroll
    for (int sp = 0; sp < SPIN_MAX; ++sp) {
        const bool ba = max4(a) == SENT, bc = max4(c) == SENT;
        if (__builtin_amdgcn_ballot_w64(ba || bc) == 0) break;
        __builtin_amdgcn_s_sleep(1);
        FENCE();
        if (ba) a = bld4<SC1>(rw, vo, sa);
        if (bc) c = bld4<SC1>(rw, vo, sc);
    }
}
DI void wait_tok(u4& t0, u4& t1, rsrc_t rw, int vo, int so) {
    if (any_sent(umax(max4(t0), max4(t1)))) {
#pragma nounroll
        for (int sp = 0; sp < SPIN_MAX; ++sp) {
            FENCE();
            t0 = bld4<SC1>(rw, vo, so); t1 = bld4<SC1>(rw, vo, so + 16384);
            if (!any_sent(umax(max4(t0), max4(t1)))) break;
        }
    }
}
DI u4 ldw1(LAS char* wb, int kk, int ft) {
    s4 a0 = __builtin_amdgcn_ds_read_tr16_b64_v4i16((LAS s4*)(wb + kk * 32 * 224 + ft * 32));
    s4 a1 = __builtin_amdgcn_ds_read_tr16_b64_v4i16((LAS s4*)(wb + (kk * 32 + 16) * 224 + ft * 32));
    u2 x0 = __builtin_bit_cast(u2, a0), x1 = __builtin_bit_cast(u2, a1);
    return (u4){x0[0], x0[1], x1[0], x1[1]};
}
DI u2 pk4(f4 a) { return (u2){pk_b(a[0], a[1]), pk_b(a[2], a[3])}; }
DI u2 silu_pk2(u32 p0, u32 p1) {
    u32 o0, o1; float a0, a1, a2, a3, t0, t1, t2, t3;
    asm("v_lshlrev_b32 %2, 16, %10\n\tv_and_b32 %3, 0xffff0000, %10\n\tv_lshlrev_b32 %4, 16, %11\n\tv_and_b32 %5, 0xffff0000, %11\n\t"
        "v_mul_f32 %6, %12, %2\n\tv_mul_f32 %7, %12, %3\n\tv_mul_f32 %8, %12, %4\n\tv_mul_f32 %9, %12, %5\n\t"
        "v_exp_f32 %6, %6\n\tv_exp_f32 %7, %7\n\tv_exp_f32 %8, %8\n\tv_exp_f32 %9, %9\n\t"
        "v_add_f32 %6, 1.0, %6\n\tv_add_f32 %7, 1.0, %7\n\tv_add_f32 %8, 1.0, %8\n\tv_add_f32 %9, 1.0, %9\n\t"
        "v_rcp_f32 %6, %6\n\tv_rcp_f32 %7, %7\n\tv_rcp_f32 %8, %8\n\tv_rcp_f32 %9, %9\n\t"
        "v_mul_f32 %2, %2, %6\n\tv_mul_f32 %3, %3, %7\n\tv_mul_f32 %4, %4, %8\n\tv_mul_f32 %5, %5, %9\n\t"
        "v_cvt_pk_bf16_f32 %0, %2, %3\n\tv_cvt_pk_bf16_f32 %1, %4, %5"
        : "=&v"(o0), "=&v"(o1), "=&v"(a0), "=&v"(a1), "=&v"(a2), "=&v"(a3), "=&v"(t0), "=&v"(t1), "=&v"(t2), "=&v"(t3)
        : "v"(p0), "v"(p1), "s"(-1.4426950408889634f));
    return (u2){o0, o1};
}
DI u4 silu_pk4(u4 p) { u2 a = silu_pk2(p[0], p[1]), b = silu_pk2(p[2], p[3]); return (u4){a[0], a[1], b[0], b[1]}; }
DI void lds_set(LAS u32* p, u32 v) {
    asm volatile("s_waitcnt lgkmcnt(0)" ::: "memory");
    __hip_atomic_store(p, v, __ATOMIC_RELAXED, __HIP_MEMORY_SCOPE_WORKGROUP);
}
DI void lds_wait(LAS u32* p, u32 v) {
#pragma nounroll
    for (int sp = 0; sp < SPIN_MAX; ++sp) {
        if (__builtin_amdgcn_readfirstlane(__hip_atomic_load(p, __ATOMIC_RELAXED, __HIP_MEMORY_SCOPE_WORKGROUP)) >= v) break;
        __builtin_amdgcn_s_sleep(1);
    }
    FENCE();
}
DI void flag_wait(GAS u32* p, u32 tag) {
#pragma nounroll
    for (int sp = 0; sp < SPIN_MAX; ++sp) {
        if (__builtin_amdgcn_ballot_w64(__hip_atomic_load(p, __ATOMIC_RELAXED, __HIP_MEMORY_SCOPE_AGENT) != tag) == 0) break;
        __builtin_amdgcn_s_sleep(2);
    }
    FENCE();
}
#define SENT4 ((u4){SENT, SENT, SENT, SENT})

__global__ __launch_bounds__(512, 2) void k_fused(const uint16_t* x, const uint16_t* wr, const uint16_t* wi, const uint16_t* w1,
                                                  const uint16_t* w2, uint16_t* out, char* ws) {
    extern __shared__ __attribute__((aligned(16))) char smem[];
    const int tid = threadIdx.x, l = tid & 63, g = l >> 4, m = l & 15;
    const int w = __builtin_amdgcn_readfirstlane(tid >> 6);
    const int b = (blockIdx.x & 7) * 32 + (blockIdx.x >> 3);
    const int mt1 = (b >> 6) * 4 + ((b >> 3) & 3), nt1 = ((b >> 5) & 1) * 8 + (b & 7);
    const int grp = b >> 4, th = (b >> 3) & 1, fb = b & 7;
    const rsrc_t rw = mkrs(ws, WS_END), ro = mkrs(out, 2097152);
    char* scr = smem + LDS_SCR;
    LAS u32* hw = (LAS u32*)((LAS char*)smem + LDS_SCR + SCR_F);
    const u32 lds0 = (u32)(uintptr_t)(LAS char*)smem;
    const float mf = (float)m, gf = (float)g, sg = (m & 1) ? -1.0f : 1.0f;
    const bool aw = w < 4;
    ST_DECL;
    unsigned long long pc0;
    asm volatile("s_getpc_b64 %0" : "=s"(pc0));
    if (tid >= 448 && l < CODE_PF)
        asm volatile("s_mov_b32 m0, %2\n\ts_nop 0\n\tglobal_load_lds_dword %0, %1"
                     :: "v"(umin((blockIdx.x >> 3) * CODE_PF + l, CODE_LINES - 1) * 128), "s"(pc0), "s"(lds0 + LDS_SCR + SCR_F + 64) : "memory");
    const u32 epoch = __hip_atomic_load((GAS u32*)(ws + WS_EP), __ATOMIC_RELAXED, __HIP_MEMORY_SCOPE_AGENT);
    FENCE();
    u32 xd[8];
    const int f1 = b >> 5, cg = b & 31;
    const bool mir = cg >= 16;
    const rsrc_t rr = mkrs(wr, 1052672), ri = mkrs(wi, 1052672);
    u32 sr_, si_;
    if (aw) {
        __builtin_amdgcn_s_sleep(A_SLEEP);
        const u32* xr = (const u32*)(x + (256 * (2 * w + ((m >> 1) & 1)) + b) * 512) + 8 * g + 2 * (m >> 2) + (m & 1);
#pragma unroll
        for (int j = 0; j < 8; ++j) xd[j] = xr[32 * j];
    } else {
        bst4<SC1>(SENT4, rw, l * 16, WS_U + b * 8192 + (w - 4) * 2048); bst4<SC1>(SENT4, rw, l * 16 + 1024, WS_U + b * 8192 + (w - 4) * 2048);
        bst4<SC1>(SENT4, rw, l < 2 ? l * 4096 : 0x7ffffff0, WS_WA + (w - 4) * 8192 + b * 16);
        FENCE();
        if (w == 7 && VPOS == 0) {
#pragma unroll
            for (int i = 0; i < 8; ++i) bst4<SC1>(SENT4, rw, l * 16 + (i & 3) * 1024, WS_V + b * 8192 + (i >> 2) * 4096);
        }
        if (w == 6) {
            const int c = (l >> 3) & 3, f_ = 8 * b + (l & 7);
            const int e1 = ((c & 1) ? (2048 - f_) & 2047 : f_) * 514 + (c >> 1) * 512;
            sr_ = __builtin_amdgcn_raw_buffer_load_b16(rr, e1, 0, 0); si_ = __builtin_amdgcn_raw_buffer_load_b16(ri, e1, 0, 0);
        }
    }
    FENCE();
    const int zo = (64 * g + (g < 2 ? m : (m + 15) & 15)) * 4;
    const int rq = ((b >> 3) + b) & 3;
    const int row0 = (tid & 255) / 14, cch0 = (tid & 255) - row0 * 14;
    const char* wsrc = (const char*)w1 + nt1 * 224;
    u32 w1o[7];
    auto w1_burst = [&](int i0, int n) {
#pragma unroll
        for (int i = i0; i < i0 + n; ++i) {
            const int q = (i / 7 + rq) & 3, j = i % 7;
            glds16(wsrc + q * (128 * 3584), w1o[j], lds0 + (w - 4) * 1024 + q * 28672 + j * 4096);
        }
    };
    const int xoff = (256 * w + b) * 1024 + m * 32 + g * 8;
    u2 xl[2];
    float c16[4], s16[4];
    f2 twc[2], tws[2];
    u4 Gre3, Gim3, SGre, SGim, GA[2];
    auto consts0 = [&]() {
#pragma unroll
        for (int u = 0; u < 4; ++u) {
            float a = (4.0f * gf + u) * (mf * (1.0f / 16));
            c16[u] = cosr(a); s16[u] = sinr(a);
            twc[u >> 1][u & 1] = cosr(a * (1.0f / 32)); tws[u >> 1][u & 1] = sinr(a * (1.0f / 32));
        }
    };
    auto consts3 = [&]() {
#pragma unroll
        for (int u = 0; u < 4; ++u) {
            const float k = 4.0f * gf + u;
            const bool k0 = u == 0 && g == 0;
            float cc = c16[u], ss = s16[u];
            Gre3[u] = pk_b(cc, -ss); Gim3[u] = pk_b(ss, cc);
            float a2 = (2.0f * k + 1.0f) * (mf * (1.0f / 32));
            u32 sp = pk_b(g < 2 ? 2.f * cc : 2.f * cosr(a2), g < 2 ? -2.f * ss : -2.f * sinr(a2));
            SGre[u] = k0 ? pk_b(1.0f, sg) : (g < 2 ? sp : 0u);
            SGim[u] = g < 2 ? 0u : sp;
            float a4 = k * (mf * (1.0f / 32)), c4 = 2.f * cosr(a4), s4_ = -2.f * sinr(a4);
            GA[0][u] = k0 ? pk_b(1.0f, sg) : pk_b(c4, s4_);
            GA[1][u] = k0 ? pk_b(1.0f, sg) : ((u & 1) ? pk_b(-c4, -s4_) : pk_b(c4, s4_));
        }
        PIN(Gre3); PIN(Gim3); PIN(SGre); PIN(SGim); PIN(GA[0]); PIN(GA[1]); PIN(twc[0]); PIN(twc[1]); PIN(tws[0]); PIN(tws[1]);
    };
    const int pw = (w + 4 * th) & 7;
    GAS u32* fl = (GAS u32*)(ws + WS_FLAG);
    int ko[7];
    {
        const int r6 = (fb < 6 ? fb : fb - 5) + 3 * (grp & 1);
#pragma unroll
        for (int ks = 0; ks < 6; ++ks) { int k = ks + r6; k = k >= 6 ? k - 6 : k; ko[ks] = k >= 6 ? k - 6 : k; }
        ko[6] = 6;
    }
    u4 W[7][4];
    const u32 w2vo = (l >> 3) * 1024 + (((l & 7) ^ (((l >> 4) & 1) << 2)) * 16);
    auto w2_glds = [&](int p_, int ks, u32 dst) {
        const int q = ko[ks];
        const char* src = (const char*)w2 + (224 * p_ + (ks == 6 ? 96 : (q < 3 ? 32 * q : 16 + 32 * q))) * 1024 + fb * 128;
        glds16(src, w2vo, dst); glds16(src + 8192, w2vo, dst + 1024);
        glds16(src + (ks == 6 ? 112 : 16) * 1024, w2vo, dst + 2048); glds16(src + (ks == 6 ? 120 : 24) * 1024, w2vo, dst + 3072);
    };
    LAS char* w2rd = (LAS char*)smem + (4 * g + (m >> 2)) * 128 + (((m & 3) ^ (((m >> 3) & 1) << 2)) * 16);
    auto w2_read = [&](u4 (&f)[4], int off) {
#pragma unroll
        for (int t = 0; t < 4; ++t) {
            LAS char* p = (LAS char*)((u32)(uintptr_t)w2rd ^ ((t >> 1) * 64)) + off + (t & 1) * 8;
            u2 x0 = __builtin_bit_cast(u2, __builtin_amdgcn_ds_read_tr16_b64_v4i16((LAS s4*)p));
            u2 x1 = __builtin_bit_cast(u2, __builtin_amdgcn_ds_read_tr16_b64_v4i16((LAS s4*)(p + 2048)));
            f[t] = (u4){x0[0], x0[1], x1[0], x1[1]};
        }
    };
    auto bg = [&](int i0, int i1) { w1_burst(i0, i1 - i0); };
    u32 tag;
    if (__builtin_expect(aw, 1)) {
        if (tid < 8) hw[tid] = 0;
        FENCE();
        ST(2);
        {
            u4 FAre, FAim, Fre, Fim, Sre, Sim; u2 Mb; f2 pc[2], ps[2];
            consts0();
            float c0[8], s0[8];
#pragma unroll
            for (int j = 0; j < 8; ++j) {
                float a = (gf + 4.0f * j) * (mf * (1.0f / 32));
                c0[j] = cosr(a); s0[j] = m ? -sinr(a) : ((g & 1) ? -1.0f : 1.0f);
            }
            const float mo = (2.0f * mf + 1.0f) * (1.0f / 32);
#pragma unroll
            for (int u = 0; u < 4; ++u) {
                FAre[u] = pk_h(c0[2 * u], c0[2 * u + 1]); FAim[u] = pk_h(s0[2 * u], s0[2 * u + 1]);
                float cc = c16[u], ss = s16[u];
                Fre[u] = pk_h(cc, ss); Fim[u] = pk_h(-ss, cc);
                float a2 = (4.0f * gf + u) * mo;
                u32 re = pk_h(m < 8 ? cc : cosr(a2), 0.f), im = pk_h(m < 8 ? (m ? -ss : ((u & 1) ? -1.0f : 1.0f)) : -sinr(a2), 0.f);
                Sre[u] = m < 8 ? re : re << 16; Sim[u] = m < 8 ? im : im << 16;
                pc[u & 1][u >> 1] = twc[u >> 1][u & 1]; ps[u & 1][u >> 1] = tws[u >> 1][u & 1];
            }
#pragma unroll
            for (int e = 0; e < 2; ++e) {
                float a = (float)(((m >> 1) * (256 * (2 * g + e) + b)) & 2047) * (1.0f / 2048);
                float cc = cosr(a), ss = sinr(a);
                Mb[e] = (m & 1) ? pk_h(-ss, cc) : pk_h(cc, ss);
            }
            PIN(FAre); PIN(FAim); PIN(Fre); PIN(Fim); PIN(Sre); PIN(Sim); PIN(Mb); PIN(pc[0]); PIN(pc[1]); PIN(ps[0]); PIN(ps[1]);
            ST(3);
            f2 v[8];
#pragma unroll
            for (int j = 0; j < 8; ++j) v[j] = bf_2(xd[j]);
            const u32 x0 = __builtin_amdgcn_readlane(xd[0], 0), x2 = __builtin_amdgcn_readlane(xd[0], 2);
            const float c = bf_lo((m & 2) ? x2 : x0);
            f2 q0 = {0.f, 0.f}, q1 = {0.f, 0.f};
#pragma unroll
            for (int j = 0; j < 8; ++j) { v[j] -= c; if (j & 1) q1 = fma2(v[j], v[j], q1); else q0 = fma2(v[j], v[j], q0); }
            f2 s2 = ((v[0] + v[1]) + (v[2] + v[3])) + ((v[4] + v[5]) + (v[6] + v[7]));
            q0 += q1;
            float s1 = s2[0] + s2[1], sq = q0[0] + q0[1];
            red_lanes<false>(s1, sq);
            s1 *= 1.0f / 512;
            const float inv = ln_inv(__builtin_fmaxf(sq * (1.0f / 512) - s1 * s1, 0.f));
            const float off = (c - rbf(c + s1)) * inv;
#pragma unroll
            for (int j = 0; j < 8; ++j) v[j] = fma2(v[j], (f2){inv, inv}, (f2){off, off});
            u4 B[2];
#pragma unroll
            for (int p = 0; p < 2; ++p) {
                u4 A = {pk_h(v[0][p], v[1][p]), pk_h(v[2][p], v[3][p]), pk_h(v[4][p], v[5][p]), pk_h(v[6][p], v[7][p])};
                f4 Dre = mma_h(A, FAre, Z4), Dim = mma_h(A, FAim, Z4);
#pragma unroll
                for (int r = 0; r < 2; ++r) {
                    f2 re = r ? hi2(Dre) : lo2(Dre), im = r ? hi2(Dim) : lo2(Dim);
                    f2 tr = fma2(im, ps[p], re * pc[p]), ti = fma2(-re, ps[p], im * pc[p]);
                    B[r][p] = pk_h(tr[0], ti[0]); B[r][2 + p] = pk_h(tr[1], ti[1]);
                }
            }
#pragma unroll
            for (int r = 0; r < 2; ++r) {
                f4 Gre = mma_h(Fre, B[r], Z4), Gim = mma_h(Fim, B[r], Z4), Hre = mma_h(Sre, B[r], Z4), Him = mma_h(Sim, B[r], Z4);
#pragma unroll
                for (int i = 0; i < 4; ++i) *(u32*)(scr + (2 * w + r) * 1024 + zo + i * 64) = pk_h(m ? Gre[i] : Hre[i], m ? Gim[i] : Him[i]);
            }
            BARRIER();
            ST(18);
            u4 r0 = *(const u4*)(scr + (2 * g) * 1024 + w * 256 + m * 16);
            u4 r1 = *(const u4*)(scr + (2 * g + 1) * 1024 + w * 256 + m * 16);
            u4 o0, o1;
#pragma unroll
            for (int t = 0; t < 4; ++t) {
                f4 D = mma16_h(Mb, (u2){r0[t], r1[t]}, Z4);
                o0[t] = clean(pk_h(D[0], D[1])); o1[t] = clean(pk_h(D[2], D[3]));
            }
            bst4<SC1>(o0, rw, g * 2048 + m * 16, WS_U + b * 8192 + w * 256);
            bst4<SC1>(o1, rw, g * 2048 + m * 16 + 1024, WS_U + b * 8192 + w * 256);
            if (w == 0 && m == 0) {
                __builtin_amdgcn_raw_buffer_store_b32(o0[0], rw, ((8 - 2 * g) & 7) * 4096, WS_WA + b * 16, SC1);
                __builtin_amdgcn_raw_buffer_store_b32(o1[0], rw, (7 - 2 * g) * 4096, WS_WA + b * 16, SC1);
            }
        }
        ST(4);
        u4 Fre, Fim, Gre, Gim; u2 Nb; f2 tc[2], ts[2];
#pragma unroll
        for (int u = 0; u < 4; ++u) {
            float cc = c16[u], ss = s16[u];
            Fre[u] = pk_h(0.25f * cc, 0.25f * ss); Fim[u] = pk_h(-0.25f * ss, 0.25f * cc);
            Gre[u] = pk_b(cc * (1.0f / 256), -ss * (1.0f / 256)); Gim[u] = pk_b(ss * (1.0f / 256), cc * (1.0f / 256));
            float a3 = (4.0f * gf + u) * (mf * (1.0f / 256));
            tc[u >> 1][u & 1] = cosr(a3); ts[u >> 1][u & 1] = sinr(a3);
        }
        PIN(Fre); PIN(Fim); PIN(Gre); PIN(Gim); PIN(tc[0]); PIN(tc[1]); PIN(ts[0]); PIN(ts[1]);
        xl[0] = *(const u2*)((const char*)x + xoff); xl[1] = *(const u2*)((const char*)x + xoff + 512);
        FENCE();
        tag = __builtin_amdgcn_readfirstlane(epoch) + 1u;
        consts3();
#pragma unroll
        for (int e = 0; e < 2; ++e) {
            float a1 = (float)(((2 * g + e) * (256 * (m >> 1) + b)) & 2047) * (1.0f / 2048);
            float c1 = cosr(a1), s1 = sinr(a1);
            Nb[e] = (m & 1) ? pk_b(s1, c1) : pk_b(c1, -s1);
        }
        PIN(Nb);
        WAITV(0);
        PIN(xl[0]); PIN(xl[1]);
        ST(19);
        BARRIER();
        ST(5);
        u4 d[2], rec[4];
        const int uo = (tid >> 1) * 8192 + (tid & 1) * 16, us = WS_U + f1 * 1024 + cg * 32;
        d[0] = bld4<SC1>(rw, uo, us); d[1] = bld4<SC1>(rw, uo, us + 128 * 8192);
        poll2(d[0], d[1], rw, uo, us, us + 128 * 8192);
        char* sp = scr + SCR_S + (tid ^ ((tid >> 4) & 1)) * 16;
        *(u4*)sp = d[0]; *(u4*)(sp + 4096) = d[1];
        BARRIER();
        if (b == 0 && tid == 0) __hip_atomic_store((GAS u32*)(ws + WS_EP), tag, __ATOMIC_RELAXED, __HIP_MEMORY_SCOPE_AGENT);
        const int ro_ = (64 * g + m) * 16, rs_ = WS_WA + f1 * 4096;
        if (cg == 0 && w == 0) {
#pragma unroll
            for (int i = 0; i < 4; ++i) rec[i] = bld4<SC1>(rw, ro_ + i * 256, rs_);
        }
        ST(6);
        u2 ld[4], wl[4];
        const int so = (64 * g + m) * 32 + (((w >> 1) ^ (m >> 3)) * 16) + (w & 1) * 8;
#pragma unroll
        for (int q = 0; q < 4; ++q) ld[q] = *(const u2*)(scr + SCR_S + so + 512 * q);
#pragma unroll
        for (int q = 0; q < 4; ++q) wl[q] = *(const u2*)(scr + SCR_S + so + 8192 + 512 * q);
        FENCE();
        auto fwd = [&](u4 A, f4& Xr, f4& Xi) {
            f4 Dre = mma_h(A, Fre, Z4), Dim = mma_h(A, Fim, Z4);
            f2 r0 = fma2(lo2(Dim), ts[0], lo2(Dre) * tc[0]), i0 = fma2(-lo2(Dre), ts[0], lo2(Dim) * tc[0]);
            f2 r1 = fma2(hi2(Dim), ts[1], hi2(Dre) * tc[1]), i1 = fma2(-hi2(Dre), ts[1], hi2(Dim) * tc[1]);
            u4 B = {pk_h(r0[0], i0[0]), pk_h(r0[1], i0[1]), pk_h(r1[0], i1[0]), pk_h(r1[1], i1[1])};
            Xr = mma_h(Fre, B, Z4); Xi = mma_h(Fim, B, Z4);
        };
        f4 Xre[2], Xim[2];
#pragma unroll
        for (int e = 0; e < 2; ++e) fwd((u4){ld[0][e], ld[1][e], ld[2][e], ld[3][e]}, Xre[e], Xim[e]);
        f2 yr[2][2], yi[2][2];
#pragma unroll
        for (int e = 0; e < 2; ++e)
#pragma unroll
            for (int hh = 0; hh < 2; ++hh) {
                f2 wre = {bf_lo(wl[2 * hh][e]), bf_lo(wl[2 * hh + 1][e])}, wim = {bf_hi(wl[2 * hh][e]), bf_hi(wl[2 * hh + 1][e])};
                f2 xr = hh ? hi2(Xre[e]) : lo2(Xre[e]), xi = hh ? hi2(Xim[e]) : lo2(Xim[e]);
                yr[e][hh] = fma2(-xi, wim, xr * wre); yi[e][hh] = fma2(xr, wim, xi * wre);
            }
        if (cg == 0 && w == 0) {
#pragma nounroll
            for (int sp = 0; sp < SPIN_MAX; ++sp) {
                u32 mx = 0;
#pragma unroll
                for (int i = 0; i < 4; ++i) mx = umax(mx, umax(umax(rec[i][0], rec[i][1]), rec[i][2]));
                if (!any_sent(mx)) break;
                FENCE();
#pragma unroll
                for (int i = 0; i < 4; ++i) rec[i] = bld4<SC1>(rw, ro_ + i * 256, rs_);
            }
            f4 Mr, Mi;
            fwd((u4){rec[0][0], rec[1][0], rec[2][0], rec[3][0]}, Mr, Mi);
            f2* sx = (f2*)(scr + SCR_X);
#pragma unroll
            for (int i = 0; i < 4; ++i) sx[m + 16 * (4 * g + i)] = (f2){Mr[i], Mi[i]};
#pragma unroll
            for (int i = 0; i < 4; ++i) {
                f2 xm = sx[((f1 ? 255 : 256) - (m + 16 * (4 * g + i))) & 255];
                float ar = bf_lo(rec[i][1]), ai = bf_hi(rec[i][1]), br = bf_lo(rec[i][2]), bi = bf_hi(rec[i][2]);
                yr[0][i >> 1][i & 1] = Xre[0][i] * ar - Xim[0][i] * ai + xm[0] * br + xm[1] * bi;
                yi[0][i >> 1][i & 1] = Xre[0][i] * ai + Xim[0][i] * ar + xm[0] * bi - xm[1] * br;
            }
        }
        char* pv = scr;
#pragma unroll
        for (int e = 0; e < 2; ++e) {
            u4 Y = {pk_b(yr[e][0][0], yi[e][0][0]), pk_b(yr[e][0][1], yi[e][0][1]), pk_b(yr[e][1][0], yi[e][1][0]), pk_b(yr[e][1][1], yi[e][1][1])};
            f4 Dre = mma_b(Y, Gre, Z4), Dim = mma_b(Y, Gim, Z4);
            f2 r0 = fma2(-lo2(Dim), ts[0], lo2(Dre) * tc[0]), i0 = fma2(lo2(Dre), ts[0], lo2(Dim) * tc[0]);
            f2 r1 = fma2(-hi2(Dim), ts[1], hi2(Dre) * tc[1]), i1 = fma2(hi2(Dre), ts[1], hi2(Dim) * tc[1]);
            u4 A = {pk_b(r0[0], i0[0]), pk_b(r0[1], i0[1]), pk_b(r1[0], i1[0]), pk_b(r1[1], i1[1])};
            f4 Ere = mma_b(A, Gre, Z4), Eim = mma_b(A, Gim, Z4);
            u4 o;
#pragma unroll
            for (int i = 0; i < 4; ++i) o[i] = clean(pk_b(Ere[i], Eim[i]));
            *(u4*)(pv + (2 * w + e) * 1024 + ((m * 4 + g) ^ ((w >> 1) * 4)) * 16) = o;
        }
        BARRIER();
        ST(7);
        {
            const u32* q = (const u32*)(pv + (tid & 1) * 4096 + (((tid >> 1) * 4) ^ ((tid & 1) * 64)));
            const u4 o0 = {q[0], q[256], q[512], q[768]}, o1 = {q[128], q[384], q[640], q[896]};
            SCHED();
            bst4<SC1>(o0, rw, tid * 16, WS_V + b * 8192); bst4<SC1>(o1, rw, tid * 16, WS_V + b * 8192 + 4096);
        }
        WAITV(0);
        ST(8);
        const int vo = (g * 64 + (m >> 1)) * 8192 + (m & 1) * 16, vs = WS_V + w * 8 * 8192 + b * 32;
        u4 v0 = bld4<SC1>(rw, vo, vs), v1 = bld4<SC1>(rw, vo, vs + 32 * 8192);
        poll2(v0, v1, rw, vo, vs, vs + 32 * 8192);
        ST(9);
        u4 z0, z1;
#pragma unroll
        for (int t = 0; t < 4; ++t) {
            f4 D = mma16_b(Nb, (u2){v0[t], v1[t]}, Z4);
            z0[t] = pk_b(D[0], D[1]); z1[t] = pk_b(D[2], D[3]);
        }
        *(u4*)(scr + SCR_X + g * 2048 + w * 256 + m * 16) = z0;
        *(u4*)(scr + SCR_X + g * 2048 + 1024 + w * 256 + m * 16) = z1;
    } else {
        auto announce = [&](int which) {
            if (l < 8) __hip_atomic_store((GAS u32*)(ws + WS_FA) + (which * 2048 + l * 256 + b) * FS, tag, __ATOMIC_RELAXED, __HIP_MEMORY_SCOPE_AGENT);
            FENCE();
        };
        auto all_announced = [&](int which) {
#pragma nounroll
            for (int sp = 0; sp < SPIN_MAX; ++sp) {
                FENCE();
                u4 f;
#pragma unroll
                for (int i = 0; i < 4; ++i) f[i] = __builtin_amdgcn_raw_buffer_load_b32(rw, (l + 64 * i) * 4 * FS, WS_FA + (which * 8192 + (b >> 5) * 1024) * FS, SC1);
                if (__builtin_amdgcn_ballot_w64(f[0] != tag || f[1] != tag || f[2] != tag || f[3] != tag) == 0) break;
                __builtin_amdgcn_s_sleep(4);
            }
        };
        FENCE();
        ST(2);
        tag = __builtin_amdgcn_readfirstlane(epoch) + 1u;
        if (w == 6) WAITV(2); else if (w == 7 && VPOS == 0) WAITV(8); else WAITV(0);
        if (l < 2) __hip_atomic_store((GAS u32*)(ws + WS_FA) + ((2 * (w - 4) + l) * 256 + b) * FS, tag, __ATOMIC_RELAXED, __HIP_MEMORY_SCOPE_AGENT);
        FENCE();
        ST(18);
#pragma unroll
        for (int jj = 0; jj < 7; ++jj) {
            const int cj_ = (4 * jj) % 14, rj = 18 * jj + (4 * jj) / 14;
            w1o[jj] = (row0 + rj) * 3584 + (cch0 + cj_) * 16 + (cch0 + cj_ >= 14 ? 3584 - 224 : 0);
            PIN(w1o[jj]);
        }
        BARRIER();
        if (w == 6) {
            float re = bf_lo(sr_), im = bf_lo(si_);
            re += dpp<0x128>(re); im -= dpp<0x128>(im);
            u32 a = __builtin_bit_cast(u32, re), a2 = a, c = __builtin_bit_cast(u32, im), c2 = c;
            asm("" : "+v"(a2), "+v"(c2));
            auto pr = __builtin_amdgcn_permlane16_swap(a, a2, false, false);
            auto pi = __builtin_amdgcn_permlane16_swap(c, c2, false, false);
            const float ar = __builtin_bit_cast(float, (u32)pr[0]), br = __builtin_bit_cast(float, (u32)pr[1]);
            const float ai = __builtin_bit_cast(float, (u32)pi[0]), bi = __builtin_bit_cast(float, (u32)pi[1]);
            if (l < 8) {
                __builtin_amdgcn_raw_buffer_store_b32(clean(pk_b(0.25f * (ar + br), 0.25f * (ai + bi))), rw, l * 4096 + 4, WS_WA + b * 16, SC1);
                __builtin_amdgcn_raw_buffer_store_b32(clean(pk_b(0.25f * (ar - br), 0.25f * (ai - bi))), rw, l * 4096 + 8, WS_WA + b * 16, SC1);
            }
        }
        const int f = f1 + 8 * (tid & 255), k0 = 16 * cg - 8 * (cg & 1);
        const int e = mir ? ((2048 - f) & 2047) * 514 + (504 - k0) * 2 : f * 514 + k0 * 2;
        consts0();
        if (w == 7 && VPOS == 0) { WAITV(0); announce(1); }
        if (TLBPOS == 0) {
        if (w == TLBWAVE) {
            const char* p = l < 5 ? ws + (size_t)umin(WS_H1 + l * (2u << 20) + (b & 31) * 128, WS_END - 4)
                          : (l < 7 ? (const char*)w1 : l < 9 ? (const char*)w2 : (const char*)out)
                            + ((l & 1) ? 0 : (l < 9 ? 1835008 - 4096 : 2097152 - 4096)) + (b & 31) * 128;
            if (l < 11) asm volatile("s_mov_b32 m0, %1\n\ts_nop 0\n\tglobal_load_lds_dword %0, off" :: "v"(p), "s"(lds0 + LDS_SCR + 48896) : "memory");
        }
        }
        __builtin_amdgcn_s_sleep(GA_SLEEP);
        const u4 fa = bld4<0>(mkrs((cg & 1) ? wi : wr, 1052672), e, 0);
        FENCE();
        __builtin_amdgcn_s_sleep(GB_SLEEP);
        if (w == 4) all_announced(0);
        const u4 fb = bld4<0>(mkrs((cg & 1) ? wr : wi, 1052672), e, 0);
        xl[0] = *(const u2*)((const char*)x + xoff); xl[1] = *(const u2*)((const char*)x + xoff + 512);
        FENCE();
        ST(4);
        BARRIER();
        {
            u32 o[8];
            const u4 fr = (cg & 1) ? fb : fa, fi = (cg & 1) ? fa : fb;
#pragma unroll
            for (int c = 0; c < 8; ++c)
                o[c] = mir ? __builtin_amdgcn_perm(fi[3 - (c >> 1)], fr[3 - (c >> 1)], (c & 1) ? 0x05040100u : 0x07060302u) ^ 0x80000000u
                           : __builtin_amdgcn_perm(fi[c >> 1], fr[c >> 1], (c & 1) ? 0x07060302u : 0x05040100u);
            char* wp = scr + SCR_S + 8192 + (tid & 255) * 32;
            *(u4*)(wp + (m & 8) * 2) = (u4){o[0], o[1], o[2], o[3]};
            *(u4*)(wp + ((m & 8) * 2 ^ 16)) = (u4){o[4], o[5], o[6], o[7]};
        }
        FENCE();
        if (w == 7 && VPOS == 1) {
#pragma unroll
            for (int i = 0; i < 8; ++i) bst4<SC1>(SENT4, rw, l * 16 + (i & 3) * 1024, WS_V + b * 8192 + (i >> 2) * 4096);
            WAITV(0); announce(1);
        }
        if (LPOS == 1) {
#pragma unroll
        for (int i = 0; i < 2; ++i) bst4<SC1>(SENT4, rw, (l >> 3) * 256 + (l & 7) * 16 + i * 2048, WS_LN2 + (b >> 1) * 16384 + (b & 1) * 128 + (w - 4) * 4096);
        }
        PIN(xl[0]); PIN(xl[1]);
        if (TLBPOS == 1) {
        if (w == TLBWAVE) {
            const char* p = l < 5 ? ws + (size_t)umin(WS_H1 + l * (2u << 20) + (b & 31) * 128, WS_END - 4)
                          : (l < 7 ? (const char*)w1 : l < 9 ? (const char*)w2 : (const char*)out)
                            + ((l & 1) ? 0 : (l < 9 ? 1835008 - 4096 : 2097152 - 4096)) + (b & 31) * 128;
            if (l < 11) asm volatile("s_mov_b32 m0, %1\n\ts_nop 0\n\tglobal_load_lds_dword %0, off" :: "v"(p), "s"(lds0 + LDS_SCR + 48896) : "memory");
        }
        }
        consts3();
        ST(5);
        BARRIER();
        if (LPOS == 0) {
#pragma unroll
        for (int i = 0; i < 2; ++i) bst4<SC1>(SENT4, rw, (l >> 3) * 256 + (l & 7) * 16 + i * 2048, WS_LN2 + (b >> 1) * 16384 + (b & 1) * 128 + (w - 4) * 4096);
        }
        if (w == 5) all_announced(1);
        if (cg == 0) __builtin_amdgcn_s_sleep(SP_SLEEP);
        if (cg != 0 || SP_BURST) bg(0, BG5);
        if (cg != 0 || SP_BURST) WAITV(BG5); else WAITV(0);
        ST(6);
        BARRIER();
        __builtin_amdgcn_s_sleep(3);
        bg(BG5, BG5b);
        ST(7);
        __builtin_amdgcn_s_sleep(BG_DELAY);
        if (cg == 0 && !SP_BURST) bg(0, BG5);
        bg(BG5b, BG6);
    }
    BARRIER();
    {
        const u32* zp = (const u32*)(scr + SCR_X + w * 1024 + zo);
        u4 A = {zp[0], zp[16], zp[32], zp[48]};
        f4 Dre = mma_b(A, Gre3, Z4), Dim = mma_b(A, Gim3, Z4), Hre = mma_b(A, SGre, Z4), Him = mma_b(A, SGim, Z4);
        if (g == 0) { Dre[0] = Hre[0]; Dim[0] = Him[0]; }
        f2 r0 = fma2(-lo2(Dim), tws[0], lo2(Dre) * twc[0]), i0 = fma2(lo2(Dre), tws[0], lo2(Dim) * twc[0]);
        f2 r1 = fma2(-hi2(Dim), tws[1], hi2(Dre) * twc[1]), i1 = fma2(hi2(Dre), tws[1], hi2(Dim) * twc[1]);
        u4 A2 = {pk_b(r0[0], i0[0]), pk_b(r0[1], i0[1]), pk_b(r1[0], i1[0]), pk_b(r1[1], i1[1])};
        f2 h[4]; u2 hp[2];
#pragma unroll
        for (int t = 0; t < 2; ++t) {
            f4 Yv = mma_b(A2, GA[t], Z4);
            u32 h0 = pk_b2(bf_2(xl[t][0]) + bf_2(pk_b(Yv[0], Yv[1]))), h1 = pk_b2(bf_2(xl[t][1]) + bf_2(pk_b(Yv[2], Yv[3])));
            hp[t] = (u2){h0, h1};
            h[2 * t] = bf_2(h0); h[2 * t + 1] = bf_2(h1);
        }
        const float c = bf_lo(__builtin_amdgcn_readfirstlane(hp[0][0]));
        f2 q2 = {0.f, 0.f}, s2 = {0.f, 0.f};
#pragma unroll
        for (int j = 0; j < 4; ++j) { f2 d = h[j] - c; q2 = fma2(d, d, q2); s2 += d; }
        float s1 = s2[0] + s2[1], sq = q2[0] + q2[1];
        red_lanes<true>(s1, sq);
        s1 *= 1.0f / 512;
        const float inv = ln_inv(__builtin_fmaxf(sq * (1.0f / 512) - s1 * s1, 0.f));
        const float mb = rbf(c + s1);
#pragma unroll
        for (int t = 0; t < 2; ++t)
            *(u2*)(scr + ((8 * t + (m >> 1)) * 4 + g) * 128 + (w ^ (m >> 1)) * 16 + (m & 1) * 8) =
                (u2){clean(pk_b2(bf_2(pk_b2(h[2 * t] - mb)) * inv)), clean(pk_b2(bf_2(pk_b2(h[2 * t + 1] - mb)) * inv))};
        WAITV(0);
        BARRIER();
        u4 lo = *(const u4*)(scr + (tid ^ ((tid >> 5) & 7)) * 16);
        bst4<SC1>(lo, rw, (tid >> 3) * 256 + (tid & 7) * 16, WS_LN2 + (b >> 1) * 16384 + (b & 1) * 128);
        FENCE();
        if (aw && l == 0) __hip_atomic_fetch_add(hw + 6, 1u, __ATOMIC_RELAXED, __HIP_MEMORY_SCOPE_WORKGROUP);
        if (aw) WAITV(0); else __builtin_amdgcn_s_sleep(H_SLEEP);
        bst2<SC1>(hp[0], ro, xoff, 0); bst2<SC1>(hp[1], ro, xoff + 512, 0);
        FENCE();
        ST(10);
    }
    auto stage_out = [&](int r) {
        w2_read(W[r], LDS_SCR + (w & 3) * 8192 + (r & 1) * 4096);
        asm volatile("s_waitcnt lgkmcnt(0)" ::: "memory");
        PIN(W[r][0]); PIN(W[r][1]); PIN(W[r][2]); PIN(W[r][3]);
        if (r + 2 < 7) w2_glds(pw, r + 2, lds0 + LDS_SCR + (w & 3) * 8192 + (r & 1) * 4096);
    };
    auto publish = [&]() {
        u32 prev = 0;
        if (l == 0) prev = __hip_atomic_fetch_add(hw + 4, 1u, __ATOMIC_RELAXED, __HIP_MEMORY_SCOPE_WORKGROUP);
        if (__builtin_amdgcn_readfirstlane(prev) == 7u && l < 2)
            __hip_atomic_store(fl + (l ? 4096 + (b >> 1) * 32 + (b & 1) : (mt1 * 8 + (nt1 >> 1)) * 32 + (nt1 & 1)), tag, __ATOMIC_RELAXED, __HIP_MEMORY_SCOPE_AGENT);
        FENCE();
    };
    {
        const int tg = w & 3;
        char* xch = smem + XCH + tg * 3072;
        const int hoff = WS_H1 + (mt1 * 8 + 2 * tg) * (56 * 1024) + (nt1 >> 1) * 7168;
        if (__builtin_expect(aw, 1)) {
            const int toff = WS_LN2 + (mt1 * 8 + 2 * tg) * 16384;
            LAS char* wb0 = (LAS char*)smem + (4 * g + (m >> 2)) * 224 + (m & 3) * 8;
            LAS char* wb[4];
            int tq[4];
#pragma unroll
            for (int q = 0; q < 4; ++q) { const int qq = (q + rq) & 3; wb[q] = wb0 + qq * 28672; tq[q] = toff + qq * 4096; }
            u4 T[2][16], A[2][7];
            auto ldt = [&](int kk) {
#pragma unroll
                for (int tt = 0; tt < 2; ++tt) T[tt][kk] = bld4<SC1>(rw, l * 16 + (kk & 3) * 1024, tq[kk >> 2] + tt * 16384);
            };
#pragma unroll
            for (int i = 0; i < 7; ++i) A[0][i] = ldw1(wb[0], 0, i);
            if (l == 0) __hip_atomic_fetch_add(hw + 5, 1u, __ATOMIC_RELAXED, __HIP_MEMORY_SCOPE_WORKGROUP);
            ST(11);
#pragma unroll
            for (int kk = 0; kk < G1_DEPTH; ++kk) { ldt(kk); SCHED(); }
            f4 acc[2][7];
#pragma unroll
            for (int tt = 0; tt < 2; ++tt)
#pragma unroll
                for (int i = 0; i < 7; ++i) acc[tt][i] = Z4;
#pragma unroll
            for (int kk = 0; kk < 16; ++kk) {
                if ((kk & 1) == 0) {
                    if (__builtin_expect(any_sent(umax(umax(max4(T[0][kk]), max4(T[1][kk])), umax(max4(T[0][kk + 1]), max4(T[1][kk + 1])))), 0)) {
                        wait_tok(T[0][kk], T[1][kk], rw, l * 16, tq[kk >> 2] + (kk & 3) * 1024);
                        wait_tok(T[0][kk + 1], T[1][kk + 1], rw, l * 16, tq[kk >> 2] + ((kk + 1) & 3) * 1024);
                    }
                    if (kk == 0) ST(12);
                    if (kk == (BG6 / 7) * 4 - 2) lds_wait(hw + 5, 8u);
                }
                if (kk + G1_DEPTH < 16) ldt(kk + G1_DEPTH);
                if (kk < 15) {
#pragma unroll
                    for (int i = 0; i < 7; ++i) A[(kk + 1) & 1][i] = ldw1(wb[(kk + 1) >> 2], (kk + 1) & 3, i);
                }
#pragma unroll
                for (int i = 0; i < 7; ++i)
#pragma unroll
                    for (int tt = 0; tt < 2; ++tt) acc[tt][i] = mma_b(A[kk & 1][i], T[tt][kk], acc[tt][i]);
#pragma unroll
                for (int i = 0; i < 14; ++i) {
                    __builtin_amdgcn_sched_group_barrier(0x8, 1, 0);
                    if (i == 1 || i == 7) __builtin_amdgcn_sched_group_barrier(0x20, 1, 0);
                    __builtin_amdgcn_sched_group_barrier(0x100, 1, 0);
                    if ((kk & 1) && i >= 3) __builtin_amdgcn_sched_group_barrier(0x2, 1, 0);
                }
                if (kk & 1) {} else SCHED();
            }
#pragma unroll
            for (int q = 0; q < 3; ++q) {
                u2 p0 = pk4(acc[1][2 * q]), p1 = pk4(acc[1][2 * q + 1]);
                *(u4*)(xch + q * 1024 + l * 16) = (u4){p0[0], p0[1], p1[0], p1[1]};
            }
            lds_set(hw + tg, 1u);
            ST(13);
#pragma unroll
            for (int q = 0; q < 3; ++q) {
                u2 p0 = pk4(acc[0][2 * q]), p1 = pk4(acc[0][2 * q + 1]);
                bst4<SC1>(silu_pk4((u4){p0[0], p0[1], p1[0], p1[1]}), rw, l * 16, hoff + ((nt1 & 1) * 3 + q) * 1024);
            }
#pragma unroll
            for (int tt = 0; tt < 2; ++tt) { u2 p = pk4(acc[tt][6]); bst2<SC1>(silu_pk2(p[0], p[1]), rw, l * 8, hoff + tt * (56 * 1024) + 6144 + (nt1 & 1) * 512); }
        } else {
            bg(BG6, 28);
            if (l == 0) __hip_atomic_fetch_add(hw + 6, 1u, __ATOMIC_RELAXED, __HIP_MEMORY_SCOPE_WORKGROUP);
            lds_wait(hw + 6, 8u);
            w2_glds(pw, 0, lds0 + LDS_SCR + tg * 8192);
            w2_glds(pw, 1, lds0 + LDS_SCR + tg * 8192 + 4096);
            WAITV(8);
            if (l == 0) __hip_atomic_fetch_add(hw + 5, 1u, __ATOMIC_RELAXED, __HIP_MEMORY_SCOPE_WORKGROUP);
#pragma unroll
            for (int r = 0; r < B_R; ++r) { if (r < 6) WAITV(4); else WAITV(0); stage_out(r); }
            ST(12);
            lds_wait(hw + tg, 1u);
            ST(13);
#pragma unroll
            for (int q = 0; q < 3; ++q)
                bst4<SC1>(silu_pk4(*(const u4*)(xch + q * 1024 + l * 16)), rw, l * 16, hoff + 56 * 1024 + ((nt1 & 1) * 3 + q) * 1024);
        }
        ST(14);
        WAITV(0);
        publish();
        ST(21);
        if (__builtin_expect(aw, 1)) {
            lds_wait(hw + 4, 8u);
#pragma unroll
            for (int ks = 0; ks < 7; ++ks) w2_glds(pw, ks, lds0 + tg * 28672 + ks * 4096);
        } else {
            if (B_R < 7) stage_out(B_R);
#pragma unroll
            for (int r = B_R + 1; r < 7; ++r) { if (r + 1 < 7) WAITV(4); else WAITV(0); stage_out(r); }
        }
    }
    {
        ST(15);
        const int rot = w & 3;
        if (aw) {
            flag_wait(fl + ((l & 4) ? 4096 + (grp * 8 + th * 4 + rot) * 32 : (grp * 8 + (pw ^ ((l & 2) * 2))) * 32) + (l & 1), tag);
            lds_set(hw + 8 + rot, tag);
        } else {
#pragma nounroll
            for (int sp = 0; sp < SPIN_MAX; ++sp) {
                if (__builtin_amdgcn_readfirstlane(__hip_atomic_load(hw + 8 + rot, __ATOMIC_RELAXED, __HIP_MEMORY_SCOPE_WORKGROUP)) == tag) break;
                __builtin_amdgcn_s_sleep(1);
            }
            FENCE();
        }
        ST(16);
        const int toff = WS_H1 + (grp * 8 + th * 4) * (56 * 1024) + pw * 7168;
        int to[4];
#pragma unroll
        for (int tt = 0; tt < 4; ++tt) to[tt] = toff + ((tt + rot) & 3) * (56 * 1024);
        u4 T[7][4];
        auto ldtok = [&](int ks) {
#pragma unroll
            for (int tt = 0; tt < 4; ++tt) {
                if (ks < 6) T[ks][tt] = bld4<SC1>(rw, l * 16, to[tt] + ko[ks] * 1024);
                else {
                    u2 a = bld2<SC1>(rw, l * 8 + 2048, to[tt] + 4096), c = bld2<SC1>(rw, l * 8 + 2560, to[tt] + 4096);
                    T[ks][tt] = (u4){a[0], a[1], c[0], c[1]};
                }
            }
            SCHED();
        };
#pragma unroll
        for (int ks = 0; ks < G2_DEPTH; ++ks) ldtok(ks);
        if (aw) {
#pragma unroll
            for (int ks = 0; ks < 7; ++ks) w2_read(W[ks], rot * 28672 + ks * 4096);
            asm volatile("s_waitcnt lgkmcnt(0)" ::: "memory");
        }
        if (l == 0) __hip_atomic_fetch_add(hw + 7, 1u, __ATOMIC_RELAXED, __HIP_MEMORY_SCOPE_WORKGROUP);
        const int rho = grp * 128 + th * 64 + rot * 16 + m;
        const int ooff = (256 * (rho & 7) + (rho >> 3)) * 1024 + fb * 128 + (w >> 2) * 64 + g * 16;
        const u4 hres = bld4<SC1>(ro, ooff, 0);
        SCHED();
        f4 acc[4][4];
#pragma unroll
        for (int tt = 0; tt < 4; ++tt)
#pragma unroll
            for (int t = 0; t < 4; ++t) acc[tt][t] = Z4;
#pragma unroll
        for (int ks = 0; ks < 7; ++ks) {
            if (ks + G2_DEPTH < 7) { PIN(T[ks][0]); ldtok(ks + G2_DEPTH); }
#pragma unroll
            for (int tt = 0; tt < 4; ++tt)
#pragma unroll
                for (int t = 0; t < 4; ++t) {
                    acc[tt][t] = mma_b(W[ks][t], T[ks][tt], acc[tt][t]);
                }
            SCHED();
        }
        ST(17);
        lds_wait(hw + 7, 8u);
        char* dp = smem + w * 16384 + l * 16;
#pragma unroll
        for (int tt = 1; tt < 4; ++tt)
#pragma unroll
            for (int t = 0; t < 4; ++t) *(f4*)(dp + (((tt + rot) & 3) * 4 + t) * 1024) = acc[tt][t];
        f4 s0, s1;
        if (aw) { *(f4*)(dp + (rot * 4 + 2) * 1024) = acc[0][2]; *(f4*)(dp + (rot * 4 + 3) * 1024) = acc[0][3]; s0 = acc[0][0]; s1 = acc[0][1]; }
        else { *(f4*)(dp + (rot * 4) * 1024) = acc[0][0]; *(f4*)(dp + (rot * 4 + 1) * 1024) = acc[0][1]; s0 = acc[0][2]; s1 = acc[0][3]; }
        BARRIER();
        f4 r0[7], r1[7];
#pragma unroll
        for (int j = 1; j < 8; ++j) {
            const char* rp = smem + (((w + j) & 7) * 16 + rot * 4 + (w >> 2) * 2) * 1024 + l * 16;
            r0[j - 1] = *(const f4*)rp; r1[j - 1] = *(const f4*)(rp + 1024);
        }
        SCHED();
        s0 = ((s0 + r0[0]) + (r0[1] + r0[2])) + ((r0[3] + r0[4]) + (r0[5] + r0[6]));
        s1 = ((s1 + r1[0]) + (r1[1] + r1[2])) + ((r1[3] + r1[4]) + (r1[5] + r1[6]));
        u2 p0 = pk4(s0), p1 = pk4(s1);
        bst4<NT>((u4){pk_b2(bf_2(hres[0]) + bf_2(p0[0])), pk_b2(bf_2(hres[1]) + bf_2(p0[1])),
                           pk_b2(bf_2(hres[2]) + bf_2(p1[0])), pk_b2(bf_2(hres[3]) + bf_2(p1[1]))}, ro, ooff, 0);
    }
    ST_END(ws, 0);
}

extern "C" void kernel_launch(void* const* d_in, const int* in_sizes, int n_in, void* d_out, int out_size,
                              void* d_ws, size_t ws_size, hipStream_t stream) {
    (void)hipFuncSetAttribute((const void*)k_fused, hipFuncAttributeMaxDynamicSharedMemorySize, LDS_B);
    k_fused<<<256, 512, LDS_B, stream>>>((const uint16_t*)d_in[0], (const uint16_t*)d_in[1], (const uint16_t*)d_in[2],
                                         (const uint16_t*)d_in[4], (const uint16_t*)d_in[5], (uint16_t*)d_out, (char*)d_ws);
}
```

```cpp
#define BRR 7
#include <hip/hip_runtime.h>
#include <hip/hip_bf16.h>
#include <stdint.h>

typedef _Float16 h8 __attribute__((ext_vector_type(8)));
typedef _Float16 h4 __attribute__((ext_vector_type(4)));
typedef _Float16 h2 __attribute__((ext_vector_type(2)));
typedef __bf16 b8 __attribute__((ext_vector_type(8)));
typedef __bf16 b2 __attribute__((ext_vector_type(2)));
typedef short s4 __attribute__((ext_vector_type(4)));
typedef float f4 __attribute__((ext_vector_type(4)));
typedef float f2 __attribute__((ext_vector_type(2)));
typedef unsigned u32;
typedef u32 u4 __attribute__((ext_vector_type(4)));
typedef u32 u2 __attribute__((ext_vector_type(2)));
#define LAS __attribute__((address_space(3)))
#define GAS __attribute__((address_space(1)))
#define DI __device__ __forceinline__
#define FENCE() asm volatile("" ::: "memory")
#define PIN(x) asm volatile("" : "+v"(x))
#define SCHED() __builtin_amdgcn_sched_barrier(0)
#define WAITV(n) asm volatile("s_waitcnt vmcnt(%0)" :: "n"(n) : "memory")
#define BARRIER() do { asm volatile("s_waitcnt lgkmcnt(0)" ::: "memory"); __builtin_amdgcn_s_barrier(); FENCE(); } while (0)
constexpr int SC1 = 16, NT = 2;
constexpr u32 SENT = 0xffffffffu;
constexpr int SPIN_MAX = 1 << 20;

DI u32 pk_h(float a, float b) { f2 v = {a, b}; h2 r = __builtin_convertvector(v, h2); return __builtin_bit_cast(u32, r); }
DI u32 pk_b(float a, float b) { f2 v = {a, b}; b2 r = __builtin_convertvector(v, b2); return __builtin_bit_cast(u32, r); }
DI float bf_lo(u32 v) { return __builtin_bit_cast(float, v << 16); }
DI float bf_hi(u32 v) { return __builtin_bit_cast(float, v & 0xffff0000u); }
DI f2 bf_2(u32 v) { return (f2){bf_lo(v), bf_hi(v)}; }
DI u32 pk_b2(f2 v) { return pk_b(v[0], v[1]); }
DI float rbf(float a) { return bf_lo(pk_b(a, a)); }
DI float cosr(float rev) { return __builtin_amdgcn_cosf(rev); }
DI float sinr(float rev) { return __builtin_amdgcn_sinf(rev); }
DI f2 lo2(f4 v) { return __builtin_shufflevector(v, v, 0, 1); }
DI f2 hi2(f4 v) { return __builtin_shufflevector(v, v, 2, 3); }
DI f2 fma2(f2 a, f2 b, f2 c) { return __builtin_elementwise_fma(a, b, c); }
DI u32 umin(u32 a, u32 b) { return a < b ? a : b; }
DI u32 umax(u32 a, u32 b) { return a > b ? a : b; }
DI u32 clean(u32 v) { return umin(v, SENT - 1); }
DI u32 max4(u4 v) { return umax(umax(umax(v[0], v[1]), v[2]), v[3]); }
#define Z4 ((f4){0, 0, 0, 0})
DI f4 mma_h(u4 a, u4 b, f4 c) { return __builtin_amdgcn_mfma_f32_16x16x32_f16(__builtin_bit_cast(h8, a), __builtin_bit_cast(h8, b), c, 0, 0, 0); }
DI f4 mma_b(u4 a, u4 b, f4 c) { return __builtin_amdgcn_mfma_f32_16x16x32_bf16(__builtin_bit_cast(b8, a), __builtin_bit_cast(b8, b), c, 0, 0, 0); }
DI f4 mma16_b(u2 a, u2 b, f4 c) { return __builtin_amdgcn_mfma_f32_16x16x16bf16_1k(__builtin_bit_cast(s4, a), __builtin_bit_cast(s4, b), c, 0, 0, 0); }
DI f4 mma16_h(u2 a, u2 b, f4 c) { return __builtin_amdgcn_mfma_f32_16x16x16f16(__builtin_bit_cast(h4, a), __builtin_bit_cast(h4, b), c, 0, 0, 0); }
typedef __amdgpu_buffer_rsrc_t rsrc_t;
DI rsrc_t mkrs(const void* p, u32 bytes) { return __builtin_amdgcn_make_buffer_rsrc((void*)p, 0, bytes, 0x00020000); }
template <int AUX> DI u4 bld4(rsrc_t r, int v, int s) { return __builtin_amdgcn_raw_buffer_load_b128(r, v, s, AUX); }
template <int AUX> DI u2 bld2(rsrc_t r, int v, int s) { return __builtin_amdgcn_raw_buffer_load_b64(r, v, s, AUX); }
template <int AUX> DI void bst4(u4 d, rsrc_t r, int v, int s) {
    __builtin_amdgcn_raw_buffer_store_b128(d, r, v, s, AUX);
    asm volatile("s_nop 1" :: "v"(d));
}
template <int AUX> DI void bst2(u2 d, rsrc_t r, int v, int s) { __builtin_amdgcn_raw_buffer_store_b64(d, r, v, s, AUX); }

template <int CTRL> DI float dpp(float v) {
    return __builtin_bit_cast(float, __builtin_amdgcn_update_dpp(0, __builtin_bit_cast(int, v), CTRL, 0xf, 0xf, true));
}
DI float swap_add16(float v) {
    u32 x = __builtin_bit_cast(u32, v), y = x;
    asm("" : "+v"(y));
    auto r = __builtin_amdgcn_permlane16_swap(x, y, false, false);
    return __builtin_bit_cast(float, (u32)r[0]) + __builtin_bit_cast(float, (u32)r[1]);
}
DI float swap_add32(float v) {
    u32 x = __builtin_bit_cast(u32, v), y = x;
    asm("" : "+v"(y));
    auto r = __builtin_amdgcn_permlane32_swap(x, y, false, false);
    return __builtin_bit_cast(float, (u32)r[0]) + __builtin_bit_cast(float, (u32)r[1]);
}
template <bool ALL> DI void red_lanes(float& a, float& b) {
    a += dpp<0xB1>(a); b += dpp<0xB1>(b);
    if (ALL) { a += dpp<0x4E>(a); b += dpp<0x4E>(b); }
    a += dpp<0x124>(a); b += dpp<0x124>(b);
    a += dpp<0x128>(a); b += dpp<0x128>(b);
    a = swap_add16(a); b = swap_add16(b);
    a = swap_add32(a); b = swap_add32(b);
}
DI float ln_inv(float var) { return __builtin_amdgcn_rcpf(rbf(__builtin_amdgcn_sqrtf(rbf(rbf(var) + 1e-5f)))); }

#define ST_DECL
#define ST(i)
#define ST_END(ws, kid)

#define FS 1
constexpr u32 WS_U = 0;
constexpr u32 WS_V = WS_U + (2u << 20);
constexpr u32 WS_LN2 = WS_V + (2u << 20);
constexpr u32 WS_WA = WS_LN2 + (2u << 20);
constexpr u32 WS_FA = WS_WA + 32768u;
constexpr u32 WS_FLAG = WS_FA + 16384u * FS;
constexpr u32 WS_EP = WS_FLAG + 32768u;
constexpr u32 WS_H1 = WS_WA + (512u << 10);
constexpr u32 WS_END = WS_H1 + 7340032u;

#define BGA 10
#define BGB 10
#define BGC 21
#define G_SLEEP 16
#define GA_SLEEP 4
#define GB_SLEEP 8
#define A_SLEEP 0
#define VPOS 0
#define LPOS 1
#define SP_SLEEP 6
#define SP_BURST 1
#define EARLY_STAGE 1
#define STG_SLEEP 8
#define H_SLEEP 2
#define BG_DELAY 14
#define PRO_SLEEP 4
constexpr int BG5 = BGA, BG5b = BGB, BG6 = BGC;
constexpr int G1_DEPTH = 10, G2_DEPTH = 3;
constexpr int B_R = BRR;
constexpr int CODE_LINES = 290, CODE_PF = 10;
constexpr int LDS_SCR = 114688;
constexpr int SCR_X = 8448;
constexpr int SCR_S = SCR_X + 20480;
constexpr int SCR_F = 49024;
constexpr int XCH = 147456;
constexpr int LDS_B = 163840;

DI void glds16(const void* sbase, u32 voff, u32 lds_dst) {
    asm volatile("s_mov_b32 m0, %2\n\ts_nop 0\n\tglobal_load_lds_dwordx4 %0, %1" :: "v"(voff), "s"(sbase), "s"(lds_dst) : "memory");
}
DI bool any_sent(u32 v) { return __builtin_amdgcn_ballot_w64(v == SENT) != 0; }
DI void poll2(u4& a, u4& c, rsrc_t rw, int vo, int sa, int sc) {
#pragma nounroll
    for (int sp = 0; sp < SPIN_MAX; ++sp) {
        const bool ba = max4(a) == SENT, bc = max4(c) == SENT;
        if (__builtin_amdgcn_ballot_w64(ba || bc) == 0) break;
        __builtin_amdgcn_s_sleep(1);
        FENCE();
        if (ba) a = bld4<SC1>(rw, vo, sa);
        if (bc) c = bld4<SC1>(rw, vo, sc);
    }
}
DI void wait_tok(u4& t0, u4& t1, rsrc_t rw, int vo, int so) {
    if (any_sent(umax(max4(t0), max4(t1)))) {
#pragma nounroll
        for (int sp = 0; sp < SPIN_MAX; ++sp) {
            FENCE();
            t0 = bld4<SC1>(rw, vo, so); t1 = bld4<SC1>(rw, vo, so + 16384);
            if (!any_sent(umax(max4(t0), max4(t1)))) break;
        }
    }
}
DI u4 ldw1(LAS char* wb, int kk, int ft) {
    s4 a0 = __builtin_amdgcn_ds_read_tr16_b64_v4i16((LAS s4*)(wb + kk * 32 * 224 + ft * 32));
    s4 a1 = __builtin_amdgcn_ds_read_tr16_b64_v4i16((LAS s4*)(wb + (kk * 32 + 16) * 224 + ft * 32));
    u2 x0 = __builtin_bit_cast(u2, a0), x1 = __builtin_bit_cast(u2, a1);
    return (u4){x0[0], x0[1], x1[0], x1[1]};
}
DI u2 pk4(f4 a) { return (u2){pk_b(a[0], a[1]), pk_b(a[2], a[3])}; }
DI u2 silu_pk2(u32 p0, u32 p1) {
    u32 o0, o1; float a0, a1, a2, a3, t0, t1, t2, t3;
    asm("v_lshlrev_b32 %2, 16, %10\n\tv_and_b32 %3, 0xffff0000, %10\n\tv_lshlrev_b32 %4, 16, %11\n\tv_and_b32 %5, 0xffff0000, %11\n\t"
        "v_mul_f32 %6, %12, %2\n\tv_mul_f32 %7, %12, %3\n\tv_mul_f32 %8, %12, %4\n\tv_mul_f32 %9, %12, %5\n\t"
        "v_exp_f32 %6, %6\n\tv_exp_f32 %7, %7\n\tv_exp_f32 %8, %8\n\tv_exp_f32 %9, %9\n\t"
        "v_add_f32 %6, 1.0, %6\n\tv_add_f32 %7, 1.0, %7\n\tv_add_f32 %8, 1.0, %8\n\tv_add_f32 %9, 1.0, %9\n\t"
        "v_rcp_f32 %6, %6\n\tv_rcp_f32 %7, %7\n\tv_rcp_f32 %8, %8\n\tv_rcp_f32 %9, %9\n\t"
        "v_mul_f32 %2, %2, %6\n\tv_mul_f32 %3, %3, %7\n\tv_mul_f32 %4, %4, %8\n\tv_mul_f32 %5, %5, %9\n\t"
        "v_cvt_pk_bf16_f32 %0, %2, %3\n\tv_cvt_pk_bf16_f32 %1, %4, %5"
        : "=&v"(o0), "=&v"(o1), "=&v"(a0), "=&v"(a1), "=&v"(a2), "=&v"(a3), "=&v"(t0), "=&v"(t1), "=&v"(t2), "=&v"(t3)
        : "v"(p0), "v"(p1), "s"(-1.4426950408889634f));
    return (u2){o0, o1};
}
DI u4 silu_pk4(u4 p) { u2 a = silu_pk2(p[0], p[1]), b = silu_pk2(p[2], p[3]); return (u4){a[0], a[1], b[0], b[1]}; }
DI void lds_set(LAS u32* p, u32 v) {
    asm volatile("s_waitcnt lgkmcnt(0)" ::: "memory");
    __hip_atomic_store(p, v, __ATOMIC_RELAXED, __HIP_MEMORY_SCOPE_WORKGROUP);
}
DI void lds_wait(LAS u32* p, u32 v) {
#pragma nounroll
    for (int sp = 0; sp < SPIN_MAX; ++sp) {
        if (__builtin_amdgcn_readfirstlane(__hip_atomic_load(p, __ATOMIC_RELAXED, __HIP_MEMORY_SCOPE_WORKGROUP)) >= v) break;
        __builtin_amdgcn_s_sleep(1);
    }
    FENCE();
}
DI void flag_wait(GAS u32* p, u32 tag) {
#pragma nounroll
    for (int sp = 0; sp < SPIN_MAX; ++sp) {
        if (__builtin_amdgcn_ballot_w64(__hip_atomic_load(p, __ATOMIC_RELAXED, __HIP_MEMORY_SCOPE_AGENT) != tag) == 0) break;
        __builtin_amdgcn_s_sleep(2);
    }
    FENCE();
}
#define SENT4 ((u4){SENT, SENT, SENT, SENT})

__global__ __launch_bounds__(512, 2) void k_fused(const uint16_t* x, const uint16_t* wr, const uint16_t* wi, const uint16_t* w1,
                                                  const uint16_t* w2, uint16_t* out, char* ws) {
    extern __shared__ __attribute__((aligned(16))) char smem[];
    const int tid = threadIdx.x, l = tid & 63, g = l >> 4, m = l & 15;
    const int w = __builtin_amdgcn_readfirstlane(tid >> 6);
    const int b = (blockIdx.x & 7) * 32 + (blockIdx.x >> 3);
    const int mt1 = (b >> 6) * 4 + ((b >> 3) & 3), nt1 = ((b >> 5) & 1) * 8 + (b & 7);
    const int grp = b >> 4, th = (b >> 3) & 1, fb = b & 7;
    const rsrc_t rw = mkrs(ws, WS_END), ro = mkrs(out, 2097152);
    char* scr = smem + LDS_SCR;
    LAS u32* hw = (LAS u32*)((LAS char*)smem + LDS_SCR + SCR_F);
    const u32 lds0 = (u32)(uintptr_t)(LAS char*)smem;
    const float mf = (float)m, gf = (float)g, sg = (m & 1) ? -1.0f : 1.0f;
    const bool aw = w < 4;
    ST_DECL;
    unsigned long long pc0;
    asm volatile("s_getpc_b64 %0" : "=s"(pc0));
    if (tid >= 448 && l < CODE_PF)
        asm volatile("s_mov_b32 m0, %2\n\ts_nop 0\n\tglobal_load_lds_dword %0, %1"
                     :: "v"(umin((blockIdx.x >> 3) * CODE_PF + l, CODE_LINES - 1) * 128), "s"(pc0), "s"(lds0 + LDS_SCR + SCR_F + 64) : "memory");
    const u32 epoch = __hip_atomic_load((GAS u32*)(ws + WS_EP), __ATOMIC_RELAXED, __HIP_MEMORY_SCOPE_AGENT);
    FENCE();
    u32 xd[8];
    const int f1 = b >> 5, cg = b & 31;
    const bool mir = cg >= 16;
    const rsrc_t rr = mkrs(wr, 1052672), ri = mkrs(wi, 1052672);
    u32 sr_, si_;
    if (aw) {
        __builtin_amdgcn_s_sleep(A_SLEEP);
        const u32* xr = (const u32*)(x + (256 * (2 * w + ((m >> 1) & 1)) + b) * 512) + 8 * g + 2 * (m >> 2) + (m & 1);
#pragma unroll
        for (int j = 0; j < 8; ++j) xd[j] = xr[32 * j];
    } else {
        bst4<SC1>(SENT4, rw, l * 16, WS_U + b * 8192 + (w - 4) * 2048); bst4<SC1>(SENT4, rw, l * 16 + 1024, WS_U + b * 8192 + (w - 4) * 2048);
        bst4<SC1>(SENT4, rw, l < 2 ? l * 4096 : 0x7ffffff0, WS_WA + (w - 4) * 8192 + b * 16);
        FENCE();
        if (w == 7 && VPOS == 0) {
#pragma unroll
            for (int i = 0; i < 8; ++i) bst4<SC1>(SENT4, rw, l * 16 + (i & 3) * 1024, WS_V + b * 8192 + (i >> 2) * 4096);
        }
        if (w == 6) {
            const int c = (l >> 3) & 3, f_ = 8 * b + (l & 7);
            const int e1 = ((c & 1) ? (2048 - f_) & 2047 : f_) * 514 + (c >> 1) * 512;
            sr_ = __builtin_amdgcn_raw_buffer_load_b16(rr, e1, 0, 0); si_ = __builtin_amdgcn_raw_buffer_load_b16(ri, e1, 0, 0);
        }
    }
    FENCE();
    const int zo = (64 * g + (g < 2 ? m : (m + 15) & 15)) * 4;
    const int rq = ((b >> 3) + b) & 3;
    const int row0 = (tid & 255) / 14, cch0 = (tid & 255) - row0 * 14;
    const char* wsrc = (const char*)w1 + nt1 * 224;
    u32 w1o[7];
    auto w1_burst = [&](int i0, int n) {
#pragma unroll
        for (int i = i0; i < i0 + n; ++i) {
            const int q = (i / 7 + rq) & 3, j = i % 7;
            glds16(wsrc + q * (128 * 3584), w1o[j], lds0 + (w - 4) * 1024 + q * 28672 + j * 4096);
        }
    };
    const int xoff = (256 * w + b) * 1024 + m * 32 + g * 8;
    u2 xl[2];
    float c16[4], s16[4];
    f2 twc[2], tws[2];
    u4 Gre3, Gim3, SGre, SGim, GA[2];
    auto consts0 = [&]() {
#pragma unroll
        for (int u = 0; u < 4; ++u) {
            float a = (4.0f * gf + u) * (mf * (1.0f / 16));
            c16[u] = cosr(a); s16[u] = sinr(a);
            twc[u >> 1][u & 1] = cosr(a * (1.0f / 32)); tws[u >> 1][u & 1] = sinr(a * (1.0f / 32));
        }
    };
    auto consts3 = [&]() {
#pragma unroll
        for (int u = 0; u < 4; ++u) {
            const float k = 4.0f * gf + u;
            const bool k0 = u == 0 && g == 0;
            float cc = c16[u], ss = s16[u];
            Gre3[u] = pk_b(cc, -ss); Gim3[u] = pk_b(ss, cc);
            float a2 = (2.0f * k + 1.0f) * (mf * (1.0f / 32));
            u32 sp = pk_b(g < 2 ? 2.f * cc : 2.f * cosr(a2), g < 2 ? -2.f * ss : -2.f * sinr(a2));
            SGre[u] = k0 ? pk_b(1.0f, sg) : (g < 2 ? sp : 0u);
            SGim[u] = g < 2 ? 0u : sp;
            float a4 = k * (mf * (1.0f / 32)), c4 = 2.f * cosr(a4), s4_ = -2.f * sinr(a4);
            GA[0][u] = k0 ? pk_b(1.0f, sg) : pk_b(c4, s4_);
            GA[1][u] = k0 ? pk_b(1.0f, sg) : ((u & 1) ? pk_b(-c4, -s4_) : pk_b(c4, s4_));
        }
        PIN(Gre3); PIN(Gim3); PIN(SGre); PIN(SGim); PIN(GA[0]); PIN(GA[1]); PIN(twc[0]); PIN(twc[1]); PIN(tws[0]); PIN(tws[1]);
    };
    const int pw = (w + 4 * th) & 7;
    GAS u32* fl = (GAS u32*)(ws + WS_FLAG);
    int ko[7];
    {
        const int r6 = (fb < 6 ? fb : fb - 5) + 3 * (grp & 1);
#pragma unroll
        for (int ks = 0; ks < 6; ++ks) { int k = ks + r6; k = k >= 6 ? k - 6 : k; ko[ks] = k >= 6 ? k - 6 : k; }
        ko[6] = 6;
    }
    u4 W[7][4];
    const u32 w2vo = (l >> 3) * 1024 + (((l & 7) ^ (((l >> 4) & 1) << 2)) * 16);
    auto w2_glds = [&](int p_, int ks, u32 dst) {
        const int q = ko[ks];
        const char* src = (const char*)w2 + (224 * p_ + (ks == 6 ? 96 : (q < 3 ? 32 * q : 16 + 32 * q))) * 1024 + fb * 128;
        glds16(src, w2vo, dst); glds16(src + 8192, w2vo, dst + 1024);
        glds16(src + (ks == 6 ? 112 : 16) * 1024, w2vo, dst + 2048); glds16(src + (ks == 6 ? 120 : 24) * 1024, w2vo, dst + 3072);
    };
    LAS char* w2rd = (LAS char*)smem + (4 * g + (m >> 2)) * 128 + (((m & 3) ^ (((m >> 3) & 1) << 2)) * 16);
    auto w2_read = [&](u4 (&f)[4], int off) {
#pragma unroll
        for (int t = 0; t < 4; ++t) {
            LAS char* p = (LAS char*)((u32)(uintptr_t)w2rd ^ ((t >> 1) * 64)) + off + (t & 1) * 8;
            u2 x0 = __builtin_bit_cast(u2, __builtin_amdgcn_ds_read_tr16_b64_v4i16((LAS s4*)p));
            u2 x1 = __builtin_bit_cast(u2, __builtin_amdgcn_ds_read_tr16_b64_v4i16((LAS s4*)(p + 2048)));
            f[t] = (u4){x0[0], x0[1], x1[0], x1[1]};
        }
    };
    auto bg = [&](int i0, int i1) { w1_burst(i0, i1 - i0); };
    u32 tag;
    if (__builtin_expect(aw, 1)) {
        if (tid < 8) hw[tid] = 0;
        FENCE();
        ST(2);
        {
            u4 FAre, FAim, Fre, Fim, Sre, Sim; u2 Mb; f2 pc[2], ps[2];
            consts0();
            float c0[8], s0[8];
#pragma unroll
            for (int j = 0; j < 8; ++j) {
                float a = (gf + 4.0f * j) * (mf * (1.0f / 32));
                c0[j] = cosr(a); s0[j] = m ? -sinr(a) : ((g & 1) ? -1.0f : 1.0f);
            }
            const float mo = (2.0f * mf + 1.0f) * (1.0f / 32);
#pragma unroll
            for (int u = 0; u < 4; ++u) {
                FAre[u] = pk_h(c0[2 * u], c0[2 * u + 1]); FAim[u] = pk_h(s0[2 * u], s0[2 * u + 1]);
                float cc = c16[u], ss = s16[u];
                Fre[u] = pk_h(cc, ss); Fim[u] = pk_h(-ss, cc);
                float a2 = (4.0f * gf + u) * mo;
                u32 re = pk_h(m < 8 ? cc : cosr(a2), 0.f), im = pk_h(m < 8 ? (m ? -ss : ((u & 1) ? -1.0f : 1.0f)) : -sinr(a2), 0.f);
                Sre[u] = m < 8 ? re : re << 16; Sim[u] = m < 8 ? im : im << 16;
                pc[u & 1][u >> 1] = twc[u >> 1][u & 1]; ps[u & 1][u >> 1] = tws[u >> 1][u & 1];
            }
#pragma unroll
            for (int e = 0; e < 2; ++e) {
                float a = (float)(((m >> 1) * (256 * (2 * g + e) + b)) & 2047) * (1.0f / 2048);
                float cc = cosr(a), ss = sinr(a);
                Mb[e] = (m & 1) ? pk_h(-ss, cc) : pk_h(cc, ss);
            }
            PIN(FAre); PIN(FAim); PIN(Fre); PIN(Fim); PIN(Sre); PIN(Sim); PIN(Mb); PIN(pc[0]); PIN(pc[1]); PIN(ps[0]); PIN(ps[1]);
            ST(3);
            f2 v[8];
#pragma unroll
            for (int j = 0; j < 8; ++j) v[j] = bf_2(xd[j]);
            const u32 x0 = __builtin_amdgcn_readlane(xd[0], 0), x2 = __builtin_amdgcn_readlane(xd[0], 2);
            const float c = bf_lo((m & 2) ? x2 : x0);
            f2 q0 = {0.f, 0.f}, q1 = {0.f, 0.f};
#pragma unroll
            for (int j = 0; j < 8; ++j) { v[j] -= c; if (j & 1) q1 = fma2(v[j], v[j], q1); else q0 = fma2(v[j], v[j], q0); }
            f2 s2 = ((v[0] + v[1]) + (v[2] + v[3])) + ((v[4] + v[5]) + (v[6] + v[7]));
            q0 += q1;
            float s1 = s2[0] + s2[1], sq = q0[0] + q0[1];
            red_lanes<false>(s1, sq);
            s1 *= 1.0f / 512;
            const float inv = ln_inv(__builtin_fmaxf(sq * (1.0f / 512) - s1 * s1, 0.f));
            const float off = (c - rbf(c + s1)) * inv;
#pragma unroll
            for (int j = 0; j < 8; ++j) v[j] = fma2(v[j], (f2){inv, inv}, (f2){off, off});
            u4 B[2];
#pragma unroll
            for (int p = 0; p < 2; ++p) {
                u4 A = {pk_h(v[0][p], v[1][p]), pk_h(v[2][p], v[3][p]), pk_h(v[4][p], v[5][p]), pk_h(v[6][p], v[7][p])};
                f4 Dre = mma_h(A, FAre, Z4), Dim = mma_h(A, FAim, Z4);
#pragma unroll
                for (int r = 0; r < 2; ++r) {
                    f2 re = r ? hi2(Dre) : lo2(Dre), im = r ? hi2(Dim) : lo2(Dim);
                    f2 tr = fma2(im, ps[p], re * pc[p]), ti = fma2(-re, ps[p], im * pc[p]);
                    B[r][p] = pk_h(tr[0], ti[0]); B[r][2 + p] = pk_h(tr[1], ti[1]);
                }
            }
#pragma unroll
            for (int r = 0; r < 2; ++r) {
                f4 Gre = mma_h(Fre, B[r], Z4), Gim = mma_h(Fim, B[r], Z4), Hre = mma_h(Sre, B[r], Z4), Him = mma_h(Sim, B[r], Z4);
#pragma unroll
                for (int i = 0; i < 4; ++i) *(u32*)(scr + (2 * w + r) * 1024 + zo + i * 64) = pk_h(m ? Gre[i] : Hre[i], m ? Gim[i] : Him[i]);
            }
            BARRIER();
            ST(18);
            u4 r0 = *(const u4*)(scr + (2 * g) * 1024 + w * 256 + m * 16);
            u4 r1 = *(const u4*)(scr + (2 * g + 1) * 1024 + w * 256 + m * 16);
            u4 o0, o1;
#pragma unroll
            for (int t = 0; t < 4; ++t) {
                f4 D = mma16_h(Mb, (u2){r0[t], r1[t]}, Z4);
                o0[t] = clean(pk_h(D[0], D[1])); o1[t] = clean(pk_h(D[2], D[3]));
            }
            bst4<SC1>(o0, rw, g * 2048 + m * 16, WS_U + b * 8192 + w * 256);
            bst4<SC1>(o1, rw, g * 2048 + m * 16 + 1024, WS_U + b * 8192 + w * 256);
            if (w == 0 && m == 0) {
                __builtin_amdgcn_raw_buffer_store_b32(o0[0], rw, ((8 - 2 * g) & 7) * 4096, WS_WA + b * 16, SC1);
                __builtin_amdgcn_raw_buffer_store_b32(o1[0], rw, (7 - 2 * g) * 4096, WS_WA + b * 16, SC1);
            }
        }
        ST(4);
        u4 Fre, Fim, Gre, Gim; u2 Nb; f2 tc[2], ts[2];
#pragma unroll
        for (int u = 0; u < 4; ++u) {
            float cc = c16[u], ss = s16[u];
            Fre[u] = pk_h(0.25f * cc, 0.25f * ss); Fim[u] = pk_h(-0.25f * ss, 0.25f * cc);
            Gre[u] = pk_b(cc * (1.0f / 256), -ss * (1.0f / 256)); Gim[u] = pk_b(ss * (1.0f / 256), cc * (1.0f / 256));
            float a3 = (4.0f * gf + u) * (mf * (1.0f / 256));
            tc[u >> 1][u & 1] = cosr(a3); ts[u >> 1][u & 1] = sinr(a3);
        }
        PIN(Fre); PIN(Fim); PIN(Gre); PIN(Gim); PIN(tc[0]); PIN(tc[1]); PIN(ts[0]); PIN(ts[1]);
        xl[0] = *(const u2*)((const char*)x + xoff); xl[1] = *(const u2*)((const char*)x + xoff + 512);
        FENCE();
        tag = __builtin_amdgcn_readfirstlane(epoch) + 1u;
        consts3();
#pragma unroll
        for (int e = 0; e < 2; ++e) {
            float a1 = (float)(((2 * g + e) * (256 * (m >> 1) + b)) & 2047) * (1.0f / 2048);
            float c1 = cosr(a1), s1 = sinr(a1);
            Nb[e] = (m & 1) ? pk_b(s1, c1) : pk_b(c1, -s1);
        }
        PIN(Nb);
        WAITV(0);
        PIN(xl[0]); PIN(xl[1]);
        ST(19);
        BARRIER();
        ST(5);
        u4 d[2], rec[4];
        const int uo = (tid >> 1) * 8192 + (tid & 1) * 16, us = WS_U + f1 * 1024 + cg * 32;
        d[0] = bld4<SC1>(rw, uo, us); d[1] = bld4<SC1>(rw, uo, us + 128 * 8192);
        poll2(d[0], d[1], rw, uo, us, us + 128 * 8192);
        char* sp = scr + SCR_S + (tid ^ ((tid >> 4) & 1)) * 16;
        *(u4*)sp = d[0]; *(u4*)(sp + 4096) = d[1];
        BARRIER();
        if (b == 0 && tid == 0) __hip_atomic_store((GAS u32*)(ws + WS_EP), tag, __ATOMIC_RELAXED, __HIP_MEMORY_SCOPE_AGENT);
        const int ro_ = (64 * g + m) * 16, rs_ = WS_WA + f1 * 4096;
        if (cg == 0 && w == 0) {
#pragma unroll
            for (int i = 0; i < 4; ++i) rec[i] = bld4<SC1>(rw, ro_ + i * 256, rs_);
        }
        ST(6);
        u2 ld[4], wl[4];
        const int so = (64 * g + m) * 32 + (((w >> 1) ^ (m >> 3)) * 16) + (w & 1) * 8;
#pragma unroll
        for (int q = 0; q < 4; ++q) ld[q] = *(const u2*)(scr + SCR_S + so + 512 * q);
#pragma unroll
        for (int q = 0; q < 4; ++q) wl[q] = *(const u2*)(scr + SCR_S + so + 8192 + 512 * q);
        FENCE();
        auto fwd = [&](u4 A, f4& Xr, f4& Xi) {
            f4 Dre = mma_h(A, Fre, Z4), Dim = mma_h(A, Fim, Z4);
            f2 r0 = fma2(lo2(Dim), ts[0], lo2(Dre) * tc[0]), i0 = fma2(-lo2(Dre), ts[0], lo2(Dim) * tc[0]);
            f2 r1 = fma2(hi2(Dim), ts[1], hi2(Dre) * tc[1]), i1 = fma2(-hi2(Dre), ts[1], hi2(Dim) * tc[1]);
            u4 B = {pk_h(r0[0], i0[0]), pk_h(r0[1], i0[1]), pk_h(r1[0], i1[0]), pk_h(r1[1], i1[1])};
            Xr = mma_h(Fre, B, Z4); Xi = mma_h(Fim, B, Z4);
        };
        f4 Xre[2], Xim[2];
#pragma unroll
        for (int e = 0; e < 2; ++e) fwd((u4){ld[0][e], ld[1][e], ld[2][e], ld[3][e]}, Xre[e], Xim[e]);
        f2 yr[2][2], yi[2][2];
#pragma unroll
        for (int e = 0; e < 2; ++e)
#pragma unroll
            for (int hh = 0; hh < 2; ++hh) {
                f2 wre = {bf_lo(wl[2 * hh][e]), bf_lo(wl[2 * hh + 1][e])}, wim = {bf_hi(wl[2 * hh][e]), bf_hi(wl[2 * hh + 1][e])};
                f2 xr = hh ? hi2(Xre[e]) : lo2(Xre[e]), xi = hh ? hi2(Xim[e]) : lo2(Xim[e]);
                yr[e][hh] = fma2(-xi, wim, xr * wre); yi[e][hh] = fma2(xr, wim, xi * wre);
            }
        if (cg == 0 && w == 0) {
#pragma nounroll
            for (int sp = 0; sp < SPIN_MAX; ++sp) {
                u32 mx = 0;
#pragma unroll
                for (int i = 0; i < 4; ++i) mx = umax(mx, umax(umax(rec[i][0], rec[i][1]), rec[i][2]));
                if (!any_sent(mx)) break;
                FENCE();
#pragma unroll
                for (int i = 0; i < 4; ++i) rec[i] = bld4<SC1>(rw, ro_ + i * 256, rs_);
            }
            f4 Mr, Mi;
            fwd((u4){rec[0][0], rec[1][0], rec[2][0], rec[3][0]}, Mr, Mi);
            f2* sx = (f2*)(scr + SCR_X);
#pragma unroll
            for (int i = 0; i < 4; ++i) sx[m + 16 * (4 * g + i)] = (f2){Mr[i], Mi[i]};
#pragma unroll
            for (int i = 0; i < 4; ++i) {
                f2 xm = sx[((f1 ? 255 : 256) - (m + 16 * (4 * g + i))) & 255];
                float ar = bf_lo(rec[i][1]), ai = bf_hi(rec[i][1]), br = bf_lo(rec[i][2]), bi = bf_hi(rec[i][2]);
                yr[0][i >> 1][i & 1] = Xre[0][i] * ar - Xim[0][i] * ai + xm[0] * br + xm[1] * bi;
                yi[0][i >> 1][i & 1] = Xre[0][i] * ai + Xim[0][i] * ar + xm[0] * bi - xm[1] * br;
            }
        }
        char* pv = scr;
#pragma unroll
        for (int e = 0; e < 2; ++e) {
            u4 Y = {pk_b(yr[e][0][0], yi[e][0][0]), pk_b(yr[e][0][1], yi[e][0][1]), pk_b(yr[e][1][0], yi[e][1][0]), pk_b(yr[e][1][1], yi[e][1][1])};
            f4 Dre = mma_b(Y, Gre, Z4), Dim = mma_b(Y, Gim, Z4);
            f2 r0 = fma2(-lo2(Dim), ts[0], lo2(Dre) * tc[0]), i0 = fma2(lo2(Dre), ts[0], lo2(Dim) * tc[0]);
            f2 r1 = fma2(-hi2(Dim), ts[1], hi2(Dre) * tc[1]), i1 = fma2(hi2(Dre), ts[1], hi2(Dim) * tc[1]);
            u4 A = {pk_b(r0[0], i0[0]), pk_b(r0[1], i0[1]), pk_b(r1[0], i1[0]), pk_b(r1[1], i1[1])};
            f4 Ere = mma_b(A, Gre, Z4), Eim = mma_b(A, Gim, Z4);
            u4 o;
#pragma unroll
            for (int i = 0; i < 4; ++i) o[i] = clean(pk_b(Ere[i], Eim[i]));
            *(u4*)(pv + (2 * w + e) * 1024 + ((m * 4 + g) ^ ((w >> 1) * 4)) * 16) = o;
        }
        BARRIER();
        ST(7);
        {
            const u32* q = (const u32*)(pv + (tid & 1) * 4096 + (((tid >> 1) * 4) ^ ((tid & 1) * 64)));
            const u4 o0 = {q[0], q[256], q[512], q[768]}, o1 = {q[128], q[384], q[640], q[896]};
            SCHED();
            bst4<SC1>(o0, rw, tid * 16, WS_V + b * 8192); bst4<SC1>(o1, rw, tid * 16, WS_V + b * 8192 + 4096);
        }
        WAITV(0);
        ST(8);
        const int vo = (g * 64 + (m >> 1)) * 8192 + (m & 1) * 16, vs = WS_V + w * 8 * 8192 + b * 32;
        u4 v0 = bld4<SC1>(rw, vo, vs), v1 = bld4<SC1>(rw, vo, vs + 32 * 8192);
        poll2(v0, v1, rw, vo, vs, vs + 32 * 8192);
        ST(9);
        u4 z0, z1;
#pragma unroll
        for (int t = 0; t < 4; ++t) {
            f4 D = mma16_b(Nb, (u2){v0[t], v1[t]}, Z4);
            z0[t] = pk_b(D[0], D[1]); z1[t] = pk_b(D[2], D[3]);
        }
        *(u4*)(scr + SCR_X + g * 2048 + w * 256 + m * 16) = z0;
        *(u4*)(scr + SCR_X + g * 2048 + 1024 + w * 256 + m * 16) = z1;
    } else {
        auto announce = [&](int which) {
            if (l < 8) __hip_atomic_store((GAS u32*)(ws + WS_FA) + (which * 2048 + l * 256 + b) * FS, tag, __ATOMIC_RELAXED, __HIP_MEMORY_SCOPE_AGENT);
            FENCE();
        };
        auto all_announced = [&](int which) {
#pragma nounroll
            for (int sp = 0; sp < SPIN_MAX; ++sp) {
                FENCE();
                u4 f;
#pragma unroll
                for (int i = 0; i < 4; ++i) f[i] = __builtin_amdgcn_raw_buffer_load_b32(rw, (l + 64 * i) * 4 * FS, WS_FA + (which * 8192 + (b >> 5) * 1024) * FS, SC1);
                if (__builtin_amdgcn_ballot_w64(f[0] != tag || f[1] != tag || f[2] != tag || f[3] != tag) == 0) break;
                __builtin_amdgcn_s_sleep(4);
            }
        };
        FENCE();
        ST(2);
        tag = __builtin_amdgcn_readfirstlane(epoch) + 1u;
        if (w == 6) WAITV(2); else if (w == 7 && VPOS == 0) WAITV(8); else WAITV(0);
        if (l < 2) __hip_atomic_store((GAS u32*)(ws + WS_FA) + ((2 * (w - 4) + l) * 256 + b) * FS, tag, __ATOMIC_RELAXED, __HIP_MEMORY_SCOPE_AGENT);
        FENCE();
        ST(18);
#pragma unroll
        for (int jj = 0; jj < 7; ++jj) {
            const int cj_ = (4 * jj) % 14, rj = 18 * jj + (4 * jj) / 14;
            w1o[jj] = (row0 + rj) * 3584 + (cch0 + cj_) * 16 + (cch0 + cj_ >= 14 ? 3584 - 224 : 0);
            PIN(w1o[jj]);
        }
        BARRIER();
        if (w == 6) {
            float re = bf_lo(sr_), im = bf_lo(si_);
            re += dpp<0x128>(re); im -= dpp<0x128>(im);
            u32 a = __builtin_bit_cast(u32, re), a2 = a, c = __builtin_bit_cast(u32, im), c2 = c;
            asm("" : "+v"(a2), "+v"(c2));
            auto pr = __builtin_amdgcn_permlane16_swap(a, a2, false, false);
            auto pi = __builtin_amdgcn_permlane16_swap(c, c2, false, false);
            const float ar = __builtin_bit_cast(float, (u32)pr[0]), br = __builtin_bit_cast(float, (u32)pr[1]);
            const float ai = __builtin_bit_cast(float, (u32)pi[0]), bi = __builtin_bit_cast(float, (u32)pi[1]);
            if (l < 8) {
                __builtin_amdgcn_raw_buffer_store_b32(clean(pk_b(0.25f * (ar + br), 0.25f * (ai + bi))), rw, l * 4096 + 4, WS_WA + b * 16, SC1);
                __builtin_amdgcn_raw_buffer_store_b32(clean(pk_b(0.25f * (ar - br), 0.25f * (ai - bi))), rw, l * 4096 + 8, WS_WA + b * 16, SC1);
            }
        }
        const int f = f1 + 8 * (tid & 255), k0 = 16 * cg - 8 * (cg & 1);
        const int e = mir ? ((2048 - f) & 2047) * 514 + (504 - k0) * 2 : f * 514 + k0 * 2;
        consts0();
        if (w == 7 && VPOS == 0) { WAITV(0); announce(1); }
        __builtin_amdgcn_s_sleep(GA_SLEEP);
        const u4 fa = bld4<0>(mkrs((cg & 1) ? wi : wr, 1052672), e, 0);
        FENCE();
        __builtin_amdgcn_s_sleep(GB_SLEEP);
        if (w == 4) all_announced(0);
        const u4 fb = bld4<0>(mkrs((cg & 1) ? wr : wi, 1052672), e, 0);
        xl[0] = *(const u2*)((const char*)x + xoff); xl[1] = *(const u2*)((const char*)x + xoff + 512);
        FENCE();
        ST(4);
        BARRIER();
        {
            u32 o[8];
            const u4 fr = (cg & 1) ? fb : fa, fi = (cg & 1) ? fa : fb;
#pragma unroll
            for (int c = 0; c < 8; ++c)
                o[c] = mir ? __builtin_amdgcn_perm(fi[3 - (c >> 1)], fr[3 - (c >> 1)], (c & 1) ? 0x05040100u : 0x07060302u) ^ 0x80000000u
                           : __builtin_amdgcn_perm(fi[c >> 1], fr[c >> 1], (c & 1) ? 0x07060302u : 0x05040100u);
            char* wp = scr + SCR_S + 8192 + (tid & 255) * 32;
            *(u4*)(wp + (m & 8) * 2) = (u4){o[0], o[1], o[2], o[3]};
            *(u4*)(wp + ((m & 8) * 2 ^ 16)) = (u4){o[4], o[5], o[6], o[7]};
        }
        FENCE();
        if (w == 7 && VPOS == 1) {
#pragma unroll
            for (int i = 0; i < 8; ++i) bst4<SC1>(SENT4, rw, l * 16 + (i & 3) * 1024, WS_V + b * 8192 + (i >> 2) * 4096);
            WAITV(0); announce(1);
        }
        if (LPOS == 1) {
#pragma unroll
        for (int i = 0; i < 2; ++i) bst4<SC1>(SENT4, rw, (l >> 3) * 256 + (l & 7) * 16 + i * 2048, WS_LN2 + (b >> 1) * 16384 + (b & 1) * 128 + (w - 4) * 4096);
        }
        PIN(xl[0]); PIN(xl[1]);
        consts3();
        ST(5);
        BARRIER();
        if (LPOS == 0) {
#pragma unroll
        for (int i = 0; i < 2; ++i) bst4<SC1>(SENT4, rw, (l >> 3) * 256 + (l & 7) * 16 + i * 2048, WS_LN2 + (b >> 1) * 16384 + (b & 1) * 128 + (w - 4) * 4096);
        }
        if (w == 5) all_announced(1);
        if (cg == 0) __builtin_amdgcn_s_sleep(SP_SLEEP);
        if (cg != 0 || SP_BURST) bg(0, BG5);
        if (cg != 0 || SP_BURST) WAITV(BG5); else WAITV(0);
        ST(6);
        BARRIER();
        __builtin_amdgcn_s_sleep(3);
        bg(BG5, BG5b);
        ST(7);
        __builtin_amdgcn_s_sleep(BG_DELAY);
        if (cg == 0 && !SP_BURST) bg(0, BG5);
        bg(BG5b, BG6);
    }
    BARRIER();
    {
        const u32* zp = (const u32*)(scr + SCR_X + w * 1024 + zo);
        u4 A = {zp[0], zp[16], zp[32], zp[48]};
        f4 Dre = mma_b(A, Gre3, Z4), Dim = mma_b(A, Gim3, Z4), Hre = mma_b(A, SGre, Z4), Him = mma_b(A, SGim, Z4);
        if (g == 0) { Dre[0] = Hre[0]; Dim[0] = Him[0]; }
        f2 r0 = fma2(-lo2(Dim), tws[0], lo2(Dre) * twc[0]), i0 = fma2(lo2(Dre), tws[0], lo2(Dim) * twc[0]);
        f2 r1 = fma2(-hi2(Dim), tws[1], hi2(Dre) * twc[1]), i1 = fma2(hi2(Dre), tws[1], hi2(Dim) * twc[1]);
        u4 A2 = {pk_b(r0[0], i0[0]), pk_b(r0[1], i0[1]), pk_b(r1[0], i1[0]), pk_b(r1[1], i1[1])};
        f2 h[4]; u2 hp[2];
#pragma unroll
        for (int t = 0; t < 2; ++t) {
            f4 Yv = mma_b(A2, GA[t], Z4);
            u32 h0 = pk_b2(bf_2(xl[t][0]) + bf_2(pk_b(Yv[0], Yv[1]))), h1 = pk_b2(bf_2(xl[t][1]) + bf_2(pk_b(Yv[2], Yv[3])));
            hp[t] = (u2){h0, h1};
            h[2 * t] = bf_2(h0); h[2 * t + 1] = bf_2(h1);
        }
        const float c = bf_lo(__builtin_amdgcn_readfirstlane(hp[0][0]));
        f2 q2 = {0.f, 0.f}, s2 = {0.f, 0.f};
#pragma unroll
        for (int j = 0; j < 4; ++j) { f2 d = h[j] - c; q2 = fma2(d, d, q2); s2 += d; }
        float s1 = s2[0] + s2[1], sq = q2[0] + q2[1];
        red_lanes<true>(s1, sq);
        s1 *= 1.0f / 512;
        const float inv = ln_inv(__builtin_fmaxf(sq * (1.0f / 512) - s1 * s1, 0.f));
        const float mb = rbf(c + s1);
#pragma unroll
        for (int t = 0; t < 2; ++t)
            *(u2*)(scr + ((8 * t + (m >> 1)) * 4 + g) * 128 + (w ^ (m >> 1)) * 16 + (m & 1) * 8) =
                (u2){clean(pk_b2(bf_2(pk_b2(h[2 * t] - mb)) * inv)), clean(pk_b2(bf_2(pk_b2(h[2 * t + 1] - mb)) * inv))};
        WAITV(0);
        BARRIER();
        u4 lo = *(const u4*)(scr + (tid ^ ((tid >> 5) & 7)) * 16);
        bst4<SC1>(lo, rw, (tid >> 3) * 256 + (tid & 7) * 16, WS_LN2 + (b >> 1) * 16384 + (b & 1) * 128);
        FENCE();
        if (aw && l == 0) __hip_atomic_fetch_add(hw + 6, 1u, __ATOMIC_RELAXED, __HIP_MEMORY_SCOPE_WORKGROUP);
        if (aw) WAITV(0); else __builtin_amdgcn_s_sleep(H_SLEEP);
        bst2<SC1>(hp[0], ro, xoff, 0); bst2<SC1>(hp[1], ro, xoff + 512, 0);
        FENCE();
        ST(10);
    }
    auto stage_out = [&](int r) {
        w2_read(W[r], LDS_SCR + (w & 3) * 8192 + (r & 1) * 4096);
        asm volatile("s_waitcnt lgkmcnt(0)" ::: "memory");
        PIN(W[r][0]); PIN(W[r][1]); PIN(W[r][2]); PIN(W[r][3]);
        if (r + 2 < 7) w2_glds(pw, r + 2, lds0 + LDS_SCR + (w & 3) * 8192 + (r & 1) * 4096);
    };
    auto publish = [&]() {
        u32 prev = 0;
        if (l == 0) prev = __hip_atomic_fetch_add(hw + 4, 1u, __ATOMIC_RELAXED, __HIP_MEMORY_SCOPE_WORKGROUP);
        if (__builtin_amdgcn_readfirstlane(prev) == 7u && l < 2)
            __hip_atomic_store(fl + (l ? 4096 + (b >> 1) * 32 + (b & 1) : (mt1 * 8 + (nt1 >> 1)) * 32 + (nt1 & 1)), tag, __ATOMIC_RELAXED, __HIP_MEMORY_SCOPE_AGENT);
        FENCE();
    };
    {
        const int tg = w & 3;
        char* xch = smem + XCH + tg * 3072;
        const int hoff = WS_H1 + (mt1 * 8 + 2 * tg) * (56 * 1024) + (nt1 >> 1) * 7168;
        if (__builtin_expect(aw, 1)) {
            const int toff = WS_LN2 + (mt1 * 8 + 2 * tg) * 16384;
            LAS char* wb0 = (LAS char*)smem + (4 * g + (m >> 2)) * 224 + (m & 3) * 8;
            LAS char* wb[4];
            int tq[4];
#pragma unroll
            for (int q = 0; q < 4; ++q) { const int qq = (q + rq) & 3; wb[q] = wb0 + qq * 28672; tq[q] = toff + qq * 4096; }
            u4 T[2][16], A[2][7];
            auto ldt = [&](int kk) {
#pragma unroll
                for (int tt = 0; tt < 2; ++tt) T[tt][kk] = bld4<SC1>(rw, l * 16 + (kk & 3) * 1024, tq[kk >> 2] + tt * 16384);
            };
#pragma unroll
            for (int i = 0; i < 7; ++i) A[0][i] = ldw1(wb[0], 0, i);
            if (l == 0) __hip_atomic_fetch_add(hw + 5, 1u, __ATOMIC_RELAXED, __HIP_MEMORY_SCOPE_WORKGROUP);
            ST(11);
#pragma unroll
            for (int kk = 0; kk < G1_DEPTH; ++kk) { ldt(kk); SCHED(); }
            f4 acc[2][7];
#pragma unroll
            for (int tt = 0; tt < 2; ++tt)
#pragma unroll
                for (int i = 0; i < 7; ++i) acc[tt][i] = Z4;
#pragma unroll
            for (int kk = 0; kk < 16; ++kk) {
                if ((kk & 1) == 0) {
                    if (__builtin_expect(any_sent(umax(umax(max4(T[0][kk]), max4(T[1][kk])), umax(max4(T[0][kk + 1]), max4(T[1][kk + 1])))), 0)) {
                        wait_tok(T[0][kk], T[1][kk], rw, l * 16, tq[kk >> 2] + (kk & 3) * 1024);
                        wait_tok(T[0][kk + 1], T[1][kk + 1], rw, l * 16, tq[kk >> 2] + ((kk + 1) & 3) * 1024);
                    }
                    if (kk == 0) ST(12);
                    if (kk == (BG6 / 7) * 4 - 2) lds_wait(hw + 5, 8u);
                }
                if (kk + G1_DEPTH < 16) ldt(kk + G1_DEPTH);
                if (kk < 15) {
#pragma unroll
                    for (int i = 0; i < 7; ++i) A[(kk + 1) & 1][i] = ldw1(wb[(kk + 1) >> 2], (kk + 1) & 3, i);
                }
#pragma unroll
                for (int i = 0; i < 7; ++i)
#pragma unroll
                    for (int tt = 0; tt < 2; ++tt) acc[tt][i] = mma_b(A[kk & 1][i], T[tt][kk], acc[tt][i]);
#pragma unroll
                for (int i = 0; i < 14; ++i) {
                    __builtin_amdgcn_sched_group_barrier(0x8, 1, 0);
                    if (i == 1 || i == 7) __builtin_amdgcn_sched_group_barrier(0x20, 1, 0);
                    __builtin_amdgcn_sched_group_barrier(0x100, 1, 0);
                    if ((kk & 1) && i >= 3) __builtin_amdgcn_sched_group_barrier(0x2, 1, 0);
                }
                if (kk & 1) {} else SCHED();
            }
#pragma unroll
            for (int q = 0; q < 3; ++q) {
                u2 p0 = pk4(acc[1][2 * q]), p1 = pk4(acc[1][2 * q + 1]);
                *(u4*)(xch + q * 1024 + l * 16) = (u4){p0[0], p0[1], p1[0], p1[1]};
            }
            lds_set(hw + tg, 1u);
            ST(13);
#pragma unroll
            for (int q = 0; q < 3; ++q) {
                u2 p0 = pk4(acc[0][2 * q]), p1 = pk4(acc[0][2 * q + 1]);
                bst4<SC1>(silu_pk4((u4){p0[0], p0[1], p1[0], p1[1]}), rw, l * 16, hoff + ((nt1 & 1) * 3 + q) * 1024);
            }
#pragma unroll
            for (int tt = 0; tt < 2; ++tt) { u2 p = pk4(acc[tt][6]); bst2<SC1>(silu_pk2(p[0], p[1]), rw, l * 8, hoff + tt * (56 * 1024) + 6144 + (nt1 & 1) * 512); }
        } else {
            bg(BG6, 28);
            if (l == 0) __hip_atomic_fetch_add(hw + 6, 1u, __ATOMIC_RELAXED, __HIP_MEMORY_SCOPE_WORKGROUP);
            lds_wait(hw + 6, 8u);
            w2_glds(pw, 0, lds0 + LDS_SCR + tg * 8192);
            w2_glds(pw, 1, lds0 + LDS_SCR + tg * 8192 + 4096);
            WAITV(8);
            if (l == 0) __hip_atomic_fetch_add(hw + 5, 1u, __ATOMIC_RELAXED, __HIP_MEMORY_SCOPE_WORKGROUP);
#pragma unroll
            for (int r = 0; r < B_R; ++r) { if (r < 6) WAITV(4); else WAITV(0); stage_out(r); }
            ST(12);
            lds_wait(hw + tg, 1u);
            ST(13);
#pragma unroll
            for (int q = 0; q < 3; ++q)
                bst4<SC1>(silu_pk4(*(const u4*)(xch + q * 1024 + l * 16)), rw, l * 16, hoff + 56 * 1024 + ((nt1 & 1) * 3 + q) * 1024);
        }
        ST(14);
        WAITV(0);
        publish();
        ST(21);
        if (__builtin_expect(aw, 1)) {
            lds_wait(hw + 4, 8u);
#pragma unroll
            for (int ks = 0; ks < 7; ++ks) w2_glds(pw, ks, lds0 + tg * 28672 + ks * 4096);
        } else {
            if (B_R < 7) stage_out(B_R);
#pragma unroll
            for (int r = B_R + 1; r < 7; ++r) { if (r + 1 < 7) WAITV(4); else WAITV(0); stage_out(r); }
        }
    }
    {
        ST(15);
        const int rot = w & 3;
        if (aw) {
            flag_wait(fl + ((l & 4) ? 4096 + (grp * 8 + th * 4 + rot) * 32 : (grp * 8 + (pw ^ ((l & 2) * 2))) * 32) + (l & 1), tag);
            lds_set(hw + 8 + rot, tag);
        } else {
#pragma nounroll
            for (int sp = 0; sp < SPIN_MAX; ++sp) {
                if (__builtin_amdgcn_readfirstlane(__hip_atomic_load(hw + 8 + rot, __ATOMIC_RELAXED, __HIP_MEMORY_SCOPE_WORKGROUP)) == tag) break;
                __builtin_amdgcn_s_sleep(1);
            }
            FENCE();
        }
        ST(16);
        const int toff = WS_H1 + (grp * 8 + th * 4) * (56 * 1024) + pw * 7168;
        int to[4];
#pragma unroll
        for (int tt = 0; tt < 4; ++tt) to[tt] = toff + ((tt + rot) & 3) * (56 * 1024);
        u4 T[7][4];
        auto ldtok = [&](int ks) {
#pragma unroll
            for (int tt = 0; tt < 4; ++tt) {
                if (ks < 6) T[ks][tt] = bld4<SC1>(rw, l * 16, to[tt] + ko[ks] * 1024);
                else {
                    u2 a = bld2<SC1>(rw, l * 8 + 2048, to[tt] + 4096), c = bld2<SC1>(rw, l * 8 + 2560, to[tt] + 4096);
                    T[ks][tt] = (u4){a[0], a[1], c[0], c[1]};
                }
            }
            SCHED();
        };
#pragma unroll
        for (int ks = 0; ks < G2_DEPTH; ++ks) ldtok(ks);
        if (aw) {
#pragma unroll
            for (int ks = 0; ks < 7; ++ks) w2_read(W[ks], rot * 28672 + ks * 4096);
            asm volatile("s_waitcnt lgkmcnt(0)" ::: "memory");
        }
        if (l == 0) __hip_atomic_fetch_add(hw + 7, 1u, __ATOMIC_RELAXED, __HIP_MEMORY_SCOPE_WORKGROUP);
        const int rho = grp * 128 + th * 64 + rot * 16 + m;
        const int ooff = (256 * (rho & 7) + (rho >> 3)) * 1024 + fb * 128 + (w >> 2) * 64 + g * 16;
        const u4 hres = bld4<SC1>(ro, ooff, 0);
        SCHED();
        f4 acc[4][4];
#pragma unroll
        for (int tt = 0; tt < 4; ++tt)
#pragma unroll
            for (int t = 0; t < 4; ++t) acc[tt][t] = Z4;
#pragma unroll
        for (int ks = 0; ks < 7; ++ks) {
            if (ks + G2_DEPTH < 7) { PIN(T[ks][0]); ldtok(ks + G2_DEPTH); }
#pragma unroll
            for (int tt = 0; tt < 4; ++tt)
#pragma unroll
                for (int t = 0; t < 4; ++t) {
                    acc[tt][t] = mma_b(W[ks][t], T[ks][tt], acc[tt][t]);
                }
            SCHED();
        }
        ST(17);
        lds_wait(hw + 7, 8u);
        char* dp = smem + w * 16384 + l * 16;
#pragma unroll
        for (int tt = 1; tt < 4; ++tt)
#pragma unroll
            for (int t = 0; t < 4; ++t) *(f4*)(dp + (((tt + rot) & 3) * 4 + t) * 1024) = acc[tt][t];
        f4 s0, s1;
        if (aw) { *(f4*)(dp + (rot * 4 + 2) * 1024) = acc[0][2]; *(f4*)(dp + (rot * 4 + 3) * 1024) = acc[0][3]; s0 = acc[0][0]; s1 = acc[0][1]; }
        else { *(f4*)(dp + (rot * 4) * 1024) = acc[0][0]; *(f4*)(dp + (rot * 4 + 1) * 1024) = acc[0][1]; s0 = acc[0][2]; s1 = acc[0][3]; }
        BARRIER();
        f4 r0[7], r1[7];
#pragma unroll
        for (int j = 1; j < 8; ++j) {
            const char* rp = smem + (((w + j) & 7) * 16 + rot * 4 + (w >> 2) * 2) * 1024 + l * 16;
            r0[j - 1] = *(const f4*)rp; r1[j - 1] = *(const f4*)(rp + 1024);
        }
        SCHED();
        s0 = ((s0 + r0[0]) + (r0[1] + r0[2])) + ((r0[3] + r0[4]) + (r0[5] + r0[6]));
        s1 = ((s1 + r1[0]) + (r1[1] + r1[2])) + ((r1[3] + r1[4]) + (r1[5] + r1[6]));
        u2 p0 = pk4(s0), p1 = pk4(s1);
        bst4<NT>((u4){pk_b2(bf_2(hres[0]) + bf_2(p0[0])), pk_b2(bf_2(hres[1]) + bf_2(p0[1])),
                           pk_b2(bf_2(hres[2]) + bf_2(p1[0])), pk_b2(bf_2(hres[3]) + bf_2(p1[1]))}, ro, ooff, 0);
    }
    ST_END(ws, 0);
}

extern "C" void kernel_launch(void* const* d_in, const int* in_sizes, int n_in, void* d_out, int out_size,
                              void* d_ws, size_t ws_size, hipStream_t stream) {
    (void)hipFuncSetAttribute((const void*)k_fused, hipFuncAttributeMaxDynamicSharedMemorySize, LDS_B);
    k_fused<<<256, 512, LDS_B, stream>>>((const uint16_t*)d_in[0], (const uint16_t*)d_in[1], (const uint16_t*)d_in[2],
                                         (const uint16_t*)d_in[4], (const uint16_t*)d_in[5], (uint16_t*)d_out, (char*)d_ws);
}
```

```cpp
#include <hip/hip_runtime.h>
#include <hip/hip_bf16.h>
#include <stdint.h>

typedef _Float16 h8 __attribute__((ext_vector_type(8)));
typedef _Float16 h4 __attribute__((ext_vector_type(4)));
typedef _Float16 h2 __attribute__((ext_vector_type(2)));
typedef __bf16 b8 __attribute__((ext_vector_type(8)));
typedef __bf16 b2 __attribute__((ext_vector_type(2)));
typedef short s4 __attribute__((ext_vector_type(4)));
typedef float f4 __attribute__((ext_vector_type(4)));
typedef float f2 __attribute__((ext_vector_type(2)));
typedef unsigned u32;
typedef u32 u4 __attribute__((ext_vector_type(4)));
typedef u32 u2 __attribute__((ext_vector_type(2)));
#define LAS __attribute__((address_space(3)))
#define GAS __attribute__((address_space(1)))
#define DI __device__ __forceinline__
#define FENCE() asm volatile("" ::: "memory")
#define PIN(x) asm volatile("" : "+v"(x))
#define SCHED() __builtin_amdgcn_sched_barrier(0)
#define WAITV(n) asm volatile("s_waitcnt vmcnt(%0)" :: "n"(n) : "memory")
#define BARRIER() do { asm volatile("s_waitcnt lgkmcnt(0)" ::: "memory"); __builtin_amdgcn_s_barrier(); FENCE(); } while (0)
constexpr int SC1 = 16, NT = 2;
constexpr u32 SENT = 0xffffffffu;
constexpr int SPIN_MAX = 1 << 20;

DI u32 pk_h(float a, float b) { f2 v = {a, b}; h2 r = __builtin_convertvector(v, h2); return __builtin_bit_cast(u32, r); }
DI u32 pk_b(float a, float b) { f2 v = {a, b}; b2 r = __builtin_convertvector(v, b2); return __builtin_bit_cast(u32, r); }
DI float bf_lo(u32 v) { return __builtin_bit_cast(float, v << 16); }
DI float bf_hi(u32 v) { return __builtin_bit_cast(float, v & 0xffff0000u); }
DI f2 bf_2(u32 v) { return (f2){bf_lo(v), bf_hi(v)}; }
DI u32 pk_b2(f2 v) { return pk_b(v[0], v[1]); }
DI float rbf(float a) { return bf_lo(pk_b(a, a)); }
DI float cosr(float rev) { return __builtin_amdgcn_cosf(rev); }
DI float sinr(float rev) { return __builtin_amdgcn_sinf(rev); }
DI f2 lo2(f4 v) { return __builtin_shufflevector(v, v, 0, 1); }
DI f2 hi2(f4 v) { return __builtin_shufflevector(v, v, 2, 3); }
DI f2 fma2(f2 a, f2 b, f2 c) { return __builtin_elementwise_fma(a, b, c); }
DI u32 umin(u32 a, u32 b) { return a < b ? a : b; }
DI u32 umax(u32 a, u32 b) { return a > b ? a : b; }
DI u32 clean(u32 v) { return umin(v, SENT - 1); }
DI u32 max4(u4 v) { return umax(umax(umax(v[0], v[1]), v[2]), v[3]); }
#define Z4 ((f4){0, 0, 0, 0})
DI f4 mma_h(u4 a, u4 b, f4 c) { return __builtin_amdgcn_mfma_f32_16x16x32_f16(__builtin_bit_cast(h8, a), __builtin_bit_cast(h8, b), c, 0, 0, 0); }
DI f4 mma_b(u4 a, u4 b, f4 c) { return __builtin_amdgcn_mfma_f32_16x16x32_bf16(__builtin_bit_cast(b8, a), __builtin_bit_cast(b8, b), c, 0, 0, 0); }
DI f4 mma16_b(u2 a, u2 b, f4 c) { return __builtin_amdgcn_mfma_f32_16x16x16bf16_1k(__builtin_bit_cast(s4, a), __builtin_bit_cast(s4, b), c, 0, 0, 0); }
DI f4 mma16_h(u2 a, u2 b, f4 c) { return __builtin_amdgcn_mfma_f32_16x16x16f16(__builtin_bit_cast(h4, a), __builtin_bit_cast(h4, b), c, 0, 0, 0); }
typedef __amdgpu_buffer_rsrc_t rsrc_t;
DI rsrc_t mkrs(const void* p, u32 bytes) { return __builtin_amdgcn_make_buffer_rsrc((void*)p, 0, bytes, 0x00020000); }
template <int AUX> DI u4 bld4(rsrc_t r, int v, int s) { return __builtin_amdgcn_raw_buffer_load_b128(r, v, s, AUX); }
template <int AUX> DI u2 bld2(rsrc_t r, int v, int s) { return __builtin_amdgcn_raw_buffer_load_b64(r, v, s, AUX); }
template <int AUX> DI void bst4(u4 d, rsrc_t r, int v, int s) {
    __builtin_amdgcn_raw_buffer_store_b128(d, r, v, s, AUX);
    asm volatile("s_nop 1" :: "v"(d));
}
template <int AUX> DI void bst2(u2 d, rsrc_t r, int v, int s) { __builtin_amdgcn_raw_buffer_store_b64(d, r, v, s, AUX); }

template <int CTRL> DI float dpp(float v) {
    return __builtin_bit_cast(float, __builtin_amdgcn_update_dpp(0, __builtin_bit_cast(int, v), CTRL, 0xf, 0xf, true));
}
DI float swap_add16(float v) {
    u32 x = __builtin_bit_cast(u32, v), y = x;
    asm("" : "+v"(y));
    auto r = __builtin_amdgcn_permlane16_swap(x, y, false, false);
    return __builtin_bit_cast(float, (u32)r[0]) + __builtin_bit_cast(float, (u32)r[1]);
}
DI float swap_add32(float v) {
    u32 x = __builtin_bit_cast(u32, v), y = x;
    asm("" : "+v"(y));
    auto r = __builtin_amdgcn_permlane32_swap(x, y, false, false);
    return __builtin_bit_cast(float, (u32)r[0]) + __builtin_bit_cast(float, (u32)r[1]);
}
template <bool ALL> DI void red_lanes(float& a, float& b) {
    a += dpp<0xB1>(a); b += dpp<0xB1>(b);
    if (ALL) { a += dpp<0x4E>(a); b += dpp<0x4E>(b); }
    a += dpp<0x124>(a); b += dpp<0x124>(b);
    a += dpp<0x128>(a); b += dpp<0x128>(b);
    a = swap_add16(a); b = swap_add16(b);
    a = swap_add32(a); b = swap_add32(b);
}
DI float ln_inv(float var) { return __builtin_amdgcn_rcpf(rbf(__builtin_amdgcn_sqrtf(rbf(rbf(var) + 1e-5f)))); }

#define ST_DECL
#define ST(i)
#define ST_END(ws, kid)

#define FS 1
constexpr u32 WS_U = 0;
constexpr u32 WS_V = WS_U + (2u << 20);
constexpr u32 WS_LN2 = WS_V + (2u << 20);
constexpr u32 WS_WA = WS_LN2 + (2u << 20);
constexpr u32 WS_FA = WS_WA + 32768u;
constexpr u32 WS_FLAG = WS_FA + 16384u * FS;
constexpr u32 WS_EP = WS_FLAG + 32768u;
constexpr u32 WS_H1 = WS_WA + (512u << 10);
constexpr u32 WS_END = WS_H1 + 7340032u;

#define BGA 10
#define BGB 10
#define BGC 21
#define G_SLEEP 16
#define GA_SLEEP 4
#define GB_SLEEP 8
#define A_SLEEP 0
#define VPOS 0
#define LPOS 1
#define SP_SLEEP 6
#define SP_BURST 1
#define EARLY_STAGE 1
#define STG_SLEEP 8
#define H_SLEEP 2
#define BRR 5
#define TLBN 12
#define BG_DELAY 14
#define PRO_SLEEP 4
constexpr int BG5 = BGA, BG5b = BGB, BG6 = BGC;
constexpr int G1_DEPTH = 10, G2_DEPTH = 3;
constexpr int B_R = BRR;
constexpr int CODE_LINES = 290, CODE_PF = 10;
constexpr int LDS_SCR = 114688;
constexpr int SCR_X = 8448;
constexpr int SCR_S = SCR_X + 20480;
constexpr int SCR_F = 49024;
constexpr int XCH = 147456;
constexpr int LDS_B = 163840;

DI void glds16(const void* sbase, u32 voff, u32 lds_dst) {
    asm volatile("s_mov_b32 m0, %2\n\ts_nop 0\n\tglobal_load_lds_dwordx4 %0, %1" :: "v"(voff), "s"(sbase), "s"(lds_dst) : "memory");
}
DI bool any_sent(u32 v) { return __builtin_amdgcn_ballot_w64(v == SENT) != 0; }
DI void poll2(u4& a, u4& c, rsrc_t rw, int vo, int sa, int sc) {
#pragma nounroll
    for (int sp = 0; sp < SPIN_MAX; ++sp) {
        const bool ba = max4(a) == SENT, bc = max4(c) == SENT;
        if (__builtin_amdgcn_ballot_w64(ba || bc) == 0) break;
        __builtin_amdgcn_s_sleep(1);
        FENCE();
        if (ba) a = bld4<SC1>(rw, vo, sa);
        if (bc) c = bld4<SC1>(rw, vo, sc);
    }
}
DI void wait_tok(u4& t0, u4& t1, rsrc_t rw, int vo, int so) {
    if (any_sent(umax(max4(t0), max4(t1)))) {
#pragma nounroll
        for (int sp = 0; sp < SPIN_MAX; ++sp) {
            FENCE();
            t0 = bld4<SC1>(rw, vo, so); t1 = bld4<SC1>(rw, vo, so + 16384);
            if (!any_sent(umax(max4(t0), max4(t1)))) break;
        }
    }
}
DI u4 ldw1(LAS char* wb, int kk, int ft) {
    s4 a0 = __builtin_amdgcn_ds_read_tr16_b64_v4i16((LAS s4*)(wb + kk * 32 * 224 + ft * 32));
    s4 a1 = __builtin_amdgcn_ds_read_tr16_b64_v4i16((LAS s4*)(wb + (kk * 32 + 16) * 224 + ft * 32));
    u2 x0 = __builtin_bit_cast(u2, a0), x1 = __builtin_bit_cast(u2, a1);
    return (u4){x0[0], x0[1], x1[0], x1[1]};
}
DI u2 pk4(f4 a) { return (u2){pk_b(a[0], a[1]), pk_b(a[2], a[3])}; }
DI u2 silu_pk2(u32 p0, u32 p1) {
    u32 o0, o1; float a0, a1, a2, a3, t0, t1, t2, t3;
    asm("v_lshlrev_b32 %2, 16, %10\n\tv_and_b32 %3, 0xffff0000, %10\n\tv_lshlrev_b32 %4, 16, %11\n\tv_and_b32 %5, 0xffff0000, %11\n\t"
        "v_mul_f32 %6, %12, %2\n\tv_mul_f32 %7, %12, %3\n\tv_mul_f32 %8, %12, %4\n\tv_mul_f32 %9, %12, %5\n\t"
        "v_exp_f32 %6, %6\n\tv_exp_f32 %7, %7\n\tv_exp_f32 %8, %8\n\tv_exp_f32 %9, %9\n\t"
        "v_add_f32 %6, 1.0, %6\n\tv_add_f32 %7, 1.0, %7\n\tv_add_f32 %8, 1.0, %8\n\tv_add_f32 %9, 1.0, %9\n\t"
        "v_rcp_f32 %6, %6\n\tv_rcp_f32 %7, %7\n\tv_rcp_f32 %8, %8\n\tv_rcp_f32 %9, %9\n\t"
        "v_mul_f32 %2, %2, %6\n\tv_mul_f32 %3, %3, %7\n\tv_mul_f32 %4, %4, %8\n\tv_mul_f32 %5, %5, %9\n\t"
        "v_cvt_pk_bf16_f32 %0, %2, %3\n\tv_cvt_pk_bf16_f32 %1, %4, %5"
        : "=&v"(o0), "=&v"(o1), "=&v"(a0), "=&v"(a1), "=&v"(a2), "=&v"(a3), "=&v"(t0), "=&v"(t1), "=&v"(t2), "=&v"(t3)
        : "v"(p0), "v"(p1), "s"(-1.4426950408889634f));
    return (u2){o0, o1};
}
DI u4 silu_pk4(u4 p) { u2 a = silu_pk2(p[0], p[1]), b = silu_pk2(p[2], p[3]); return (u4){a[0], a[1], b[0], b[1]}; }
DI void lds_set(LAS u32* p, u32 v) {
    asm volatile("s_waitcnt lgkmcnt(0)" ::: "memory");
    __hip_atomic_store(p, v, __ATOMIC_RELAXED, __HIP_MEMORY_SCOPE_WORKGROUP);
}
DI void lds_wait(LAS u32* p, u32 v) {
#pragma nounroll
    for (int sp = 0; sp < SPIN_MAX; ++sp) {
        if (__builtin_amdgcn_readfirstlane(__hip_atomic_load(p, __ATOMIC_RELAXED, __HIP_MEMORY_SCOPE_WORKGROUP)) >= v) break;
        __builtin_amdgcn_s_sleep(1);
    }
    FENCE();
}
DI void flag_wait(GAS u32* p, u32 tag) {
#pragma nounroll
    for (int sp = 0; sp < SPIN_MAX; ++sp) {
        if (__builtin_amdgcn_ballot_w64(__hip_atomic_load(p, __ATOMIC_RELAXED, __HIP_MEMORY_SCOPE_AGENT) != tag) == 0) break;
        __builtin_amdgcn_s_sleep(2);
    }
    FENCE();
}
#define SENT4 ((u4){SENT, SENT, SENT, SENT})

__global__ __launch_bounds__(512, 2) void k_fused(const uint16_t* x, const uint16_t* wr, const uint16_t* wi, const uint16_t* w1,
                                                  const uint16_t* w2, uint16_t* out, char* ws) {
    extern __shared__ __attribute__((aligned(16))) char smem[];
    const int tid = threadIdx.x, l = tid & 63, g = l >> 4, m = l & 15;
    const int w = __builtin_amdgcn_readfirstlane(tid >> 6);
    const int b = (blockIdx.x & 7) * 32 + (blockIdx.x >> 3);
    const int mt1 = (b >> 6) * 4 + ((b >> 3) & 3), nt1 = ((b >> 5) & 1) * 8 + (b & 7);
    const int grp = b >> 4, th = (b >> 3) & 1, fb = b & 7;
    const rsrc_t rw = mkrs(ws, WS_END), ro = mkrs(out, 2097152);
    char* scr = smem + LDS_SCR;
    LAS u32* hw = (LAS u32*)((LAS char*)smem + LDS_SCR + SCR_F);
    const u32 lds0 = (u32)(uintptr_t)(LAS char*)smem;
    const float mf = (float)m, gf = (float)g, sg = (m & 1) ? -1.0f : 1.0f;
    const bool aw = w < 4;
    ST_DECL;
    unsigned long long pc0;
    asm volatile("s_getpc_b64 %0" : "=s"(pc0));
    if (tid >= 448 && l < CODE_PF)
        asm volatile("s_mov_b32 m0, %2\n\ts_nop 0\n\tglobal_load_lds_dword %0, %1"
                     :: "v"(umin((blockIdx.x >> 3) * CODE_PF + l, CODE_LINES - 1) * 128), "s"(pc0), "s"(lds0 + LDS_SCR + SCR_F + 64) : "memory");
    const u32 epoch = __hip_atomic_load((GAS u32*)(ws + WS_EP), __ATOMIC_RELAXED, __HIP_MEMORY_SCOPE_AGENT);
    FENCE();
    u32 xd[8];
    const int f1 = b >> 5, cg = b & 31;
    const bool mir = cg >= 16;
    const rsrc_t rr = mkrs(wr, 1052672), ri = mkrs(wi, 1052672);
    u32 sr_, si_;
    if (aw) {
        __builtin_amdgcn_s_sleep(A_SLEEP);
        const u32* xr = (const u32*)(x + (256 * (2 * w + ((m >> 1) & 1)) + b) * 512) + 8 * g + 2 * (m >> 2) + (m & 1);
#pragma unroll
        for (int j = 0; j < 8; ++j) xd[j] = xr[32 * j];
    } else {
        bst4<SC1>(SENT4, rw, l * 16, WS_U + b * 8192 + (w - 4) * 2048); bst4<SC1>(SENT4, rw, l * 16 + 1024, WS_U + b * 8192 + (w - 4) * 2048);
        bst4<SC1>(SENT4, rw, l < 2 ? l * 4096 : 0x7ffffff0, WS_WA + (w - 4) * 8192 + b * 16);
        FENCE();
        if (w == 7 && VPOS == 0) {
#pragma unroll
            for (int i = 0; i < 8; ++i) bst4<SC1>(SENT4, rw, l * 16 + (i & 3) * 1024, WS_V + b * 8192 + (i >> 2) * 4096);
        }
        if (w == 5) {
            const char* p = l < 6 ? ws + (size_t)umin(WS_LN2 + l * (2u << 20) + (b & 31) * 128, WS_END - 4)
                          : (l < 8 ? (const char*)w1 : l < 10 ? (const char*)w2 : (const char*)out)
                            + ((l & 1) ? (l < 10 ? 1835008 - 4096 : 2097152 - 4096) : 0) + (b & 31) * 128;
            if (l < TLBN) asm volatile("s_mov_b32 m0, %1\n\ts_nop 0\n\tglobal_load_lds_dword %0, off" :: "v"(p), "s"(lds0 + LDS_SCR + 48896) : "memory");
        }
        if (w == 6) {
            const int c = (l >> 3) & 3, f_ = 8 * b + (l & 7);
            const int e1 = ((c & 1) ? (2048 - f_) & 2047 : f_) * 514 + (c >> 1) * 512;
            sr_ = __builtin_amdgcn_raw_buffer_load_b16(rr, e1, 0, 0); si_ = __builtin_amdgcn_raw_buffer_load_b16(ri, e1, 0, 0);
        }
    }
    FENCE();
    const int zo = (64 * g + (g < 2 ? m : (m + 15) & 15)) * 4;
    const int rq = ((b >> 3) + b) & 3;
    const int row0 = (tid & 255) / 14, cch0 = (tid & 255) - row0 * 14;
    const char* wsrc = (const char*)w1 + nt1 * 224;
    u32 w1o[7];
    auto w1_burst = [&](int i0, int n) {
#pragma unroll
        for (int i = i0; i < i0 + n; ++i) {
            const int q = (i / 7 + rq) & 3, j = i % 7;
            glds16(wsrc + q * (128 * 3584), w1o[j], lds0 + (w - 4) * 1024 + q * 28672 + j * 4096);
        }
    };
    const int xoff = (256 * w + b) * 1024 + m * 32 + g * 8;
    u2 xl[2];
    float c16[4], s16[4];
    f2 twc[2], tws[2];
    u4 Gre3, Gim3, SGre, SGim, GA[2];
    auto consts0 = [&]() {
#pragma unroll
        for (int u = 0; u < 4; ++u) {
            float a = (4.0f * gf + u) * (mf * (1.0f / 16));
            c16[u] = cosr(a); s16[u] = sinr(a);
            twc[u >> 1][u & 1] = cosr(a * (1.0f / 32)); tws[u >> 1][u & 1] = sinr(a * (1.0f / 32));
        }
    };
    auto consts3 = [&]() {
#pragma unroll
        for (int u = 0; u < 4; ++u) {
            const float k = 4.0f * gf + u;
            const bool k0 = u == 0 && g == 0;
            float cc = c16[u], ss = s16[u];
            Gre3[u] = pk_b(cc, -ss); Gim3[u] = pk_b(ss, cc);
            float a2 = (2.0f * k + 1.0f) * (mf * (1.0f / 32));
            u32 sp = pk_b(g < 2 ? 2.f * cc : 2.f * cosr(a2), g < 2 ? -2.f * ss : -2.f * sinr(a2));
            SGre[u] = k0 ? pk_b(1.0f, sg) : (g < 2 ? sp : 0u);
            SGim[u] = g < 2 ? 0u : sp;
            float a4 = k * (mf * (1.0f / 32)), c4 = 2.f * cosr(a4), s4_ = -2.f * sinr(a4);
            GA[0][u] = k0 ? pk_b(1.0f, sg) : pk_b(c4, s4_);
            GA[1][u] = k0 ? pk_b(1.0f, sg) : ((u & 1) ? pk_b(-c4, -s4_) : pk_b(c4, s4_));
        }
        PIN(Gre3); PIN(Gim3); PIN(SGre); PIN(SGim); PIN(GA[0]); PIN(GA[1]); PIN(twc[0]); PIN(twc[1]); PIN(tws[0]); PIN(tws[1]);
    };
    const int pw = (w + 4 * th) & 7;
    GAS u32* fl = (GAS u32*)(ws + WS_FLAG);
    int ko[7];
    {
        const int r6 = (fb < 6 ? fb : fb - 5) + 3 * (grp & 1);
#pragma unroll
        for (int ks = 0; ks < 6; ++ks) { int k = ks + r6; k = k >= 6 ? k - 6 : k; ko[ks] = k >= 6 ? k - 6 : k; }
        ko[6] = 6;
    }
    u4 W[7][4];
    const u32 w2vo = (l >> 3) * 1024 + (((l & 7) ^ (((l >> 4) & 1) << 2)) * 16);
    auto w2_glds = [&](int p_, int ks, u32 dst) {
        const int q = ko[ks];
        const char* src = (const char*)w2 + (224 * p_ + (ks == 6 ? 96 : (q < 3 ? 32 * q : 16 + 32 * q))) * 1024 + fb * 128;
        glds16(src, w2vo, dst); glds16(src + 8192, w2vo, dst + 1024);
        glds16(src + (ks == 6 ? 112 : 16) * 1024, w2vo, dst + 2048); glds16(src + (ks == 6 ? 120 : 24) * 1024, w2vo, dst + 3072);
    };
    LAS char* w2rd = (LAS char*)smem + (4 * g + (m >> 2)) * 128 + (((m & 3) ^ (((m >> 3) & 1) << 2)) * 16);
    auto w2_read = [&](u4 (&f)[4], int off) {
#pragma unroll
        for (int t = 0; t < 4; ++t) {
            LAS char* p = (LAS char*)((u32)(uintptr_t)w2rd ^ ((t >> 1) * 64)) + off + (t & 1) * 8;
            u2 x0 = __builtin_bit_cast(u2, __builtin_amdgcn_ds_read_tr16_b64_v4i16((LAS s4*)p));
            u2 x1 = __builtin_bit_cast(u2, __builtin_amdgcn_ds_read_tr16_b64_v4i16((LAS s4*)(p + 2048)));
            f[t] = (u4){x0[0], x0[1], x1[0], x1[1]};
        }
    };
    auto bg = [&](int i0, int i1) { w1_burst(i0, i1 - i0); };
    u32 tag;
    if (__builtin_expect(aw, 1)) {
        if (tid < 8) hw[tid] = 0;
        FENCE();
        ST(2);
        {
            u4 FAre, FAim, Fre, Fim, Sre, Sim; u2 Mb; f2 pc[2], ps[2];
            consts0();
            float c0[8], s0[8];
#pragma unroll
            for (int j = 0; j < 8; ++j) {
                float a = (gf + 4.0f * j) * (mf * (1.0f / 32));
                c0[j] = cosr(a); s0[j] = m ? -sinr(a) : ((g & 1) ? -1.0f : 1.0f);
            }
            const float mo = (2.0f * mf + 1.0f) * (1.0f / 32);
#pragma unroll
            for (int u = 0; u < 4; ++u) {
                FAre[u] = pk_h(c0[2 * u], c0[2 * u + 1]); FAim[u] = pk_h(s0[2 * u], s0[2 * u + 1]);
                float cc = c16[u], ss = s16[u];
                Fre[u] = pk_h(cc, ss); Fim[u] = pk_h(-ss, cc);
                float a2 = (4.0f * gf + u) * mo;
                u32 re = pk_h(m < 8 ? cc : cosr(a2), 0.f), im = pk_h(m < 8 ? (m ? -ss : ((u & 1) ? -1.0f : 1.0f)) : -sinr(a2), 0.f);
                Sre[u] = m < 8 ? re : re << 16; Sim[u] = m < 8 ? im : im << 16;
                pc[u & 1][u >> 1] = twc[u >> 1][u & 1]; ps[u & 1][u >> 1] = tws[u >> 1][u & 1];
            }
#pragma unroll
            for (int e = 0; e < 2; ++e) {
                float a = (float)(((m >> 1) * (256 * (2 * g + e) + b)) & 2047) * (1.0f / 2048);
                float cc = cosr(a), ss = sinr(a);
                Mb[e] = (m & 1) ? pk_h(-ss, cc) : pk_h(cc, ss);
            }
            PIN(FAre); PIN(FAim); PIN(Fre); PIN(Fim); PIN(Sre); PIN(Sim); PIN(Mb); PIN(pc[0]); PIN(pc[1]); PIN(ps[0]); PIN(ps[1]);
            ST(3);
            f2 v[8];
#pragma unroll
            for (int j = 0; j < 8; ++j) v[j] = bf_2(xd[j]);
            const u32 x0 = __builtin_amdgcn_readlane(xd[0], 0), x2 = __builtin_amdgcn_readlane(xd[0], 2);
            const float c = bf_lo((m & 2) ? x2 : x0);
            f2 q0 = {0.f, 0.f}, q1 = {0.f, 0.f};
#pragma unroll
            for (int j = 0; j < 8; ++j) { v[j] -= c; if (j & 1) q1 = fma2(v[j], v[j], q1); else q0 = fma2(v[j], v[j], q0); }
            f2 s2 = ((v[0] + v[1]) + (v[2] + v[3])) + ((v[4] + v[5]) + (v[6] + v[7]));
            q0 += q1;
            float s1 = s2[0] + s2[1], sq = q0[0] + q0[1];
            red_lanes<false>(s1, sq);
            s1 *= 1.0f / 512;
            const float inv = ln_inv(__builtin_fmaxf(sq * (1.0f / 512) - s1 * s1, 0.f));
            const float off = (c - rbf(c + s1)) * inv;
#pragma unroll
            for (int j = 0; j < 8; ++j) v[j] = fma2(v[j], (f2){inv, inv}, (f2){off, off});
            u4 B[2];
#pragma unroll
            for (int p = 0; p < 2; ++p) {
                u4 A = {pk_h(v[0][p], v[1][p]), pk_h(v[2][p], v[3][p]), pk_h(v[4][p], v[5][p]), pk_h(v[6][p], v[7][p])};
                f4 Dre = mma_h(A, FAre, Z4), Dim = mma_h(A, FAim, Z4);
#pragma unroll
                for (int r = 0; r < 2; ++r) {
                    f2 re = r ? hi2(Dre) : lo2(Dre), im = r ? hi2(Dim) : lo2(Dim);
                    f2 tr = fma2(im, ps[p], re * pc[p]), ti = fma2(-re, ps[p], im * pc[p]);
                    B[r][p] = pk_h(tr[0], ti[0]); B[r][2 + p] = pk_h(tr[1], ti[1]);
                }
            }
#pragma unroll
            for (int r = 0; r < 2; ++r) {
                f4 Gre = mma_h(Fre, B[r], Z4), Gim = mma_h(Fim, B[r], Z4), Hre = mma_h(Sre, B[r], Z4), Him = mma_h(Sim, B[r], Z4);
#pragma unroll
                for (int i = 0; i < 4; ++i) *(u32*)(scr + (2 * w + r) * 1024 + zo + i * 64) = pk_h(m ? Gre[i] : Hre[i], m ? Gim[i] : Him[i]);
            }
            BARRIER();
            ST(18);
            u4 r0 = *(const u4*)(scr + (2 * g) * 1024 + w * 256 + m * 16);
            u4 r1 = *(const u4*)(scr + (2 * g + 1) * 1024 + w * 256 + m * 16);
            u4 o0, o1;
#pragma unroll
            for (int t = 0; t < 4; ++t) {
                f4 D = mma16_h(Mb, (u2){r0[t], r1[t]}, Z4);
                o0[t] = clean(pk_h(D[0], D[1])); o1[t] = clean(pk_h(D[2], D[3]));
            }
            bst4<SC1>(o0, rw, g * 2048 + m * 16, WS_U + b * 8192 + w * 256);
            bst4<SC1>(o1, rw, g * 2048 + m * 16 + 1024, WS_U + b * 8192 + w * 256);
            if (w == 0 && m == 0) {
                __builtin_amdgcn_raw_buffer_store_b32(o0[0], rw, ((8 - 2 * g) & 7) * 4096, WS_WA + b * 16, SC1);
                __builtin_amdgcn_raw_buffer_store_b32(o1[0], rw, (7 - 2 * g) * 4096, WS_WA + b * 16, SC1);
            }
        }
        ST(4);
        u4 Fre, Fim, Gre, Gim; u2 Nb; f2 tc[2], ts[2];
#pragma unroll
        for (int u = 0; u < 4; ++u) {
            float cc = c16[u], ss = s16[u];
            Fre[u] = pk_h(0.25f * cc, 0.25f * ss); Fim[u] = pk_h(-0.25f * ss, 0.25f * cc);
            Gre[u] = pk_b(cc * (1.0f / 256), -ss * (1.0f / 256)); Gim[u] = pk_b(ss * (1.0f / 256), cc * (1.0f / 256));
            float a3 = (4.0f * gf + u) * (mf * (1.0f / 256));
            tc[u >> 1][u & 1] = cosr(a3); ts[u >> 1][u & 1] = sinr(a3);
        }
        PIN(Fre); PIN(Fim); PIN(Gre); PIN(Gim); PIN(tc[0]); PIN(tc[1]); PIN(ts[0]); PIN(ts[1]);
        xl[0] = *(const u2*)((const char*)x + xoff); xl[1] = *(const u2*)((const char*)x + xoff + 512);
        FENCE();
        tag = __builtin_amdgcn_readfirstlane(epoch) + 1u;
        consts3();
#pragma unroll
        for (int e = 0; e < 2; ++e) {
            float a1 = (float)(((2 * g + e) * (256 * (m >> 1) + b)) & 2047) * (1.0f / 2048);
            float c1 = cosr(a1), s1 = sinr(a1);
            Nb[e] = (m & 1) ? pk_b(s1, c1) : pk_b(c1, -s1);
        }
        PIN(Nb);
        WAITV(0);
        PIN(xl[0]); PIN(xl[1]);
        ST(19);
        BARRIER();
        ST(5);
        u4 d[2], rec[4];
        const int uo = (tid >> 1) * 8192 + (tid & 1) * 16, us = WS_U + f1 * 1024 + cg * 32;
        d[0] = bld4<SC1>(rw, uo, us); d[1] = bld4<SC1>(rw, uo, us + 128 * 8192);
        poll2(d[0], d[1], rw, uo, us, us + 128 * 8192);
        char* sp = scr + SCR_S + (tid ^ ((tid >> 4) & 1)) * 16;
        *(u4*)sp = d[0]; *(u4*)(sp + 4096) = d[1];
        BARRIER();
        if (b == 0 && tid == 0) __hip_atomic_store((GAS u32*)(ws + WS_EP), tag, __ATOMIC_RELAXED, __HIP_MEMORY_SCOPE_AGENT);
        const int ro_ = (64 * g + m) * 16, rs_ = WS_WA + f1 * 4096;
        if (cg == 0 && w == 0) {
#pragma unroll
            for (int i = 0; i < 4; ++i) rec[i] = bld4<SC1>(rw, ro_ + i * 256, rs_);
        }
        ST(6);
        u2 ld[4], wl[4];
        const int so = (64 * g + m) * 32 + (((w >> 1) ^ (m >> 3)) * 16) + (w & 1) * 8;
#pragma unroll
        for (int q = 0; q < 4; ++q) ld[q] = *(const u2*)(scr + SCR_S + so + 512 * q);
#pragma unroll
        for (int q = 0; q < 4; ++q) wl[q] = *(const u2*)(scr + SCR_S + so + 8192 + 512 * q);
        FENCE();
        auto fwd = [&](u4 A, f4& Xr, f4& Xi) {
            f4 Dre = mma_h(A, Fre, Z4), Dim = mma_h(A, Fim, Z4);
            f2 r0 = fma2(lo2(Dim), ts[0], lo2(Dre) * tc[0]), i0 = fma2(-lo2(Dre), ts[0], lo2(Dim) * tc[0]);
            f2 r1 = fma2(hi2(Dim), ts[1], hi2(Dre) * tc[1]), i1 = fma2(-hi2(Dre), ts[1], hi2(Dim) * tc[1]);
            u4 B = {pk_h(r0[0], i0[0]), pk_h(r0[1], i0[1]), pk_h(r1[0], i1[0]), pk_h(r1[1], i1[1])};
            Xr = mma_h(Fre, B, Z4); Xi = mma_h(Fim, B, Z4);
        };
        f4 Xre[2], Xim[2];
#pragma unroll
        for (int e = 0; e < 2; ++e) fwd((u4){ld[0][e], ld[1][e], ld[2][e], ld[3][e]}, Xre[e], Xim[e]);
        f2 yr[2][2], yi[2][2];
#pragma unroll
        for (int e = 0; e < 2; ++e)
#pragma unroll
            for (int hh = 0; hh < 2; ++hh) {
                f2 wre = {bf_lo(wl[2 * hh][e]), bf_lo(wl[2 * hh + 1][e])}, wim = {bf_hi(wl[2 * hh][e]), bf_hi(wl[2 * hh + 1][e])};
                f2 xr = hh ? hi2(Xre[e]) : lo2(Xre[e]), xi = hh ? hi2(Xim[e]) : lo2(Xim[e]);
                yr[e][hh] = fma2(-xi, wim, xr * wre); yi[e][hh] = fma2(xr, wim, xi * wre);
            }
        if (cg == 0 && w == 0) {
#pragma nounroll
            for (int sp = 0; sp < SPIN_MAX; ++sp) {
                u32 mx = 0;
#pragma unroll
                for (int i = 0; i < 4; ++i) mx = umax(mx, umax(umax(rec[i][0], rec[i][1]), rec[i][2]));
                if (!any_sent(mx)) break;
                FENCE();
#pragma unroll
                for (int i = 0; i < 4; ++i) rec[i] = bld4<SC1>(rw, ro_ + i * 256, rs_);
            }
            f4 Mr, Mi;
            fwd((u4){rec[0][0], rec[1][0], rec[2][0], rec[3][0]}, Mr, Mi);
            f2* sx = (f2*)(scr + SCR_X);
#pragma unroll
            for (int i = 0; i < 4; ++i) sx[m + 16 * (4 * g + i)] = (f2){Mr[i], Mi[i]};
#pragma unroll
            for (int i = 0; i < 4; ++i) {
                f2 xm = sx[((f1 ? 255 : 256) - (m + 16 * (4 * g + i))) & 255];
                float ar = bf_lo(rec[i][1]), ai = bf_hi(rec[i][1]), br = bf_lo(rec[i][2]), bi = bf_hi(rec[i][2]);
                yr[0][i >> 1][i & 1] = Xre[0][i] * ar - Xim[0][i] * ai + xm[0] * br + xm[1] * bi;
                yi[0][i >> 1][i & 1] = Xre[0][i] * ai + Xim[0][i] * ar + xm[0] * bi - xm[1] * br;
            }
        }
        char* pv = scr;
#pragma unroll
        for (int e = 0; e < 2; ++e) {
            u4 Y = {pk_b(yr[e][0][0], yi[e][0][0]), pk_b(yr[e][0][1], yi[e][0][1]), pk_b(yr[e][1][0], yi[e][1][0]), pk_b(yr[e][1][1], yi[e][1][1])};
            f4 Dre = mma_b(Y, Gre, Z4), Dim = mma_b(Y, Gim, Z4);
            f2 r0 = fma2(-lo2(Dim), ts[0], lo2(Dre) * tc[0]), i0 = fma2(lo2(Dre), ts[0], lo2(Dim) * tc[0]);
            f2 r1 = fma2(-hi2(Dim), ts[1], hi2(Dre) * tc[1]), i1 = fma2(hi2(Dre), ts[1], hi2(Dim) * tc[1]);
            u4 A = {pk_b(r0[0], i0[0]), pk_b(r0[1], i0[1]), pk_b(r1[0], i1[0]), pk_b(r1[1], i1[1])};
            f4 Ere = mma_b(A, Gre, Z4), Eim = mma_b(A, Gim, Z4);
            u4 o;
#pragma unroll
            for (int i = 0; i < 4; ++i) o[i] = clean(pk_b(Ere[i], Eim[i]));
            *(u4*)(pv + (2 * w + e) * 1024 + ((m * 4 + g) ^ ((w >> 1) * 4)) * 16) = o;
        }
        BARRIER();
        ST(7);
        {
            const u32* q = (const u32*)(pv + (tid & 1) * 4096 + (((tid >> 1) * 4) ^ ((tid & 1) * 64)));
            const u4 o0 = {q[0], q[256], q[512], q[768]}, o1 = {q[128], q[384], q[640], q[896]};
            SCHED();
            bst4<SC1>(o0, rw, tid * 16, WS_V + b * 8192); bst4<SC1>(o1, rw, tid * 16, WS_V + b * 8192 + 4096);
        }
        WAITV(0);
        ST(8);
        const int vo = (g * 64 + (m >> 1)) * 8192 + (m & 1) * 16, vs = WS_V + w * 8 * 8192 + b * 32;
        u4 v0 = bld4<SC1>(rw, vo, vs), v1 = bld4<SC1>(rw, vo, vs + 32 * 8192);
        poll2(v0, v1, rw, vo, vs, vs + 32 * 8192);
        ST(9);
        u4 z0, z1;
#pragma unroll
        for (int t = 0; t < 4; ++t) {
            f4 D = mma16_b(Nb, (u2){v0[t], v1[t]}, Z4);
            z0[t] = pk_b(D[0], D[1]); z1[t] = pk_b(D[2], D[3]);
        }
        *(u4*)(scr + SCR_X + g * 2048 + w * 256 + m * 16) = z0;
        *(u4*)(scr + SCR_X + g * 2048 + 1024 + w * 256 + m * 16) = z1;
    } else {
        auto announce = [&](int which) {
            if (l < 8) __hip_atomic_store((GAS u32*)(ws + WS_FA) + (which * 2048 + l * 256 + b) * FS, tag, __ATOMIC_RELAXED, __HIP_MEMORY_SCOPE_AGENT);
            FENCE();
        };
        auto all_announced = [&](int which) {
#pragma nounroll
            for (int sp = 0; sp < SPIN_MAX; ++sp) {
                FENCE();
                u4 f;
#pragma unroll
                for (int i = 0; i < 4; ++i) f[i] = __builtin_amdgcn_raw_buffer_load_b32(rw, (l + 64 * i) * 4 * FS, WS_FA + (which * 8192 + (b >> 5) * 1024) * FS, SC1);
                if (__builtin_amdgcn_ballot_w64(f[0] != tag || f[1] != tag || f[2] != tag || f[3] != tag) == 0) break;
                __builtin_amdgcn_s_sleep(4);
            }
        };
        FENCE();
        ST(2);
        tag = __builtin_amdgcn_readfirstlane(epoch) + 1u;
        if (w == 6) WAITV(2); else if (w == 5) WAITV(1); else if (w == 7 && VPOS == 0) WAITV(8); else WAITV(0);
        if (l < 2) __hip_atomic_store((GAS u32*)(ws + WS_FA) + ((2 * (w - 4) + l) * 256 + b) * FS, tag, __ATOMIC_RELAXED, __HIP_MEMORY_SCOPE_AGENT);
        FENCE();
        ST(18);
#pragma unroll
        for (int jj = 0; jj < 7; ++jj) {
            const int cj_ = (4 * jj) % 14, rj = 18 * jj + (4 * jj) / 14;
            w1o[jj] = (row0 + rj) * 3584 + (cch0 + cj_) * 16 + (cch0 + cj_ >= 14 ? 3584 - 224 : 0);
            PIN(w1o[jj]);
        }
        BARRIER();
        if (w == 6) {
            float re = bf_lo(sr_), im = bf_lo(si_);
            re += dpp<0x128>(re); im -= dpp<0x128>(im);
            u32 a = __builtin_bit_cast(u32, re), a2 = a, c = __builtin_bit_cast(u32, im), c2 = c;
            asm("" : "+v"(a2), "+v"(c2));
            auto pr = __builtin_amdgcn_permlane16_swap(a, a2, false, false);
            auto pi = __builtin_amdgcn_permlane16_swap(c, c2, false, false);
            const float ar = __builtin_bit_cast(float, (u32)pr[0]), br = __builtin_bit_cast(float, (u32)pr[1]);
            const float ai = __builtin_bit_cast(float, (u32)pi[0]), bi = __builtin_bit_cast(float, (u32)pi[1]);
            if (l < 8) {
                __builtin_amdgcn_raw_buffer_store_b32(clean(pk_b(0.25f * (ar + br), 0.25f * (ai + bi))), rw, l * 4096 + 4, WS_WA + b * 16, SC1);
                __builtin_amdgcn_raw_buffer_store_b32(clean(pk_b(0.25f * (ar - br), 0.25f * (ai - bi))), rw, l * 4096 + 8, WS_WA + b * 16, SC1);
            }
        }
        const int f = f1 + 8 * (tid & 255), k0 = 16 * cg - 8 * (cg & 1);
        const int e = mir ? ((2048 - f) & 2047) * 514 + (504 - k0) * 2 : f * 514 + k0 * 2;
        consts0();
        if (w == 7 && VPOS == 0) { WAITV(0); announce(1); }
        __builtin_amdgcn_s_sleep(GA_SLEEP);
        const u4 fa = bld4<0>(mkrs((cg & 1) ? wi : wr, 1052672), e, 0);
        FENCE();
        __builtin_amdgcn_s_sleep(GB_SLEEP);
        if (w == 4) all_announced(0);
        const u4 fb = bld4<0>(mkrs((cg & 1) ? wr : wi, 1052672), e, 0);
        xl[0] = *(const u2*)((const char*)x + xoff); xl[1] = *(const u2*)((const char*)x + xoff + 512);
        FENCE();
        ST(4);
        BARRIER();
        {
            u32 o[8];
            const u4 fr = (cg & 1) ? fb : fa, fi = (cg & 1) ? fa : fb;
#pragma unroll
            for (int c = 0; c < 8; ++c)
                o[c] = mir ? __builtin_amdgcn_perm(fi[3 - (c >> 1)], fr[3 - (c >> 1)], (c & 1) ? 0x05040100u : 0x07060302u) ^ 0x80000000u
                           : __builtin_amdgcn_perm(fi[c >> 1], fr[c >> 1], (c & 1) ? 0x07060302u : 0x05040100u);
            char* wp = scr + SCR_S + 8192 + (tid & 255) * 32;
            *(u4*)(wp + (m & 8) * 2) = (u4){o[0], o[1], o[2], o[3]};
            *(u4*)(wp + ((m & 8) * 2 ^ 16)) = (u4){o[4], o[5], o[6], o[7]};
        }
        FENCE();
        if (w == 7 && VPOS == 1) {
#pragma unroll
            for (int i = 0; i < 8; ++i) bst4<SC1>(SENT4, rw, l * 16 + (i & 3) * 1024, WS_V + b * 8192 + (i >> 2) * 4096);
            WAITV(0); announce(1);
        }
        if (LPOS == 1) {
#pragma unroll
        for (int i = 0; i < 2; ++i) bst4<SC1>(SENT4, rw, (l >> 3) * 256 + (l & 7) * 16 + i * 2048, WS_LN2 + (b >> 1) * 16384 + (b & 1) * 128 + (w - 4) * 4096);
        }
        PIN(xl[0]); PIN(xl[1]);
        consts3();
        ST(5);
        BARRIER();
        if (LPOS == 0) {
#pragma unroll
        for (int i = 0; i < 2; ++i) bst4<SC1>(SENT4, rw, (l >> 3) * 256 + (l & 7) * 16 + i * 2048, WS_LN2 + (b >> 1) * 16384 + (b & 1) * 128 + (w - 4) * 4096);
        }
        if (w == 5) all_announced(1);
        if (cg == 0) __builtin_amdgcn_s_sleep(SP_SLEEP);
        if (cg != 0 || SP_BURST) bg(0, BG5);
        if (cg != 0 || SP_BURST) WAITV(BG5); else WAITV(0);
        ST(6);
        BARRIER();
        __builtin_amdgcn_s_sleep(3);
        bg(BG5, BG5b);
        ST(7);
        __builtin_amdgcn_s_sleep(BG_DELAY);
        if (cg == 0 && !SP_BURST) bg(0, BG5);
        bg(BG5b, BG6);
    }
    BARRIER();
    {
        const u32* zp = (const u32*)(scr + SCR_X + w * 1024 + zo);
        u4 A = {zp[0], zp[16], zp[32], zp[48]};
        f4 Dre = mma_b(A, Gre3, Z4), Dim = mma_b(A, Gim3, Z4), Hre = mma_b(A, SGre, Z4), Him = mma_b(A, SGim, Z4);
        if (g == 0) { Dre[0] = Hre[0]; Dim[0] = Him[0]; }
        f2 r0 = fma2(-lo2(Dim), tws[0], lo2(Dre) * twc[0]), i0 = fma2(lo2(Dre), tws[0], lo2(Dim) * twc[0]);
        f2 r1 = fma2(-hi2(Dim), tws[1], hi2(Dre) * twc[1]), i1 = fma2(hi2(Dre), tws[1], hi2(Dim) * twc[1]);
        u4 A2 = {pk_b(r0[0], i0[0]), pk_b(r0[1], i0[1]), pk_b(r1[0], i1[0]), pk_b(r1[1], i1[1])};
        f2 h[4]; u2 hp[2];
#pragma unroll
        for (int t = 0; t < 2; ++t) {
            f4 Yv = mma_b(A2, GA[t], Z4);
            u32 h0 = pk_b2(bf_2(xl[t][0]) + bf_2(pk_b(Yv[0], Yv[1]))), h1 = pk_b2(bf_2(xl[t][1]) + bf_2(pk_b(Yv[2], Yv[3])));
            hp[t] = (u2){h0, h1};
            h[2 * t] = bf_2(h0); h[2 * t + 1] = bf_2(h1);
        }
        const float c = bf_lo(__builtin_amdgcn_readfirstlane(hp[0][0]));
        f2 q2 = {0.f, 0.f}, s2 = {0.f, 0.f};
#pragma unroll
        for (int j = 0; j < 4; ++j) { f2 d = h[j] - c; q2 = fma2(d, d, q2); s2 += d; }
        float s1 = s2[0] + s2[1], sq = q2[0] + q2[1];
        red_lanes<true>(s1, sq);
        s1 *= 1.0f / 512;
        const float inv = ln_inv(__builtin_fmaxf(sq * (1.0f / 512) - s1 * s1, 0.f));
        const float mb = rbf(c + s1);
#pragma unroll
        for (int t = 0; t < 2; ++t)
            *(u2*)(scr + ((8 * t + (m >> 1)) * 4 + g) * 128 + (w ^ (m >> 1)) * 16 + (m & 1) * 8) =
                (u2){clean(pk_b2(bf_2(pk_b2(h[2 * t] - mb)) * inv)), clean(pk_b2(bf_2(pk_b2(h[2 * t + 1] - mb)) * inv))};
        WAITV(0);
        BARRIER();
        u4 lo = *(const u4*)(scr + (tid ^ ((tid >> 5) & 7)) * 16);
        bst4<SC1>(lo, rw, (tid >> 3) * 256 + (tid & 7) * 16, WS_LN2 + (b >> 1) * 16384 + (b & 1) * 128);
        FENCE();
        if (aw && l == 0) __hip_atomic_fetch_add(hw + 6, 1u, __ATOMIC_RELAXED, __HIP_MEMORY_SCOPE_WORKGROUP);
        if (aw) WAITV(0); else __builtin_amdgcn_s_sleep(H_SLEEP);
        bst2<SC1>(hp[0], ro, xoff, 0); bst2<SC1>(hp[1], ro, xoff + 512, 0);
        FENCE();
        ST(10);
    }
    auto stage_out = [&](int r) {
        w2_read(W[r], LDS_SCR + (w & 3) * 8192 + (r & 1) * 4096);
        asm volatile("s_waitcnt lgkmcnt(0)" ::: "memory");
        PIN(W[r][0]); PIN(W[r][1]); PIN(W[r][2]); PIN(W[r][3]);
        if (r + 2 < 7) w2_glds(pw, r + 2, lds0 + LDS_SCR + (w & 3) * 8192 + (r & 1) * 4096);
    };
    auto publish = [&]() {
        u32 prev = 0;
        if (l == 0) prev = __hip_atomic_fetch_add(hw + 4, 1u, __ATOMIC_RELAXED, __HIP_MEMORY_SCOPE_WORKGROUP);
        if (__builtin_amdgcn_readfirstlane(prev) == 7u && l < 2)
            __hip_atomic_store(fl + (l ? 4096 + (b >> 1) * 32 + (b & 1) : (mt1 * 8 + (nt1 >> 1)) * 32 + (nt1 & 1)), tag, __ATOMIC_RELAXED, __HIP_MEMORY_SCOPE_AGENT);
        FENCE();
    };
    {
        const int tg = w & 3;
        char* xch = smem + XCH + tg * 3072;
        const int hoff = WS_H1 + (mt1 * 8 + 2 * tg) * (56 * 1024) + (nt1 >> 1) * 7168;
        if (__builtin_expect(aw, 1)) {
            const int toff = WS_LN2 + (mt1 * 8 + 2 * tg) * 16384;
            LAS char* wb0 = (LAS char*)smem + (4 * g + (m >> 2)) * 224 + (m & 3) * 8;
            LAS char* wb[4];
            int tq[4];
#pragma unroll
            for (int q = 0; q < 4; ++q) { const int qq = (q + rq) & 3; wb[q] = wb0 + qq * 28672; tq[q] = toff + qq * 4096; }
            u4 T[2][16], A[2][7];
            auto ldt = [&](int kk) {
#pragma unroll
                for (int tt = 0; tt < 2; ++tt) T[tt][kk] = bld4<SC1>(rw, l * 16 + (kk & 3) * 1024, tq[kk >> 2] + tt * 16384);
            };
#pragma unroll
            for (int i = 0; i < 7; ++i) A[0][i] = ldw1(wb[0], 0, i);
            if (l == 0) __hip_atomic_fetch_add(hw + 5, 1u, __ATOMIC_RELAXED, __HIP_MEMORY_SCOPE_WORKGROUP);
            ST(11);
#pragma unroll
            for (int kk = 0; kk < G1_DEPTH; ++kk) { ldt(kk); SCHED(); }
            f4 acc[2][7];
#pragma unroll
            for (int tt = 0; tt < 2; ++tt)
#pragma unroll
                for (int i = 0; i < 7; ++i) acc[tt][i] = Z4;
#pragma unroll
            for (int kk = 0; kk < 16; ++kk) {
                if ((kk & 1) == 0) {
                    if (__builtin_expect(any_sent(umax(umax(max4(T[0][kk]), max4(T[1][kk])), umax(max4(T[0][kk + 1]), max4(T[1][kk + 1])))), 0)) {
                        wait_tok(T[0][kk], T[1][kk], rw, l * 16, tq[kk >> 2] + (kk & 3) * 1024);
                        wait_tok(T[0][kk + 1], T[1][kk + 1], rw, l * 16, tq[kk >> 2] + ((kk + 1) & 3) * 1024);
                    }
                    if (kk == 0) ST(12);
                    if (kk == (BG6 / 7) * 4 - 2) lds_wait(hw + 5, 8u);
                }
                if (kk + G1_DEPTH < 16) ldt(kk + G1_DEPTH);
                if (kk < 15) {
#pragma unroll
                    for (int i = 0; i < 7; ++i) A[(kk + 1) & 1][i] = ldw1(wb[(kk + 1) >> 2], (kk + 1) & 3, i);
                }
#pragma unroll
                for (int i = 0; i < 7; ++i)
#pragma unroll
                    for (int tt = 0; tt < 2; ++tt) acc[tt][i] = mma_b(A[kk & 1][i], T[tt][kk], acc[tt][i]);
#pragma unroll
                for (int i = 0; i < 14; ++i) {
                    __builtin_amdgcn_sched_group_barrier(0x8, 1, 0);
                    if (i == 1 || i == 7) __builtin_amdgcn_sched_group_barrier(0x20, 1, 0);
                    __builtin_amdgcn_sched_group_barrier(0x100, 1, 0);
                    if ((kk & 1) && i >= 3) __builtin_amdgcn_sched_group_barrier(0x2, 1, 0);
                }
                if (kk & 1) {} else SCHED();
            }
#pragma unroll
            for (int q = 0; q < 3; ++q) {
                u2 p0 = pk4(acc[1][2 * q]), p1 = pk4(acc[1][2 * q + 1]);
                *(u4*)(xch + q * 1024 + l * 16) = (u4){p0[0], p0[1], p1[0], p1[1]};
            }
            lds_set(hw + tg, 1u);
            ST(13);
#pragma unroll
            for (int q = 0; q < 3; ++q) {
                u2 p0 = pk4(acc[0][2 * q]), p1 = pk4(acc[0][2 * q + 1]);
                bst4<SC1>(silu_pk4((u4){p0[0], p0[1], p1[0], p1[1]}), rw, l * 16, hoff + ((nt1 & 1) * 3 + q) * 1024);
            }
#pragma unroll
            for (int tt = 0; tt < 2; ++tt) { u2 p = pk4(acc[tt][6]); bst2<SC1>(silu_pk2(p[0], p[1]), rw, l * 8, hoff + tt * (56 * 1024) + 6144 + (nt1 & 1) * 512); }
        } else {
            bg(BG6, 28);
            if (l == 0) __hip_atomic_fetch_add(hw + 6, 1u, __ATOMIC_RELAXED, __HIP_MEMORY_SCOPE_WORKGROUP);
            lds_wait(hw + 6, 8u);
            w2_glds(pw, 0, lds0 + LDS_SCR + tg * 8192);
            w2_glds(pw, 1, lds0 + LDS_SCR + tg * 8192 + 4096);
            WAITV(8);
            if (l == 0) __hip_atomic_fetch_add(hw + 5, 1u, __ATOMIC_RELAXED, __HIP_MEMORY_SCOPE_WORKGROUP);
#pragma unroll
            for (int r = 0; r < B_R; ++r) { if (r < 6) WAITV(4); else WAITV(0); stage_out(r); }
            ST(12);
            lds_wait(hw + tg, 1u);
            ST(13);
#pragma unroll
            for (int q = 0; q < 3; ++q)
                bst4<SC1>(silu_pk4(*(const u4*)(xch + q * 1024 + l * 16)), rw, l * 16, hoff + 56 * 1024 + ((nt1 & 1) * 3 + q) * 1024);
        }
        ST(14);
        WAITV(0);
        publish();
        ST(21);
        if (__builtin_expect(aw, 1)) {
            lds_wait(hw + 4, 8u);
#pragma unroll
            for (int ks = 0; ks < 7; ++ks) w2_glds(pw, ks, lds0 + tg * 28672 + ks * 4096);
        } else {
            if (B_R < 7) stage_out(B_R);
#pragma unroll
            for (int r = B_R + 1; r < 7; ++r) { if (r + 1 < 7) WAITV(4); else WAITV(0); stage_out(r); }
        }
    }
    {
        ST(15);
        const int rot = w & 3;
        if (aw) {
            flag_wait(fl + ((l & 4) ? 4096 + (grp * 8 + th * 4 + rot) * 32 : (grp * 8 + (pw ^ ((l & 2) * 2))) * 32) + (l & 1), tag);
            lds_set(hw + 8 + rot, tag);
        } else {
#pragma nounroll
            for (int sp = 0; sp < SPIN_MAX; ++sp) {
                if (__builtin_amdgcn_readfirstlane(__hip_atomic_load(hw + 8 + rot, __ATOMIC_RELAXED, __HIP_MEMORY_SCOPE_WORKGROUP)) == tag) break;
                __builtin_amdgcn_s_sleep(1);
            }
            FENCE();
        }
        ST(16);
        const int toff = WS_H1 + (grp * 8 + th * 4) * (56 * 1024) + pw * 7168;
        int to[4];
#pragma unroll
        for (int tt = 0; tt < 4; ++tt) to[tt] = toff + ((tt + rot) & 3) * (56 * 1024);
        u4 T[7][4];
        auto ldtok = [&](int ks) {
#pragma unroll
            for (int tt = 0; tt < 4; ++tt) {
                if (ks < 6) T[ks][tt] = bld4<SC1>(rw, l * 16, to[tt] + ko[ks] * 1024);
                else {
                    u2 a = bld2<SC1>(rw, l * 8 + 2048, to[tt] + 4096), c = bld2<SC1>(rw, l * 8 + 2560, to[tt] + 4096);
                    T[ks][tt] = (u4){a[0], a[1], c[0], c[1]};
                }
            }
            SCHED();
        };
#pragma unroll
        for (int ks = 0; ks < G2_DEPTH; ++ks) ldtok(ks);
        if (aw) {
#pragma unroll
            for (int ks = 0; ks < 7; ++ks) w2_read(W[ks], rot * 28672 + ks * 4096);
            asm volatile("s_waitcnt lgkmcnt(0)" ::: "memory");
        }
        if (l == 0) __hip_atomic_fetch_add(hw + 7, 1u, __ATOMIC_RELAXED, __HIP_MEMORY_SCOPE_WORKGROUP);
        const int rho = grp * 128 + th * 64 + rot * 16 + m;
        const int ooff = (256 * (rho & 7) + (rho >> 3)) * 1024 + fb * 128 + (w >> 2) * 64 + g * 16;
        const u4 hres = bld4<SC1>(ro, ooff, 0);
        SCHED();
        f4 acc[4][4];
#pragma unroll
        for (int tt = 0; tt < 4; ++tt)
#pragma unroll
            for (int t = 0; t < 4; ++t) acc[tt][t] = Z4;
#pragma unroll
        for (int ks = 0; ks < 7; ++ks) {
            if (ks + G2_DEPTH < 7) { PIN(T[ks][0]); ldtok(ks + G2_DEPTH); }
#pragma unroll
            for (int tt = 0; tt < 4; ++tt)
#pragma unroll
                for (int t = 0; t < 4; ++t) {
                    acc[tt][t] = mma_b(W[ks][t], T[ks][tt], acc[tt][t]);
                }
            SCHED();
        }
        ST(17);
        lds_wait(hw + 7, 8u);
        char* dp = smem + w * 16384 + l * 16;
#pragma unroll
        for (int tt = 1; tt < 4; ++tt)
#pragma unroll
            for (int t = 0; t < 4; ++t) *(f4*)(dp + (((tt + rot) & 3) * 4 + t) * 1024) = acc[tt][t];
        f4 s0, s1;
        if (aw) { *(f4*)(dp + (rot * 4 + 2) * 1024) = acc[0][2]; *(f4*)(dp + (rot * 4 + 3) * 1024) = acc[0][3]; s0 = acc[0][0]; s1 = acc[0][1]; }
        else { *(f4*)(dp + (rot * 4) * 1024) = acc[0][0]; *(f4*)(dp + (rot * 4 + 1) * 1024) = acc[0][1]; s0 = acc[0][2]; s1 = acc[0][3]; }
        BARRIER();
        f4 r0[7], r1[7];
#pragma unroll
        for (int j = 1; j < 8; ++j) {
            const char* rp = smem + (((w + j) & 7) * 16 + rot * 4 + (w >> 2) * 2) * 1024 + l * 16;
            r0[j - 1] = *(const f4*)rp; r1[j - 1] = *(const f4*)(rp + 1024);
        }
        SCHED();
        s0 = ((s0 + r0[0]) + (r0[1] + r0[2])) + ((r0[3] + r0[4]) + (r0[5] + r0[6]));
        s1 = ((s1 + r1[0]) + (r1[1] + r1[2])) + ((r1[3] + r1[4]) + (r1[5] + r1[6]));
        u2 p0 = pk4(s0), p1 = pk4(s1);
        bst4<NT>((u4){pk_b2(bf_2(hres[0]) + bf_2(p0[0])), pk_b2(bf_2(hres[1]) + bf_2(p0[1])),
                           pk_b2(bf_2(hres[2]) + bf_2(p1[0])), pk_b2(bf_2(hres[3]) + bf_2(p1[1]))}, ro, ooff, 0);
    }
    ST_END(ws, 0);
}

extern "C" void kernel_launch(void* const* d_in, const int* in_sizes, int n_in, void* d_out, int out_size,
                              void* d_ws, size_t ws_size, hipStream_t stream) {
    (void)hipFuncSetAttribute((const void*)k_fused, hipFuncAttributeMaxDynamicSharedMemorySize, LDS_B);
    k_fused<<<256, 512, LDS_B, stream>>>((const uint16_t*)d_in[0], (const uint16_t*)d_in[1], (const uint16_t*)d_in[2],
                                         (const uint16_t*)d_in[4], (const uint16_t*)d_in[5], (uint16_t*)d_out, (char*)d_ws);
}
```

```cpp
#include <hip/hip_runtime.h>
#include <hip/hip_bf16.h>
#include <stdint.h>

typedef _Float16 h8 __attribute__((ext_vector_type(8)));
typedef _Float16 h4 __attribute__((ext_vector_type(4)));
typedef _Float16 h2 __attribute__((ext_vector_type(2)));
typedef __bf16 b8 __attribute__((ext_vector_type(8)));
typedef __bf16 b2 __attribute__((ext_vector_type(2)));
typedef short s4 __attribute__((ext_vector_type(4)));
typedef float f4 __attribute__((ext_vector_type(4)));
typedef float f2 __attribute__((ext_vector_type(2)));
typedef unsigned u32;
typedef u32 u4 __attribute__((ext_vector_type(4)));
typedef u32 u2 __attribute__((ext_vector_type(2)));
#define LAS __attribute__((address_space(3)))
#define GAS __attribute__((address_space(1)))
#define DI __device__ __forceinline__
#define FENCE() asm volatile("" ::: "memory")
#define PIN(x) asm volatile("" : "+v"(x))
#define SCHED() __builtin_amdgcn_sched_barrier(0)
#define WAITV(n) asm volatile("s_waitcnt vmcnt(%0)" :: "n"(n) : "memory")
#define BARRIER() do { asm volatile("s_waitcnt lgkmcnt(0)" ::: "memory"); __builtin_amdgcn_s_barrier(); FENCE(); } while (0)
constexpr int SC1 = 16, NT = 2;
constexpr u32 SENT = 0xffffffffu;
constexpr int SPIN_MAX = 1 << 20;

DI u32 pk_h(float a, float b) { f2 v = {a, b}; h2 r = __builtin_convertvector(v, h2); return __builtin_bit_cast(u32, r); }
DI u32 pk_b(float a, float b) { f2 v = {a, b}; b2 r = __builtin_convertvector(v, b2); return __builtin_bit_cast(u32, r); }
DI float bf_lo(u32 v) { return __builtin_bit_cast(float, v << 16); }
DI float bf_hi(u32 v) { return __builtin_bit_cast(float, v & 0xffff0000u); }
DI f2 bf_2(u32 v) { return (f2){bf_lo(v), bf_hi(v)}; }
DI u32 pk_b2(f2 v) { return pk_b(v[0], v[1]); }
DI float rbf(float a) { return bf_lo(pk_b(a, a)); }
DI float cosr(float rev) { return __builtin_amdgcn_cosf(rev); }
DI float sinr(float rev) { return __builtin_amdgcn_sinf(rev); }
DI f2 lo2(f4 v) { return __builtin_shufflevector(v, v, 0, 1); }
DI f2 hi2(f4 v) { return __builtin_shufflevector(v, v, 2, 3); }
DI f2 fma2(f2 a, f2 b, f2 c) { return __builtin_elementwise_fma(a, b, c); }
DI u32 umin(u32 a, u32 b) { return a < b ? a : b; }
DI u32 umax(u32 a, u32 b) { return a > b ? a : b; }
DI u32 clean(u32 v) { return umin(v, SENT - 1); }
DI u32 max4(u4 v) { return umax(umax(umax(v[0], v[1]), v[2]), v[3]); }
#define Z4 ((f4){0, 0, 0, 0})
DI f4 mma_h(u4 a, u4 b, f4 c) { return __builtin_amdgcn_mfma_f32_16x16x32_f16(__builtin_bit_cast(h8, a), __builtin_bit_cast(h8, b), c, 0, 0, 0); }
DI f4 mma_b(u4 a, u4 b, f4 c) { return __builtin_amdgcn_mfma_f32_16x16x32_bf16(__builtin_bit_cast(b8, a), __builtin_bit_cast(b8, b), c, 0, 0, 0); }
DI f4 mma16_b(u2 a, u2 b, f4 c) { return __builtin_amdgcn_mfma_f32_16x16x16bf16_1k(__builtin_bit_cast(s4, a), __builtin_bit_cast(s4, b), c, 0, 0, 0); }
DI f4 mma16_h(u2 a, u2 b, f4 c) { return __builtin_amdgcn_mfma_f32_16x16x16f16(__builtin_bit_cast(h4, a), __builtin_bit_cast(h4, b), c, 0, 0, 0); }
typedef __amdgpu_buffer_rsrc_t rsrc_t;
DI rsrc_t mkrs(const void* p, u32 bytes) { return __builtin_amdgcn_make_buffer_rsrc((void*)p, 0, bytes, 0x00020000); }
template <int AUX> DI u4 bld4(rsrc_t r, int v, int s) { return __builtin_amdgcn_raw_buffer_load_b128(r, v, s, AUX); }
template <int AUX> DI u2 bld2(rsrc_t r, int v, int s) { return __builtin_amdgcn_raw_buffer_load_b64(r, v, s, AUX); }
template <int AUX> DI void bst4(u4 d, rsrc_t r, int v, int s) {
    __builtin_amdgcn_raw_buffer_store_b128(d, r, v, s, AUX);
    asm volatile("s_nop 1" :: "v"(d));
}
template <int AUX> DI void bst2(u2 d, rsrc_t r, int v, int s) { __builtin_amdgcn_raw_buffer_store_b64(d, r, v, s, AUX); }

template <int CTRL> DI float dpp(float v) {
    return __builtin_bit_cast(float, __builtin_amdgcn_update_dpp(0, __builtin_bit_cast(int, v), CTRL, 0xf, 0xf, true));
}
DI float swap_add16(float v) {
    u32 x = __builtin_bit_cast(u32, v), y = x;
    asm("" : "+v"(y));
    auto r = __builtin_amdgcn_permlane16_swap(x, y, false, false);
    return __builtin_bit_cast(float, (u32)r[0]) + __builtin_bit_cast(float, (u32)r[1]);
}
DI float swap_add32(float v) {
    u32 x = __builtin_bit_cast(u32, v), y = x;
    asm("" : "+v"(y));
    auto r = __builtin_amdgcn_permlane32_swap(x, y, false, false);
    return __builtin_bit_cast(float, (u32)r[0]) + __builtin_bit_cast(float, (u32)r[1]);
}
template <bool ALL> DI void red_lanes(float& a, float& b) {
    a += dpp<0xB1>(a); b += dpp<0xB1>(b);
    if (ALL) { a += dpp<0x4E>(a); b += dpp<0x4E>(b); }
    a += dpp<0x124>(a); b += dpp<0x124>(b);
    a += dpp<0x128>(a); b += dpp<0x128>(b);
    a = swap_add16(a); b = swap_add16(b);
    a = swap_add32(a); b = swap_add32(b);
}
DI float ln_inv(float var) { return __builtin_amdgcn_rcpf(rbf(__builtin_amdgcn_sqrtf(rbf(rbf(var) + 1e-5f)))); }


constexpr u32 WS_U = 0;
constexpr u32 WS_V = WS_U + (2u << 20);
constexpr u32 WS_LN2 = WS_V + (2u << 20);
constexpr u32 WS_WA = WS_LN2 + (2u << 20);
constexpr u32 WS_FA = WS_WA + 32768u;
constexpr u32 WS_FLAG = WS_FA + 16384u;
constexpr u32 WS_EP = WS_FLAG + 32768u;
constexpr u32 WS_H1 = WS_WA + (512u << 10);
constexpr u32 WS_END = WS_H1 + 7340032u;

constexpr int BG5 = 10, BG6 = 21;
constexpr int G1_DEPTH = 10, G2_DEPTH = 3;
constexpr int B_R = 5;
constexpr int CODE_LINES = 290, CODE_PF = 10;
constexpr int LDS_SCR = 114688;
constexpr int SCR_X = 8448;
constexpr int SCR_S = SCR_X + 20480;
constexpr int SCR_F = 49024;
constexpr int XCH = 147456;
constexpr int LDS_B = 163840;

DI void glds16(const void* sbase, u32 voff, u32 lds_dst) {
    asm volatile("s_mov_b32 m0, %2\n\ts_nop 0\n\tglobal_load_lds_dwordx4 %0, %1" :: "v"(voff), "s"(sbase), "s"(lds_dst) : "memory");
}
DI bool any_sent(u32 v) { return __builtin_amdgcn_ballot_w64(v == SENT) != 0; }
DI void poll2(u4& a, u4& c, rsrc_t rw, int vo, int sa, int sc) {
#pragma nounroll
    for (int sp = 0; sp < SPIN_MAX; ++sp) {
        const bool ba = max4(a) == SENT, bc = max4(c) == SENT;
        if (__builtin_amdgcn_ballot_w64(ba || bc) == 0) break;
        __builtin_amdgcn_s_sleep(1);
        FENCE();
        if (ba) a = bld4<SC1>(rw, vo, sa);
        if (bc) c = bld4<SC1>(rw, vo, sc);
    }
}
DI void wait_tok(u4& t0, u4& t1, rsrc_t rw, int vo, int so) {
    if (any_sent(umax(max4(t0), max4(t1)))) {
#pragma nounroll
        for (int sp = 0; sp < SPIN_MAX; ++sp) {
            FENCE();
            t0 = bld4<SC1>(rw, vo, so); t1 = bld4<SC1>(rw, vo, so + 16384);
            if (!any_sent(umax(max4(t0), max4(t1)))) break;
        }
    }
}
DI u4 ldw1(LAS char* wb, int kk, int ft) {
    s4 a0 = __builtin_amdgcn_ds_read_tr16_b64_v4i16((LAS s4*)(wb + kk * 32 * 224 + ft * 32));
    s4 a1 = __builtin_amdgcn_ds_read_tr16_b64_v4i16((LAS s4*)(wb + (kk * 32 + 16) * 224 + ft * 32));
    u2 x0 = __builtin_bit_cast(u2, a0), x1 = __builtin_bit_cast(u2, a1);
    return (u4){x0[0], x0[1], x1[0], x1[1]};
}
DI u2 pk4(f4 a) { return (u2){pk_b(a[0], a[1]), pk_b(a[2], a[3])}; }
DI u2 silu_pk2(u32 p0, u32 p1) {
    u32 o0, o1; float a0, a1, a2, a3, t0, t1, t2, t3;
    asm("v_lshlrev_b32 %2, 16, %10\n\tv_and_b32 %3, 0xffff0000, %10\n\tv_lshlrev_b32 %4, 16, %11\n\tv_and_b32 %5, 0xffff0000, %11\n\t"
        "v_mul_f32 %6, %12, %2\n\tv_mul_f32 %7, %12, %3\n\tv_mul_f32 %8, %12, %4\n\tv_mul_f32 %9, %12, %5\n\t"
        "v_exp_f32 %6, %6\n\tv_exp_f32 %7, %7\n\tv_exp_f32 %8, %8\n\tv_exp_f32 %9, %9\n\t"
        "v_add_f32 %6, 1.0, %6\n\tv_add_f32 %7, 1.0, %7\n\tv_add_f32 %8, 1.0, %8\n\tv_add_f32 %9, 1.0, %9\n\t"
        "v_rcp_f32 %6, %6\n\tv_rcp_f32 %7, %7\n\tv_rcp_f32 %8, %8\n\tv_rcp_f32 %9, %9\n\t"
        "v_mul_f32 %2, %2, %6\n\tv_mul_f32 %3, %3, %7\n\tv_mul_f32 %4, %4, %8\n\tv_mul_f32 %5, %5, %9\n\t"
        "v_cvt_pk_bf16_f32 %0, %2, %3\n\tv_cvt_pk_bf16_f32 %1, %4, %5"
        : "=&v"(o0), "=&v"(o1), "=&v"(a0), "=&v"(a1), "=&v"(a2), "=&v"(a3), "=&v"(t0), "=&v"(t1), "=&v"(t2), "=&v"(t3)
        : "v"(p0), "v"(p1), "s"(-1.4426950408889634f));
    return (u2){o0, o1};
}
DI u4 silu_pk4(u4 p) { u2 a = silu_pk2(p[0], p[1]), b = silu_pk2(p[2], p[3]); return (u4){a[0], a[1], b[0], b[1]}; }
DI void lds_set(LAS u32* p, u32 v) {
    asm volatile("s_waitcnt lgkmcnt(0)" ::: "memory");
    __hip_atomic_store(p, v, __ATOMIC_RELAXED, __HIP_MEMORY_SCOPE_WORKGROUP);
}
DI void lds_wait(LAS u32* p, u32 v) {
#pragma nounroll
    for (int sp = 0; sp < SPIN_MAX; ++sp) {
        if (__builtin_amdgcn_readfirstlane(__hip_atomic_load(p, __ATOMIC_RELAXED, __HIP_MEMORY_SCOPE_WORKGROUP)) >= v) break;
        __builtin_amdgcn_s_sleep(1);
    }
    FENCE();
}
DI void flag_wait(GAS u32* p, u32 tag) {
#pragma nounroll
    for (int sp = 0; sp < SPIN_MAX; ++sp) {
        if (__builtin_amdgcn_ballot_w64(__hip_atomic_load(p, __ATOMIC_RELAXED, __HIP_MEMORY_SCOPE_AGENT) != tag) == 0) break;
        __builtin_amdgcn_s_sleep(2);
    }
    FENCE();
}
#define SENT4 ((u4){SENT, SENT, SENT, SENT})

__global__ __launch_bounds__(512, 2) void k_fused(const uint16_t* x, const uint16_t* wr, const uint16_t* wi, const uint16_t* w1,
                                                  const uint16_t* w2, uint16_t* out, char* ws) {
    extern __shared__ __attribute__((aligned(16))) char smem[];
    const int tid = threadIdx.x, l = tid & 63, g = l >> 4, m = l & 15;
    const int w = __builtin_amdgcn_readfirstlane(tid >> 6);
    const int b = (blockIdx.x & 7) * 32 + (blockIdx.x >> 3);
    const int mt1 = (b >> 6) * 4 + ((b >> 3) & 3), nt1 = ((b >> 5) & 1) * 8 + (b & 7);
    const int grp = b >> 4, th = (b >> 3) & 1, fb = b & 7;
    const rsrc_t rw = mkrs(ws, WS_END), ro = mkrs(out, 2097152);
    char* scr = smem + LDS_SCR;
    LAS u32* hw = (LAS u32*)((LAS char*)smem + LDS_SCR + SCR_F);
    const u32 lds0 = (u32)(uintptr_t)(LAS char*)smem;
    const float mf = (float)m, gf = (float)g, sg = (m & 1) ? -1.0f : 1.0f;
    const bool aw = w < 4;
    unsigned long long pc0;
    asm volatile("s_getpc_b64 %0" : "=s"(pc0));
    if (tid >= 448 && l < CODE_PF)
        asm volatile("s_mov_b32 m0, %2\n\ts_nop 0\n\tglobal_load_lds_dword %0, %1"
                     :: "v"(umin((blockIdx.x >> 3) * CODE_PF + l, CODE_LINES - 1) * 128), "s"(pc0), "s"(lds0 + LDS_SCR + SCR_F + 64) : "memory");
    const u32 epoch = __hip_atomic_load((GAS u32*)(ws + WS_EP), __ATOMIC_RELAXED, __HIP_MEMORY_SCOPE_AGENT);
    FENCE();
    u32 xd[8];
    const int f1 = b >> 5, cg = b & 31;
    const bool mir = cg >= 16;
    const rsrc_t rr = mkrs(wr, 1052672), ri = mkrs(wi, 1052672);
    u32 sr_, si_;
    if (aw) {
        const u32* xr = (const u32*)(x + (256 * (2 * w + ((m >> 1) & 1)) + b) * 512) + 8 * g + 2 * (m >> 2) + (m & 1);
#pragma unroll
        for (int j = 0; j < 8; ++j) xd[j] = xr[32 * j];
    } else {
        bst4<SC1>(SENT4, rw, l * 16, WS_U + b * 8192 + (w - 4) * 2048); bst4<SC1>(SENT4, rw, l * 16 + 1024, WS_U + b * 8192 + (w - 4) * 2048);
        bst4<SC1>(SENT4, rw, l < 2 ? l * 4096 : 0x7ffffff0, WS_WA + (w - 4) * 8192 + b * 16);
        FENCE();
        if (w == 7) {
#pragma unroll
            for (int i = 0; i < 8; ++i) bst4<SC1>(SENT4, rw, l * 16 + (i & 3) * 1024, WS_V + b * 8192 + (i >> 2) * 4096);
        }
        if (w == 6) {
            const int c = (l >> 3) & 3, f_ = 8 * b + (l & 7);
            const int e1 = ((c & 1) ? (2048 - f_) & 2047 : f_) * 514 + (c >> 1) * 512;
            sr_ = __builtin_amdgcn_raw_buffer_load_b16(rr, e1, 0, 0); si_ = __builtin_amdgcn_raw_buffer_load_b16(ri, e1, 0, 0);
        }
    }
    FENCE();
    const int zo = (64 * g + (g < 2 ? m : (m + 15) & 15)) * 4;
    const int rq = ((b >> 3) + b) & 3;
    const int row0 = (tid & 255) / 14, cch0 = (tid & 255) - row0 * 14;
    const char* wsrc = (const char*)w1 + nt1 * 224;
    u32 w1o[7];
    auto w1_burst = [&](int i0, int n) {
#pragma unroll
        for (int i = i0; i < i0 + n; ++i) {
            const int q = (i / 7 + rq) & 3, j = i % 7;
            glds16(wsrc + q * (128 * 3584), w1o[j], lds0 + (w - 4) * 1024 + q * 28672 + j * 4096);
        }
    };
    const int xoff = (256 * w + b) * 1024 + m * 32 + g * 8;
    u2 xl[2];
    float c16[4], s16[4];
    f2 twc[2], tws[2];
    u4 Gre3, Gim3, SGre, SGim, GA[2];
    auto consts0 = [&]() {
#pragma unroll
        for (int u = 0; u < 4; ++u) {
            float a = (4.0f * gf + u) * (mf * (1.0f / 16));
            c16[u] = cosr(a); s16[u] = sinr(a);
            twc[u >> 1][u & 1] = cosr(a * (1.0f / 32)); tws[u >> 1][u & 1] = sinr(a * (1.0f / 32));
        }
    };
    auto consts3 = [&]() {
#pragma unroll
        for (int u = 0; u < 4; ++u) {
            const float k = 4.0f * gf + u;
            const bool k0 = u == 0 && g == 0;
            float cc = c16[u], ss = s16[u];
            Gre3[u] = pk_b(cc, -ss); Gim3[u] = pk_b(ss, cc);
            float a2 = (2.0f * k + 1.0f) * (mf * (1.0f / 32));
            u32 sp = pk_b(g < 2 ? 2.f * cc : 2.f * cosr(a2), g < 2 ? -2.f * ss : -2.f * sinr(a2));
            SGre[u] = k0 ? pk_b(1.0f, sg) : (g < 2 ? sp : 0u);
            SGim[u] = g < 2 ? 0u : sp;
            float a4 = k * (mf * (1.0f / 32)), c4 = 2.f * cosr(a4), s4_ = -2.f * sinr(a4);
            GA[0][u] = k0 ? pk_b(1.0f, sg) : pk_b(c4, s4_);
            GA[1][u] = k0 ? pk_b(1.0f, sg) : ((u & 1) ? pk_b(-c4, -s4_) : pk_b(c4, s4_));
        }
        PIN(Gre3); PIN(Gim3); PIN(SGre); PIN(SGim); PIN(GA[0]); PIN(GA[1]); PIN(twc[0]); PIN(twc[1]); PIN(tws[0]); PIN(tws[1]);
    };
    const int pw = (w + 4 * th) & 7;
    GAS u32* fl = (GAS u32*)(ws + WS_FLAG);
    int ko[7];
    {
        const int r6 = (fb < 6 ? fb : fb - 5) + 3 * (grp & 1);
#pragma unroll
        for (int ks = 0; ks < 6; ++ks) { int k = ks + r6; k = k >= 6 ? k - 6 : k; ko[ks] = k >= 6 ? k - 6 : k; }
        ko[6] = 6;
    }
    u4 W[7][4];
    const u32 w2vo = (l >> 3) * 1024 + (((l & 7) ^ (((l >> 4) & 1) << 2)) * 16);
    auto w2_glds = [&](int p_, int ks, u32 dst) {
        const int q = ko[ks];
        const char* src = (const char*)w2 + (224 * p_ + (ks == 6 ? 96 : (q < 3 ? 32 * q : 16 + 32 * q))) * 1024 + fb * 128;
        glds16(src, w2vo, dst); glds16(src + 8192, w2vo, dst + 1024);
        glds16(src + (ks == 6 ? 112 : 16) * 1024, w2vo, dst + 2048); glds16(src + (ks == 6 ? 120 : 24) * 1024, w2vo, dst + 3072);
    };
    LAS char* w2rd = (LAS char*)smem + (4 * g + (m >> 2)) * 128 + (((m & 3) ^ (((m >> 3) & 1) << 2)) * 16);
    auto w2_read = [&](u4 (&f)[4], int off) {
#pragma unroll
        for (int t = 0; t < 4; ++t) {
            LAS char* p = (LAS char*)(uintptr_t)((u32)(uintptr_t)w2rd ^ ((t >> 1) * 64)) + off + (t & 1) * 8;
            u2 x0 = __builtin_bit_cast(u2, __builtin_amdgcn_ds_read_tr16_b64_v4i16((LAS s4*)p));
            u2 x1 = __builtin_bit_cast(u2, __builtin_amdgcn_ds_read_tr16_b64_v4i16((LAS s4*)(p + 2048)));
            f[t] = (u4){x0[0], x0[1], x1[0], x1[1]};
        }
    };
    auto bg = [&](int i0, int i1) { w1_burst(i0, i1 - i0); };
    u32 tag;
    if (__builtin_expect(aw, 1)) {
        if (tid < 8) hw[tid] = 0;
        FENCE();
        {
            u4 FAre, FAim, Fre, Fim, Sre, Sim; u2 Mb; f2 pc[2], ps[2];
            consts0();
            float c0[8], s0[8];
#pragma unroll
            for (int j = 0; j < 8; ++j) {
                float a = (gf + 4.0f * j) * (mf * (1.0f / 32));
                c0[j] = cosr(a); s0[j] = m ? -sinr(a) : ((g & 1) ? -1.0f : 1.0f);
            }
            const float mo = (2.0f * mf + 1.0f) * (1.0f / 32);
#pragma unroll
            for (int u = 0; u < 4; ++u) {
                FAre[u] = pk_h(c0[2 * u], c0[2 * u + 1]); FAim[u] = pk_h(s0[2 * u], s0[2 * u + 1]);
                float cc = c16[u], ss = s16[u];
                Fre[u] = pk_h(cc, ss); Fim[u] = pk_h(-ss, cc);
                float a2 = (4.0f * gf + u) * mo;
                u32 re = pk_h(m < 8 ? cc : cosr(a2), 0.f), im = pk_h(m < 8 ? (m ? -ss : ((u & 1) ? -1.0f : 1.0f)) : -sinr(a2), 0.f);
                Sre[u] = m < 8 ? re : re << 16; Sim[u] = m < 8 ? im : im << 16;
                pc[u & 1][u >> 1] = twc[u >> 1][u & 1]; ps[u & 1][u >> 1] = tws[u >> 1][u & 1];
            }
#pragma unroll
            for (int e = 0; e < 2; ++e) {
                float a = (float)(((m >> 1) * (256 * (2 * g + e) + b)) & 2047) * (1.0f / 2048);
                float cc = cosr(a), ss = sinr(a);
                Mb[e] = (m & 1) ? pk_h(-ss, cc) : pk_h(cc, ss);
            }
            PIN(FAre); PIN(FAim); PIN(Fre); PIN(Fim); PIN(Sre); PIN(Sim); PIN(Mb); PIN(pc[0]); PIN(pc[1]); PIN(ps[0]); PIN(ps[1]);
            SCHED();
            f2 v[8];
#pragma unroll
            for (int j = 0; j < 8; ++j) v[j] = bf_2(xd[j]);
            const u32 x0 = __builtin_amdgcn_readlane(xd[0], 0), x2 = __builtin_amdgcn_readlane(xd[0], 2);
            const float c = bf_lo((m & 2) ? x2 : x0);
            f2 q0 = {0.f, 0.f}, q1 = {0.f, 0.f};
#pragma unroll
            for (int j = 0; j < 8; ++j) { v[j] -= c; if (j & 1) q1 = fma2(v[j], v[j], q1); else q0 = fma2(v[j], v[j], q0); }
            f2 s2 = ((v[0] + v[1]) + (v[2] + v[3])) + ((v[4] + v[5]) + (v[6] + v[7]));
            q0 += q1;
            float s1 = s2[0] + s2[1], sq = q0[0] + q0[1];
            red_lanes<false>(s1, sq);
            s1 *= 1.0f / 512;
            const float inv = ln_inv(__builtin_fmaxf(sq * (1.0f / 512) - s1 * s1, 0.f));
            const float off = (c - rbf(c + s1)) * inv;
#pragma unroll
            for (int j = 0; j < 8; ++j) v[j] = fma2(v[j], (f2){inv, inv}, (f2){off, off});
            u4 B[2];
#pragma unroll
            for (int p = 0; p < 2; ++p) {
                u4 A = {pk_h(v[0][p], v[1][p]), pk_h(v[2][p], v[3][p]), pk_h(v[4][p], v[5][p]), pk_h(v[6][p], v[7][p])};
                f4 Dre = mma_h(A, FAre, Z4), Dim = mma_h(A, FAim, Z4);
#pragma unroll
                for (int r = 0; r < 2; ++r) {
                    f2 re = r ? hi2(Dre) : lo2(Dre), im = r ? hi2(Dim) : lo2(Dim);
                    f2 tr = fma2(im, ps[p], re * pc[p]), ti = fma2(-re, ps[p], im * pc[p]);
                    B[r][p] = pk_h(tr[0], ti[0]); B[r][2 + p] = pk_h(tr[1], ti[1]);
                }
            }
#pragma unroll
            for (int r = 0; r < 2; ++r) {
                f4 Gre = mma_h(Fre, B[r], Z4), Gim = mma_h(Fim, B[r], Z4), Hre = mma_h(Sre, B[r], Z4), Him = mma_h(Sim, B[r], Z4);
#pragma unroll
                for (int i = 0; i < 4; ++i) *(u32*)(scr + (2 * w + r) * 1024 + zo + i * 64) = pk_h(m ? Gre[i] : Hre[i], m ? Gim[i] : Him[i]);
            }
            BARRIER();
            u4 r0 = *(const u4*)(scr + (2 * g) * 1024 + w * 256 + m * 16);
            u4 r1 = *(const u4*)(scr + (2 * g + 1) * 1024 + w * 256 + m * 16);
            u4 o0, o1;
#pragma unroll
            for (int t = 0; t < 4; ++t) {
                f4 D = mma16_h(Mb, (u2){r0[t], r1[t]}, Z4);
                o0[t] = clean(pk_h(D[0], D[1])); o1[t] = clean(pk_h(D[2], D[3]));
            }
            bst4<SC1>(o0, rw, g * 2048 + m * 16, WS_U + b * 8192 + w * 256);
            bst4<SC1>(o1, rw, g * 2048 + m * 16 + 1024, WS_U + b * 8192 + w * 256);
            if (w == 0 && m == 0) {
                __builtin_amdgcn_raw_buffer_store_b32(o0[0], rw, ((8 - 2 * g) & 7) * 4096, WS_WA + b * 16, SC1);
                __builtin_amdgcn_raw_buffer_store_b32(o1[0], rw, (7 - 2 * g) * 4096, WS_WA + b * 16, SC1);
            }
        }
        u4 Fre, Fim, Gre, Gim; u2 Nb; f2 tc[2], ts[2];
#pragma unroll
        for (int u = 0; u < 4; ++u) {
            float cc = c16[u], ss = s16[u];
            Fre[u] = pk_h(0.25f * cc, 0.25f * ss); Fim[u] = pk_h(-0.25f * ss, 0.25f * cc);
            Gre[u] = pk_b(cc * (1.0f / 256), -ss * (1.0f / 256)); Gim[u] = pk_b(ss * (1.0f / 256), cc * (1.0f / 256));
            float a3 = (4.0f * gf + u) * (mf * (1.0f / 256));
            tc[u >> 1][u & 1] = cosr(a3); ts[u >> 1][u & 1] = sinr(a3);
        }
        PIN(Fre); PIN(Fim); PIN(Gre); PIN(Gim); PIN(tc[0]); PIN(tc[1]); PIN(ts[0]); PIN(ts[1]);
        xl[0] = *(const u2*)((const char*)x + xoff); xl[1] = *(const u2*)((const char*)x + xoff + 512);
        FENCE();
        tag = __builtin_amdgcn_readfirstlane(epoch) + 1u;
        consts3();
#pragma unroll
        for (int e = 0; e < 2; ++e) {
            float a1 = (float)(((2 * g + e) * (256 * (m >> 1) + b)) & 2047) * (1.0f / 2048);
            float c1 = cosr(a1), s1 = sinr(a1);
            Nb[e] = (m & 1) ? pk_b(s1, c1) : pk_b(c1, -s1);
        }
        PIN(Nb);
        WAITV(0);
        PIN(xl[0]); PIN(xl[1]);
        BARRIER();
        u4 d[2], rec[4];
        const int uo = (tid >> 1) * 8192 + (tid & 1) * 16, us = WS_U + f1 * 1024 + cg * 32;
        d[0] = bld4<SC1>(rw, uo, us); d[1] = bld4<SC1>(rw, uo, us + 128 * 8192);
        poll2(d[0], d[1], rw, uo, us, us + 128 * 8192);
        char* sp = scr + SCR_S + (tid ^ ((tid >> 4) & 1)) * 16;
        *(u4*)sp = d[0]; *(u4*)(sp + 4096) = d[1];
        BARRIER();
        if (b == 0 && tid == 0) __hip_atomic_store((GAS u32*)(ws + WS_EP), tag, __ATOMIC_RELAXED, __HIP_MEMORY_SCOPE_AGENT);
        const int ro_ = (64 * g + m) * 16, rs_ = WS_WA + f1 * 4096;
        if (cg == 0 && w == 0) {
#pragma unroll
            for (int i = 0; i < 4; ++i) rec[i] = bld4<SC1>(rw, ro_ + i * 256, rs_);
        }
        u2 ld[4], wl[4];
        const int so = (64 * g + m) * 32 + (((w >> 1) ^ (m >> 3)) * 16) + (w & 1) * 8;
#pragma unroll
        for (int q = 0; q < 4; ++q) ld[q] = *(const u2*)(scr + SCR_S + so + 512 * q);
#pragma unroll
        for (int q = 0; q < 4; ++q) wl[q] = *(const u2*)(scr + SCR_S + so + 8192 + 512 * q);
        FENCE();
        auto fwd = [&](u4 A, f4& Xr, f4& Xi) {
            f4 Dre = mma_h(A, Fre, Z4), Dim = mma_h(A, Fim, Z4);
            f2 r0 = fma2(lo2(Dim), ts[0], lo2(Dre) * tc[0]), i0 = fma2(-lo2(Dre), ts[0], lo2(Dim) * tc[0]);
            f2 r1 = fma2(hi2(Dim), ts[1], hi2(Dre) * tc[1]), i1 = fma2(-hi2(Dre), ts[1], hi2(Dim) * tc[1]);
            u4 B = {pk_h(r0[0], i0[0]), pk_h(r0[1], i0[1]), pk_h(r1[0], i1[0]), pk_h(r1[1], i1[1])};
            Xr = mma_h(Fre, B, Z4); Xi = mma_h(Fim, B, Z4);
        };
        f4 Xre[2], Xim[2];
#pragma unroll
        for (int e = 0; e < 2; ++e) fwd((u4){ld[0][e], ld[1][e], ld[2][e], ld[3][e]}, Xre[e], Xim[e]);
        f2 yr[2][2], yi[2][2];
#pragma unroll
        for (int e = 0; e < 2; ++e)
#pragma unroll
            for (int hh = 0; hh < 2; ++hh) {
                f2 wre = {bf_lo(wl[2 * hh][e]), bf_lo(wl[2 * hh + 1][e])}, wim = {bf_hi(wl[2 * hh][e]), bf_hi(wl[2 * hh + 1][e])};
                f2 xr = hh ? hi2(Xre[e]) : lo2(Xre[e]), xi = hh ? hi2(Xim[e]) : lo2(Xim[e]);
                yr[e][hh] = fma2(-xi, wim, xr * wre); yi[e][hh] = fma2(xr, wim, xi * wre);
            }
        if (cg == 0 && w == 0) {
#pragma nounroll
            for (int sp = 0; sp < SPIN_MAX; ++sp) {
                u32 mx = 0;
#pragma unroll
                for (int i = 0; i < 4; ++i) mx = umax(mx, umax(umax(rec[i][0], rec[i][1]), rec[i][2]));
                if (!any_sent(mx)) break;
                FENCE();
#pragma unroll
                for (int i = 0; i < 4; ++i) rec[i] = bld4<SC1>(rw, ro_ + i * 256, rs_);
            }
            f4 Mr, Mi;
            fwd((u4){rec[0][0], rec[1][0], rec[2][0], rec[3][0]}, Mr, Mi);
            f2* sx = (f2*)(scr + SCR_X);
#pragma unroll
            for (int i = 0; i < 4; ++i) sx[m + 16 * (4 * g + i)] = (f2){Mr[i], Mi[i]};
#pragma unroll
            for (int i = 0; i < 4; ++i) {
                f2 xm = sx[((f1 ? 255 : 256) - (m + 16 * (4 * g + i))) & 255];
                float ar = bf_lo(rec[i][1]), ai = bf_hi(rec[i][1]), br = bf_lo(rec[i][2]), bi = bf_hi(rec[i][2]);
                yr[0][i >> 1][i & 1] = Xre[0][i] * ar - Xim[0][i] * ai + xm[0] * br + xm[1] * bi;
                yi[0][i >> 1][i & 1] = Xre[0][i] * ai + Xim[0][i] * ar + xm[0] * bi - xm[1] * br;
            }
        }
        char* pv = scr;
#pragma unroll
        for (int e = 0; e < 2; ++e) {
            u4 Y = {pk_b(yr[e][0][0], yi[e][0][0]), pk_b(yr[e][0][1], yi[e][0][1]), pk_b(yr[e][1][0], yi[e][1][0]), pk_b(yr[e][1][1], yi[e][1][1])};
            f4 Dre = mma_b(Y, Gre, Z4), Dim = mma_b(Y, Gim, Z4);
            f2 r0 = fma2(-lo2(Dim), ts[0], lo2(Dre) * tc[0]), i0 = fma2(lo2(Dre), ts[0], lo2(Dim) * tc[0]);
            f2 r1 = fma2(-hi2(Dim), ts[1], hi2(Dre) * tc[1]), i1 = fma2(hi2(Dre), ts[1], hi2(Dim) * tc[1]);
            u4 A = {pk_b(r0[0], i0[0]), pk_b(r0[1], i0[1]), pk_b(r1[0], i1[0]), pk_b(r1[1], i1[1])};
            f4 Ere = mma_b(A, Gre, Z4), Eim = mma_b(A, Gim, Z4);
            u4 o;
#pragma unroll
            for (int i = 0; i < 4; ++i) o[i] = clean(pk_b(Ere[i], Eim[i]));
            *(u4*)(pv + (2 * w + e) * 1024 + ((m * 4 + g) ^ ((w >> 1) * 4)) * 16) = o;
        }
        BARRIER();
        {
            const u32* q = (const u32*)(pv + (tid & 1) * 4096 + (((tid >> 1) * 4) ^ ((tid & 1) * 64)));
            const u4 o0 = {q[0], q[256], q[512], q[768]}, o1 = {q[128], q[384], q[640], q[896]};
            SCHED();
            bst4<SC1>(o0, rw, tid * 16, WS_V + b * 8192); bst4<SC1>(o1, rw, tid * 16, WS_V + b * 8192 + 4096);
        }
        WAITV(0);
        const int vo = (g * 64 + (m >> 1)) * 8192 + (m & 1) * 16, vs = WS_V + w * 8 * 8192 + b * 32;
        u4 v0 = bld4<SC1>(rw, vo, vs), v1 = bld4<SC1>(rw, vo, vs + 32 * 8192);
        poll2(v0, v1, rw, vo, vs, vs + 32 * 8192);
        u4 z0, z1;
#pragma unroll
        for (int t = 0; t < 4; ++t) {
            f4 D = mma16_b(Nb, (u2){v0[t], v1[t]}, Z4);
            z0[t] = pk_b(D[0], D[1]); z1[t] = pk_b(D[2], D[3]);
        }
        *(u4*)(scr + SCR_X + g * 2048 + w * 256 + m * 16) = z0;
        *(u4*)(scr + SCR_X + g * 2048 + 1024 + w * 256 + m * 16) = z1;
    } else {
        auto announce = [&](int which) {
            if (l < 8) __hip_atomic_store((GAS u32*)(ws + WS_FA) + (which * 2048 + l * 256 + b), tag, __ATOMIC_RELAXED, __HIP_MEMORY_SCOPE_AGENT);
            FENCE();
        };
        auto all_announced = [&](int which) {
#pragma nounroll
            for (int sp = 0; sp < SPIN_MAX; ++sp) {
                FENCE();
                u4 f;
#pragma unroll
                for (int i = 0; i < 4; ++i) f[i] = __builtin_amdgcn_raw_buffer_load_b32(rw, (l + 64 * i) * 4, WS_FA + (which * 8192 + (b >> 5) * 1024), SC1);
                if (__builtin_amdgcn_ballot_w64(f[0] != tag || f[1] != tag || f[2] != tag || f[3] != tag) == 0) break;
                __builtin_amdgcn_s_sleep(4);
            }
        };
        FENCE();
        tag = __builtin_amdgcn_readfirstlane(epoch) + 1u;
        if (w == 6) WAITV(2); else if (w == 7) WAITV(8); else WAITV(0);
        if (l < 2) __hip_atomic_store((GAS u32*)(ws + WS_FA) + ((2 * (w - 4) + l) * 256 + b), tag, __ATOMIC_RELAXED, __HIP_MEMORY_SCOPE_AGENT);
        FENCE();
#pragma unroll
        for (int jj = 0; jj < 7; ++jj) {
            const int cj_ = (4 * jj) % 14, rj = 18 * jj + (4 * jj) / 14;
            w1o[jj] = (row0 + rj) * 3584 + (cch0 + cj_) * 16 + (cch0 + cj_ >= 14 ? 3584 - 224 : 0);
            PIN(w1o[jj]);
        }
        BARRIER();
        if (w == 6) {
            float re = bf_lo(sr_), im = bf_lo(si_);
            re += dpp<0x128>(re); im -= dpp<0x128>(im);
            u32 a = __builtin_bit_cast(u32, re), a2 = a, c = __builtin_bit_cast(u32, im), c2 = c;
            asm("" : "+v"(a2), "+v"(c2));
            auto pr = __builtin_amdgcn_permlane16_swap(a, a2, false, false);
            auto pi = __builtin_amdgcn_permlane16_swap(c, c2, false, false);
            const float ar = __builtin_bit_cast(float, (u32)pr[0]), br = __builtin_bit_cast(float, (u32)pr[1]);
            const float ai = __builtin_bit_cast(float, (u32)pi[0]), bi = __builtin_bit_cast(float, (u32)pi[1]);
            if (l < 8) {
                __builtin_amdgcn_raw_buffer_store_b32(clean(pk_b(0.25f * (ar + br), 0.25f * (ai + bi))), rw, l * 4096 + 4, WS_WA + b * 16, SC1);
                __builtin_amdgcn_raw_buffer_store_b32(clean(pk_b(0.25f * (ar - br), 0.25f * (ai - bi))), rw, l * 4096 + 8, WS_WA + b * 16, SC1);
            }
        }
        const int f = f1 + 8 * (tid & 255), k0 = 16 * cg - 8 * (cg & 1);
        const int e = mir ? ((2048 - f) & 2047) * 514 + (504 - k0) * 2 : f * 514 + k0 * 2;
        consts0();
        if (w == 7) { WAITV(0); announce(1); }
        __builtin_amdgcn_s_sleep(4);
        const u4 fa = bld4<0>(mkrs((cg & 1) ? wi : wr, 1052672), e, 0);
        FENCE();
        __builtin_amdgcn_s_sleep(8);
        if (w == 4) all_announced(0);
        const u4 fb = bld4<0>(mkrs((cg & 1) ? wr : wi, 1052672), e, 0);
        xl[0] = *(const u2*)((const char*)x + xoff); xl[1] = *(const u2*)((const char*)x + xoff + 512);
        FENCE();
        BARRIER();
        {
            u32 o[8];
            const u4 fr = (cg & 1) ? fb : fa, fi = (cg & 1) ? fa : fb;
#pragma unroll
            for (int c = 0; c < 8; ++c)
                o[c] = mir ? __builtin_amdgcn_perm(fi[3 - (c >> 1)], fr[3 - (c >> 1)], (c & 1) ? 0x05040100u : 0x07060302u) ^ 0x80000000u
                           : __builtin_amdgcn_perm(fi[c >> 1], fr[c >> 1], (c & 1) ? 0x07060302u : 0x05040100u);
            char* wp = scr + SCR_S + 8192 + (tid & 255) * 32;
            *(u4*)(wp + (m & 8) * 2) = (u4){o[0], o[1], o[2], o[3]};
            *(u4*)(wp + ((m & 8) * 2 ^ 16)) = (u4){o[4], o[5], o[6], o[7]};
        }
        FENCE();
#pragma unroll
        for (int i = 0; i < 2; ++i) bst4<SC1>(SENT4, rw, (l >> 3) * 256 + (l & 7) * 16 + i * 2048, WS_LN2 + (b >> 1) * 16384 + (b & 1) * 128 + (w - 4) * 4096);
        PIN(xl[0]); PIN(xl[1]);
        consts3();
        BARRIER();
        if (w == 5) all_announced(1);
        if (cg == 0) __builtin_amdgcn_s_sleep(6);
        bg(0, BG5);
        WAITV(BG5);
        BARRIER();
        __builtin_amdgcn_s_sleep(17);
        bg(BG5, BG6);
    }
    BARRIER();
    {
        const u32* zp = (const u32*)(scr + SCR_X + w * 1024 + zo);
        u4 A = {zp[0], zp[16], zp[32], zp[48]};
        f4 Dre = mma_b(A, Gre3, Z4), Dim = mma_b(A, Gim3, Z4), Hre = mma_b(A, SGre, Z4), Him = mma_b(A, SGim, Z4);
        if (g == 0) { Dre[0] = Hre[0]; Dim[0] = Him[0]; }
        f2 r0 = fma2(-lo2(Dim), tws[0], lo2(Dre) * twc[0]), i0 = fma2(lo2(Dre), tws[0], lo2(Dim) * twc[0]);
        f2 r1 = fma2(-hi2(Dim), tws[1], hi2(Dre) * twc[1]), i1 = fma2(hi2(Dre), tws[1], hi2(Dim) * twc[1]);
        u4 A2 = {pk_b(r0[0], i0[0]), pk_b(r0[1], i0[1]), pk_b(r1[0], i1[0]), pk_b(r1[1], i1[1])};
        f2 h[4]; u2 hp[2];
#pragma unroll
        for (int t = 0; t < 2; ++t) {
            f4 Yv = mma_b(A2, GA[t], Z4);
            u32 h0 = pk_b2(bf_2(xl[t][0]) + bf_2(pk_b(Yv[0], Yv[1]))), h1 = pk_b2(bf_2(xl[t][1]) + bf_2(pk_b(Yv[2], Yv[3])));
            hp[t] = (u2){h0, h1};
            h[2 * t] = bf_2(h0); h[2 * t + 1] = bf_2(h1);
        }
        const float c = bf_lo(__builtin_amdgcn_readfirstlane(hp[0][0]));
        f2 q2 = {0.f, 0.f}, s2 = {0.f, 0.f};
#pragma unroll
        for (int j = 0; j < 4; ++j) { f2 d = h[j] - c; q2 = fma2(d, d, q2); s2 += d; }
        float s1 = s2[0] + s2[1], sq = q2[0] + q2[1];
        red_lanes<true>(s1, sq);
        s1 *= 1.0f / 512;
        const float inv = ln_inv(__builtin_fmaxf(sq * (1.0f / 512) - s1 * s1, 0.f));
        const float mb = rbf(c + s1);
#pragma unroll
        for (int t = 0; t < 2; ++t)
            *(u2*)(scr + ((8 * t + (m >> 1)) * 4 + g) * 128 + (w ^ (m >> 1)) * 16 + (m & 1) * 8) =
                (u2){clean(pk_b2(bf_2(pk_b2(h[2 * t] - mb)) * inv)), clean(pk_b2(bf_2(pk_b2(h[2 * t + 1] - mb)) * inv))};
        WAITV(0);
        BARRIER();
        u4 lo = *(const u4*)(scr + (tid ^ ((tid >> 5) & 7)) * 16);
        bst4<SC1>(lo, rw, (tid >> 3) * 256 + (tid & 7) * 16, WS_LN2 + (b >> 1) * 16384 + (b & 1) * 128);
        FENCE();
        if (aw && l == 0) __hip_atomic_fetch_add(hw + 6, 1u, __ATOMIC_RELAXED, __HIP_MEMORY_SCOPE_WORKGROUP);
        if (aw) WAITV(0); else __builtin_amdgcn_s_sleep(2);
        bst2<SC1>(hp[0], ro, xoff, 0); bst2<SC1>(hp[1], ro, xoff + 512, 0);
        FENCE();
    }
    auto stage_out = [&](int r) {
        w2_read(W[r], LDS_SCR + (w & 3) * 8192 + (r & 1) * 4096);
        asm volatile("s_waitcnt lgkmcnt(0)" ::: "memory");
        PIN(W[r][0]); PIN(W[r][1]); PIN(W[r][2]); PIN(W[r][3]);
        if (r + 2 < 7) w2_glds(pw, r + 2, lds0 + LDS_SCR + (w & 3) * 8192 + (r & 1) * 4096);
    };
    auto publish = [&]() {
        u32 prev = 0;
        if (l == 0) prev = __hip_atomic_fetch_add(hw + 4, 1u, __ATOMIC_RELAXED, __HIP_MEMORY_SCOPE_WORKGROUP);
        if (__builtin_amdgcn_readfirstlane(prev) == 7u && l < 2)
            __hip_atomic_store(fl + (l ? 4096 + (b >> 1) * 32 + (b & 1) : (mt1 * 8 + (nt1 >> 1)) * 32 + (nt1 & 1)), tag, __ATOMIC_RELAXED, __HIP_MEMORY_SCOPE_AGENT);
        FENCE();
    };
    {
        const int tg = w & 3;
        char* xch = smem + XCH + tg * 3072;
        const int hoff = WS_H1 + (mt1 * 8 + 2 * tg) * (56 * 1024) + (nt1 >> 1) * 7168;
        if (__builtin_expect(aw, 1)) {
            const int toff = WS_LN2 + (mt1 * 8 + 2 * tg) * 16384;
            LAS char* wb0 = (LAS char*)smem + (4 * g + (m >> 2)) * 224 + (m & 3) * 8;
            LAS char* wb[4];
            int tq[4];
#pragma unroll
            for (int q = 0; q < 4; ++q) { const int qq = (q + rq) & 3; wb[q] = wb0 + qq * 28672; tq[q] = toff + qq * 4096; }
            u4 T[2][16], A[2][7];
            auto ldt = [&](int kk) {
#pragma unroll
                for (int tt = 0; tt < 2; ++tt) T[tt][kk] = bld4<SC1>(rw, l * 16 + (kk & 3) * 1024, tq[kk >> 2] + tt * 16384);
            };
#pragma unroll
            for (int i = 0; i < 7; ++i) A[0][i] = ldw1(wb[0], 0, i);
            if (l == 0) __hip_atomic_fetch_add(hw + 5, 1u, __ATOMIC_RELAXED, __HIP_MEMORY_SCOPE_WORKGROUP);
#pragma unroll
            for (int kk = 0; kk < G1_DEPTH; ++kk) { ldt(kk); SCHED(); }
            f4 acc[2][7];
#pragma unroll
            for (int tt = 0; tt < 2; ++tt)
#pragma unroll
                for (int i = 0; i < 7; ++i) acc[tt][i] = Z4;
#pragma unroll
            for (int kk = 0; kk < 16; ++kk) {
                if ((kk & 1) == 0) {
                    if (__builtin_expect(any_sent(umax(umax(max4(T[0][kk]), max4(T[1][kk])), umax(max4(T[0][kk + 1]), max4(T[1][kk + 1])))), 0)) {
                        wait_tok(T[0][kk], T[1][kk], rw, l * 16, tq[kk >> 2] + (kk & 3) * 1024);
                        wait_tok(T[0][kk + 1], T[1][kk + 1], rw, l * 16, tq[kk >> 2] + ((kk + 1) & 3) * 1024);
                    }
                    if (kk == (BG6 / 7) * 4 - 2) lds_wait(hw + 5, 8u);
                }
                if (kk + G1_DEPTH < 16) ldt(kk + G1_DEPTH);
                if (kk < 15) {
#pragma unroll
                    for (int i = 0; i < 7; ++i) A[(kk + 1) & 1][i] = ldw1(wb[(kk + 1) >> 2], (kk + 1) & 3, i);
                }
#pragma unroll
                for (int i = 0; i < 7; ++i)
#pragma unroll
                    for (int tt = 0; tt < 2; ++tt) acc[tt][i] = mma_b(A[kk & 1][i], T[tt][kk], acc[tt][i]);
#pragma unroll
                for (int i = 0; i < 14; ++i) {
                    __builtin_amdgcn_sched_group_barrier(0x8, 1, 0);
                    if (i == 1 || i == 7) __builtin_amdgcn_sched_group_barrier(0x20, 1, 0);
                    __builtin_amdgcn_sched_group_barrier(0x100, 1, 0);
                    if ((kk & 1) && i >= 3) __builtin_amdgcn_sched_group_barrier(0x2, 1, 0);
                }
                if (kk & 1) {} else SCHED();
            }
#pragma unroll
            for (int q = 0; q < 3; ++q) {
                u2 p0 = pk4(acc[1][2 * q]), p1 = pk4(acc[1][2 * q + 1]);
                *(u4*)(xch + q * 1024 + l * 16) = (u4){p0[0], p0[1], p1[0], p1[1]};
            }
            lds_set(hw + tg, 1u);
#pragma unroll
            for (int q = 0; q < 3; ++q) {
                u2 p0 = pk4(acc[0][2 * q]), p1 = pk4(acc[0][2 * q + 1]);
                bst4<SC1>(silu_pk4((u4){p0[0], p0[1], p1[0], p1[1]}), rw, l * 16, hoff + ((nt1 & 1) * 3 + q) * 1024);
            }
#pragma unroll
            for (int tt = 0; tt < 2; ++tt) { u2 p = pk4(acc[tt][6]); bst2<SC1>(silu_pk2(p[0], p[1]), rw, l * 8, hoff + tt * (56 * 1024) + 6144 + (nt1 & 1) * 512); }
        } else {
            bg(BG6, 28);
            if (l == 0) __hip_atomic_fetch_add(hw + 6, 1u, __ATOMIC_RELAXED, __HIP_MEMORY_SCOPE_WORKGROUP);
            lds_wait(hw + 6, 8u);
            w2_glds(pw, 0, lds0 + LDS_SCR + tg * 8192);
            w2_glds(pw, 1, lds0 + LDS_SCR + tg * 8192 + 4096);
            WAITV(8);
            if (l == 0) __hip_atomic_fetch_add(hw + 5, 1u, __ATOMIC_RELAXED, __HIP_MEMORY_SCOPE_WORKGROUP);
#pragma unroll
            for (int r = 0; r < B_R; ++r) { if (r < 6) WAITV(4); else WAITV(0); stage_out(r); }
            lds_wait(hw + tg, 1u);
#pragma unroll
            for (int q = 0; q < 3; ++q)
                bst4<SC1>(silu_pk4(*(const u4*)(xch + q * 1024 + l * 16)), rw, l * 16, hoff + 56 * 1024 + ((nt1 & 1) * 3 + q) * 1024);
        }
        WAITV(0);
        publish();
        if (__builtin_expect(aw, 1)) {
            lds_wait(hw + 4, 8u);
#pragma unroll
            for (int ks = 0; ks < 7; ++ks) w2_glds(pw, ks, lds0 + tg * 28672 + ks * 4096);
        } else {
            if (B_R < 7) stage_out(B_R);
#pragma unroll
            for (int r = B_R + 1; r < 7; ++r) { if (r + 1 < 7) WAITV(4); else WAITV(0); stage_out(r); }
        }
    }
    {
        const int rot = w & 3;
        if (aw) {
            flag_wait(fl + ((l & 4) ? 4096 + (grp * 8 + th * 4 + rot) * 32 : (grp * 8 + (pw ^ ((l & 2) * 2))) * 32) + (l & 1), tag);
            lds_set(hw + 8 + rot, tag);
        } else {
#pragma nounroll
            for (int sp = 0; sp < SPIN_MAX; ++sp) {
                if (__builtin_amdgcn_readfirstlane(__hip_atomic_load(hw + 8 + rot, __ATOMIC_RELAXED, __HIP_MEMORY_SCOPE_WORKGROUP)) == tag) break;
                __builtin_amdgcn_s_sleep(1);
            }
            FENCE();
        }
        const int toff = WS_H1 + (grp * 8 + th * 4) * (56 * 1024) + pw * 7168;
        int to[4];
#pragma unroll
        for (int tt = 0; tt < 4; ++tt) to[tt] = toff + ((tt + rot) & 3) * (56 * 1024);
        u4 T[7][4];
        auto ldtok = [&](int ks) {
#pragma unroll
            for (int tt = 0; tt < 4; ++tt) {
                if (ks < 6) T[ks][tt] = bld4<SC1>(rw, l * 16, to[tt] + ko[ks] * 1024);
                else {
                    u2 a = bld2<SC1>(rw, l * 8 + 2048, to[tt] + 4096), c = bld2<SC1>(rw, l * 8 + 2560, to[tt] + 4096);
                    T[ks][tt] = (u4){a[0], a[1], c[0], c[1]};
                }
            }
            SCHED();
        };
#pragma unroll
        for (int ks = 0; ks < G2_DEPTH; ++ks) ldtok(ks);
        if (aw) {
#pragma unroll
            for (int ks = 0; ks < 7; ++ks) w2_read(W[ks], rot * 28672 + ks * 4096);
            asm volatile("s_waitcnt lgkmcnt(0)" ::: "memory");
        }
        if (l == 0) __hip_atomic_fetch_add(hw + 7, 1u, __ATOMIC_RELAXED, __HIP_MEMORY_SCOPE_WORKGROUP);
        const int rho = grp * 128 + th * 64 + rot * 16 + m;
        const int ooff = (256 * (rho & 7) + (rho >> 3)) * 1024 + fb * 128 + (w >> 2) * 64 + g * 16;
        const u4 hres = bld4<SC1>(ro, ooff, 0);
        SCHED();
        f4 acc[4][4];
#pragma unroll
        for (int tt = 0; tt < 4; ++tt)
#pragma unroll
            for (int t = 0; t < 4; ++t) acc[tt][t] = Z4;
#pragma unroll
        for (int ks = 0; ks < 7; ++ks) {
            if (ks + G2_DEPTH < 7) { PIN(T[ks][0]); ldtok(ks + G2_DEPTH); }
#pragma unroll
            for (int tt = 0; tt < 4; ++tt)
#pragma unroll
                for (int t = 0; t < 4; ++t) {
                    acc[tt][t] = mma_b(W[ks][t], T[ks][tt], acc[tt][t]);
                }
            SCHED();
        }
        lds_wait(hw + 7, 8u);
        char* dp = smem + w * 16384 + l * 16;
#pragma unroll
        for (int tt = 1; tt < 4; ++tt)
#pragma unroll
            for (int t = 0; t < 4; ++t) *(f4*)(dp + (((tt + rot) & 3) * 4 + t) * 1024) = acc[tt][t];
        f4 s0, s1;
        if (aw) { *(f4*)(dp + (rot * 4 + 2) * 1024) = acc[0][2]; *(f4*)(dp + (rot * 4 + 3) * 1024) = acc[0][3]; s0 = acc[0][0]; s1 = acc[0][1]; }
        else { *(f4*)(dp + (rot * 4) * 1024) = acc[0][0]; *(f4*)(dp + (rot * 4 + 1) * 1024) = acc[0][1]; s0 = acc[0][2]; s1 = acc[0][3]; }
        BARRIER();
        f4 r0[7], r1[7];
#pragma unroll
        for (int j = 1; j < 8; ++j) {
            const char* rp = smem + (((w + j) & 7) * 16 + rot * 4 + (w >> 2) * 2) * 1024 + l * 16;
            r0[j - 1] = *(const f4*)rp; r1[j - 1] = *(const f4*)(rp + 1024);
        }
        SCHED();
        s0 = ((s0 + r0[0]) + (r0[1] + r0[2])) + ((r0[3] + r0[4]) + (r0[5] + r0[6]));
        s1 = ((s1 + r1[0]) + (r1[1] + r1[2])) + ((r1[3] + r1[4]) + (r1[5] + r1[6]));
        u2 p0 = pk4(s0), p1 = pk4(s1);
        bst4<NT>((u4){pk_b2(bf_2(hres[0]) + bf_2(p0[0])), pk_b2(bf_2(hres[1]) + bf_2(p0[1])),
                           pk_b2(bf_2(hres[2]) + bf_2(p1[0])), pk_b2(bf_2(hres[3]) + bf_2(p1[1]))}, ro, ooff, 0);
    }
}

extern "C" void kernel_launch(void* const* d_in, const int* in_sizes, int n_in, void* d_out, int out_size,
                              void* d_ws, size_t ws_size, hipStream_t stream) {
    (void)hipFuncSetAttribute((const void*)k_fused, hipFuncAttributeMaxDynamicSharedMemorySize, LDS_B);
    k_fused<<<256, 512, LDS_B, stream>>>((const uint16_t*)d_in[0], (const uint16_t*)d_in[1], (const uint16_t*)d_in[2],
                                         (const uint16_t*)d_in[4], (const uint16_t*)d_in[5], (uint16_t*)d_out, (char*)d_ws);
}
```
